```python
import torch

def _softmax(x, axis=-1):
    return torch.softmax(x, dim=axis)
def _gelu(x):
    return torch.nn.functional.gelu(x, approximate="tanh")
def _silu(x):
    return torch.nn.functional.silu(x)
def _sigmoid(x):
    return torch.sigmoid(x)
def _layernorm(x, axis=-1, eps=1e-5):
    m = x.mean(dim=axis, keepdim=True)
    v = x.var(dim=axis, keepdim=True, unbiased=False)
    return (x - m) / torch.sqrt(v + eps)
def _scatter_add(tgt, idx, src, axis=0):
    return tgt.clone().index_add_(axis, idx, src)
def _segment_sum(src, seg, n):
    out = torch.zeros((n,) + src.shape[1:], dtype=src.dtype)
    return out.index_add_(0, seg, src)
def _pad(x, pw):
    flat = tuple(v for pair in reversed(pw) for v in pair)
    return torch.nn.functional.pad(x, flat)

kernel_name = 'concat_heads__softmax_full_mha__sliding_local_dilated__prompt'

def setup_inputs():

    _w = (lambda a: (lambda t: t.to(torch.bfloat16) if t.is_floating_point() else t)(torch.as_tensor(a)))
    def _A_setup():
        B, S, D = 1, 2048, 448
        return {
            "x": _w((lambda *s: torch.randn(*s, dtype=torch.bfloat16) * 0.2)(B, S, D)),
            "wqkv": _w((lambda *s: torch.randn(*s, dtype=torch.bfloat16) * 0.8)(D, 3 * D)),
            "wo": _w((lambda *s: torch.randn(*s, dtype=torch.bfloat16) * 0.2)(D, D)),
        }
    _A_in = _A_setup()

    def _B_setup():
        B, S, D = 1, 2048, 448
        return {
            "x": _w((lambda *s: torch.randn(*s, dtype=torch.bfloat16) * 0.4)(B, S, D)),
            "wqkv": _w((lambda *s: torch.randn(*s, dtype=torch.bfloat16) * 0.8)(D, 3 * D)),
            "wo": _w((lambda *s: torch.randn(*s, dtype=torch.bfloat16) * 0.1)(D, D)),
        }
    _B_in = _B_setup()

    return {
        "A_x": _A_in["x"],
        "A_wqkv": _A_in["wqkv"],
        "A_wo": _A_in["wo"],
        "B_x": _B_in["x"],
        "B_wqkv": _B_in["wqkv"],
        "B_wo": _B_in["wo"],
    }

def reference(A_x, A_wqkv, A_wo, B_x, B_wqkv, B_wo):
    def _A_ref(x, wqkv, wo):
        B, S, D = x.shape
        H = 16; Dh = D // H
        qkv = (lambda x, *d: x.permute(*d))((x @ wqkv).reshape(B, S, 3, H, Dh), 2, 0, 3, 1, 4)
        q, k, v = qkv[0], qkv[1], qkv[2]
        scale = Dh ** -0.5
        scores = torch.einsum("bhqd,bhkd->bhqk", q, k) * scale
        p = _softmax(scores, axis=-1)
        o = torch.einsum("bhqk,bhkd->bhqd", p, v)
        return (lambda x, *d: x.permute(*d))(o, 0, 2, 1, 3).reshape(B, S, D) @ wo

    def _B_ref(x, wqkv, wo):
        B, S, D = x.shape
        H = 16; Dh = D // H
        W, dil = 8, 2
        qkv = (lambda x, *d: x.permute(*d))((x @ wqkv).reshape(B, S, 3, H, Dh), 2, 0, 3, 1, 4)
        q, k, v = qkv[0], qkv[1], qkv[2]
        scale = Dh ** -0.5
        pos = torch.arange(S)
        off = torch.arange(W) * dil
        idx = pos[:, None] - off[None, :]
        mask = idx >= 0
        idx_c = torch.where(mask, idx, 0).reshape(-1)
        kg = k[:, :, idx_c, :].reshape(B, H, S, W, Dh)
        vg = v[:, :, idx_c, :].reshape(B, H, S, W, Dh)
        logits = torch.einsum("bhsd,bhswd->bhsw", q, kg) * scale
        logits = torch.where(mask[None, None, :, :], logits, -1e9)
        attn = _softmax(logits, axis=-1)
        o = torch.einsum("bhsw,bhswd->bhsd", attn, vg)
        return (lambda x, *d: x.permute(*d))(o, 0, 2, 1, 3).reshape(B, S, D) @ wo

    a = _A_ref(A_x, A_wqkv, A_wo)
    b = _B_ref(B_x, B_wqkv, B_wo)
    return torch.concatenate([a, b], axis=-1)
```

```cpp
#include <hip/hip_runtime.h>
#include <hip/hip_bf16.h>
#include <stdint.h>

#define MIMIC 0

typedef unsigned short u16;
constexpr int S = 2048, D = 448, H = 16, DH = 28, N3 = 1344;

__device__ __forceinline__ float bf2f(u16 v) { return __uint_as_float(((unsigned)v) << 16); }
__device__ __forceinline__ u16 f2bf(float f) { unsigned u = __float_as_uint(f); u = (u + 0x7FFF + ((u >> 16) & 1)) >> 16; return (u16)u; }
__device__ __forceinline__ float rbf(float f) { return bf2f(f2bf(f)); }

__global__ void gemm_naive(const u16* A, const u16* B, u16* C, int M, int N, int K, int ldc) {
    int n = blockIdx.x * 64 + threadIdx.x % 64; int m = blockIdx.y * 4 + threadIdx.x / 64;
    if (n >= N || m >= M) return;
    float acc = 0.f;
    for (int k = 0; k < K; ++k) acc += bf2f(A[m * K + k]) * bf2f(B[k * N + n]);
    C[m * ldc + n] = f2bf(acc);
}

__global__ void attnA(const u16* qkv, u16* o) {
    __shared__ float sc[S];
    __shared__ float red[64];
    int h = blockIdx.y, q = blockIdx.x, t = threadIdx.x;
    float qv[DH];
    for (int d = 0; d < DH; ++d) qv[d] = bf2f(qkv[q * N3 + h * DH + d]);
    const float scale = 0.18898223650461362f;
    float mx = -INFINITY;
    for (int k = t; k < S; k += 64) {
        float s = 0.f;
        for (int d = 0; d < DH; ++d) s += qv[d] * bf2f(qkv[k * N3 + 448 + h * DH + d]);
        s = s * scale;
        sc[k] = s; mx = fmaxf(mx, s);
    }
    red[t] = mx; __syncthreads();
    for (int i = 0; i < 64; ++i) mx = fmaxf(mx, red[i]);
    __syncthreads();
    float sum = 0.f;
    for (int k = t; k < S; k += 64) { float p = __expf(sc[k] - mx); sc[k] = p; sum += p; }
    red[t] = sum; __syncthreads();
    sum = 0.f; for (int i = 0; i < 64; ++i) sum += red[i];
    __syncthreads();
    if (t < DH) {
        float acc = 0.f;
        for (int k = 0; k < S; ++k) {
            float p = sc[k] / sum;
            acc += p * bf2f(qkv[k * N3 + 896 + h * DH + t]);
        }
        o[q * D + h * DH + t] = f2bf(acc);
    }
}

__global__ void attnB(const u16* qkv, u16* o) {
    int idx = blockIdx.x * blockDim.x + threadIdx.x;
    if (idx >= S * H) return;
    int s = idx / H, h = idx % H;
    const float scale = 0.18898223650461362f;
    float lg[8]; float mx = -INFINITY;
    for (int w = 0; w < 8; ++w) {
        int k = s - 2 * w; float a = 0.f;
        if (k >= 0) { for (int d = 0; d < DH; ++d) a += bf2f(qkv[s * N3 + h * DH + d]) * bf2f(qkv[k * N3 + 448 + h * DH + d]);
            a *= scale;
        }
        else a = -1e9f;
        lg[w] = a; mx = fmaxf(mx, a);
    }
    float sum = 0.f;
    for (int w = 0; w < 8; ++w) { lg[w] = __expf(lg[w] - mx); sum += lg[w]; }
    for (int d = 0; d < DH; ++d) {
        float a = 0.f;
        for (int w = 0; w < 8; ++w) { int k = s - 2 * w; if (k < 0) k = 0; float p = lg[w] / sum;
            a += p * bf2f(qkv[k * N3 + 896 + h * DH + d]); }
        o[s * D + h * DH + d] = f2bf(a);
    }
}

extern "C" void kernel_launch(void* const* d_in, const int* in_sizes, int n_in, void* d_out, int out_size, void* d_ws, size_t ws_size, hipStream_t stream) {
    const u16* Ax = (const u16*)d_in[0]; const u16* Aw = (const u16*)d_in[1]; const u16* Ao = (const u16*)d_in[2];
    const u16* Bx = (const u16*)d_in[3]; const u16* Bw = (const u16*)d_in[4]; const u16* Bo = (const u16*)d_in[5];
    u16* out = (u16*)d_out;
    u16* qkvA = (u16*)d_ws; u16* qkvB = qkvA + S * N3; u16* oA = qkvB + S * N3; u16* oB = oA + S * D;
    gemm_naive<<<dim3(N3 / 64, S / 4), 256, 0, stream>>>(Ax, Aw, qkvA, S, N3, D, N3);
    gemm_naive<<<dim3(N3 / 64, S / 4), 256, 0, stream>>>(Bx, Bw, qkvB, S, N3, D, N3);
    attnA<<<dim3(S, H), 64, 0, stream>>>(qkvA, oA);
    attnB<<<(S * H + 255) / 256, 256, 0, stream>>>(qkvB, oB);
    gemm_naive<<<dim3(D / 64, S / 4), 256, 0, stream>>>(oA, Ao, out, S, D, D, 2 * D);
    gemm_naive<<<dim3(D / 64, S / 4), 256, 0, stream>>>(oB, Bo, out + D, S, D, D, 2 * D);
}
```

```cpp
#include <hip/hip_runtime.h>
#include <hip/hip_bf16.h>
#include <stdint.h>

typedef unsigned short u16;
typedef short bf16x8 __attribute__((ext_vector_type(8)));
typedef short s16x4 __attribute__((ext_vector_type(4)));
typedef float f32x4 __attribute__((ext_vector_type(4)));
typedef float f32x16 __attribute__((ext_vector_type(16)));
typedef unsigned u32x4 __attribute__((ext_vector_type(4)));
typedef unsigned u32x2 __attribute__((ext_vector_type(2)));
typedef __attribute__((address_space(3))) const char* lds_cptr;
typedef __attribute__((address_space(3))) char* lds_ptr;

constexpr int S = 2048, D = 448, H = 16, DH = 28, N3 = 1344;
constexpr int TILE_B = 56 * 512;
constexpr size_t QC_BYTES = (size_t)64 * TILE_B;
constexpr size_t V_BYTES = (size_t)16 * 64 * 2048;
constexpr size_t WS_QA = 0, WS_KA = WS_QA + QC_BYTES, WS_VA = WS_KA + QC_BYTES;
constexpr size_t WS_QB = WS_VA + V_BYTES, WS_KB = WS_QB + QC_BYTES, WS_VB = WS_KB + QC_BYTES;
constexpr size_t WS_OA = WS_VB + V_BYTES, WS_OB = WS_OA + QC_BYTES, WS_END = WS_OB + QC_BYTES;
constexpr float QSCALE = 0.18898223650461362f * 1.4426950408889634f;

#define MFMA16(a, b, c) __builtin_amdgcn_mfma_f32_16x16x32_bf16(a, b, c, 0, 0, 0)
#define MFMA32(a, b, c) __builtin_amdgcn_mfma_f32_32x32x16_bf16(a, b, c, 0, 0, 0)

__device__ __forceinline__ unsigned cvtpk(float lo, float hi) {
    typedef float f2 __attribute__((ext_vector_type(2))); typedef __bf16 b2 __attribute__((ext_vector_type(2)));
    f2 v = {lo, hi}; b2 r = __builtin_convertvector(v, b2); return __builtin_bit_cast(unsigned, r);
}
__device__ __forceinline__ void glds16(const void* sbase, unsigned voff, unsigned lds_base) {
    unsigned sv;
    asm volatile("s_mov_b32 %0, m0\n\ts_mov_b32 m0, %3\n\ts_nop 0\n\tglobal_load_lds_dwordx4 %1, %2\n\ts_mov_b32 m0, %0" : "=&s"(sv) : "v"(voff), "s"(sbase), "s"(lds_base) : "memory");
}
__device__ __forceinline__ s16x4 vtr(lds_cptr p) {
    typedef short v4i16_t __attribute__((ext_vector_type(4)));
    return __builtin_bit_cast(s16x4, __builtin_amdgcn_ds_read_tr16_b64_v4i16((__attribute__((address_space(3))) v4i16_t*)p));
}
__device__ __forceinline__ bf16x8 cat8(s16x4 a, s16x4 b) { return (bf16x8){a[0], a[1], a[2], a[3], b[0], b[1], b[2], b[3]}; }
__device__ __forceinline__ bf16x8 ldsrd128(lds_cptr p) { return *(const __attribute__((address_space(3))) bf16x8*)p; }
#define RSRC(p, bytes) __builtin_amdgcn_make_buffer_rsrc((void*)(p), 0, (int)(bytes), 0x00020000)
template <int AUX> __device__ __forceinline__ u32x4 bload128(__amdgpu_buffer_rsrc_t r, unsigned voff, unsigned soff) { return __builtin_amdgcn_raw_buffer_load_b128(r, voff, soff, AUX); }
template <int AUX> __device__ __forceinline__ void bstore128(u32x4 v, __amdgpu_buffer_rsrc_t r, unsigned voff) { __builtin_amdgcn_raw_buffer_store_b128(v, r, voff, 0, AUX); }
template <int AUX> __device__ __forceinline__ void bstore64(u32x2 v, __amdgpu_buffer_rsrc_t r, unsigned voff) { __builtin_amdgcn_raw_buffer_store_b64(v, r, voff, 0, AUX); }

constexpr int STG = 32768;

template <int AUX>
__device__ __forceinline__ void qkv_epilogue(const f32x4 (&aq)[2], const f32x4 (&ak)[2], const f32x4 (&av)[2], char* ws, size_t offQ, size_t offK, size_t offV,
                                             int rt, int ct, int wid, int lane) {
    const int tb = wid >> 2, c = wid & 3, rho = lane >> 4, t16 = lane & 15;
    const int tile = rt * 2 + tb;
    {
        const unsigned off = ((unsigned)(tile * 56 + 8 * ct + 2 * c) * 32u) * 16u + (unsigned)lane * 16u;
        unsigned q0a = cvtpk(aq[0][0] * QSCALE, aq[0][1] * QSCALE), q0b = cvtpk(aq[0][2] * QSCALE, aq[0][3] * QSCALE);
        unsigned q1a = cvtpk(aq[1][0] * QSCALE, aq[1][1] * QSCALE), q1b = cvtpk(aq[1][2] * QSCALE, aq[1][3] * QSCALE);
        auto ra = __builtin_amdgcn_permlane16_swap(q0a, q1a, false, false);
        auto rb = __builtin_amdgcn_permlane16_swap(q0b, q1b, false, false);
        bstore128<AUX>((u32x4){ra[0], rb[0], ra[1], rb[1]}, RSRC(ws + offQ, QC_BYTES), off);
        unsigned k0a = cvtpk(ak[0][0], ak[0][1]), k0b = cvtpk(ak[0][2], ak[0][3]);
        unsigned k1a = cvtpk(ak[1][0], ak[1][1]), k1b = cvtpk(ak[1][2], ak[1][3]);
        auto sa = __builtin_amdgcn_permlane16_swap(k0a, k1a, false, false);
        auto sb = __builtin_amdgcn_permlane16_swap(k0b, k1b, false, false);
        bstore128<AUX>((u32x4){sa[0], sb[0], sa[1], sb[1]}, RSRC(ws + offK, QC_BYTES), off);
    }
    {
        unsigned v0a = cvtpk(av[0][0], av[0][1]), v0b = cvtpk(av[0][2], av[0][3]);
        unsigned v1a = cvtpk(av[1][0], av[1][1]), v1b = cvtpk(av[1][2], av[1][3]);
        auto ra = __builtin_amdgcn_permlane32_swap(v0a, v1a, false, false);
        auto rb = __builtin_amdgcn_permlane32_swap(v0b, v1b, false, false);
        const int n = 64 * ct + 16 * c + t16, h = n / 28, d = n - 28 * h;
        const unsigned off = (unsigned)(h * 64 + tile) * 2048u + (unsigned)(lane >> 4) * 512u + (unsigned)d * 16u;
        const auto rs = RSRC(ws + offV, V_BYTES);
        bstore128<AUX>((u32x4){ra[0], rb[0], ra[1], rb[1]}, rs, off);
        if (d == 27) bstore128<AUX>((u32x4){0x3F803F80u, 0x3F803F80u, 0x3F803F80u, 0x3F803F80u}, rs, off + 16u);
    }
}

template <int AUX>
__device__ __forceinline__ void qkv_tile(const u16* x, const u16* w, char* ws, size_t offQ, size_t offK, size_t offV, int rt, int ct, char* lds) {
    const int tid = threadIdx.x, lane = tid & 63, wid = __builtin_amdgcn_readfirstlane(tid >> 6);
    const unsigned lds0 = (unsigned)(uintptr_t)lds;
    const int drow = 8 * wid + (lane >> 3), dpos = lane & 7;
    const unsigned xoff = (unsigned)drow * 896u + (unsigned)((dpos ^ ((drow >> 1) & 7)) << 4);
    const int f2 = ((drow >> 1) & 1) | (((drow >> 3) & 1) << 1);
    const unsigned woff = (unsigned)drow * 2688u + (unsigned)((((dpos >> 1) ^ f2) * 2 + (dpos & 1)) << 4);
    const char* xb = (const char*)x + (size_t)rt * 64 * 896;
    const char* wb = (const char*)w + (size_t)ct * 128;
#define QKV_ISSUE(st) do { const unsigned sl_ = lds0 + ((st) & 3) * STG + wid * 1024; \
        glds16(xb + (st) * 128, xoff, sl_); \
        glds16(wb + (size_t)(st) * 64 * 2688, woff, sl_ + 8192); \
        glds16(wb + (size_t)(st) * 64 * 2688 + 896, woff, sl_ + 16384); \
        glds16(wb + (size_t)(st) * 64 * 2688 + 1792, woff, sl_ + 24576); } while (0)
    QKV_ISSUE(0); QKV_ISSUE(1); QKV_ISSUE(2);
    const int tb = wid >> 2, c = wid & 3, g = lane >> 4, t16 = lane & 15;
    const unsigned xr0 = (unsigned)(tb * 32 + t16) * 128u + (unsigned)((g ^ (t16 >> 1)) << 4);
    const int q = t16 >> 2, p = lane & 3;
    const int rf2 = ((q >> 1) & 1) | ((g & 1) << 1);
    const unsigned wr0 = 8192u + (unsigned)(8 * g + q) * 128u + (unsigned)((c ^ rf2) * 32) + (unsigned)p * 8u;
    f32x4 aq[2] = {}, ak[2] = {}, av[2] = {};
#pragma unroll
    for (int st = 0; st < 7; ++st) {
        if (st <= 4) asm volatile("s_waitcnt vmcnt(8) lgkmcnt(0)\n\ts_barrier" ::: "memory");
        else if (st == 5) asm volatile("s_waitcnt vmcnt(4) lgkmcnt(0)\n\ts_barrier" ::: "memory");
        else asm volatile("s_waitcnt vmcnt(0) lgkmcnt(0)\n\ts_barrier" ::: "memory");
        if (st + 3 < 7) QKV_ISSUE(st + 3);
        lds_cptr sl = (lds_cptr)lds + (st & 3) * STG;
#pragma unroll
        for (int ks = 0; ks < 2; ++ks) {
            const bf16x8 x0 = ldsrd128(sl + (xr0 ^ (ks * 64))), x1 = ldsrd128(sl + (xr0 ^ (ks * 64)) + 2048);
            lds_cptr wp = sl + wr0 + ks * 4096;
            const bf16x8 wq = cat8(vtr(wp), vtr(wp + 512)), wk = cat8(vtr(wp + 8192), vtr(wp + 8192 + 512)), wv = cat8(vtr(wp + 16384), vtr(wp + 16384 + 512));
            aq[0] = MFMA16(wq, x0, aq[0]); aq[1] = MFMA16(wq, x1, aq[1]);
            ak[0] = MFMA16(wk, x0, ak[0]); ak[1] = MFMA16(wk, x1, ak[1]);
            av[0] = MFMA16(x0, wv, av[0]); av[1] = MFMA16(x1, wv, av[1]);
        }
    }
    qkv_epilogue<AUX>(aq, ak, av, ws, offQ, offK, offV, rt, ct, wid, lane);
}

__global__ void __launch_bounds__(512, 2) k_qkv(const u16* xA, const u16* wA, const u16* xB, const u16* wB, char* ws) {
    extern __shared__ __attribute__((aligned(16))) char lds[];
    int id = blockIdx.x; const bool br = id >= 224; if (br) id -= 224;
    const int rt = id / 7, ct = id % 7;
    if (!br) qkv_tile<0>(xA, wA, ws, WS_QA, WS_KA, WS_VA, rt, ct, lds);
    else qkv_tile<0>(xB, wB, ws, WS_QB, WS_KB, WS_VB, rt, ct, lds);
}

__device__ __forceinline__ bf16x8 asb(u32x4 v) { return __builtin_bit_cast(bf16x8, v); }
__device__ __forceinline__ float max16(const f32x16& s) {
    float a = fmaxf(fmaxf(s[0], s[1]), s[2]), b = fmaxf(fmaxf(s[3], s[4]), s[5]);
    a = fmaxf(fmaxf(a, s[6]), s[7]); b = fmaxf(fmaxf(b, s[8]), s[9]); a = fmaxf(fmaxf(a, s[10]), s[11]); b = fmaxf(fmaxf(b, s[12]), s[13]);
    a = fmaxf(fmaxf(a, s[14]), s[15]); return fmaxf(a, b);
}
__device__ __forceinline__ float xhalf_max(float m) {
    auto r = __builtin_amdgcn_permlane32_swap(__float_as_uint(m), __float_as_uint(m), false, false);
    return fmaxf(__uint_as_float(r[0]), __uint_as_float(r[1]));
}
template <int ODD> __device__ __forceinline__ void patch(u32x4& f0, u32x4& f1, bool hi, unsigned a, unsigned b) {
    if (ODD) { f0[0] = hi ? f0[0] : a; f0[1] = hi ? f0[1] : b; }
    else { f1[2] = hi ? a : f1[2]; f1[3] = hi ? b : f1[3]; }
}
__device__ __forceinline__ void exp_pack(const f32x16& s, u32x4& p0, u32x4& p1) {
    float e[16];
#pragma unroll
    for (int r = 0; r < 16; ++r) e[r] = __builtin_amdgcn_exp2f(s[r]);
    p0 = (u32x4){cvtpk(e[0], e[1]), cvtpk(e[2], e[3]), cvtpk(e[4], e[5]), cvtpk(e[6], e[7])};
    p1 = (u32x4){cvtpk(e[8], e[9]), cvtpk(e[10], e[11]), cvtpk(e[12], e[13]), cvtpk(e[14], e[15])};
}

template <int AUX, int ODD>
__device__ __forceinline__ void attnA_body(char* ws, int h, int rb, char* lds) {
    const int tid = threadIdx.x, lane = tid & 63, wid = __builtin_amdgcn_readfirstlane(tid >> 6);
    const bool hi = lane >= 32;
    const int m0 = (28 * h) >> 3;
    const auto rQ = RSRC(ws + WS_QA, QC_BYTES), rK = RSRC(ws + WS_KA, QC_BYTES), rV = RSRC(ws + WS_VA, V_BYTES);
    const unsigned lo16 = (unsigned)lane * 16u;
    u32x4 qf[4][2];
#pragma unroll
    for (int j = 0; j < 4; ++j) { const unsigned b = (unsigned)((rb * 4 + j) * 56 + m0) * 512u; qf[j][0] = bload128<AUX>(rQ, lo16, b); qf[j][1] = bload128<AUX>(rQ, lo16, b + 1024u); }
    const unsigned kbase = (unsigned)((wid * 8) * 56 + m0) * 512u, vbase = (unsigned)(h * 64 + wid * 8) * 2048u;
    u32x4 kf[2][2], vf[2][2];
    kf[0][0] = bload128<AUX>(rK, lo16, kbase); kf[0][1] = bload128<AUX>(rK, lo16, kbase + 1024u);
    vf[0][0] = bload128<AUX>(rV, lo16, vbase); vf[0][1] = bload128<AUX>(rV, lo16, vbase + 1024u);
    float* mx = (float*)(lds + 131072);
    patch<ODD>(kf[0][0], kf[0][1], hi, 0x3F803F80u, 0u);
    {
        const int j = wid & 3;
        u32x4 a0 = j == 0 ? qf[0][0] : j == 1 ? qf[1][0] : j == 2 ? qf[2][0] : qf[3][0];
        u32x4 a1 = j == 0 ? qf[0][1] : j == 1 ? qf[1][1] : j == 2 ? qf[2][1] : qf[3][1];
        patch<ODD>(a0, a1, hi, 0u, 0u);
        f32x16 s = {}; s = MFMA32(asb(kf[0][0]), asb(a0), s); s = MFMA32(asb(kf[0][1]), asb(a1), s);
        const float m = xhalf_max(max16(s));
        if (!hi) mx[(wid >> 2) * 128 + j * 32 + lane] = m;
    }
    __syncthreads();
    float mref[4];
#pragma unroll
    for (int j = 0; j < 4; ++j) {
        const float m = fmaxf(mx[j * 32 + (lane & 31)], mx[128 + j * 32 + (lane & 31)]);
        const unsigned mh = cvtpk(-m, 0.f) & 0xffffu; const float r = -m - __uint_as_float(mh << 16);
        const unsigned pk = mh | (cvtpk(r, 0.f) << 16);
        patch<ODD>(qf[j][0], qf[j][1], hi, pk, 0u);
        mref[j] = m;
    }
    f32x16 o[4] = {};
#pragma unroll
    for (int t = 0; t < 8; ++t) {
        const int cur = t & 1, nxt = cur ^ 1;
        if (t + 1 < 8) {
            kf[nxt][0] = bload128<AUX>(rK, lo16, kbase + (unsigned)(t + 1) * TILE_B); kf[nxt][1] = bload128<AUX>(rK, lo16, kbase + (unsigned)(t + 1) * TILE_B + 1024u);
            vf[nxt][0] = bload128<AUX>(rV, lo16, vbase + (unsigned)(t + 1) * 2048u); vf[nxt][1] = bload128<AUX>(rV, lo16, vbase + (unsigned)(t + 1) * 2048u + 1024u);
        }
        if (t > 0) patch<ODD>(kf[cur][0], kf[cur][1], hi, 0x3F803F80u, 0u);
#pragma unroll
        for (int j = 0; j < 4; ++j) {
            f32x16 s = {}; s = MFMA32(asb(kf[cur][0]), asb(qf[j][0]), s); s = MFMA32(asb(kf[cur][1]), asb(qf[j][1]), s);
            u32x4 p0, p1; exp_pack(s, p0, p1);
            o[j] = MFMA32(asb(vf[cur][0]), asb(p0), o[j]); o[j] = MFMA32(asb(vf[cur][1]), asb(p1), o[j]);
        }
    }
#pragma unroll
    for (int j = 0; j < 4; ++j)
#pragma unroll
        for (int rq = 0; rq < 4; ++rq)
            *(f32x4*)(lds + ((wid * 4 + j) * 4 + rq) * 1024 + lane * 16) = (f32x4){o[j][4 * rq], o[j][4 * rq + 1], o[j][4 * rq + 2], o[j][4 * rq + 3]};
    __syncthreads();
    {
        const int j = wid >> 1, rq0 = (wid & 1) * 2;
        f32x4 a0 = {}, a1 = {}; float l = 0.f;
#pragma unroll
        for (int w = 0; w < 8; ++w) {
            const char* b = lds + (w * 4 + j) * 4096;
            a0 += *(const f32x4*)(b + rq0 * 1024 + lane * 16); a1 += *(const f32x4*)(b + (rq0 + 1) * 1024 + lane * 16);
            l += *(const float*)(b + 3 * 1024 + (32 + (lane & 31)) * 16);
        }
        const float inv = 1.0f / l;
        const auto rO = RSRC(ws + WS_OA, QC_BYTES);
        const int tile = rb * 4 + j, q = lane & 31;
        {   const int col = 28 * h + 8 * rq0 + (hi ? 4 : 0);
            bstore64<AUX>((u32x2){cvtpk(a0[0] * inv, a0[1] * inv), cvtpk(a0[2] * inv, a0[3] * inv)}, rO, (unsigned)((tile * 56 + (col >> 3)) * 32 + q) * 16u + (unsigned)((col >> 2) & 1) * 8u); }
        if (!(rq0 == 2 && hi)) {
            const int col = 28 * h + 8 * (rq0 + 1) + (hi ? 4 : 0);
            bstore64<AUX>((u32x2){cvtpk(a1[0] * inv, a1[1] * inv), cvtpk(a1[2] * inv, a1[3] * inv)}, rO, (unsigned)((tile * 56 + (col >> 3)) * 32 + q) * 16u + (unsigned)((col >> 2) & 1) * 8u); }
    }
    (void)mref;
}

__global__ void __launch_bounds__(512, 2) k_attnA(char* ws) {
    extern __shared__ __attribute__((aligned(16))) char lds[];
    const int id = blockIdx.x, h = (id & 7) * 2 + ((id >> 3) & 1), rb = id >> 4;
    if (h & 1) attnA_body<0, 1>(ws, h, rb, lds); else attnA_body<0, 0>(ws, h, rb, lds);
}

template <int AUX, int ODD>
__device__ __forceinline__ void attnB_body(char* ws, int h, int rb, int j, int lane) {
    const bool hi = lane >= 32;
    const int m0 = (28 * h) >> 3, T = rb * 4 + j, Tp = T > 0 ? T - 1 : 0;
    const auto rQ = RSRC(ws + WS_QB, QC_BYTES), rK = RSRC(ws + WS_KB, QC_BYTES), rV = RSRC(ws + WS_VB, V_BYTES);
    const unsigned lo16 = (unsigned)lane * 16u;
    u32x4 q0 = bload128<AUX>(rQ, lo16, (unsigned)(T * 56 + m0) * 512u), q1 = bload128<AUX>(rQ, lo16, (unsigned)(T * 56 + m0) * 512u + 1024u);
    u32x4 ka0 = bload128<AUX>(rK, lo16, (unsigned)(Tp * 56 + m0) * 512u), ka1 = bload128<AUX>(rK, lo16, (unsigned)(Tp * 56 + m0) * 512u + 1024u);
    u32x4 kb0 = bload128<AUX>(rK, lo16, (unsigned)(T * 56 + m0) * 512u), kb1 = bload128<AUX>(rK, lo16, (unsigned)(T * 56 + m0) * 512u + 1024u);
    u32x4 va0 = bload128<AUX>(rV, lo16, (unsigned)(h * 64 + Tp) * 2048u), va1 = bload128<AUX>(rV, lo16, (unsigned)(h * 64 + Tp) * 2048u + 1024u);
    u32x4 vb0 = bload128<AUX>(rV, lo16, (unsigned)(h * 64 + T) * 2048u), vb1 = bload128<AUX>(rV, lo16, (unsigned)(h * 64 + T) * 2048u + 1024u);
    patch<ODD>(q0, q1, hi, 0u, 0u); patch<ODD>(ka0, ka1, hi, 0u, 0u); patch<ODD>(kb0, kb1, hi, 0u, 0u);
    f32x16 sa = {}, sb = {};
    sa = MFMA32(asb(ka0), asb(q0), sa); sa = MFMA32(asb(ka1), asb(q1), sa);
    sb = MFMA32(asb(kb0), asb(q0), sb); sb = MFMA32(asb(kb1), asb(q1), sb);
    const int ql = lane & 31;
#pragma unroll
    for (int r = 0; r < 16; ++r) {
        const int kl = (r & 3) + 8 * (r >> 2) + (hi ? 4 : 0);
        const int da = ql + 32 - kl, db = ql - kl;
        const bool va = T > 0 && da <= 14 && !(da & 1), vb = db >= 0 && db <= 14 && !(db & 1);
        sa[r] = va ? sa[r] : -INFINITY; sb[r] = vb ? sb[r] : -INFINITY;
    }
    const float m = xhalf_max(fmaxf(max16(sa), max16(sb)));
#pragma unroll
    for (int r = 0; r < 16; ++r) { sa[r] -= m; sb[r] -= m; }
    u32x4 pa0, pa1, pb0, pb1; exp_pack(sa, pa0, pa1); exp_pack(sb, pb0, pb1);
    f32x16 o = {};
    o = MFMA32(asb(va0), asb(pa0), o); o = MFMA32(asb(va1), asb(pa1), o); o = MFMA32(asb(vb0), asb(pb0), o); o = MFMA32(asb(vb1), asb(pb1), o);
    float l = o[12]; { auto r = __builtin_amdgcn_permlane32_swap(__float_as_uint(l), __float_as_uint(l), false, false); l = __uint_as_float(r[1]); }
    const float inv = 1.0f / l;
    const auto rO = RSRC(ws + WS_OB, QC_BYTES);
#pragma unroll
    for (int rq = 0; rq < 4; ++rq) {
        if (rq == 3 && hi) continue;
        const int col = 28 * h + 8 * rq + (hi ? 4 : 0);
        bstore64<AUX>((u32x2){cvtpk(o[4 * rq] * inv, o[4 * rq + 1] * inv), cvtpk(o[4 * rq + 2] * inv, o[4 * rq + 3] * inv)}, rO, (unsigned)((T * 56 + (col >> 3)) * 32 + ql) * 16u + (unsigned)((col >> 2) & 1) * 8u);
    }
}
__global__ void __launch_bounds__(256) k_attnB(char* ws) {
    const int id = blockIdx.x, h = (id & 7) * 2 + ((id >> 3) & 1), rb = id >> 4;
    const int lane = threadIdx.x & 63, j = __builtin_amdgcn_readfirstlane(threadIdx.x >> 6);
    if (h & 1) attnB_body<0, 1>(ws, h, rb, j, lane); else attnB_body<0, 0>(ws, h, rb, j, lane);
}

__device__ __forceinline__ void oproj_issue_w(const u16* wo, int ct, unsigned lds_base, int wid, int lane) {
    const int drow = 8 * wid + (lane >> 3), dpos = lane & 7;
    const int f2 = ((drow >> 1) & 1) | (((drow >> 3) & 1) << 1);
    const unsigned woff = (unsigned)drow * 896u + (unsigned)((((dpos >> 1) ^ f2) * 2 + (dpos & 1)) << 4);
    const char* wb = (const char*)wo + (size_t)ct * 128;
#pragma unroll
    for (int i = 0; i < 7; ++i) glds16(wb + (size_t)i * 64 * 896, woff, lds_base + i * 8192 + wid * 1024);
}
template <int AUX>
__device__ __forceinline__ void oproj_compute(char* ws, size_t offO, u16* out, int colbase, int rt, int ct, char* ldsw, char* ldsr, int wid, int lane) {
    const int tb = wid & 3, kh = wid >> 2, g = lane >> 4, t16 = lane & 15;
    const auto rO = RSRC(ws + offO, QC_BYTES);
    const int tile = rt * 2 + (tb >> 1);
    const unsigned ooff = (unsigned)g * 512u + (unsigned)((tb & 1) * 16 + t16) * 16u;
    u32x4 of[7];
#pragma unroll
    for (int i = 0; i < 7; ++i) of[i] = bload128<AUX>(rO, ooff, (unsigned)(tile * 56 + 4 * (kh * 7 + i)) * 512u);
    const int q = t16 >> 2, p = lane & 3;
    const int rf2 = ((q >> 1) & 1) | ((g & 1) << 1);
    f32x4 acc[4] = {};
#pragma unroll
    for (int i = 0; i < 7; ++i) {
#pragma unroll
        for (int c = 0; c < 4; ++c) {
            lds_cptr wp = (lds_cptr)ldsw + (unsigned)((kh * 7 + i) * 32 + 8 * g + q) * 128u + (unsigned)((c ^ rf2) * 32) + (unsigned)p * 8u;
            const bf16x8 wf = cat8(vtr(wp), vtr(wp + 512));
            acc[c] = MFMA16(wf, asb(of[i]), acc[c]);
        }
    }
    if (kh == 1) {
#pragma unroll
        for (int c = 0; c < 4; ++c) *(f32x4*)(ldsr + (tb * 4 + c) * 1024 + lane * 16) = acc[c];
    }
    __syncthreads();
    if (kh == 0) {
#pragma unroll
        for (int c = 0; c < 4; ++c) acc[c] += *(const f32x4*)(ldsr + (tb * 4 + c) * 1024 + lane * 16);
        const int rho = lane >> 4;
        const int token = rt * 64 + tb * 16 + t16;
#pragma unroll
        for (int pr = 0; pr < 2; ++pr) {
            unsigned a0 = cvtpk(acc[2 * pr][0], acc[2 * pr][1]), a1 = cvtpk(acc[2 * pr][2], acc[2 * pr][3]);
            unsigned b0 = cvtpk(acc[2 * pr + 1][0], acc[2 * pr + 1][1]), b1 = cvtpk(acc[2 * pr + 1][2], acc[2 * pr + 1][3]);
            auto r0 = __builtin_amdgcn_permlane16_swap(a0, b0, false, false);
            auto r1 = __builtin_amdgcn_permlane16_swap(a1, b1, false, false);
            const int col = colbase + 64 * ct + 16 * (2 * pr + (rho & 1)) + 8 * (rho >> 1);
            *(u32x4*)(out + (size_t)token * 896 + col) = (u32x4){r0[0], r1[0], r0[1], r1[1]};
        }
    }
}
__global__ void __launch_bounds__(512, 2) k_oproj(char* ws, const u16* woA, const u16* woB, u16* out) {
    extern __shared__ __attribute__((aligned(16))) char lds[];
    int id = blockIdx.x; const bool br = id >= 224; if (br) id -= 224;
    const int rt = id / 7, ct = id % 7;
    const int lane = threadIdx.x & 63, wid = __builtin_amdgcn_readfirstlane(threadIdx.x >> 6);
    oproj_issue_w(br ? woB : woA, ct, (unsigned)(uintptr_t)lds, wid, lane);
    asm volatile("s_waitcnt vmcnt(0)\n\ts_barrier" ::: "memory");
    oproj_compute<0>(ws, br ? WS_OB : WS_OA, out, br ? 448 : 0, rt, ct, lds, lds + 57344, wid, lane);
}

extern "C" void kernel_launch(void* const* d_in, const int* in_sizes, int n_in, void* d_out, int out_size, void* d_ws, size_t ws_size, hipStream_t stream) {
    const u16* xA = (const u16*)d_in[0]; const u16* wA = (const u16*)d_in[1]; const u16* woA = (const u16*)d_in[2];
    const u16* xB = (const u16*)d_in[3]; const u16* wB = (const u16*)d_in[4]; const u16* woB = (const u16*)d_in[5];
    char* ws = (char*)d_ws; u16* out = (u16*)d_out;
    hipFuncSetAttribute((const void*)k_qkv, hipFuncAttributeMaxDynamicSharedMemorySize, 4 * STG);
    hipFuncSetAttribute((const void*)k_attnA, hipFuncAttributeMaxDynamicSharedMemorySize, 131072 + 1024);
    hipFuncSetAttribute((const void*)k_oproj, hipFuncAttributeMaxDynamicSharedMemorySize, 57344 + 16384);
    k_qkv<<<448, 512, 4 * STG, stream>>>(xA, wA, xB, wB, ws);
    k_attnA<<<256, 512, 131072 + 1024, stream>>>(ws);
    k_attnB<<<256, 256, 0, stream>>>(ws);
    k_oproj<<<448, 512, 57344 + 16384, stream>>>(ws, woA, woB, out);
}
```

```cpp
#include <hip/hip_runtime.h>
#include <hip/hip_bf16.h>
#include <stdint.h>

typedef unsigned short u16;
typedef unsigned long long u64;
typedef short bf16x8 __attribute__((ext_vector_type(8)));
typedef short s16x4 __attribute__((ext_vector_type(4)));
typedef float f32x4 __attribute__((ext_vector_type(4)));
typedef float f32x16 __attribute__((ext_vector_type(16)));
typedef unsigned u32x4 __attribute__((ext_vector_type(4)));
typedef unsigned u32x2 __attribute__((ext_vector_type(2)));
typedef __attribute__((address_space(3))) const char* lds_cptr;
typedef __attribute__((address_space(1))) u64 gu64;

constexpr int TILE_B = 56 * 512;
constexpr size_t QC_BYTES = (size_t)64 * TILE_B;
constexpr size_t V_BYTES = (size_t)16 * 64 * 2048;
constexpr size_t WS_QA = 0, WS_KA = WS_QA + QC_BYTES, WS_VA = WS_KA + QC_BYTES;
constexpr size_t WS_QB = WS_VA + V_BYTES, WS_KB = WS_QB + QC_BYTES, WS_VB = WS_KB + QC_BYTES;
constexpr size_t WS_OA = WS_VB + V_BYTES, WS_OB = WS_OA + QC_BYTES, WS_FLAGS = WS_OB + QC_BYTES;
constexpr int F1 = 0, F2 = 256, F3 = 512, F4 = 768, F0 = 1024, FEP = 1056;
constexpr float QSCALE = 0.18898223650461362f * 1.4426950408889634f;
constexpr int STG = 32768;
constexpr int LDS_MX = 131072, LDS_BAD = LDS_MX + 1024, LDS_TOTAL = LDS_MX + 2048;
constexpr unsigned SPIN_MAX = 1u << 18;

#define STAMP(i)

#define MFMA16(a, b, c) __builtin_amdgcn_mfma_f32_16x16x32_bf16(a, b, c, 0, 0, 0)
#define MFMA32(a, b, c) __builtin_amdgcn_mfma_f32_32x32x16_bf16(a, b, c, 0, 0, 0)
#define PIN(x) asm volatile("" : "+v"(x))
#define SC1 16

__device__ __forceinline__ unsigned cvtpk(float lo, float hi) {
    typedef float f2 __attribute__((ext_vector_type(2))); typedef __bf16 b2 __attribute__((ext_vector_type(2)));
    f2 v = {lo, hi}; b2 r = __builtin_convertvector(v, b2); return __builtin_bit_cast(unsigned, r);
}
__device__ __forceinline__ void glds16(const void* sbase, unsigned voff, unsigned lds_base) {
    unsigned sv;
    asm volatile("s_mov_b32 %0, m0\n\ts_mov_b32 m0, %3\n\ts_nop 0\n\tglobal_load_lds_dwordx4 %1, %2\n\ts_mov_b32 m0, %0" : "=&s"(sv) : "v"(voff), "s"(sbase), "s"(lds_base) : "memory");
}
__device__ __forceinline__ s16x4 vtr(lds_cptr p) {
    typedef short v4i16_t __attribute__((ext_vector_type(4)));
    return __builtin_bit_cast(s16x4, __builtin_amdgcn_ds_read_tr16_b64_v4i16((__attribute__((address_space(3))) v4i16_t*)p));
}
__device__ __forceinline__ bf16x8 cat8(s16x4 a, s16x4 b) { return (bf16x8){a[0], a[1], a[2], a[3], b[0], b[1], b[2], b[3]}; }
__device__ __forceinline__ bf16x8 ldsrd128(lds_cptr p) { return *(const __attribute__((address_space(3))) bf16x8*)p; }
#define RSRC(p, bytes) __builtin_amdgcn_make_buffer_rsrc((void*)(p), 0, (int)(bytes), 0x00020000)
__device__ __forceinline__ u32x4 bload128(__amdgpu_buffer_rsrc_t r, unsigned voff, unsigned soff) { return __builtin_amdgcn_raw_buffer_load_b128(r, voff, soff, SC1); }
__device__ __forceinline__ void bstore128(u32x4 v, __amdgpu_buffer_rsrc_t r, unsigned voff) { __builtin_amdgcn_raw_buffer_store_b128(v, r, voff, 0, SC1); }
__device__ __forceinline__ void bstore64(u32x2 v, __amdgpu_buffer_rsrc_t r, unsigned voff) { __builtin_amdgcn_raw_buffer_store_b64(v, r, voff, 0, SC1); }
__device__ __forceinline__ bf16x8 asb(u32x4 v) { return __builtin_bit_cast(bf16x8, v); }

__device__ __forceinline__ gu64* flagp(char* ws, int idx) { return (gu64*)(ws + WS_FLAGS + (size_t)idx * 128); }
__device__ __forceinline__ void flag_set(char* ws, int idx, u64 tag) { __hip_atomic_store(flagp(ws, idx), tag, __ATOMIC_RELAXED, __HIP_MEMORY_SCOPE_AGENT); }
__device__ __forceinline__ void flag_wait(char* ws, int idx, u64 tag) {
    gu64* p = flagp(ws, idx);
    for (unsigned spins = 0; spins < SPIN_MAX; ++spins) {
        const u64 v = __hip_atomic_load(p, __ATOMIC_RELAXED, __HIP_MEMORY_SCOPE_AGENT);
        if (__all(v == tag)) break;
        __builtin_amdgcn_s_sleep(1);
    }
}
#define BARRIER() asm volatile("s_waitcnt lgkmcnt(0)\n\ts_barrier" ::: "memory")
#define DRAIN_BARRIER() asm volatile("s_waitcnt vmcnt(0) lgkmcnt(0)\n\ts_barrier" ::: "memory")

__device__ __forceinline__ void qkv_epilogue(const f32x4 (&aq)[2], const f32x4 (&ak)[2], const f32x4 (&av)[2], char* ws, size_t offQ, size_t offK, size_t offV, float qs,
                                             int rt, int ct, int wid, int lane) {
    const int tb = wid >> 2, c = wid & 3, t16 = lane & 15;
    const int tile = rt * 2 + tb;
    {
        const unsigned off = (unsigned)(tile * 56 + 8 * ct + 2 * c) * 512u + (unsigned)lane * 16u;
        unsigned q0a = cvtpk(aq[0][0] * qs, aq[0][1] * qs), q0b = cvtpk(aq[0][2] * qs, aq[0][3] * qs);
        unsigned q1a = cvtpk(aq[1][0] * qs, aq[1][1] * qs), q1b = cvtpk(aq[1][2] * qs, aq[1][3] * qs);
        auto ra = __builtin_amdgcn_permlane16_swap(q0a, q1a, false, false);
        auto rb = __builtin_amdgcn_permlane16_swap(q0b, q1b, false, false);
        bstore128((u32x4){ra[0], rb[0], ra[1], rb[1]}, RSRC(ws + offQ, QC_BYTES), off);
        unsigned k0a = cvtpk(ak[0][0], ak[0][1]), k0b = cvtpk(ak[0][2], ak[0][3]);
        unsigned k1a = cvtpk(ak[1][0], ak[1][1]), k1b = cvtpk(ak[1][2], ak[1][3]);
        auto sa = __builtin_amdgcn_permlane16_swap(k0a, k1a, false, false);
        auto sb = __builtin_amdgcn_permlane16_swap(k0b, k1b, false, false);
        bstore128((u32x4){sa[0], sb[0], sa[1], sb[1]}, RSRC(ws + offK, QC_BYTES), off);
    }
    {
        unsigned v0a = cvtpk(av[0][0], av[0][1]), v0b = cvtpk(av[0][2], av[0][3]);
        unsigned v1a = cvtpk(av[1][0], av[1][1]), v1b = cvtpk(av[1][2], av[1][3]);
        auto ra = __builtin_amdgcn_permlane32_swap(v0a, v1a, false, false);
        auto rb = __builtin_amdgcn_permlane32_swap(v0b, v1b, false, false);
        const int n = 64 * ct + 16 * c + t16, h = n / 28, d = n - 28 * h;
        const unsigned off = (unsigned)(h * 64 + tile) * 2048u + (unsigned)(lane >> 4) * 512u + (unsigned)d * 16u;
        const auto rs = RSRC(ws + offV, V_BYTES);
        bstore128((u32x4){ra[0], rb[0], ra[1], rb[1]}, rs, off);
        if (d == 27) bstore128((u32x4){0x3F803F80u, 0x3F803F80u, 0x3F803F80u, 0x3F803F80u}, rs, off + 16u);
    }
}

__device__ __forceinline__ void qkv_phase(const u16* xA, const u16* wA, const u16* xB, const u16* wB, char* ws, int rt, int ct, u64 tag, char* lds, int wid, int lane) {
    const unsigned lds0 = (unsigned)(uintptr_t)lds;
    const int drow = 8 * wid + (lane >> 3), dpos = lane & 7;
    const unsigned xoff = (unsigned)drow * 896u + (unsigned)((dpos ^ ((drow >> 1) & 7)) << 4);
    const int f2 = ((drow >> 1) & 1) | (((drow >> 3) & 1) << 1);
    const unsigned woff = (unsigned)drow * 2688u + (unsigned)((((dpos >> 1) ^ f2) * 2 + (dpos & 1)) << 4);
    const int rot = ((rt & 7) + 2 * (ct & 3)) % 7;
    const char* xa = (const char*)xA + (size_t)rt * 64 * 896; const char* wa = (const char*)wA + (size_t)ct * 128;
    const char* xb = (const char*)xB + (size_t)rt * 64 * 896; const char* wb = (const char*)wB + (size_t)ct * 128;
#define QKV_ISSUE(g) do { int st_ = ((g) % 7) + rot; st_ = st_ >= 7 ? st_ - 7 : st_; const char* x_ = ((g) < 7 ? xa : xb) + st_ * 128; const char* w_ = ((g) < 7 ? wa : wb) + (size_t)st_ * (64 * 2688); \
        const unsigned sl_ = lds0 + ((g) & 3) * STG + wid * 1024; \
        glds16(x_, xoff, sl_); glds16(w_, woff, sl_ + 8192); glds16(w_ + 896, woff, sl_ + 16384); glds16(w_ + 1792, woff, sl_ + 24576); } while (0)
    QKV_ISSUE(0); QKV_ISSUE(1); QKV_ISSUE(2);
    const int tb = wid >> 2, c = wid & 3, g4 = lane >> 4, t16 = lane & 15;
    const unsigned xr0 = (unsigned)(tb * 32 + t16) * 128u + (unsigned)((g4 ^ (t16 >> 1)) << 4);
    const int q = t16 >> 2, p = lane & 3;
    const int rf2 = ((q >> 1) & 1) | ((g4 & 1) << 1);
    const unsigned wr0 = 8192u + (unsigned)(8 * g4 + q) * 128u + (unsigned)((c ^ rf2) * 32) + (unsigned)p * 8u;
    f32x4 aq[2] = {}, ak[2] = {}, av[2] = {};
#pragma unroll
    for (int g = 0; g < 14; ++g) {
        if (g == 7 || g == 8) asm volatile("s_waitcnt vmcnt(11) lgkmcnt(0)\n\ts_barrier" ::: "memory");
        else if (g <= 11) asm volatile("s_waitcnt vmcnt(8) lgkmcnt(0)\n\ts_barrier" ::: "memory");
        else if (g == 12) asm volatile("s_waitcnt vmcnt(4) lgkmcnt(0)\n\ts_barrier" ::: "memory");
        else asm volatile("s_waitcnt vmcnt(0) lgkmcnt(0)\n\ts_barrier" ::: "memory");
        if (g == 0) STAMP(1);
        if (g == 6) STAMP(2);
        if (g == 9 && threadIdx.x == 0) flag_set(ws, F1 + ct * 32 + rt, tag);
        if (g + 3 < 14) QKV_ISSUE(g + 3);
        lds_cptr sl = (lds_cptr)lds + (g & 3) * STG;
#pragma unroll
        for (int ks = 0; ks < 2; ++ks) {
            const bf16x8 x0 = ldsrd128(sl + (xr0 ^ (ks * 64))), x1 = ldsrd128(sl + (xr0 ^ (ks * 64)) + 2048);
            lds_cptr wp = sl + wr0 + ks * 4096;
            const bf16x8 wq = cat8(vtr(wp), vtr(wp + 512)), wk = cat8(vtr(wp + 8192), vtr(wp + 8192 + 512)), wv = cat8(vtr(wp + 16384), vtr(wp + 16384 + 512));
            aq[0] = MFMA16(wq, x0, aq[0]); aq[1] = MFMA16(wq, x1, aq[1]);
            ak[0] = MFMA16(wk, x0, ak[0]); ak[1] = MFMA16(wk, x1, ak[1]);
            av[0] = MFMA16(x0, wv, av[0]); av[1] = MFMA16(x1, wv, av[1]);
        }
        if (g == 6) {
            qkv_epilogue(aq, ak, av, ws, WS_QA, WS_KA, WS_VA, QSCALE, rt, ct, wid, lane);
            aq[0] = aq[1] = ak[0] = ak[1] = av[0] = av[1] = (f32x4){};
            STAMP(3);
        }
    }
    qkv_epilogue(aq, ak, av, ws, WS_QB, WS_KB, WS_VB, 1.0f, rt, ct, wid, lane);
}

__device__ __forceinline__ float max16(const f32x16& s) {
    float a = fmaxf(fmaxf(s[0], s[1]), s[2]), b = fmaxf(fmaxf(s[3], s[4]), s[5]);
    a = fmaxf(fmaxf(a, s[6]), s[7]); b = fmaxf(fmaxf(b, s[8]), s[9]); a = fmaxf(fmaxf(a, s[10]), s[11]); b = fmaxf(fmaxf(b, s[12]), s[13]);
    a = fmaxf(fmaxf(a, s[14]), s[15]); return fmaxf(a, b);
}
__device__ __forceinline__ float xhalf_max(float m) {
    auto r = __builtin_amdgcn_permlane32_swap(__float_as_uint(m), __float_as_uint(m), false, false);
    return fmaxf(__uint_as_float(r[0]), __uint_as_float(r[1]));
}
template <int ODD> __device__ __forceinline__ void patch(u32x4& f0, u32x4& f1, bool hi, unsigned a, unsigned b) {
    if (ODD) { f0[0] = hi ? f0[0] : a; f0[1] = hi ? f0[1] : b; }
    else { f1[2] = hi ? a : f1[2]; f1[3] = hi ? b : f1[3]; }
}
__device__ __forceinline__ void exp_pack(const f32x16& s, u32x4& p0, u32x4& p1) {
    float e[16];
#pragma unroll
    for (int r = 0; r < 16; ++r) e[r] = __builtin_amdgcn_exp2f(s[r]);
    p0 = (u32x4){cvtpk(e[0], e[1]), cvtpk(e[2], e[3]), cvtpk(e[4], e[5]), cvtpk(e[6], e[7])};
    p1 = (u32x4){cvtpk(e[8], e[9]), cvtpk(e[10], e[11]), cvtpk(e[12], e[13]), cvtpk(e[14], e[15])};
}
__device__ __forceinline__ bool merge_store(const f32x16 (&o)[4], char* ws, int h, int rb, char* lds, int wid, int lane) {
    const bool hi = lane >= 32;
#pragma unroll
    for (int j = 0; j < 4; ++j)
#pragma unroll
        for (int rq = 0; rq < 4; ++rq)
            *(f32x4*)(lds + ((wid * 4 + j) * 4 + rq) * 1024 + lane * 16) = (f32x4){o[j][4 * rq], o[j][4 * rq + 1], o[j][4 * rq + 2], o[j][4 * rq + 3]};
    BARRIER();
    const int j = wid >> 1, rq0 = (wid & 1) * 2;
    f32x4 a0 = {}, a1 = {}; float l = 0.f;
#pragma unroll
    for (int w = 0; w < 8; ++w) {
        const char* b = lds + (w * 4 + j) * 4096;
        a0 += *(const f32x4*)(b + rq0 * 1024 + lane * 16); a1 += *(const f32x4*)(b + (rq0 + 1) * 1024 + lane * 16);
        l += *(const float*)(b + 3 * 1024 + (32 + (lane & 31)) * 16);
    }
    const float inv = 1.0f / l;
    const auto rO = RSRC(ws + WS_OA, QC_BYTES);
    const int tile = rb * 4 + j, q = lane & 31;
    a0 *= inv; a1 *= inv;
    const bool last = rq0 == 2 && hi;
    {   const int col = 28 * h + 8 * rq0 + (hi ? 4 : 0);
        bstore64((u32x2){cvtpk(a0[0], a0[1]), cvtpk(a0[2], a0[3])}, rO, (unsigned)((tile * 56 + (col >> 3)) * 32 + q) * 16u + (unsigned)((col >> 2) & 1) * 8u); }
    if (!last) {
        const int col = 28 * h + 8 * (rq0 + 1) + (hi ? 4 : 0);
        bstore64((u32x2){cvtpk(a1[0], a1[1]), cvtpk(a1[2], a1[3])}, rO, (unsigned)((tile * 56 + (col >> 3)) * 32 + q) * 16u + (unsigned)((col >> 2) & 1) * 8u); }
    const float big = 3.0e38f;
    bool ok = l > 1.0e-18f && l < 1.0e30f;
    ok = ok && fabsf(a0[0]) < big && fabsf(a0[1]) < big && fabsf(a0[2]) < big && fabsf(a0[3]) < big;
    ok = ok && (last || (fabsf(a1[0]) < big && fabsf(a1[1]) < big && fabsf(a1[2]) < big && fabsf(a1[3]) < big));
    return !ok;
}

template <int ODD>
__device__ __forceinline__ void attnA_safe(char* ws, int h, int rb, char* lds, int wid, int lane, f32x16 (&o)[4]) {
    const bool hi = lane >= 32; const int m0 = (28 * h) >> 3;
    const auto rQ = RSRC(ws + WS_QA, QC_BYTES), rK = RSRC(ws + WS_KA, QC_BYTES), rV = RSRC(ws + WS_VA, V_BYTES);
    const unsigned lo16 = (unsigned)lane * 16u;
    float* mx = (float*)(lds + LDS_MX);
#pragma unroll
    for (int j = 0; j < 4; ++j) {
        const unsigned b = (unsigned)((rb * 4 + j) * 56 + m0) * 512u;
        u32x4 q0 = bload128(rQ, lo16, b), q1 = bload128(rQ, lo16, b + 1024u);
        patch<ODD>(q0, q1, hi, 0u, 0u);
        float m = -3.0e38f; f32x16 acc = {};
#pragma unroll 1
        for (int t = 0; t < 8; ++t) {
            const unsigned ko = (unsigned)((wid * 8 + t) * 56 + m0) * 512u, vo = (unsigned)(h * 64 + wid * 8 + t) * 2048u;
            u32x4 k0 = bload128(rK, lo16, ko), k1 = bload128(rK, lo16, ko + 1024u), v0 = bload128(rV, lo16, vo), v1 = bload128(rV, lo16, vo + 1024u);
            patch<ODD>(k0, k1, hi, 0u, 0u);
            f32x16 s = {}; s = MFMA32(asb(k0), asb(q0), s); s = MFMA32(asb(k1), asb(q1), s);
            const float mn = fmaxf(m, xhalf_max(max16(s)));
            const float al = __builtin_amdgcn_exp2f(m - mn); m = mn;
#pragma unroll
            for (int r = 0; r < 16; ++r) { s[r] -= mn; acc[r] *= al; }
            u32x4 p0, p1; exp_pack(s, p0, p1);
            acc = MFMA32(asb(v0), asb(p0), acc); acc = MFMA32(asb(v1), asb(p1), acc);
        }
        BARRIER();
        if (!hi) mx[wid * 32 + lane] = m;
        BARRIER();
        float g = m;
#pragma unroll
        for (int w = 0; w < 8; ++w) g = fmaxf(g, mx[w * 32 + (lane & 31)]);
        const float f = __builtin_amdgcn_exp2f(m - g);
#pragma unroll
        for (int r = 0; r < 16; ++r) acc[r] *= f;
        o[j] = acc;
    }
}

template <int ODD>
__device__ __forceinline__ void attnA_phase(char* ws, int h, int rb, u64 tag, int f2idx, char* lds, int wid, int lane) {
    const bool hi = lane >= 32;
    const int m0 = (28 * h) >> 3;
    const auto rQ = RSRC(ws + WS_QA, QC_BYTES), rK = RSRC(ws + WS_KA, QC_BYTES), rV = RSRC(ws + WS_VA, V_BYTES);
    const unsigned lo16 = (unsigned)lane * 16u;
    u32x4 qf[4][2];
#pragma unroll
    for (int j = 0; j < 4; ++j) { const unsigned b = (unsigned)((rb * 4 + j) * 56 + m0) * 512u; qf[j][0] = bload128(rQ, lo16, b); qf[j][1] = bload128(rQ, lo16, b + 1024u); }
    const unsigned kbase = (unsigned)((wid * 8) * 56 + m0) * 512u, vbase = (unsigned)(h * 64 + wid * 8) * 2048u;
    u32x4 kf[2][2], vf[2][2];
    const int rot = rb & 7;
    {   const unsigned k0 = kbase + (unsigned)rot * TILE_B, v0 = vbase + (unsigned)rot * 2048u, k1 = kbase + (unsigned)((rot + 1) & 7) * TILE_B;
        kf[0][0] = bload128(rK, lo16, k0); kf[0][1] = bload128(rK, lo16, k0 + 1024u);
        vf[0][0] = bload128(rV, lo16, v0); vf[0][1] = bload128(rV, lo16, v0 + 1024u);
        kf[1][0] = bload128(rK, lo16, k1); kf[1][1] = bload128(rK, lo16, k1 + 1024u); }
    float* mx = (float*)(lds + LDS_MX);
    patch<ODD>(kf[0][0], kf[0][1], hi, 0x3F803F80u, 0u);
    {
        const int j = wid & 3;
        u32x4 a0 = j == 0 ? qf[0][0] : j == 1 ? qf[1][0] : j == 2 ? qf[2][0] : qf[3][0];
        u32x4 a1 = j == 0 ? qf[0][1] : j == 1 ? qf[1][1] : j == 2 ? qf[2][1] : qf[3][1];
        patch<ODD>(a0, a1, hi, 0u, 0u);
        f32x16 s = {}; s = MFMA32(asb(kf[0][0]), asb(a0), s); s = MFMA32(asb(kf[0][1]), asb(a1), s);
        const float m = xhalf_max(max16(s));
        if (!hi) mx[(wid >> 2) * 128 + j * 32 + lane] = m;
        if (threadIdx.x == 0) *(int*)(lds + LDS_BAD) = 0;
    }
    BARRIER();
    if (f2idx >= 0 && threadIdx.x == 0) flag_set(ws, f2idx, tag);
    STAMP(6);
#pragma unroll
    for (int j = 0; j < 4; ++j) {
        const float m = fmaxf(mx[j * 32 + (lane & 31)], mx[128 + j * 32 + (lane & 31)]);
        const unsigned mh = cvtpk(-m, 0.f) & 0xffffu; const float r = -m - __uint_as_float(mh << 16);
        const unsigned pk = mh | (cvtpk(r, 0.f) << 16);
        patch<ODD>(qf[j][0], qf[j][1], hi, pk, 0u);
    }
    f32x16 o[4] = {};
#define SLOT(Sc, Pc0, Pc1, Sn, kn0, kn1, qn0, qn1, Op, vp0, vp1, Pp0, Pp1, INJ) do { \
        float e0, e1, e2, e3, e4, e5, e6, e7, e8, e9, e10, e11, e12, e13, e14, e15; \
        asm volatile("v_mfma_f32_32x32x16_bf16 %0, %5, %6, 0\n\tv_exp_f32 %1, %7\n\tv_exp_f32 %2, %8\n\tv_exp_f32 %3, %9\n\tv_exp_f32 %4, %10" \
            : "=&v"(Sn), "=&v"(e0), "=&v"(e1), "=&v"(e2), "=&v"(e3) : "v"(kn0), "v"(qn0), "v"(Sc[0]), "v"(Sc[1]), "v"(Sc[2]), "v"(Sc[3])); \
        asm volatile("v_mfma_f32_32x32x16_bf16 %0, %7, %8, %0\n\tv_exp_f32 %1, %9\n\tv_exp_f32 %2, %10\n\tv_exp_f32 %3, %11\n\tv_exp_f32 %4, %12\n\tv_cvt_pk_bf16_f32 %5, %13, %14\n\tv_cvt_pk_bf16_f32 %6, %15, %16" \
            : "+v"(Sn), "=&v"(e4), "=&v"(e5), "=&v"(e6), "=&v"(e7), "=&v"(Pc0[0]), "=&v"(Pc0[1]) : "v"(kn1), "v"(qn1), "v"(Sc[4]), "v"(Sc[5]), "v"(Sc[6]), "v"(Sc[7]), "v"(e0), "v"(e1), "v"(e2), "v"(e3)); \
        INJ; \
        asm volatile("v_mfma_f32_32x32x16_bf16 %0, %7, %8, %0\n\tv_exp_f32 %1, %9\n\tv_exp_f32 %2, %10\n\tv_exp_f32 %3, %11\n\tv_exp_f32 %4, %12\n\tv_cvt_pk_bf16_f32 %5, %13, %14\n\tv_cvt_pk_bf16_f32 %6, %15, %16" \
            : "+v"(Op), "=&v"(e8), "=&v"(e9), "=&v"(e10), "=&v"(e11), "=&v"(Pc0[2]), "=&v"(Pc0[3]) : "v"(vp0), "v"(Pp0), "v"(Sc[8]), "v"(Sc[9]), "v"(Sc[10]), "v"(Sc[11]), "v"(e4), "v"(e5), "v"(e6), "v"(e7)); \
        asm volatile("v_mfma_f32_32x32x16_bf16 %0, %9, %10, %0\n\tv_exp_f32 %1, %11\n\tv_exp_f32 %2, %12\n\tv_exp_f32 %3, %13\n\tv_exp_f32 %4, %14\n\tv_cvt_pk_bf16_f32 %5, %15, %16\n\tv_cvt_pk_bf16_f32 %6, %17, %18\n\tv_cvt_pk_bf16_f32 %7, %1, %2\n\tv_cvt_pk_bf16_f32 %8, %3, %4" \
            : "+v"(Op), "=&v"(e12), "=&v"(e13), "=&v"(e14), "=&v"(e15), "=&v"(Pc1[0]), "=&v"(Pc1[1]), "=&v"(Pc1[2]), "=&v"(Pc1[3]) : "v"(vp1), "v"(Pp1), "v"(Sc[12]), "v"(Sc[13]), "v"(Sc[14]), "v"(Sc[15]), "v"(e8), "v"(e9), "v"(e10), "v"(e11)); \
    } while (0)
    f32x16 sA, sB; u32x4 pA0, pA1, pB0 = {}, pB1 = {};
    vf[1][0] = (u32x4){}; vf[1][1] = (u32x4){};
    asm volatile("s_nop 1\n\tv_mfma_f32_32x32x16_bf16 %0, %1, %2, 0\n\tv_mfma_f32_32x32x16_bf16 %0, %3, %4, %0\n\ts_nop 7\n\ts_nop 3" : "=&v"(sA) : "v"(kf[0][0]), "v"(qf[0][0]), "v"(kf[0][1]), "v"(qf[0][1]));
#define PATCHK(kx) do { PIN(kx[0]); PIN(kx[1]); patch<ODD>(kx[0], kx[1], hi, 0x3F803F80u, 0u); PIN(kx[0]); PIN(kx[1]); } while (0)
#define LOADK(kc, tt) do { const unsigned o_ = kbase + (unsigned)(((tt) + rot) & 7) * TILE_B; kc[0] = bload128(rK, lo16, o_); kc[1] = bload128(rK, lo16, o_ + 1024u); } while (0)
#define LOADV(vx, tt) do { const unsigned o_ = vbase + (unsigned)(((tt) + rot) & 7) * 2048u; vx[0] = bload128(rV, lo16, o_); vx[1] = bload128(rV, lo16, o_ + 1024u); } while (0)
#define TILE(kc, kx, vc, vx, t) do { \
        SLOT(sA, pA0, pA1, sB, kc[0], kc[1], qf[1][0], qf[1][1], o[3], vx[0], vx[1], pB0, pB1, (void)0); \
        LOADV(vx, (t) + 1); \
        SLOT(sB, pB0, pB1, sA, kc[0], kc[1], qf[2][0], qf[2][1], o[0], vc[0], vc[1], pA0, pA1, (void)0); \
        SLOT(sA, pA0, pA1, sB, kc[0], kc[1], qf[3][0], qf[3][1], o[1], vc[0], vc[1], pB0, pB1, PATCHK(kx)); \
        LOADK(kc, (t) + 2); \
        SLOT(sB, pB0, pB1, sA, kx[0], kx[1], qf[0][0], qf[0][1], o[2], vc[0], vc[1], pA0, pA1, (void)0); \
    } while (0)
#pragma unroll 1
    for (int t = 0; t < 8; t += 2) {
        if (wid >= 4) __builtin_amdgcn_s_setprio(1); else __builtin_amdgcn_s_setprio(0);
        TILE(kf[0], kf[1], vf[0], vf[1], t);
        if (wid >= 4) __builtin_amdgcn_s_setprio(0); else __builtin_amdgcn_s_setprio(1);
        TILE(kf[1], kf[0], vf[1], vf[0], t + 1);
    }
    asm volatile("s_nop 1\n\tv_mfma_f32_32x32x16_bf16 %0, %1, %2, %0\n\tv_mfma_f32_32x32x16_bf16 %0, %3, %4, %0\n\ts_nop 7\n\ts_nop 3" : "+v"(o[3]) : "v"(vf[1][0]), "v"(pB0), "v"(vf[1][1]), "v"(pB1));
    asm volatile("" : "+v"(o[0]), "+v"(o[1]), "+v"(o[2]));
    __builtin_amdgcn_s_setprio(0);
    STAMP(7);
    const bool bad = merge_store(o, ws, h, rb, lds, wid, lane);
    if (__any(bad) && lane == 0) *(int*)(lds + LDS_BAD) = 1;
    STAMP(8);
    DRAIN_BARRIER();
    if (*(volatile int*)(lds + LDS_BAD)) {
        attnA_safe<ODD>(ws, h, rb, lds, wid, lane, o);
        BARRIER();
        merge_store(o, ws, h, rb, lds, wid, lane);
        DRAIN_BARRIER();
    }
    if (threadIdx.x == 0) flag_set(ws, F3 + rb * 16 + h, tag);
}

template <int ODD>
__device__ __forceinline__ void attnB_wave(char* ws, int h, int rb, int j, int lane) {
    const bool hi = lane >= 32;
    const int m0 = (28 * h) >> 3, T = rb * 4 + j, Tp = T > 0 ? T - 1 : 0;
    const auto rQ = RSRC(ws + WS_QB, QC_BYTES), rK = RSRC(ws + WS_KB, QC_BYTES), rV = RSRC(ws + WS_VB, V_BYTES);
    const unsigned lo16 = (unsigned)lane * 16u;
    u32x4 q0 = bload128(rQ, lo16, (unsigned)(T * 56 + m0) * 512u), q1 = bload128(rQ, lo16, (unsigned)(T * 56 + m0) * 512u + 1024u);
    u32x4 ka0 = bload128(rK, lo16, (unsigned)(Tp * 56 + m0) * 512u), ka1 = bload128(rK, lo16, (unsigned)(Tp * 56 + m0) * 512u + 1024u);
    u32x4 kb0 = bload128(rK, lo16, (unsigned)(T * 56 + m0) * 512u), kb1 = bload128(rK, lo16, (unsigned)(T * 56 + m0) * 512u + 1024u);
    u32x4 va0 = bload128(rV, lo16, (unsigned)(h * 64 + Tp) * 2048u), va1 = bload128(rV, lo16, (unsigned)(h * 64 + Tp) * 2048u + 1024u);
    u32x4 vb0 = bload128(rV, lo16, (unsigned)(h * 64 + T) * 2048u), vb1 = bload128(rV, lo16, (unsigned)(h * 64 + T) * 2048u + 1024u);
    patch<ODD>(q0, q1, hi, 0u, 0u); patch<ODD>(ka0, ka1, hi, 0u, 0u); patch<ODD>(kb0, kb1, hi, 0u, 0u);
    f32x16 sa = {}, sb = {};
    sa = MFMA32(asb(ka0), asb(q0), sa); sa = MFMA32(asb(ka1), asb(q1), sa);
    sb = MFMA32(asb(kb0), asb(q0), sb); sb = MFMA32(asb(kb1), asb(q1), sb);
    const int ql = lane & 31;
#pragma unroll
    for (int r = 0; r < 16; ++r) {
        const int kl = (r & 3) + 8 * (r >> 2) + (hi ? 4 : 0);
        const int da = ql + 32 - kl, db = ql - kl;
        const bool va = T > 0 && da <= 14 && !(da & 1), vb = db >= 0 && db <= 14 && !(db & 1);
        sa[r] = va ? sa[r] : -INFINITY; sb[r] = vb ? sb[r] : -INFINITY;
    }
    const float m = xhalf_max(fmaxf(max16(sa), max16(sb)));
#pragma unroll
    for (int r = 0; r < 16; ++r) { sa[r] = (sa[r] - m) * QSCALE; sb[r] = (sb[r] - m) * QSCALE; }
    u32x4 pa0, pa1, pb0, pb1; exp_pack(sa, pa0, pa1); exp_pack(sb, pb0, pb1);
    f32x16 o = {};
    o = MFMA32(asb(va0), asb(pa0), o); o = MFMA32(asb(va1), asb(pa1), o); o = MFMA32(asb(vb0), asb(pb0), o); o = MFMA32(asb(vb1), asb(pb1), o);
    float l = o[12]; { auto r = __builtin_amdgcn_permlane32_swap(__float_as_uint(l), __float_as_uint(l), false, false); l = __uint_as_float(r[1]); }
    const float inv = 1.0f / l;
    const auto rO = RSRC(ws + WS_OB, QC_BYTES);
#pragma unroll
    for (int rq = 0; rq < 4; ++rq) {
        if (rq == 3 && hi) continue;
        const int col = 28 * h + 8 * rq + (hi ? 4 : 0);
        bstore64((u32x2){cvtpk(o[4 * rq] * inv, o[4 * rq + 1] * inv), cvtpk(o[4 * rq + 2] * inv, o[4 * rq + 3] * inv)}, rO, (unsigned)((T * 56 + (col >> 3)) * 32 + ql) * 16u + (unsigned)((col >> 2) & 1) * 8u);
    }
}

__device__ __forceinline__ void oproj_issue_w(const u16* wo, int ct, unsigned lds_base, int wid, int lane) {
    const int drow = 8 * wid + (lane >> 3), dpos = lane & 7;
    const int f2 = ((drow >> 1) & 1) | (((drow >> 3) & 1) << 1);
    const unsigned woff = (unsigned)drow * 896u + (unsigned)((((dpos >> 1) ^ f2) * 2 + (dpos & 1)) << 4);
    const char* wb = (const char*)wo + (size_t)ct * 128;
#pragma unroll
    for (int i = 0; i < 7; ++i) glds16(wb + (size_t)i * 64 * 896, woff, lds_base + i * 8192 + wid * 1024);
}
__device__ __forceinline__ void oproj_compute(char* ws, size_t offO, u16* out, int colbase, int rt, int ct, char* ldsw, char* ldsr, int wid, int lane) {
    const int tb = wid & 3, kh = wid >> 2, g = lane >> 4, t16 = lane & 15;
    const auto rO = RSRC(ws + offO, QC_BYTES);
    const int tile = rt * 2 + (tb >> 1);
    const unsigned ooff = (unsigned)g * 512u + (unsigned)((tb & 1) * 16 + t16) * 16u;
    u32x4 of[7];
#pragma unroll
    for (int i = 0; i < 7; ++i) of[i] = bload128(rO, ooff, (unsigned)(tile * 56 + 4 * (kh * 7 + i)) * 512u);
    const int q = t16 >> 2, p = lane & 3;
    const int rf2 = ((q >> 1) & 1) | ((g & 1) << 1);
    f32x4 acc[4] = {};
#pragma unroll
    for (int i = 0; i < 7; ++i) {
#pragma unroll
        for (int c = 0; c < 4; ++c) {
            lds_cptr wp = (lds_cptr)ldsw + (unsigned)((kh * 7 + i) * 32 + 8 * g + q) * 128u + (unsigned)((c ^ rf2) * 32) + (unsigned)p * 8u;
            const bf16x8 wf = cat8(vtr(wp), vtr(wp + 512));
            acc[c] = MFMA16(wf, asb(of[i]), acc[c]);
        }
    }
    if (kh == 1) {
#pragma unroll
        for (int c = 0; c < 4; ++c) *(f32x4*)(ldsr + (tb * 4 + c) * 1024 + lane * 16) = acc[c];
    }
    BARRIER();
    if (kh == 0) {
#pragma unroll
        for (int c = 0; c < 4; ++c) acc[c] += *(const f32x4*)(ldsr + (tb * 4 + c) * 1024 + lane * 16);
        const int rho = lane >> 4;
        const int token = rt * 64 + tb * 16 + t16;
#pragma unroll
        for (int pr = 0; pr < 2; ++pr) {
            unsigned a0 = cvtpk(acc[2 * pr][0], acc[2 * pr][1]), a1 = cvtpk(acc[2 * pr][2], acc[2 * pr][3]);
            unsigned b0 = cvtpk(acc[2 * pr + 1][0], acc[2 * pr + 1][1]), b1 = cvtpk(acc[2 * pr + 1][2], acc[2 * pr + 1][3]);
            auto r0 = __builtin_amdgcn_permlane16_swap(a0, b0, false, false);
            auto r1 = __builtin_amdgcn_permlane16_swap(a1, b1, false, false);
            const int col = colbase + 64 * ct + 16 * (2 * pr + (rho & 1)) + 8 * (rho >> 1);
            *(u32x4*)(out + (size_t)token * 896 + col) = (u32x4){r0[0], r1[0], r0[1], r1[1]};
        }
    }
}

__global__ void __launch_bounds__(512, 2) fused_kernel(const u16* xA, const u16* wA, const u16* woA, const u16* xB, const u16* wB, const u16* woB, u16* out, char* ws) {
    extern __shared__ __attribute__((aligned(16))) char lds[];
    const int lane = threadIdx.x & 63, wid = __builtin_amdgcn_readfirstlane(threadIdx.x >> 6);
    const int id = blockIdx.x, xcd = id & 7, loc = id >> 3;
    STAMP(0);
    const u64 tag = __hip_atomic_load(flagp(ws, FEP), __ATOMIC_RELAXED, __HIP_MEMORY_SCOPE_AGENT) + 1;
    const int gb = xcd & 1, grt = 8 * (xcd >> 1) + (loc & 7);
    const int qct = gb * 4 + (loc >> 3);
    const bool has_qkv = qct < 7;
    if (has_qkv) qkv_phase(xA, wA, xB, wB, ws, grt, qct, tag, lds, wid, lane);
    else if (threadIdx.x == 0) flag_set(ws, F0 + (xcd >> 1) * 8 + (loc & 7), tag);
    STAMP(4);
    const int h = xcd * 2 + (loc & 1), rb = loc >> 1;
    const int ct0 = (28 * h) >> 6, ct1 = (28 * h + 27) >> 6;
    if (wid == 0) flag_wait(ws, F1 + (lane < 32 ? ct0 : ct1) * 32 + (lane & 31), tag);
    BARRIER();
    STAMP(5);
    if (h & 1) attnA_phase<1>(ws, h, rb, tag, has_qkv ? F2 + qct * 32 + grt : -1, lds, wid, lane);
    else attnA_phase<0>(ws, h, rb, tag, has_qkv ? F2 + qct * 32 + grt : -1, lds, wid, lane);
    STAMP(9);
    const int ort = 4 * xcd + (loc & 3), oct = loc >> 2;
    const bool has_o = oct < 7;
    const unsigned lds0 = (unsigned)(uintptr_t)lds;
    if (has_o) { oproj_issue_w(woB, oct, lds0 + 57344, wid, lane); oproj_issue_w(woA, oct, lds0, wid, lane); }
    if (wid < 4) {
        { const int i = lane % 6, r = 2 * rb - 1 + i % 3; flag_wait(ws, F2 + (i < 3 ? ct0 : ct1) * 32 + (r < 0 ? 0 : r), tag); }
        if (h & 1) attnB_wave<1>(ws, h, rb, wid, lane); else attnB_wave<0>(ws, h, rb, wid, lane);
    }
    DRAIN_BARRIER();
    if (threadIdx.x == 0) flag_set(ws, F4 + rb * 16 + h, tag);
    STAMP(10);
    if (has_o) {
        if (wid == 0) flag_wait(ws, F3 + (ort >> 1) * 16 + (lane & 15), tag);
        BARRIER();
        STAMP(11);
        oproj_compute(ws, WS_OA, out, 0, ort, oct, lds, lds + 114688, wid, lane);
        STAMP(12);
        if (wid == 0) flag_wait(ws, F4 + (ort >> 1) * 16 + (lane & 15), tag);
        BARRIER();
        STAMP(13);
        oproj_compute(ws, WS_OB, out, 448, ort, oct, lds + 57344, lds + 114688, wid, lane);
    }
    STAMP(14);
    if (id == 0 && wid == 0) {
        flag_wait(ws, F0 + (lane & 31), tag);
        if (lane == 0) flag_set(ws, FEP, tag);
    }
}

extern "C" void kernel_launch(void* const* d_in, const int* in_sizes, int n_in, void* d_out, int out_size, void* d_ws, size_t ws_size, hipStream_t stream) {
    const u16* xA = (const u16*)d_in[0]; const u16* wA = (const u16*)d_in[1]; const u16* woA = (const u16*)d_in[2];
    const u16* xB = (const u16*)d_in[3]; const u16* wB = (const u16*)d_in[4]; const u16* woB = (const u16*)d_in[5];
    (void)hipFuncSetAttribute((const void*)fused_kernel, hipFuncAttributeMaxDynamicSharedMemorySize, LDS_TOTAL);
    fused_kernel<<<256, 512, LDS_TOTAL, stream>>>(xA, wA, woA, xB, wB, woB, (u16*)d_out, (char*)d_ws);
}
```

```cpp
#include <hip/hip_runtime.h>
#include <hip/hip_bf16.h>
#include <stdint.h>

typedef unsigned short u16;
typedef unsigned long long u64;
typedef short bf16x8 __attribute__((ext_vector_type(8)));
typedef short s16x4 __attribute__((ext_vector_type(4)));
typedef float f32x4 __attribute__((ext_vector_type(4)));
typedef float f32x16 __attribute__((ext_vector_type(16)));
typedef unsigned u32x4 __attribute__((ext_vector_type(4)));
typedef unsigned u32x2 __attribute__((ext_vector_type(2)));
typedef __attribute__((address_space(3))) const char* lds_cptr;
typedef __attribute__((address_space(1))) u64 gu64;

constexpr int TILE_B = 56 * 512;
constexpr size_t QC_BYTES = (size_t)64 * TILE_B;
constexpr size_t V_BYTES = (size_t)16 * 64 * 2048;
constexpr size_t WS_QA = 0, WS_KA = WS_QA + QC_BYTES, WS_VA = WS_KA + QC_BYTES;
constexpr size_t WS_QB = WS_VA + V_BYTES, WS_KB = WS_QB + QC_BYTES, WS_VB = WS_KB + QC_BYTES;
constexpr size_t WS_OA = WS_VB + V_BYTES, WS_OB = WS_OA + QC_BYTES, WS_FLAGS = WS_OB + QC_BYTES;
constexpr int F1 = 0, F2 = 256, F3 = 512, F4 = 768, F0 = 1024, FEP = 1056;
constexpr float QSCALE = 0.18898223650461362f * 1.4426950408889634f;
constexpr int STG = 32768;
constexpr int LDS_MX = 147456, LDS_BAD = LDS_MX + 1024, LDS_TOTAL = LDS_MX + 2048;
constexpr unsigned SPIN_MAX = 1u << 18;

#define STAMP(i)
#define STAMP_INIT()
#define STAMP_DUMP()
#define LDS_EXTRA 0

#define MFMA16(a, b, c) __builtin_amdgcn_mfma_f32_16x16x32_bf16(a, b, c, 0, 0, 0)
#define MFMA32(a, b, c) __builtin_amdgcn_mfma_f32_32x32x16_bf16(a, b, c, 0, 0, 0)
#define PIN(x) asm volatile("" : "+v"(x))
#define SBAR() __builtin_amdgcn_sched_barrier(0)
#define SC1 16

__device__ __forceinline__ unsigned cvtpk(float lo, float hi) {
    typedef float f2 __attribute__((ext_vector_type(2))); typedef __bf16 b2 __attribute__((ext_vector_type(2)));
    f2 v = {lo, hi}; b2 r = __builtin_convertvector(v, b2); return __builtin_bit_cast(unsigned, r);
}
__device__ __forceinline__ void glds16(const void* sbase, unsigned voff, unsigned lds_base) {
    asm volatile("s_mov_b32 m0, %2\n\ts_nop 0\n\tglobal_load_lds_dwordx4 %0, %1" :: "v"(voff), "s"(sbase), "s"(lds_base) : "memory");
}
__device__ __forceinline__ s16x4 vtr(lds_cptr p) {
    typedef short v4i16_t __attribute__((ext_vector_type(4)));
    return __builtin_bit_cast(s16x4, __builtin_amdgcn_ds_read_tr16_b64_v4i16((__attribute__((address_space(3))) v4i16_t*)p));
}
__device__ __forceinline__ bf16x8 cat8(s16x4 a, s16x4 b) { return (bf16x8){a[0], a[1], a[2], a[3], b[0], b[1], b[2], b[3]}; }
__device__ __forceinline__ bf16x8 ldsrd128(lds_cptr p) { return *(const __attribute__((address_space(3))) bf16x8*)p; }
#define RSRC(p, bytes) __builtin_amdgcn_make_buffer_rsrc((void*)(p), 0, (int)(bytes), 0x00020000)
__device__ __forceinline__ u32x4 bload128(__amdgpu_buffer_rsrc_t r, unsigned voff, unsigned soff) { return __builtin_amdgcn_raw_buffer_load_b128(r, voff, soff, SC1); }
__device__ __forceinline__ void bstore128(u32x4 v, __amdgpu_buffer_rsrc_t r, unsigned voff) { __builtin_amdgcn_raw_buffer_store_b128(v, r, voff, 0, SC1); }
__device__ __forceinline__ void bstore64(u32x2 v, __amdgpu_buffer_rsrc_t r, unsigned voff) { __builtin_amdgcn_raw_buffer_store_b64(v, r, voff, 0, SC1); }
__device__ __forceinline__ bf16x8 asb(u32x4 v) { return __builtin_bit_cast(bf16x8, v); }

__device__ __forceinline__ gu64* flagp(char* ws, int idx) { return (gu64*)(ws + WS_FLAGS + (size_t)idx * 128); }
__device__ __forceinline__ void flag_set(char* ws, int idx, u64 tag) { __hip_atomic_store(flagp(ws, idx), tag, __ATOMIC_RELAXED, __HIP_MEMORY_SCOPE_AGENT); }
__device__ __forceinline__ void flag_wait(char* ws, int idx, u64 tag) {
    gu64* p = flagp(ws, idx);
    for (unsigned spins = 0; spins < SPIN_MAX; ++spins) {
        const u64 v = __hip_atomic_load(p, __ATOMIC_RELAXED, __HIP_MEMORY_SCOPE_AGENT);
        if (__all(v == tag)) break;
        __builtin_amdgcn_s_sleep(1);
    }
}
__device__ __forceinline__ int opaque_lane() { int l; asm volatile("v_mbcnt_lo_u32_b32 %0, -1, 0\n\tv_mbcnt_hi_u32_b32 %0, -1, %0" : "=v"(l)); return l; }
__device__ __forceinline__ int f2_index(int h, int rb, int lane) { const int i = lane % 6, r = 2 * rb - 1 + i % 3; return F2 + (i < 3 ? (28 * h) >> 6 : (28 * h + 27) >> 6) * 32 + (r < 0 ? 0 : r); }
#define BARRIER() asm volatile("s_waitcnt lgkmcnt(0)\n\ts_barrier" ::: "memory")
#define DRAIN_BARRIER() asm volatile("s_waitcnt vmcnt(0) lgkmcnt(0)\n\ts_barrier" ::: "memory")

__device__ __forceinline__ void qkv_epilogue(const f32x4 (&aq)[2], const f32x4 (&ak)[2], const f32x4 (&av)[2], char* ws, size_t offQ, size_t offK, size_t offV, float qs,
                                             int rt, int ct, int tb, int c, int lane) {
    const int t16 = lane & 15;
    const int tile = rt * 2 + tb;
    {
        const unsigned off = (unsigned)(tile * 56 + 8 * ct + 2 * c) * 512u + (unsigned)lane * 16u;
        unsigned q0a = cvtpk(aq[0][0] * qs, aq[0][1] * qs), q0b = cvtpk(aq[0][2] * qs, aq[0][3] * qs);
        unsigned q1a = cvtpk(aq[1][0] * qs, aq[1][1] * qs), q1b = cvtpk(aq[1][2] * qs, aq[1][3] * qs);
        auto ra = __builtin_amdgcn_permlane16_swap(q0a, q1a, false, false);
        auto rb = __builtin_amdgcn_permlane16_swap(q0b, q1b, false, false);
        bstore128((u32x4){ra[0], rb[0], ra[1], rb[1]}, RSRC(ws + offQ, QC_BYTES), off);
        unsigned k0a = cvtpk(ak[0][0], ak[0][1]), k0b = cvtpk(ak[0][2], ak[0][3]);
        unsigned k1a = cvtpk(ak[1][0], ak[1][1]), k1b = cvtpk(ak[1][2], ak[1][3]);
        auto sa = __builtin_amdgcn_permlane16_swap(k0a, k1a, false, false);
        auto sb = __builtin_amdgcn_permlane16_swap(k0b, k1b, false, false);
        bstore128((u32x4){sa[0], sb[0], sa[1], sb[1]}, RSRC(ws + offK, QC_BYTES), off);
    }
    {
        unsigned v0a = cvtpk(av[0][0], av[0][1]), v0b = cvtpk(av[0][2], av[0][3]);
        unsigned v1a = cvtpk(av[1][0], av[1][1]), v1b = cvtpk(av[1][2], av[1][3]);
        auto ra = __builtin_amdgcn_permlane32_swap(v0a, v1a, false, false);
        auto rb = __builtin_amdgcn_permlane32_swap(v0b, v1b, false, false);
        const int n = 64 * ct + 16 * c + t16, h = n / 28, d = n - 28 * h;
        const unsigned off = (unsigned)(h * 64 + tile) * 2048u + (unsigned)(lane >> 4) * 512u + (unsigned)d * 16u;
        const auto rs = RSRC(ws + offV, V_BYTES);
        bstore128((u32x4){ra[0], rb[0], ra[1], rb[1]}, rs, off);
        if (d == 27) bstore128((u32x4){0x3F803F80u, 0x3F803F80u, 0x3F803F80u, 0x3F803F80u}, rs, off + 16u);
    }
}

struct AttnPre { u32x4 qf[4][2], kf[2][2], vf0[2]; };
__device__ __forceinline__ void attn_pre_load(AttnPre& a, char* ws, int h, int rb, int wid, int lane) {
    const int m0 = (28 * h) >> 3, rot = rb & 7;
    const auto rQ = RSRC(ws + WS_QA, QC_BYTES), rK = RSRC(ws + WS_KA, QC_BYTES), rV = RSRC(ws + WS_VA, V_BYTES);
    const unsigned lo16 = (unsigned)lane * 16u;
    const unsigned kbase = (unsigned)((wid * 8) * 56 + m0) * 512u, vbase = (unsigned)(h * 64 + wid * 8) * 2048u;
    const unsigned k0 = kbase + (unsigned)rot * TILE_B, v0 = vbase + (unsigned)rot * 2048u, k1 = kbase + (unsigned)((rot + 1) & 7) * TILE_B;
    a.kf[0][0] = bload128(rK, lo16, k0); a.kf[0][1] = bload128(rK, lo16, k0 + 1024u);
#pragma unroll
    for (int j = 0; j < 4; ++j) { const unsigned b = (unsigned)((rb * 4 + j) * 56 + m0) * 512u; a.qf[j][0] = bload128(rQ, lo16, b); a.qf[j][1] = bload128(rQ, lo16, b + 1024u); }
    a.vf0[0] = bload128(rV, lo16, v0); a.vf0[1] = bload128(rV, lo16, v0 + 1024u);
    a.kf[1][0] = bload128(rK, lo16, k1); a.kf[1][1] = bload128(rK, lo16, k1 + 1024u);
}

struct QkvFrag { bf16x8 x[2][4], w[2][3]; };
struct QkvAcc { f32x4 q[4], k[4], v[4]; };
#define QEPI(A, oQ, oK, oV, qs) do { { const f32x4 q2[2] = {A.q[0], A.q[1]}, k2[2] = {A.k[0], A.k[1]}, v2[2] = {A.v[0], A.v[1]}; qkv_epilogue(q2, k2, v2, ws, oQ, oK, oV, qs, rt, ct, 0, wid, lane); } \
        { const f32x4 q2[2] = {A.q[2], A.q[3]}, k2[2] = {A.k[2], A.k[3]}, v2[2] = {A.v[2], A.v[3]}; qkv_epilogue(q2, k2, v2, ws, oQ, oK, oV, qs, rt, ct, 1, wid, lane); } } while (0)

__device__ __forceinline__ void qkv_phase(const u16* xA, const u16* wA, const u16* xB, const u16* wB, char* ws, int rt, int ct, u64 tag, char* lds, int wid, int lane,
                                          QkvAcc& acc, int f1idx) {
    if (wid >= 4) {
        const unsigned lds0 = (unsigned)(uintptr_t)lds;
        const int lw = wid - 4, r8 = lane >> 3, dpos = lane & 7;
        unsigned xoff[2], woff[2];
#pragma unroll
        for (int i = 0; i < 2; ++i) { const int row = 16 * lw + 8 * i + r8;
            xoff[i] = (unsigned)row * 896u + (unsigned)((dpos ^ ((row >> 1) & 7)) << 4);
            const int f2 = ((row >> 1) & 1) | (((row >> 3) & 1) << 1);
            woff[i] = (unsigned)row * 2688u + (unsigned)((((dpos >> 1) ^ f2) * 2 + (dpos & 1)) << 4); }
        const int rot = __builtin_amdgcn_readfirstlane(((rt & 7) + 2 * (ct & 3)) % 7);
        const char* xa = (const char*)xA + (size_t)rt * 64 * 896; const char* wa = (const char*)wA + (size_t)ct * 128;
        const char* xb = (const char*)xB + (size_t)rt * 64 * 896; const char* wb = (const char*)wB + (size_t)ct * 128;
#define QKV_ISSUE(g) do { int st_ = ((g) % 7) + rot; st_ = st_ >= 7 ? st_ - 7 : st_; const char* x_ = ((g) < 7 ? xa : xb) + st_ * 128; const char* w_ = ((g) < 7 ? wa : wb) + (size_t)st_ * (64 * 2688); \
        const unsigned sl_ = lds0 + ((g) & 3) * STG + lw * 2048; \
        glds16(x_, xoff[0], sl_); glds16(x_, xoff[1], sl_ + 1024); \
        glds16(w_, woff[0], sl_ + 8192); glds16(w_, woff[1], sl_ + 8192 + 1024); glds16(w_ + 896, woff[0], sl_ + 16384); glds16(w_ + 896, woff[1], sl_ + 16384 + 1024); \
        glds16(w_ + 1792, woff[0], sl_ + 24576); glds16(w_ + 1792, woff[1], sl_ + 24576 + 1024); } while (0)
        QKV_ISSUE(0); QKV_ISSUE(1);
        u64 fl = 0;
#pragma unroll
        for (int g = 0; g < 14; ++g) {
            if (g <= 12) asm volatile("s_waitcnt vmcnt(8) lgkmcnt(0)\n\ts_barrier" ::: "memory");
            else asm volatile("s_waitcnt vmcnt(0) lgkmcnt(0)\n\ts_barrier" ::: "memory");
            if (g == 0) STAMP(1);
            if (g + 2 < 14) QKV_ISSUE(g + 2);
            if (g == 12 && wid == 4) fl = __hip_atomic_load(flagp(ws, f1idx), __ATOMIC_RELAXED, __HIP_MEMORY_SCOPE_AGENT);
        }
        if (wid == 4 && !__all(fl == tag)) flag_wait(ws, f1idx, tag);
        BARRIER();
        return;
    }
    const int c = wid, g4 = lane >> 4, t16 = lane & 15;
    const unsigned xr0 = (unsigned)t16 * 128u + (unsigned)((g4 ^ (t16 >> 1)) << 4);
    const int q = t16 >> 2, p = lane & 3;
    const int rf2 = ((q >> 1) & 1) | ((g4 & 1) << 1);
    const unsigned wr0 = 8192u + (unsigned)(8 * g4 + q) * 128u + (unsigned)((c ^ rf2) * 32) + (unsigned)p * 8u;
#pragma unroll
    for (int i = 0; i < 4; ++i) acc.q[i] = acc.k[i] = acc.v[i] = (f32x4){};
    QkvFrag fr[2];
#define QMF(i) do { if (g > 0) { constexpr int ks_ = (i) / 12, s_ = ((i) % 12) / 4, b_ = (i) % 4; const QkvFrag& f_ = fr[(g - 1) & 1]; \
        if (s_ == 0) acc.q[b_] = MFMA16(f_.w[ks_][0], f_.x[ks_][b_], acc.q[b_]); if (s_ == 1) acc.k[b_] = MFMA16(f_.w[ks_][1], f_.x[ks_][b_], acc.k[b_]); \
        if (s_ == 2) acc.v[b_] = MFMA16(f_.x[ks_][b_], f_.w[ks_][2], acc.v[b_]); } SBAR(); } while (0)
#define QRX(ks, b) do { if (g < 14) fr[g & 1].x[ks][b] = ldsrd128(sl + (xr0 ^ ((ks) * 64)) + (b) * 2048); } while (0)
#define QRW(ks, s) do { if (g < 14) { lds_cptr wp_ = sl + wr0 + (ks) * 4096 + (s) * 8192; fr[g & 1].w[ks][s] = cat8(vtr(wp_), vtr(wp_ + 512)); } } while (0)
#pragma unroll
    for (int g = 0; g < 15; ++g) {
        if (g == 10) asm volatile("s_waitcnt vmcnt(0) lgkmcnt(0)\n\ts_barrier" ::: "memory");
        else if (g < 14) asm volatile("s_waitcnt lgkmcnt(0)\n\ts_barrier" ::: "memory");
        SBAR();
        if (g == 7) STAMP(2);
        lds_cptr sl = (lds_cptr)lds + (g & 3) * STG;
        if (g == 10 && threadIdx.x == 0) flag_set(ws, F1 + ct * 32 + rt, tag);
        SBAR();
        QRX(0, 0); QMF(0); QRX(0, 1); QMF(1); QRX(0, 2); QMF(2); QRX(0, 3); QMF(3); QRW(0, 0); QMF(4); QRW(0, 1); QMF(5); QRW(0, 2); QMF(6);
        QRX(1, 0); QMF(7); QRX(1, 1); QMF(8); QRX(1, 2); QMF(9); QRX(1, 3); QMF(10); QRW(1, 0); QMF(11); QRW(1, 1); QMF(12); QRW(1, 2); QMF(13);
        QMF(14); QMF(15); QMF(16); QMF(17); QMF(18); QMF(19); QMF(20); QMF(21); QMF(22); QMF(23);
        if (g == 7) {
            QEPI(acc, WS_QA, WS_KA, WS_VA, QSCALE);
#pragma unroll
            for (int i = 0; i < 4; ++i) acc.q[i] = acc.k[i] = acc.v[i] = (f32x4){};
            STAMP(3);
        }
    }
    BARRIER();
}

__device__ __forceinline__ float max16(const f32x16& s) {
    float a = fmaxf(fmaxf(s[0], s[1]), s[2]), b = fmaxf(fmaxf(s[3], s[4]), s[5]);
    a = fmaxf(fmaxf(a, s[6]), s[7]); b = fmaxf(fmaxf(b, s[8]), s[9]); a = fmaxf(fmaxf(a, s[10]), s[11]); b = fmaxf(fmaxf(b, s[12]), s[13]);
    a = fmaxf(fmaxf(a, s[14]), s[15]); return fmaxf(a, b);
}
__device__ __forceinline__ float xhalf_max(float m) {
    auto r = __builtin_amdgcn_permlane32_swap(__float_as_uint(m), __float_as_uint(m), false, false);
    return fmaxf(__uint_as_float(r[0]), __uint_as_float(r[1]));
}
__device__ __forceinline__ void patch(u32x4& f0, u32x4& f1, bool pe, bool po, unsigned a, unsigned b) {
    f0[0] = po ? a : f0[0]; f0[1] = po ? b : f0[1]; f1[2] = pe ? a : f1[2]; f1[3] = pe ? b : f1[3];
}
__device__ __forceinline__ void exp_pack(const f32x16& s, u32x4& p0, u32x4& p1) {
    float e[16];
#pragma unroll
    for (int r = 0; r < 16; ++r) e[r] = __builtin_amdgcn_exp2f(s[r]);
    p0 = (u32x4){cvtpk(e[0], e[1]), cvtpk(e[2], e[3]), cvtpk(e[4], e[5]), cvtpk(e[6], e[7])};
    p1 = (u32x4){cvtpk(e[8], e[9]), cvtpk(e[10], e[11]), cvtpk(e[12], e[13]), cvtpk(e[14], e[15])};
}
__device__ __forceinline__ bool merge_store(const f32x16 (&o)[4], char* ws, int h, int rb, char* lds, int wid, int lane) {
    const bool hi = lane >= 32;
#pragma unroll
    for (int j = 0; j < 4; ++j)
#pragma unroll
        for (int rq = 0; rq < 4; ++rq)
            *(f32x4*)(lds + ((wid * 4 + j) * 4 + rq) * 1024 + lane * 16) = (f32x4){o[j][4 * rq], o[j][4 * rq + 1], o[j][4 * rq + 2], o[j][4 * rq + 3]};
    BARRIER();
    const int j = wid >> 1, rq0 = (wid & 1) * 2;
    f32x4 a0 = {}, a1 = {}; float l = 0.f;
#pragma unroll
    for (int w = 0; w < 8; ++w) {
        const char* b = lds + (w * 4 + j) * 4096;
        a0 += *(const f32x4*)(b + rq0 * 1024 + lane * 16); a1 += *(const f32x4*)(b + (rq0 + 1) * 1024 + lane * 16);
        l += *(const float*)(b + 3 * 1024 + (32 + (lane & 31)) * 16);
    }
    const float inv = 1.0f / l;
    const auto rO = RSRC(ws + WS_OA, QC_BYTES);
    const int tile = rb * 4 + j, q = lane & 31;
    a0 *= inv; a1 *= inv;
    const bool last = rq0 == 2 && hi;
    {   const int col = 28 * h + 8 * rq0 + (hi ? 4 : 0);
        bstore64((u32x2){cvtpk(a0[0], a0[1]), cvtpk(a0[2], a0[3])}, rO, (unsigned)((tile * 56 + (col >> 3)) * 32 + q) * 16u + (unsigned)((col >> 2) & 1) * 8u); }
    if (!last) {
        const int col = 28 * h + 8 * (rq0 + 1) + (hi ? 4 : 0);
        bstore64((u32x2){cvtpk(a1[0], a1[1]), cvtpk(a1[2], a1[3])}, rO, (unsigned)((tile * 56 + (col >> 3)) * 32 + q) * 16u + (unsigned)((col >> 2) & 1) * 8u); }
    const float big = 3.0e38f;
    bool ok = l > 1.0e-18f && l < 1.0e30f;
    ok = ok && fabsf(a0[0]) < big && fabsf(a0[1]) < big && fabsf(a0[2]) < big && fabsf(a0[3]) < big;
    ok = ok && (last || (fabsf(a1[0]) < big && fabsf(a1[1]) < big && fabsf(a1[2]) < big && fabsf(a1[3]) < big));
    return !ok;
}

__device__ __forceinline__ void attnA_safe(char* ws, int h, int rb, char* lds, int wid, int lane, f32x16 (&o)[4]) {
    const bool hi = lane >= 32, pe = hi && !(h & 1), po = !hi && (h & 1); const int m0 = (28 * h) >> 3;
    const auto rQ = RSRC(ws + WS_QA, QC_BYTES), rK = RSRC(ws + WS_KA, QC_BYTES), rV = RSRC(ws + WS_VA, V_BYTES);
    const unsigned lo16 = (unsigned)lane * 16u;
    float* mx = (float*)(lds + LDS_MX);
#pragma unroll
    for (int j = 0; j < 4; ++j) {
        const unsigned b = (unsigned)((rb * 4 + j) * 56 + m0) * 512u;
        u32x4 q0 = bload128(rQ, lo16, b), q1 = bload128(rQ, lo16, b + 1024u);
        patch(q0, q1, pe, po, 0u, 0u);
        float m = -3.0e38f; f32x16 acc = {};
#pragma unroll 1
        for (int t = 0; t < 8; ++t) {
            const unsigned ko = (unsigned)((wid * 8 + t) * 56 + m0) * 512u, vo = (unsigned)(h * 64 + wid * 8 + t) * 2048u;
            u32x4 k0 = bload128(rK, lo16, ko), k1 = bload128(rK, lo16, ko + 1024u), v0 = bload128(rV, lo16, vo), v1 = bload128(rV, lo16, vo + 1024u);
            patch(k0, k1, pe, po, 0u, 0u);
            f32x16 s = {}; s = MFMA32(asb(k0), asb(q0), s); s = MFMA32(asb(k1), asb(q1), s);
            const float mn = fmaxf(m, xhalf_max(max16(s)));
            const float al = __builtin_amdgcn_exp2f(m - mn); m = mn;
#pragma unroll
            for (int r = 0; r < 16; ++r) { s[r] -= mn; acc[r] *= al; }
            u32x4 p0, p1; exp_pack(s, p0, p1);
            acc = MFMA32(asb(v0), asb(p0), acc); acc = MFMA32(asb(v1), asb(p1), acc);
        }
        BARRIER();
        if (!hi) mx[wid * 32 + lane] = m;
        BARRIER();
        float g = m;
#pragma unroll
        for (int w = 0; w < 8; ++w) g = fmaxf(g, mx[w * 32 + (lane & 31)]);
        const float f = __builtin_amdgcn_exp2f(m - g);
#pragma unroll
        for (int r = 0; r < 16; ++r) acc[r] *= f;
        o[j] = acc;
    }
}

struct AttnBPre { u32x4 q, kp, kc; u32x2 v[2][2]; };
__device__ __forceinline__ void attnB_load(AttnBPre& a, char* ws, int h, int rb, int wid, int lane) {
    const int m0 = (28 * h) >> 3, T = rb * 4 + (wid >> 1), sub = wid & 1;
    const int Tp = sub ? T : (T > 0 ? T - 1 : 0), subp = sub ^ 1;
    const auto rQ = RSRC(ws + WS_QB, QC_BYTES), rK = RSRC(ws + WS_KB, QC_BYTES), rV = RSRC(ws + WS_VB, V_BYTES);
    const int g = lane >> 4, t16 = lane & 15;
    const unsigned co = (unsigned)g * 512u + (unsigned)t16 * 16u;
    a.q = bload128(rQ, co, (unsigned)(T * 56 + m0) * 512u + (unsigned)sub * 256u);
    a.kp = bload128(rK, co, (unsigned)(Tp * 56 + m0) * 512u + (unsigned)subp * 256u);
    a.kc = bload128(rK, co, (unsigned)(T * 56 + m0) * 512u + (unsigned)sub * 256u);
    const unsigned vo = (unsigned)(g & 1) * 512u + (unsigned)t16 * 16u + (unsigned)(g >> 1) * 8u;
#pragma unroll
    for (int db = 0; db < 2; ++db) {
        a.v[db][0] = __builtin_amdgcn_raw_buffer_load_b64(rV, vo + db * 256u, (unsigned)(h * 64 + Tp) * 2048u + (unsigned)subp * 1024u, SC1);
        a.v[db][1] = __builtin_amdgcn_raw_buffer_load_b64(rV, vo + db * 256u, (unsigned)(h * 64 + T) * 2048u + (unsigned)sub * 1024u, SC1);
    }
}
__device__ __forceinline__ void attnB_compute(AttnBPre& a, char* ws, int h, int rb, int wid, int lane) {
    const int T = rb * 4 + (wid >> 1), sub = wid & 1, g = lane >> 4, ql = lane & 15;
    const bool padl = (h & 1) ? g == 0 : g == 3;
    if (h & 1) { a.q[0] = padl ? 0u : a.q[0]; a.q[1] = padl ? 0u : a.q[1]; a.kp[0] = padl ? 0u : a.kp[0]; a.kp[1] = padl ? 0u : a.kp[1]; a.kc[0] = padl ? 0u : a.kc[0]; a.kc[1] = padl ? 0u : a.kc[1]; }
    else { a.q[2] = padl ? 0u : a.q[2]; a.q[3] = padl ? 0u : a.q[3]; a.kp[2] = padl ? 0u : a.kp[2]; a.kp[3] = padl ? 0u : a.kp[3]; a.kc[2] = padl ? 0u : a.kc[2]; a.kc[3] = padl ? 0u : a.kc[3]; }
    f32x4 sp = {}, sc = {};
    sp = MFMA16(asb(a.kp), asb(a.q), sp); sc = MFMA16(asb(a.kc), asb(a.q), sc);
    const bool hasp = sub || T > 0;
    float m = -INFINITY;
#pragma unroll
    for (int r = 0; r < 4; ++r) {
        const int dc = ql - (4 * g + r), dp = dc + 16;
        sp[r] = (hasp && dp <= 14 && !(dp & 1)) ? sp[r] : -INFINITY; sc[r] = (dc >= 0 && !(dc & 1)) ? sc[r] : -INFINITY;
        m = fmaxf(m, fmaxf(sp[r], sc[r]));
    }
    { auto r = __builtin_amdgcn_permlane16_swap(__float_as_uint(m), __float_as_uint(m), false, false); m = fmaxf(__uint_as_float(r[0]), __uint_as_float(r[1])); }
    m = xhalf_max(m);
    float e[8];
#pragma unroll
    for (int r = 0; r < 4; ++r) { e[r] = __builtin_amdgcn_exp2f((sp[r] - m) * QSCALE); e[4 + r] = __builtin_amdgcn_exp2f((sc[r] - m) * QSCALE); }
    const u32x4 pf = {cvtpk(e[0], e[1]), cvtpk(e[2], e[3]), cvtpk(e[4], e[5]), cvtpk(e[6], e[7])};
    f32x4 o0 = {}, o1 = {};
    o0 = MFMA16(asb((u32x4){a.v[0][0][0], a.v[0][0][1], a.v[0][1][0], a.v[0][1][1]}), asb(pf), o0);
    o1 = MFMA16(asb((u32x4){a.v[1][0][0], a.v[1][0][1], a.v[1][1][0], a.v[1][1][1]}), asb(pf), o1);
    const float l = __uint_as_float(__builtin_amdgcn_ds_bpermute((48 + ql) * 4, __float_as_uint(o1[0])));
    const float inv = 1.0f / l;
    const auto rO = RSRC(ws + WS_OB, QC_BYTES);
    const unsigned tok = (unsigned)(sub * 16 + ql) * 16u;
    { const int col = 28 * h + 4 * g; bstore64((u32x2){cvtpk(o0[0] * inv, o0[1] * inv), cvtpk(o0[2] * inv, o0[3] * inv)}, rO, (unsigned)(T * 56 + (col >> 3)) * 512u + tok + (unsigned)((col >> 2) & 1) * 8u); }
    if (g < 3) { const int col = 28 * h + 16 + 4 * g; bstore64((u32x2){cvtpk(o1[0] * inv, o1[1] * inv), cvtpk(o1[2] * inv, o1[3] * inv)}, rO, (unsigned)(T * 56 + (col >> 3)) * 512u + tok + (unsigned)((col >> 2) & 1) * 8u); }
}

__device__ __forceinline__ void attnA_phase(char* ws, int h, int rb, u64 tag, int f2idx, AttnPre& pre, char* lds, int wid, int lane) {
    const bool hi = lane >= 32, pe = hi && !(h & 1), po = !hi && (h & 1);
    const int m0 = (28 * h) >> 3;
    const auto rK = RSRC(ws + WS_KA, QC_BYTES), rV = RSRC(ws + WS_VA, V_BYTES);
    const unsigned lo16 = (unsigned)lane * 16u;
    u32x4 (&qf)[4][2] = pre.qf; u32x4 (&kf)[2][2] = pre.kf; u32x4 vf[2][2];
    vf[0][0] = pre.vf0[0]; vf[0][1] = pre.vf0[1];
    const unsigned kbase = (unsigned)((wid * 8) * 56 + m0) * 512u, vbase = (unsigned)(h * 64 + wid * 8) * 2048u;
    const int rot = rb & 7;
    float* mx = (float*)(lds + LDS_MX);
    patch(kf[0][0], kf[0][1], pe, po, 0x3F803F80u, 0u);
    {
        const int j = wid & 3;
        u32x4 a0 = j == 0 ? qf[0][0] : j == 1 ? qf[1][0] : j == 2 ? qf[2][0] : qf[3][0];
        u32x4 a1 = j == 0 ? qf[0][1] : j == 1 ? qf[1][1] : j == 2 ? qf[2][1] : qf[3][1];
        patch(a0, a1, pe, po, 0u, 0u);
        f32x16 s = {}; s = MFMA32(asb(kf[0][0]), asb(a0), s); s = MFMA32(asb(kf[0][1]), asb(a1), s);
        const float m = xhalf_max(max16(s));
        if (!hi) mx[(wid >> 2) * 128 + j * 32 + lane] = m;
        if (threadIdx.x == 0) *(int*)(lds + LDS_BAD) = 0;
    }
    BARRIER();
    STAMP(6);
#pragma unroll
    for (int j = 0; j < 4; ++j) {
        const float m = fmaxf(mx[j * 32 + (lane & 31)], mx[128 + j * 32 + (lane & 31)]);
        const unsigned mh = cvtpk(-m, 0.f) & 0xffffu; const float r = -m - __uint_as_float(mh << 16);
        const unsigned pk = mh | (cvtpk(r, 0.f) << 16);
        patch(qf[j][0], qf[j][1], pe, po, pk, 0u);
    }
#pragma unroll
    for (int j = 0; j < 4; ++j) { PIN(qf[j][0]); PIN(qf[j][1]); }
    f32x16 o[4];
#define VEXP(d, s) asm volatile("v_exp_f32 %0, %1" : "=v"(d) : "v"(s))
#define VCVT(d, a, b) asm volatile("v_cvt_pk_bf16_f32 %0, %1, %2" : "=v"(d) : "v"(a), "v"(b))
#define MF0(d, a, b) asm volatile("v_mfma_f32_32x32x16_bf16 %0, %1, %2, 0" : "=&v"(d) : "v"(a), "v"(b))
#define MFA(d, a, b) asm volatile("v_mfma_f32_32x32x16_bf16 %0, %1, %2, %0" : "+v"(d) : "v"(a), "v"(b))
#define SLOT(PV, QK, Sc, Pc0, Pc1, Sn, kn0, kn1, qn0, qn1, Op, vp0, vp1, Pp0, Pp1, INJ) do { \
        float e0, e1, e2, e3, e4, e5, e6, e7, e8, e9, e10, e11, e12, e13, e14, e15; \
        if (QK) MF0(Sn, kn0, qn0); \
        VEXP(e0, Sc[0]); VEXP(e1, Sc[1]); VEXP(e2, Sc[2]); VEXP(e3, Sc[3]); \
        if (QK) MFA(Sn, kn1, qn1); \
        VEXP(e4, Sc[4]); VEXP(e5, Sc[5]); VEXP(e6, Sc[6]); VEXP(e7, Sc[7]); VCVT(Pc0[0], e0, e1); VCVT(Pc0[1], e2, e3); \
        INJ; \
        if ((PV) == 2) MFA(Op, vp0, Pp0); else if ((PV) == 1) MF0(Op, vp0, Pp0); \
        VEXP(e8, Sc[8]); VEXP(e9, Sc[9]); VEXP(e10, Sc[10]); VEXP(e11, Sc[11]); VCVT(Pc0[2], e4, e5); VCVT(Pc0[3], e6, e7); \
        if (PV) MFA(Op, vp1, Pp1); \
        VEXP(e12, Sc[12]); VEXP(e13, Sc[13]); VEXP(e14, Sc[14]); VEXP(e15, Sc[15]); VCVT(Pc1[0], e8, e9); VCVT(Pc1[1], e10, e11); VCVT(Pc1[2], e12, e13); VCVT(Pc1[3], e14, e15); \
    } while (0)
    f32x16 sA, sB; u32x4 pA0, pA1, pB0, pB1;
    asm volatile("s_nop 1\n\tv_mfma_f32_32x32x16_bf16 %0, %1, %2, 0\n\tv_mfma_f32_32x32x16_bf16 %0, %3, %4, %0\n\ts_nop 7\n\ts_nop 3" : "=&v"(sA) : "v"(kf[0][0]), "v"(qf[0][0]), "v"(kf[0][1]), "v"(qf[0][1]));
#define PATCHK(kx) do { PIN(kx[0]); PIN(kx[1]); patch(kx[0], kx[1], pe, po, 0x3F803F80u, 0u); PIN(kx[0]); PIN(kx[1]); } while (0)
#define LOADK(kc, tt) do { if ((tt) < 8) { const unsigned o_ = kbase + (unsigned)(((tt) + rot) & 7) * TILE_B; kc[0] = bload128(rK, lo16, o_); kc[1] = bload128(rK, lo16, o_ + 1024u); } } while (0)
#define LOADV(vx, tt) do { if ((tt) < 8) { const unsigned o_ = vbase + (unsigned)(((tt) + rot) & 7) * 2048u; vx[0] = bload128(rV, lo16, o_); vx[1] = bload128(rV, lo16, o_ + 1024u); } } while (0)
#define TILE(kc, kx, vc, vx, t) do { \
        SLOT((t) == 0 ? 0 : (t) == 1 ? 1 : 2, true, sA, pA0, pA1, sB, kc[0], kc[1], qf[1][0], qf[1][1], o[3], vx[0], vx[1], pB0, pB1, (void)0); \
        LOADV(vx, (t) + 1); \
        SLOT((t) == 0 ? 1 : 2, true, sB, pB0, pB1, sA, kc[0], kc[1], qf[2][0], qf[2][1], o[0], vc[0], vc[1], pA0, pA1, (void)0); \
        SLOT((t) == 0 ? 1 : 2, true, sA, pA0, pA1, sB, kc[0], kc[1], qf[3][0], qf[3][1], o[1], vc[0], vc[1], pB0, pB1, if ((t) < 7) PATCHK(kx)); \
        LOADK(kc, (t) + 2); \
        SLOT((t) == 0 ? 1 : 2, (t) < 7, sB, pB0, pB1, sA, kx[0], kx[1], qf[0][0], qf[0][1], o[2], vc[0], vc[1], pA0, pA1, (void)0); \
    } while (0)
    AttnBPre bpre; u64 flb = 0; bool bready = false;
#pragma unroll
    for (int t = 0; t < 8; t += 2) {
        if (t == 0) __builtin_amdgcn_s_setprio(3); else if (t == 2) __builtin_amdgcn_s_setprio(2); else if (t == 4) __builtin_amdgcn_s_setprio(1); else __builtin_amdgcn_s_setprio(0);
        TILE(kf[0], kf[1], vf[0], vf[1], t);
        TILE(kf[1], kf[0], vf[1], vf[0], t + 1);
        if (t == 0) {
            asm volatile("s_waitcnt vmcnt(4)\n\ts_barrier" ::: "memory");
            if (f2idx >= 0 && threadIdx.x == 0) flag_set(ws, f2idx, tag);
        } else if (t == 2) flb = __hip_atomic_load(flagp(ws, f2_index(h, rb, opaque_lane())), __ATOMIC_RELAXED, __HIP_MEMORY_SCOPE_AGENT);
        else if (t == 4) { bready = __all(flb == tag); attnB_load(bpre, ws, h, rb, wid, opaque_lane()); }
    }
    asm volatile("s_nop 1\n\tv_mfma_f32_32x32x16_bf16 %0, %1, %2, %0\n\tv_mfma_f32_32x32x16_bf16 %0, %3, %4, %0\n\ts_nop 7\n\ts_nop 3" : "+v"(o[3]) : "v"(vf[1][0]), "v"(pB0), "v"(vf[1][1]), "v"(pB1));
    asm volatile("" : "+v"(o[0]), "+v"(o[1]), "+v"(o[2]));
    __builtin_amdgcn_s_setprio(0);
    STAMP(7);
    lane = opaque_lane();
    if (!bready) { flag_wait(ws, f2_index(h, rb, lane), tag); attnB_load(bpre, ws, h, rb, wid, lane); }
    attnB_compute(bpre, ws, h, rb, wid, lane);
    STAMP(15);
    const bool bad = merge_store(o, ws, h, rb, lds, wid, lane);
    if (__any(bad) && lane == 0) *(int*)(lds + LDS_BAD) = 1;
    STAMP(8);
    DRAIN_BARRIER();
    if (*(volatile int*)(lds + LDS_BAD)) {
        attnA_safe(ws, h, rb, lds, wid, lane, o);
        BARRIER();
        merge_store(o, ws, h, rb, lds, wid, lane);
        DRAIN_BARRIER();
    }
    if (threadIdx.x == 0) flag_set(ws, F3 + rb * 16 + h, tag);
}

__device__ __forceinline__ void oproj_issue_w(const u16* woA, const u16* woB, int ct, unsigned lds_base, int wid, int lane) {
    if (wid == 7) return;
    const int r8 = lane >> 3, dpos = lane & 7;
#pragma unroll
    for (int i = 0; i < 16; ++i) {
        const int pc = wid * 16 + i;
        const int br = pc >= 56, pr = pc - br * 56;
        const int f2 = ((r8 >> 1) & 1) | ((pr & 1) << 1);
        const unsigned woff = (unsigned)r8 * 896u + (unsigned)((((dpos >> 1) ^ f2) * 2 + (dpos & 1)) << 4);
        glds16((const char*)(br ? woB : woA) + (size_t)ct * 128 + (size_t)pr * 8 * 896, woff, lds_base + pc * 1024);
    }
}
__device__ __forceinline__ void oproj_compute(char* ws, u16* out, int rt, int ct, char* lds, int wid, int lane) {
    const int tb = wid & 3, kh = wid >> 2, g = lane >> 4, t16 = lane & 15;
    const auto rA = RSRC(ws + WS_OA, QC_BYTES), rB = RSRC(ws + WS_OB, QC_BYTES);
    const int tile = rt * 2 + (tb >> 1);
    const unsigned ooff = (unsigned)g * 512u + (unsigned)((tb & 1) * 16 + t16) * 16u;
    u32x4 of[2][7];
#pragma unroll
    for (int i = 0; i < 7; ++i) of[0][i] = bload128(rA, ooff, (unsigned)(tile * 56 + 4 * (kh * 7 + i)) * 512u);
#pragma unroll
    for (int i = 0; i < 7; ++i) of[1][i] = bload128(rB, ooff, (unsigned)(tile * 56 + 4 * (kh * 7 + i)) * 512u);
    asm volatile("s_waitcnt vmcnt(14)\n\ts_barrier" ::: "memory");
    const int q = t16 >> 2, p = lane & 3;
    const int rf2 = ((q >> 1) & 1) | ((g & 1) << 1);
    f32x4 acc[2][4] = {};
#pragma unroll
    for (int br = 0; br < 2; ++br)
#pragma unroll
        for (int i = 0; i < 7; ++i)
#pragma unroll
            for (int c = 0; c < 4; ++c) {
                lds_cptr wp = (lds_cptr)lds + br * 57344 + (unsigned)((kh * 7 + i) * 32 + 8 * g + q) * 128u + (unsigned)((c ^ rf2) * 32) + (unsigned)p * 8u;
                const bf16x8 wf = cat8(vtr(wp), vtr(wp + 512));
                acc[br][c] = MFMA16(wf, asb(of[br][i]), acc[br][c]);
            }
    char* ldsr = lds + 114688;
    {   const int give = kh ? 0 : 1;
#pragma unroll
        for (int c = 0; c < 4; ++c) *(f32x4*)(ldsr + ((give * 4 + tb) * 4 + c) * 1024 + lane * 16) = give ? acc[1][c] : acc[0][c];
    }
    BARRIER();
    const int keep = kh;
    f32x4 r[4];
#pragma unroll
    for (int c = 0; c < 4; ++c) r[c] = (keep ? acc[1][c] : acc[0][c]) + *(const f32x4*)(ldsr + ((keep * 4 + tb) * 4 + c) * 1024 + lane * 16);
    const int rho = lane >> 4, token = rt * 64 + tb * 16 + t16;
#pragma unroll
    for (int pr = 0; pr < 2; ++pr) {
        unsigned a0 = cvtpk(r[2 * pr][0], r[2 * pr][1]), a1 = cvtpk(r[2 * pr][2], r[2 * pr][3]);
        unsigned b0 = cvtpk(r[2 * pr + 1][0], r[2 * pr + 1][1]), b1 = cvtpk(r[2 * pr + 1][2], r[2 * pr + 1][3]);
        auto r0 = __builtin_amdgcn_permlane16_swap(a0, b0, false, false);
        auto r1 = __builtin_amdgcn_permlane16_swap(a1, b1, false, false);
        const int col = keep * 448 + 64 * ct + 16 * (2 * pr + (rho & 1)) + 8 * (rho >> 1);
        *(u32x4*)(out + (size_t)token * 896 + col) = (u32x4){r0[0], r1[0], r0[1], r1[1]};
    }
}

__global__ void __launch_bounds__(512, 2) fused_kernel(const u16* xA, const u16* wA, const u16* woA, const u16* xB, const u16* wB, const u16* woB, u16* out, char* ws) {
    extern __shared__ __attribute__((aligned(16))) char lds[];
    int lane = threadIdx.x & 63; const int wid = __builtin_amdgcn_readfirstlane(threadIdx.x >> 6);
    const int id = blockIdx.x, xcd = id & 7, loc = id >> 3;
    STAMP_INIT();
    STAMP(0);
    const u64 tag = __hip_atomic_load(flagp(ws, FEP), __ATOMIC_RELAXED, __HIP_MEMORY_SCOPE_AGENT) + 1;
    const int gb = xcd & 1, grt = 8 * (xcd >> 1) + (loc & 7), qct = gb * 4 + (loc >> 3);
    const bool has_qkv = qct < 7;
    const int h = xcd * 2 + (loc & 1), rb = loc >> 1;
    const int ct0 = (28 * h) >> 6, ct1 = (28 * h + 27) >> 6;
    const int f1idx = F1 + (lane < 32 ? ct0 : ct1) * 32 + (lane & 31);
    QkvAcc acc;
    if (has_qkv) qkv_phase(xA, wA, xB, wB, ws, grt, qct, tag, lds, wid, lane, acc, f1idx);
    else {
        if (threadIdx.x == 0) flag_set(ws, F0 + (xcd >> 1) * 8 + (loc & 7), tag);
        if (wid == 4) flag_wait(ws, f1idx, tag);
        BARRIER();
    }
    STAMP(4);
    AttnPre pre;
    attn_pre_load(pre, ws, h, rb, wid, lane);
    if (has_qkv && wid < 4) { const int rt = grt, ct = qct; QEPI(acc, WS_QB, WS_KB, WS_VB, 1.0f); }
    STAMP(5);
    const u64 tags = (u64)(unsigned)__builtin_amdgcn_readfirstlane((unsigned)tag) | ((u64)(unsigned)__builtin_amdgcn_readfirstlane((unsigned)(tag >> 32)) << 32);
    attnA_phase(ws, h, rb, tags, has_qkv ? F2 + qct * 32 + grt : -1, pre, lds, wid, lane);
    STAMP(9);
    lane = opaque_lane();
    const int ort = 4 * xcd + (loc & 3), oct = loc >> 2;
    if (oct < 7) {
        oproj_issue_w(woA, woB, oct, (unsigned)(uintptr_t)lds, wid, lane);
        if (wid == 7) flag_wait(ws, F3 + (ort >> 1) * 16 + (lane & 15), tags);
        asm volatile("s_barrier" ::: "memory");
        STAMP(11);
        oproj_compute(ws, out, ort, oct, lds, wid, lane);
    }
    STAMP(14);
    if (id == 0 && wid == 0) {
        flag_wait(ws, F0 + (lane & 31), tags);
        if (lane == 0) flag_set(ws, FEP, tags);
    }
    STAMP_DUMP();
}

extern "C" void kernel_launch(void* const* d_in, const int* in_sizes, int n_in, void* d_out, int out_size, void* d_ws, size_t ws_size, hipStream_t stream) {
    const u16* xA = (const u16*)d_in[0]; const u16* wA = (const u16*)d_in[1]; const u16* woA = (const u16*)d_in[2];
    const u16* xB = (const u16*)d_in[3]; const u16* wB = (const u16*)d_in[4]; const u16* woB = (const u16*)d_in[5];
    (void)hipFuncSetAttribute((const void*)fused_kernel, hipFuncAttributeMaxDynamicSharedMemorySize, LDS_TOTAL + LDS_EXTRA);
    fused_kernel<<<256, 512, LDS_TOTAL + LDS_EXTRA, stream>>>(xA, wA, woA, xB, wB, woB, (u16*)d_out, (char*)d_ws);
}
```

```cpp
#include <hip/hip_runtime.h>
#include <hip/hip_bf16.h>
#include <stdint.h>

typedef unsigned short u16;
typedef unsigned long long u64;
typedef short bf16x8 __attribute__((ext_vector_type(8)));
typedef short s16x4 __attribute__((ext_vector_type(4)));
typedef float f32x4 __attribute__((ext_vector_type(4)));
typedef float f32x16 __attribute__((ext_vector_type(16)));
typedef unsigned u32x4 __attribute__((ext_vector_type(4)));
typedef unsigned u32x2 __attribute__((ext_vector_type(2)));
typedef __attribute__((address_space(3))) const char* lds_cptr;
typedef __attribute__((address_space(1))) u64 gu64;

constexpr int TILE_B = 56 * 512;
constexpr size_t QC_BYTES = (size_t)64 * TILE_B;
constexpr size_t V_BYTES = (size_t)16 * 64 * 2048;
constexpr size_t WS_QA = 0, WS_KA = WS_QA + QC_BYTES, WS_VA = WS_KA + QC_BYTES;
constexpr size_t WS_QB = WS_VA + V_BYTES, WS_KB = WS_QB + QC_BYTES, WS_VB = WS_KB + QC_BYTES;
constexpr size_t WS_OA = WS_VB + V_BYTES, WS_OB = WS_OA + QC_BYTES, WS_FLAGS = WS_OB + QC_BYTES;
constexpr int F1 = 0, F2 = 256, F3 = 512, F4 = 768, F0 = 1024, FEP = 1056;
constexpr float QSCALE = 0.18898223650461362f * 1.4426950408889634f;
constexpr int STG = 32768;
constexpr int LDS_Q = 147456, LDS_CTL = LDS_Q + 8192, LDS_MX = LDS_CTL + 64, LDS_TOTAL = LDS_MX + 1024 + 64;
constexpr int CTL_F1 = 0, CTL_F2CNT = 4, CTL_BAD = 8;
constexpr unsigned SPIN_MAX = 1u << 18;

#define STAMP(i)
#define STAMP_INIT()
#define STAMP_DUMP()
#define LDS_EXTRA 0

#define MFMA16(a, b, c) __builtin_amdgcn_mfma_f32_16x16x32_bf16(a, b, c, 0, 0, 0)
#define MFMA32(a, b, c) __builtin_amdgcn_mfma_f32_32x32x16_bf16(a, b, c, 0, 0, 0)
#define PIN(x) asm volatile("" : "+v"(x))
#define SBAR() __builtin_amdgcn_sched_barrier(0)
#define SC1 16

__device__ __forceinline__ unsigned cvtpk(float lo, float hi) {
    typedef float f2 __attribute__((ext_vector_type(2))); typedef __bf16 b2 __attribute__((ext_vector_type(2)));
    f2 v = {lo, hi}; b2 r = __builtin_convertvector(v, b2); return __builtin_bit_cast(unsigned, r);
}
__device__ __forceinline__ void glds16(const void* sbase, unsigned voff, unsigned lds_base) {
    asm volatile("s_mov_b32 m0, %2\n\ts_nop 0\n\tglobal_load_lds_dwordx4 %0, %1" :: "v"(voff), "s"(sbase), "s"(lds_base) : "memory");
}
__device__ __forceinline__ s16x4 vtr(lds_cptr p) {
    typedef short v4i16_t __attribute__((ext_vector_type(4)));
    return __builtin_bit_cast(s16x4, __builtin_amdgcn_ds_read_tr16_b64_v4i16((__attribute__((address_space(3))) v4i16_t*)p));
}
__device__ __forceinline__ bf16x8 cat8(s16x4 a, s16x4 b) { return (bf16x8){a[0], a[1], a[2], a[3], b[0], b[1], b[2], b[3]}; }
__device__ __forceinline__ bf16x8 ldsrd128(lds_cptr p) { return *(const __attribute__((address_space(3))) bf16x8*)p; }
#define RSRC(p, bytes) __builtin_amdgcn_make_buffer_rsrc((void*)(p), 0, (int)(bytes), 0x00020000)
__device__ __forceinline__ u32x4 bload128(__amdgpu_buffer_rsrc_t r, unsigned voff, unsigned soff) { return __builtin_amdgcn_raw_buffer_load_b128(r, voff, soff, SC1); }
__device__ __forceinline__ void bstore128(u32x4 v, __amdgpu_buffer_rsrc_t r, unsigned voff) { __builtin_amdgcn_raw_buffer_store_b128(v, r, voff, 0, SC1); }
__device__ __forceinline__ void bstore64(u32x2 v, __amdgpu_buffer_rsrc_t r, unsigned voff) { __builtin_amdgcn_raw_buffer_store_b64(v, r, voff, 0, SC1); }
__device__ __forceinline__ bf16x8 asb(u32x4 v) { return __builtin_bit_cast(bf16x8, v); }

__device__ __forceinline__ gu64* flagp(char* ws, int idx) { return (gu64*)(ws + WS_FLAGS + (size_t)idx * 128); }
__device__ __forceinline__ void flag_set(char* ws, int idx, u64 tag) { __hip_atomic_store(flagp(ws, idx), tag, __ATOMIC_RELAXED, __HIP_MEMORY_SCOPE_AGENT); }
__device__ __forceinline__ void flag_wait(char* ws, int idx, u64 tag) {
    gu64* p = flagp(ws, idx);
    for (unsigned spins = 0; spins < SPIN_MAX; ++spins) {
        const u64 v = __hip_atomic_load(p, __ATOMIC_RELAXED, __HIP_MEMORY_SCOPE_AGENT);
        if (__all(v == tag)) break;
        __builtin_amdgcn_s_sleep(1);
    }
}
__device__ __forceinline__ int opaque_lane() { int l; asm volatile("v_mbcnt_lo_u32_b32 %0, -1, 0\n\tv_mbcnt_hi_u32_b32 %0, -1, %0" : "=v"(l)); return l; }
__device__ __forceinline__ int f2_index(int h, int rb, int lane) { const int i = lane % 6, r = 2 * rb - 1 + i % 3; return F2 + (i < 3 ? (28 * h) >> 6 : (28 * h + 27) >> 6) * 32 + (r < 0 ? 0 : r); }
typedef __attribute__((address_space(3))) int* lds_iptr;
__device__ __forceinline__ int ctl_load(char* lds, int off) { return __hip_atomic_load((lds_iptr)(lds + LDS_CTL + off), __ATOMIC_RELAXED, __HIP_MEMORY_SCOPE_WORKGROUP); }
__device__ __forceinline__ void ctl_store(char* lds, int off, int v) { __hip_atomic_store((lds_iptr)(lds + LDS_CTL + off), v, __ATOMIC_RELAXED, __HIP_MEMORY_SCOPE_WORKGROUP); }
__device__ __forceinline__ void ctl_wait(char* lds, int off) {
    for (unsigned spins = 0; spins < (SPIN_MAX << 6); ++spins) { if (__builtin_amdgcn_readfirstlane(ctl_load(lds, off))) break; __builtin_amdgcn_s_sleep(1); }
    asm volatile("" ::: "memory");
}
#define BARRIER() asm volatile("s_waitcnt lgkmcnt(0)\n\ts_barrier" ::: "memory")
#define DRAIN_BARRIER() asm volatile("s_waitcnt vmcnt(0) lgkmcnt(0)\n\ts_barrier" ::: "memory")

struct QkvFrag { bf16x8 x[2][4], w[2][3]; };
struct QkvAcc { f32x4 q[4], k[4], v[4]; };
struct QkvOut { u32x4 q[2], k[2], v[2]; };
__device__ __forceinline__ void qkv_pack(QkvOut& o, const QkvAcc& a, float qs) {
#pragma unroll
    for (int tb = 0; tb < 2; ++tb) {
        const f32x4 q0 = a.q[2 * tb] * qs, q1 = a.q[2 * tb + 1] * qs, k0 = a.k[2 * tb], k1 = a.k[2 * tb + 1], v0 = a.v[2 * tb], v1 = a.v[2 * tb + 1];
        { auto ra = __builtin_amdgcn_permlane16_swap(cvtpk(q0[0], q0[1]), cvtpk(q1[0], q1[1]), false, false); auto rb = __builtin_amdgcn_permlane16_swap(cvtpk(q0[2], q0[3]), cvtpk(q1[2], q1[3]), false, false);
          o.q[tb] = (u32x4){ra[0], rb[0], ra[1], rb[1]}; }
        { auto ra = __builtin_amdgcn_permlane16_swap(cvtpk(k0[0], k0[1]), cvtpk(k1[0], k1[1]), false, false); auto rb = __builtin_amdgcn_permlane16_swap(cvtpk(k0[2], k0[3]), cvtpk(k1[2], k1[3]), false, false);
          o.k[tb] = (u32x4){ra[0], rb[0], ra[1], rb[1]}; }
        { auto ra = __builtin_amdgcn_permlane32_swap(cvtpk(v0[0], v0[1]), cvtpk(v1[0], v1[1]), false, false); auto rb = __builtin_amdgcn_permlane32_swap(cvtpk(v0[2], v0[3]), cvtpk(v1[2], v1[3]), false, false);
          o.v[tb] = (u32x4){ra[0], rb[0], ra[1], rb[1]}; }
    }
}
template <int BR, int I>
__device__ __forceinline__ void qkv_store(const QkvOut& o, char* ws, int rt, int ct, int c, int lane) {
    constexpr int tb = I / 3, kind = I % 3;
    const int tile = rt * 2 + tb;
    if (kind < 2) {
        const unsigned off = (unsigned)(tile * 56 + 8 * ct + 2 * c) * 512u + (unsigned)lane * 16u;
        if (kind == 0) bstore128(o.q[tb], RSRC(ws + (BR ? WS_QB : WS_QA), QC_BYTES), off); else bstore128(o.k[tb], RSRC(ws + (BR ? WS_KB : WS_KA), QC_BYTES), off);
    } else {
        const int n = 64 * ct + 16 * c + (lane & 15), h = n / 28, d = n - 28 * h;
        const unsigned off = (unsigned)(h * 64 + tile) * 2048u + (unsigned)(lane >> 4) * 512u + (unsigned)d * 16u;
        const auto rs = RSRC(ws + (BR ? WS_VB : WS_VA), V_BYTES);
        bstore128(o.v[tb], rs, off);
        if (d == 27) bstore128((u32x4){0x3F803F80u, 0x3F803F80u, 0x3F803F80u, 0x3F803F80u}, rs, off + 16u);
    }
}

__device__ __forceinline__ void qkv_phase(const u16* xA, const u16* wA, const u16* xB, const u16* wB, char* ws, int rt, int ct, u64 tag, char* lds, int wid, int lane, QkvOut& outB) {
    if (wid >= 4) {
        const unsigned lds0 = (unsigned)(uintptr_t)lds;
        const int lw = wid - 4, r8 = lane >> 3, dpos = lane & 7;
        unsigned xoff[2], woff[2];
#pragma unroll
        for (int i = 0; i < 2; ++i) { const int row = 16 * lw + 8 * i + r8;
            xoff[i] = (unsigned)row * 896u + (unsigned)((dpos ^ ((row >> 1) & 7)) << 4);
            const int f2 = ((row >> 1) & 1) | (((row >> 3) & 1) << 1);
            woff[i] = (unsigned)row * 2688u + (unsigned)((((dpos >> 1) ^ f2) * 2 + (dpos & 1)) << 4); }
        const int rot = __builtin_amdgcn_readfirstlane(((rt & 7) + 2 * (ct & 3)) % 7);
        const char* xa = (const char*)xA + (size_t)rt * 64 * 896; const char* wa = (const char*)wA + (size_t)ct * 128;
        const char* xb = (const char*)xB + (size_t)rt * 64 * 896; const char* wb = (const char*)wB + (size_t)ct * 128;
#define QKV_ISSUE(g) do { int st_ = ((g) % 7) + rot; st_ = st_ >= 7 ? st_ - 7 : st_; const char* x_ = ((g) < 7 ? xa : xb) + st_ * 128; const char* w_ = ((g) < 7 ? wa : wb) + (size_t)st_ * (64 * 2688); \
        const unsigned sl_ = lds0 + ((g) & 3) * STG + lw * 2048; \
        glds16(x_, xoff[0], sl_); glds16(x_, xoff[1], sl_ + 1024); \
        glds16(w_, woff[0], sl_ + 8192); glds16(w_, woff[1], sl_ + 8192 + 1024); glds16(w_ + 896, woff[0], sl_ + 16384); glds16(w_ + 896, woff[1], sl_ + 16384 + 1024); \
        glds16(w_ + 1792, woff[0], sl_ + 24576); glds16(w_ + 1792, woff[1], sl_ + 24576 + 1024); } while (0)
        QKV_ISSUE(0); QKV_ISSUE(1);
#pragma unroll
        for (int g = 0; g < 14; ++g) {
            if (g <= 12) asm volatile("s_waitcnt vmcnt(8) lgkmcnt(0)\n\ts_barrier" ::: "memory");
            else asm volatile("s_waitcnt vmcnt(0) lgkmcnt(0)\n\ts_barrier" ::: "memory");
            if (g == 0) STAMP(1);
            if (g + 2 < 14) QKV_ISSUE(g + 2);
        }
        return;
    }
    const int c = wid, g4 = lane >> 4, t16 = lane & 15;
    const unsigned xr0 = (unsigned)t16 * 128u + (unsigned)((g4 ^ (t16 >> 1)) << 4);
    const int q = t16 >> 2, p = lane & 3;
    const int rf2 = ((q >> 1) & 1) | ((g4 & 1) << 1);
    const unsigned wr0 = 8192u + (unsigned)(8 * g4 + q) * 128u + (unsigned)((c ^ rf2) * 32) + (unsigned)p * 8u;
    QkvAcc acc; QkvOut outA;
#pragma unroll
    for (int i = 0; i < 4; ++i) acc.q[i] = acc.k[i] = acc.v[i] = (f32x4){};
    QkvFrag fr[2];
#define QMF(i) do { if (g > 0) { constexpr int ks_ = (i) / 12, s_ = ((i) % 12) / 4, b_ = (i) % 4; const QkvFrag& f_ = fr[(g - 1) & 1]; \
        if (s_ == 0) acc.q[b_] = MFMA16(f_.w[ks_][0], f_.x[ks_][b_], acc.q[b_]); if (s_ == 1) acc.k[b_] = MFMA16(f_.w[ks_][1], f_.x[ks_][b_], acc.k[b_]); \
        if (s_ == 2) acc.v[b_] = MFMA16(f_.x[ks_][b_], f_.w[ks_][2], acc.v[b_]); } SBAR(); } while (0)
#define QRX(ks, b) do { if (g < 14) fr[g & 1].x[ks][b] = ldsrd128(sl + (xr0 ^ ((ks) * 64)) + (b) * 2048); } while (0)
#define QRW(ks, s) do { if (g < 14) { lds_cptr wp_ = sl + wr0 + (ks) * 4096 + (s) * 8192; fr[g & 1].w[ks][s] = cat8(vtr(wp_), vtr(wp_ + 512)); } } while (0)
#define QST(gg, i) do { if (g == (gg)) { qkv_store<0, i>(outA, ws, rt, ct, c, lane); SBAR(); } } while (0)
#pragma unroll
    for (int g = 0; g < 15; ++g) {
        if (g == 12) asm volatile("s_waitcnt vmcnt(0) lgkmcnt(0)\n\ts_barrier" ::: "memory");
        else if (g < 14) asm volatile("s_waitcnt lgkmcnt(0)\n\ts_barrier" ::: "memory");
        SBAR();
        if (g == 7) STAMP(2);
        lds_cptr sl = (lds_cptr)lds + (g & 3) * STG;
        if (g == 12 && threadIdx.x == 0) flag_set(ws, F1 + ct * 32 + rt, tag);
        SBAR();
        QRX(0, 0); QMF(0); QRX(0, 1); QMF(1); QRX(0, 2); QMF(2); QRX(0, 3); QMF(3); QST(8, 0); QST(9, 4); QRW(0, 0); QMF(4); QRW(0, 1); QMF(5); QRW(0, 2); QMF(6);
        QRX(1, 0); QMF(7); QRX(1, 1); QMF(8); QRX(1, 2); QMF(9); QST(8, 1); QST(9, 5); QRX(1, 3); QMF(10); QRW(1, 0); QMF(11); QRW(1, 1); QMF(12); QRW(1, 2); QMF(13);
        QMF(14); QMF(15); QST(8, 2); QMF(16); QMF(17); QMF(18); QMF(19); QMF(20); QMF(21); QST(8, 3); QMF(22); QMF(23);
        if (g == 7) {
            qkv_pack(outA, acc, QSCALE);
#pragma unroll
            for (int i = 0; i < 4; ++i) acc.q[i] = acc.k[i] = acc.v[i] = (f32x4){};
            STAMP(3);
        }
    }
    qkv_pack(outB, acc, 1.0f);
}

__device__ __forceinline__ float max16(const f32x16& s) {
    float a = fmaxf(fmaxf(s[0], s[1]), s[2]), b = fmaxf(fmaxf(s[3], s[4]), s[5]);
    a = fmaxf(fmaxf(a, s[6]), s[7]); b = fmaxf(fmaxf(b, s[8]), s[9]); a = fmaxf(fmaxf(a, s[10]), s[11]); b = fmaxf(fmaxf(b, s[12]), s[13]);
    a = fmaxf(fmaxf(a, s[14]), s[15]); return fmaxf(a, b);
}
__device__ __forceinline__ float xhalf_max(float m) {
    auto r = __builtin_amdgcn_permlane32_swap(__float_as_uint(m), __float_as_uint(m), false, false);
    return fmaxf(__uint_as_float(r[0]), __uint_as_float(r[1]));
}
__device__ __forceinline__ void patch(u32x4& f0, u32x4& f1, bool pe, bool po, unsigned a, unsigned b) {
    f0[0] = po ? a : f0[0]; f0[1] = po ? b : f0[1]; f1[2] = pe ? a : f1[2]; f1[3] = pe ? b : f1[3];
}
__device__ __forceinline__ void exp_pack(const f32x16& s, u32x4& p0, u32x4& p1) {
    float e[16];
#pragma unroll
    for (int r = 0; r < 16; ++r) e[r] = __builtin_amdgcn_exp2f(s[r]);
    p0 = (u32x4){cvtpk(e[0], e[1]), cvtpk(e[2], e[3]), cvtpk(e[4], e[5]), cvtpk(e[6], e[7])};
    p1 = (u32x4){cvtpk(e[8], e[9]), cvtpk(e[10], e[11]), cvtpk(e[12], e[13]), cvtpk(e[14], e[15])};
}
__device__ __forceinline__ bool merge_store(const f32x16 (&o)[4], char* ws, int h, int rb, char* lds, int wid, int lane) {
    const bool hi = lane >= 32;
#pragma unroll
    for (int j = 0; j < 4; ++j)
#pragma unroll
        for (int rq = 0; rq < 4; ++rq)
            *(f32x4*)(lds + ((wid * 4 + j) * 4 + rq) * 1024 + lane * 16) = (f32x4){o[j][4 * rq], o[j][4 * rq + 1], o[j][4 * rq + 2], o[j][4 * rq + 3]};
    const int j = wid >> 1, rq0 = (wid & 1) * 2;
    const int tile = rb * 4 + j, q = lane & 31;
    const int col0 = 28 * h + 8 * rq0 + (hi ? 4 : 0), col1 = col0 + 8;
    const unsigned off0 = (unsigned)((tile * 56 + (col0 >> 3)) * 32 + q) * 16u + (unsigned)((col0 >> 2) & 1) * 8u, off1 = (unsigned)((tile * 56 + (col1 >> 3)) * 32 + q) * 16u + (unsigned)((col1 >> 2) & 1) * 8u;
    BARRIER();
    STAMP(10);
    f32x4 p0[8], p1[8]; float pl[8];
#pragma unroll
    for (int w = 0; w < 8; ++w) {
        const char* b = lds + (w * 4 + j) * 4096;
        p0[w] = *(const f32x4*)(b + rq0 * 1024 + lane * 16); p1[w] = *(const f32x4*)(b + (rq0 + 1) * 1024 + lane * 16);
        pl[w] = *(const float*)(b + 3 * 1024 + (32 + q) * 16);
    }
    f32x4 a0 = ((p0[0] + p0[1]) + (p0[2] + p0[3])) + ((p0[4] + p0[5]) + (p0[6] + p0[7])), a1 = ((p1[0] + p1[1]) + (p1[2] + p1[3])) + ((p1[4] + p1[5]) + (p1[6] + p1[7]));
    const float l = ((pl[0] + pl[1]) + (pl[2] + pl[3])) + ((pl[4] + pl[5]) + (pl[6] + pl[7]));
    const float inv = __builtin_amdgcn_rcpf(l);
    const auto rO = RSRC(ws + WS_OA, QC_BYTES);
    a0 *= inv; a1 *= inv;
    const bool last = rq0 == 2 && hi;
    bstore64((u32x2){cvtpk(a0[0], a0[1]), cvtpk(a0[2], a0[3])}, rO, off0);
    if (!last) bstore64((u32x2){cvtpk(a1[0], a1[1]), cvtpk(a1[2], a1[3])}, rO, off1);
    float z = a0[0] * 0.f; z = fmaf(a0[1], 0.f, z); z = fmaf(a0[2], 0.f, z); z = fmaf(a0[3], 0.f, z);
    float z1 = a1[0] * 0.f; z1 = fmaf(a1[1], 0.f, z1); z1 = fmaf(a1[2], 0.f, z1); z1 = fmaf(a1[3], 0.f, z1);
    const bool ok = l > 1.0e-18f && l < 1.0e30f && z == 0.f && (last || z1 == 0.f);
    return !ok;
}

__device__ __forceinline__ void attnA_safe(char* ws, int h, int rb, char* lds, int wid, int lane, f32x16 (&o)[4]) {
    const bool hi = lane >= 32, pe = hi && !(h & 1), po = !hi && (h & 1); const int m0 = (28 * h) >> 3;
    const auto rQ = RSRC(ws + WS_QA, QC_BYTES), rK = RSRC(ws + WS_KA, QC_BYTES), rV = RSRC(ws + WS_VA, V_BYTES);
    const unsigned lo16 = (unsigned)lane * 16u;
    float* mx = (float*)(lds + LDS_MX);
#pragma unroll
    for (int j = 0; j < 4; ++j) {
        const unsigned b = (unsigned)((rb * 4 + j) * 56 + m0) * 512u;
        u32x4 q0 = bload128(rQ, lo16, b), q1 = bload128(rQ, lo16, b + 1024u);
        patch(q0, q1, pe, po, 0u, 0u);
        float m = -3.0e38f; f32x16 acc = {};
#pragma unroll 1
        for (int t = 0; t < 8; ++t) {
            const unsigned ko = (unsigned)((wid * 8 + t) * 56 + m0) * 512u, vo = (unsigned)(h * 64 + wid * 8 + t) * 2048u;
            u32x4 k0 = bload128(rK, lo16, ko), k1 = bload128(rK, lo16, ko + 1024u), v0 = bload128(rV, lo16, vo), v1 = bload128(rV, lo16, vo + 1024u);
            patch(k0, k1, pe, po, 0u, 0u);
            f32x16 s = {}; s = MFMA32(asb(k0), asb(q0), s); s = MFMA32(asb(k1), asb(q1), s);
            const float mn = fmaxf(m, xhalf_max(max16(s)));
            const float al = __builtin_amdgcn_exp2f(m - mn); m = mn;
#pragma unroll
            for (int r = 0; r < 16; ++r) { s[r] -= mn; acc[r] *= al; }
            u32x4 p0, p1; exp_pack(s, p0, p1);
            acc = MFMA32(asb(v0), asb(p0), acc); acc = MFMA32(asb(v1), asb(p1), acc);
        }
        BARRIER();
        if (!hi) mx[wid * 32 + lane] = m;
        BARRIER();
        float g = m;
#pragma unroll
        for (int w = 0; w < 8; ++w) g = fmaxf(g, mx[w * 32 + (lane & 31)]);
        const float f = __builtin_amdgcn_exp2f(m - g);
#pragma unroll
        for (int r = 0; r < 16; ++r) acc[r] *= f;
        o[j] = acc;
    }
}

struct AttnBPre { u32x4 q, kp, kc; u32x2 v[2][2]; };
__device__ __forceinline__ void attnB_load(AttnBPre& a, char* ws, int h, int rb, int wid, int lane) {
    const int m0 = (28 * h) >> 3, T = rb * 4 + (wid >> 1), sub = wid & 1;
    const int Tp = sub ? T : (T > 0 ? T - 1 : 0), subp = sub ^ 1;
    const auto rQ = RSRC(ws + WS_QB, QC_BYTES), rK = RSRC(ws + WS_KB, QC_BYTES), rV = RSRC(ws + WS_VB, V_BYTES);
    const int g = lane >> 4, t16 = lane & 15;
    const unsigned co = (unsigned)g * 512u + (unsigned)t16 * 16u;
    a.q = bload128(rQ, co, (unsigned)(T * 56 + m0) * 512u + (unsigned)sub * 256u);
    a.kp = bload128(rK, co, (unsigned)(Tp * 56 + m0) * 512u + (unsigned)subp * 256u);
    a.kc = bload128(rK, co, (unsigned)(T * 56 + m0) * 512u + (unsigned)sub * 256u);
    const unsigned vo = (unsigned)(g & 1) * 512u + (unsigned)t16 * 16u + (unsigned)(g >> 1) * 8u;
#pragma unroll
    for (int db = 0; db < 2; ++db) {
        a.v[db][0] = __builtin_amdgcn_raw_buffer_load_b64(rV, vo + db * 256u, (unsigned)(h * 64 + Tp) * 2048u + (unsigned)subp * 1024u, SC1);
        a.v[db][1] = __builtin_amdgcn_raw_buffer_load_b64(rV, vo + db * 256u, (unsigned)(h * 64 + T) * 2048u + (unsigned)sub * 1024u, SC1);
    }
}
__device__ __forceinline__ void attnB_compute(AttnBPre& a, char* ws, int h, int rb, int wid, int lane) {
    const int T = rb * 4 + (wid >> 1), sub = wid & 1, g = lane >> 4, ql = lane & 15;
    const bool padl = (h & 1) ? g == 0 : g == 3;
    if (h & 1) { a.q[0] = padl ? 0u : a.q[0]; a.q[1] = padl ? 0u : a.q[1]; a.kp[0] = padl ? 0u : a.kp[0]; a.kp[1] = padl ? 0u : a.kp[1]; a.kc[0] = padl ? 0u : a.kc[0]; a.kc[1] = padl ? 0u : a.kc[1]; }
    else { a.q[2] = padl ? 0u : a.q[2]; a.q[3] = padl ? 0u : a.q[3]; a.kp[2] = padl ? 0u : a.kp[2]; a.kp[3] = padl ? 0u : a.kp[3]; a.kc[2] = padl ? 0u : a.kc[2]; a.kc[3] = padl ? 0u : a.kc[3]; }
    f32x4 sp = {}, sc = {};
    sp = MFMA16(asb(a.kp), asb(a.q), sp); sc = MFMA16(asb(a.kc), asb(a.q), sc);
    const bool hasp = sub || T > 0;
    float m = -INFINITY;
#pragma unroll
    for (int r = 0; r < 4; ++r) {
        const int dc = ql - (4 * g + r), dp = dc + 16;
        sp[r] = (hasp && dp <= 14 && !(dp & 1)) ? sp[r] : -INFINITY; sc[r] = (dc >= 0 && !(dc & 1)) ? sc[r] : -INFINITY;
        m = fmaxf(m, fmaxf(sp[r], sc[r]));
    }
    { auto r = __builtin_amdgcn_permlane16_swap(__float_as_uint(m), __float_as_uint(m), false, false); m = fmaxf(__uint_as_float(r[0]), __uint_as_float(r[1])); }
    m = xhalf_max(m);
    float e[8];
#pragma unroll
    for (int r = 0; r < 4; ++r) { e[r] = __builtin_amdgcn_exp2f((sp[r] - m) * QSCALE); e[4 + r] = __builtin_amdgcn_exp2f((sc[r] - m) * QSCALE); }
    const u32x4 pf = {cvtpk(e[0], e[1]), cvtpk(e[2], e[3]), cvtpk(e[4], e[5]), cvtpk(e[6], e[7])};
    f32x4 o0 = {}, o1 = {};
    o0 = MFMA16(asb((u32x4){a.v[0][0][0], a.v[0][0][1], a.v[0][1][0], a.v[0][1][1]}), asb(pf), o0);
    o1 = MFMA16(asb((u32x4){a.v[1][0][0], a.v[1][0][1], a.v[1][1][0], a.v[1][1][1]}), asb(pf), o1);
    const float l = __uint_as_float(__builtin_amdgcn_ds_bpermute((48 + ql) * 4, __float_as_uint(o1[0])));
    const float inv = __builtin_amdgcn_rcpf(l);
    const auto rO = RSRC(ws + WS_OB, QC_BYTES);
    const unsigned tok = (unsigned)(sub * 16 + ql) * 16u;
    { const int col = 28 * h + 4 * g; bstore64((u32x2){cvtpk(o0[0] * inv, o0[1] * inv), cvtpk(o0[2] * inv, o0[3] * inv)}, rO, (unsigned)(T * 56 + (col >> 3)) * 512u + tok + (unsigned)((col >> 2) & 1) * 8u); }
    if (g < 3) { const int col = 28 * h + 16 + 4 * g; bstore64((u32x2){cvtpk(o1[0] * inv, o1[1] * inv), cvtpk(o1[2] * inv, o1[3] * inv)}, rO, (unsigned)(T * 56 + (col >> 3)) * 512u + tok + (unsigned)((col >> 2) & 1) * 8u); }
}

__device__ __forceinline__ void attnA_phase(char* ws, const u16* woA, const u16* woB, int oct, int h, int rb, u64 tag, int rt, int ct, bool has_qkv, const QkvOut& outB, char* lds, int wid, int lane) {
    const bool hi = lane >= 32, pe = hi && !(h & 1), po = !hi && (h & 1);
    const int m0 = (28 * h) >> 3;
    const auto rK = RSRC(ws + WS_KA, QC_BYTES), rV = RSRC(ws + WS_VA, V_BYTES);
    const unsigned lo16 = (unsigned)lane * 16u;
    const unsigned kbase = (unsigned)((wid * 8) * 56 + m0) * 512u, vbase = (unsigned)(h * 64 + wid * 8) * 2048u;
    const int rot = rb & 7;
    u32x4 qf[4][2], kf[2][2], vf[2][2];
    {   const unsigned k0 = kbase + (unsigned)rot * TILE_B, v0 = vbase + (unsigned)rot * 2048u, k1 = kbase + (unsigned)((rot + 1) & 7) * TILE_B;
        kf[0][0] = bload128(rK, lo16, k0); kf[0][1] = bload128(rK, lo16, k0 + 1024u);
        if (wid >= 4) { const unsigned b = (unsigned)((rb * 4 + wid - 4) * 56 + m0) * 512u; const auto rQ = RSRC(ws + WS_QA, QC_BYTES); qf[0][0] = bload128(rQ, lo16, b); qf[0][1] = bload128(rQ, lo16, b + 1024u); }
        vf[0][0] = bload128(rV, lo16, v0); vf[0][1] = bload128(rV, lo16, v0 + 1024u);
        kf[1][0] = bload128(rK, lo16, k1); kf[1][1] = bload128(rK, lo16, k1 + 1024u); }
    patch(kf[0][0], kf[0][1], pe, po, 0x3F803F80u, 0u);
    if (wid >= 4) {
        patch(qf[0][0], qf[0][1], pe, po, 0u, 0u);
        f32x16 s = {}; s = MFMA32(asb(kf[0][0]), asb(qf[0][0]), s); s = MFMA32(asb(kf[0][1]), asb(qf[0][1]), s);
        const float m = xhalf_max(max16(s));
        const unsigned mh = cvtpk(-m, 0.f) & 0xffffu; const float r = -m - __uint_as_float(mh << 16);
        patch(qf[0][0], qf[0][1], pe, po, mh | (cvtpk(r, 0.f) << 16), 0u);
        *(u32x4*)(lds + LDS_Q + (wid - 4) * 2048 + lane * 16) = qf[0][0]; *(u32x4*)(lds + LDS_Q + (wid - 4) * 2048 + 1024 + lane * 16) = qf[0][1];
    }
    BARRIER();
    STAMP(6);
#pragma unroll
    for (int j = 0; j < 4; ++j) { qf[j][0] = *(const u32x4*)(lds + LDS_Q + j * 2048 + lane * 16); qf[j][1] = *(const u32x4*)(lds + LDS_Q + j * 2048 + 1024 + lane * 16); }
    if (has_qkv && wid < 4) {
        qkv_store<1, 0>(outB, ws, rt, ct, wid, lane); qkv_store<1, 1>(outB, ws, rt, ct, wid, lane); qkv_store<1, 2>(outB, ws, rt, ct, wid, lane);
        qkv_store<1, 3>(outB, ws, rt, ct, wid, lane); qkv_store<1, 4>(outB, ws, rt, ct, wid, lane); qkv_store<1, 5>(outB, ws, rt, ct, wid, lane);
    }
#pragma unroll
    for (int j = 0; j < 4; ++j) { PIN(qf[j][0]); PIN(qf[j][1]); }
    PIN(kf[0][0]); PIN(kf[0][1]);
    f32x16 o[4];
#define VEXP(d, s) asm volatile("v_exp_f32 %0, %1" : "=v"(d) : "v"(s))
#define VCVT(d, a, b) asm volatile("v_cvt_pk_bf16_f32 %0, %1, %2" : "=v"(d) : "v"(a), "v"(b))
#define MF0(d, a, b) asm volatile("v_mfma_f32_32x32x16_bf16 %0, %1, %2, 0" : "=&v"(d) : "v"(a), "v"(b))
#define MFA(d, a, b) asm volatile("v_mfma_f32_32x32x16_bf16 %0, %1, %2, %0" : "+v"(d) : "v"(a), "v"(b))
#define SLOT(PV, QK, Sc, Pc0, Pc1, Sn, kn0, kn1, qn0, qn1, Op, vp0, vp1, Pp0, Pp1, INJ) do { \
        float e0, e1, e2, e3, e4, e5, e6, e7, e8, e9, e10, e11, e12, e13, e14, e15; \
        if (QK) MF0(Sn, kn0, qn0); \
        VEXP(e0, Sc[0]); VEXP(e1, Sc[1]); VEXP(e2, Sc[2]); VEXP(e3, Sc[3]); \
        if (QK) MFA(Sn, kn1, qn1); \
        VEXP(e4, Sc[4]); VEXP(e5, Sc[5]); VEXP(e6, Sc[6]); VEXP(e7, Sc[7]); VCVT(Pc0[0], e0, e1); VCVT(Pc0[1], e2, e3); \
        INJ; \
        if ((PV) == 2) MFA(Op, vp0, Pp0); else if ((PV) == 1) MF0(Op, vp0, Pp0); \
        VEXP(e8, Sc[8]); VEXP(e9, Sc[9]); VEXP(e10, Sc[10]); VEXP(e11, Sc[11]); VCVT(Pc0[2], e4, e5); VCVT(Pc0[3], e6, e7); \
        if (PV) MFA(Op, vp1, Pp1); \
        VEXP(e12, Sc[12]); VEXP(e13, Sc[13]); VEXP(e14, Sc[14]); VEXP(e15, Sc[15]); VCVT(Pc1[0], e8, e9); VCVT(Pc1[1], e10, e11); VCVT(Pc1[2], e12, e13); VCVT(Pc1[3], e14, e15); \
    } while (0)
    f32x16 sA, sB; u32x4 pA0, pA1, pB0, pB1;
    asm volatile("s_nop 1\n\tv_mfma_f32_32x32x16_bf16 %0, %1, %2, 0\n\tv_mfma_f32_32x32x16_bf16 %0, %3, %4, %0\n\ts_nop 7\n\ts_nop 3" : "=&v"(sA) : "v"(kf[0][0]), "v"(qf[0][0]), "v"(kf[0][1]), "v"(qf[0][1]));
#define PATCHK(kx) do { PIN(kx[0]); PIN(kx[1]); patch(kx[0], kx[1], pe, po, 0x3F803F80u, 0u); PIN(kx[0]); PIN(kx[1]); } while (0)
#define LOADK(kc, tt) do { if ((tt) < 8) { const unsigned o_ = kbase + (unsigned)(((tt) + rot) & 7) * TILE_B; kc[0] = bload128(rK, lo16, o_); kc[1] = bload128(rK, lo16, o_ + 1024u); } } while (0)
#define LOADV(vx, tt) do { if ((tt) < 8) { const unsigned o_ = vbase + (unsigned)(((tt) + rot) & 7) * 2048u; vx[0] = bload128(rV, lo16, o_); vx[1] = bload128(rV, lo16, o_ + 1024u); } } while (0)
#define TILE(kc, kx, vc, vx, t) do { \
        SLOT((t) == 0 ? 0 : (t) == 1 ? 1 : 2, true, sA, pA0, pA1, sB, kc[0], kc[1], qf[1][0], qf[1][1], o[3], vx[0], vx[1], pB0, pB1, (void)0); \
        LOADV(vx, (t) + 1); \
        SLOT((t) == 0 ? 1 : 2, true, sB, pB0, pB1, sA, kc[0], kc[1], qf[2][0], qf[2][1], o[0], vc[0], vc[1], pA0, pA1, (void)0); \
        SLOT((t) == 0 ? 1 : 2, true, sA, pA0, pA1, sB, kc[0], kc[1], qf[3][0], qf[3][1], o[1], vc[0], vc[1], pB0, pB1, if ((t) < 7) PATCHK(kx)); \
        LOADK(kc, (t) + 2); \
        SLOT((t) == 0 ? 1 : 2, (t) < 7, sB, pB0, pB1, sA, kx[0], kx[1], qf[0][0], qf[0][1], o[2], vc[0], vc[1], pA0, pA1, (void)0); \
    } while (0)
    AttnBPre bpre; u64 flb = 0; bool bready = false; unsigned pf0 = 0, pf1 = 0;
#pragma unroll
    for (int t = 0; t < 8; t += 2) {
        if (t == 0) __builtin_amdgcn_s_setprio(3); else if (t == 2) __builtin_amdgcn_s_setprio(2); else if (t == 4) __builtin_amdgcn_s_setprio(1); else __builtin_amdgcn_s_setprio(0);
        TILE(kf[0], kf[1], vf[0], vf[1], t);
        TILE(kf[1], kf[0], vf[1], vf[0], t + 1);
        if (t == 0) {
            if (has_qkv && wid < 4) {
                asm volatile("s_waitcnt vmcnt(4)" ::: "memory");
                if (opaque_lane() == 0 && __hip_atomic_fetch_add((lds_iptr)(lds + LDS_CTL + CTL_F2CNT), 1, __ATOMIC_RELAXED, __HIP_MEMORY_SCOPE_WORKGROUP) == 3) flag_set(ws, F2 + ct * 32 + rt, tag);
            }
        } else if (t == 2) {
            const int ln = opaque_lane();
            flb = __hip_atomic_load(flagp(ws, f2_index(h, rb, ln)), __ATOMIC_RELAXED, __HIP_MEMORY_SCOPE_AGENT);
            if (oct < 7) {
                const int r = min(wid * 56 + ln, 447);
                pf0 = *(const unsigned*)((const char*)woA + (size_t)r * 896 + oct * 128); pf1 = *(const unsigned*)((const char*)woB + (size_t)r * 896 + oct * 128);
            }
        } else if (t == 4) { bready = __all(flb == tag); attnB_load(bpre, ws, h, rb, wid, opaque_lane()); }
    }
    asm volatile("s_nop 1\n\tv_mfma_f32_32x32x16_bf16 %0, %1, %2, %0\n\tv_mfma_f32_32x32x16_bf16 %0, %3, %4, %0\n\ts_nop 7\n\ts_nop 3" : "+v"(o[3]) : "v"(vf[1][0]), "v"(pB0), "v"(vf[1][1]), "v"(pB1));
    asm volatile("" : "+v"(o[0]), "+v"(o[1]), "+v"(o[2]));
    asm volatile("" :: "v"(pf0), "v"(pf1));
    STAMP(7);
    lane = opaque_lane();
    const bool bad = merge_store(o, ws, h, rb, lds, wid, lane);
    if (__any(bad) && lane == 0) ctl_store(lds, CTL_BAD, 1);
    STAMP(8);
    if (!bready) { flag_wait(ws, f2_index(h, rb, lane), tag); attnB_load(bpre, ws, h, rb, wid, lane); }
    attnB_compute(bpre, ws, h, rb, wid, lane);
    STAMP(15);
    DRAIN_BARRIER();
    if (__builtin_amdgcn_readfirstlane(ctl_load(lds, CTL_BAD))) {
        attnA_safe(ws, h, rb, lds, wid, lane, o);
        BARRIER();
        merge_store(o, ws, h, rb, lds, wid, lane);
        DRAIN_BARRIER();
    }
    if (threadIdx.x == 0) flag_set(ws, F3 + rb * 16 + h, tag);
}

__device__ __forceinline__ void oproj_issue_w(const u16* woA, const u16* woB, int ct, unsigned lds_base, int wid, int lane) {
    if (wid == 7) return;
    const int r8 = lane >> 3, dpos = lane & 7;
    const unsigned woff = (unsigned)r8 * 896u + (unsigned)((dpos ^ ((r8 & 2) << 1)) << 4);
#pragma unroll
    for (int i = 0; i < 16; ++i) {
        const int pc = wid * 16 + i;
        const int br = pc >= 56, pr = pc - br * 56;
        glds16((const char*)(br ? woB : woA) + (size_t)ct * 128 + (size_t)pr * 8 * 896, woff, lds_base + pc * 1024);
    }
}
__device__ __forceinline__ void oproj_compute(char* ws, u16* out, int rt, int ct, char* lds, int wid, int lane) {
    const int br = wid >> 2, tt = (wid >> 1) & 1, kh = wid & 1;
    const auto rO = RSRC(ws + (br ? WS_OB : WS_OA), QC_BYTES);
    const int tile = rt * 2 + tt;
    u32x4 of[14];
#pragma unroll
    for (int i = 0; i < 14; ++i) of[i] = bload128(rO, (unsigned)lane * 16u, (unsigned)(tile * 56 + 2 * (kh * 14 + i)) * 512u);
    const int g16 = lane >> 4, q = (lane & 15) >> 2, p = lane & 3;
    bf16x8 wf[2][14];
#pragma unroll
    for (int i = 0; i < 14; ++i)
#pragma unroll
        for (int c = 0; c < 2; ++c) {
            const int row = (kh * 14 + i) * 16 + 8 * (g16 >> 1) + q;
            lds_cptr wp = (lds_cptr)lds + br * 57344 + (unsigned)row * 128u + (unsigned)(((2 * c + (g16 & 1)) ^ (q & 2)) * 32) + (unsigned)p * 8u;
            wf[c][i] = cat8(vtr(wp), vtr(wp + 512));
        }
    f32x16 acc[2] = {};
#pragma unroll
    for (int i = 0; i < 14; ++i) { acc[0] = MFMA32(wf[0][i], asb(of[i]), acc[0]); acc[1] = MFMA32(wf[1][i], asb(of[i]), acc[1]); }
    char* ldsr = lds + 114688;
    {   const f32x16& gv = kh ? acc[0] : acc[1];
#pragma unroll
        for (int rq = 0; rq < 4; ++rq) *(f32x4*)(ldsr + (wid * 4 + rq) * 1024 + lane * 16) = (f32x4){gv[4 * rq], gv[4 * rq + 1], gv[4 * rq + 2], gv[4 * rq + 3]};
    }
    BARRIER();
    f32x16 r = kh ? acc[1] : acc[0];
#pragma unroll
    for (int rq = 0; rq < 4; ++rq) { const f32x4 x = *(const f32x4*)(ldsr + ((wid ^ 1) * 4 + rq) * 1024 + lane * 16); r[4 * rq] += x[0]; r[4 * rq + 1] += x[1]; r[4 * rq + 2] += x[2]; r[4 * rq + 3] += x[3]; }
    u16* orow = out + (size_t)(rt * 64 + tt * 32 + (lane & 31)) * 896 + br * 448 + ct * 64 + kh * 32 + (lane >> 5) * 8;
#pragma unroll
    for (int pr = 0; pr < 2; ++pr) {
        const unsigned a0 = cvtpk(r[8 * pr], r[8 * pr + 1]), a1 = cvtpk(r[8 * pr + 2], r[8 * pr + 3]), b0 = cvtpk(r[8 * pr + 4], r[8 * pr + 5]), b1 = cvtpk(r[8 * pr + 6], r[8 * pr + 7]);
        auto r0 = __builtin_amdgcn_permlane32_swap(a0, b0, false, false);
        auto r1 = __builtin_amdgcn_permlane32_swap(a1, b1, false, false);
        __builtin_amdgcn_raw_buffer_store_b128((u32x4){r0[0], r1[0], r0[1], r1[1]}, RSRC(out, 2048 * 896 * 2), (unsigned)((orow + pr * 16) - out) * 2u, 0, 17);
    }
}

__global__ void __launch_bounds__(512, 2) fused_kernel(const u16* xA, const u16* wA, const u16* woA, const u16* xB, const u16* wB, const u16* woB, u16* out, char* ws) {
    extern __shared__ __attribute__((aligned(16))) char lds[];
    int lane = threadIdx.x & 63; const int wid = __builtin_amdgcn_readfirstlane(threadIdx.x >> 6);
    const int id = blockIdx.x, xcd = id & 7, loc = id >> 3;
    STAMP_INIT();
    STAMP(0);
    if (threadIdx.x < 4) ctl_store(lds, threadIdx.x * 4, 0);
    const u64 tag = __hip_atomic_load(flagp(ws, FEP), __ATOMIC_RELAXED, __HIP_MEMORY_SCOPE_AGENT) + 1;
    const int gb = xcd & 1, grt = 8 * (xcd >> 1) + (loc & 7), qct = gb * 4 + (loc >> 3);
    const bool has_qkv = qct < 7;
    const int h = xcd * 2 + (loc & 1), rb = loc >> 1;
    const int ort = 4 * xcd + (loc & 3), oct = loc >> 2;
    QkvOut outB;
    if (has_qkv) qkv_phase(xA, wA, xB, wB, ws, grt, qct, tag, lds, wid, lane, outB);
    else { if (threadIdx.x == 0) flag_set(ws, F0 + (xcd >> 1) * 8 + (loc & 7), tag); BARRIER(); }
    STAMP(4);
    if (wid == 4) {
        flag_wait(ws, F1 + (lane < 32 ? (28 * h) >> 6 : (28 * h + 27) >> 6) * 32 + (lane & 31), tag);
        if (lane == 0) ctl_store(lds, CTL_F1, 1);
    } else ctl_wait(lds, CTL_F1);
    STAMP(5);
    const u64 tags = (u64)(unsigned)__builtin_amdgcn_readfirstlane((unsigned)tag) | ((u64)(unsigned)__builtin_amdgcn_readfirstlane((unsigned)(tag >> 32)) << 32);
    attnA_phase(ws, woA, woB, oct, h, rb, tags, grt, qct, has_qkv, outB, lds, wid, lane);
    STAMP(9);
    lane = opaque_lane();
    if (oct < 7) {
        oproj_issue_w(woA, woB, oct, (unsigned)(uintptr_t)lds, wid, lane);
        if (wid == 7) flag_wait(ws, F3 + (ort >> 1) * 16 + (lane & 15), tags);
        DRAIN_BARRIER();
        STAMP(11);
        oproj_compute(ws, out, ort, oct, lds, wid, lane);
    }
    STAMP(14);
    if (id == 0 && wid == 0) {
        flag_wait(ws, F0 + (lane & 31), tags);
        if (lane == 0) flag_set(ws, FEP, tags);
    }
    STAMP_DUMP();
}

extern "C" void kernel_launch(void* const* d_in, const int* in_sizes, int n_in, void* d_out, int out_size, void* d_ws, size_t ws_size, hipStream_t stream) {
    const u16* xA = (const u16*)d_in[0]; const u16* wA = (const u16*)d_in[1]; const u16* woA = (const u16*)d_in[2];
    const u16* xB = (const u16*)d_in[3]; const u16* wB = (const u16*)d_in[4]; const u16* woB = (const u16*)d_in[5];
    (void)hipFuncSetAttribute((const void*)fused_kernel, hipFuncAttributeMaxDynamicSharedMemorySize, LDS_TOTAL + LDS_EXTRA);
    fused_kernel<<<256, 512, LDS_TOTAL + LDS_EXTRA, stream>>>(xA, wA, woA, xB, wB, woB, (u16*)d_out, (char*)d_ws);
}
```

```cpp
#include <hip/hip_runtime.h>
#include <hip/hip_bf16.h>
#include <stdint.h>

typedef unsigned short u16;
typedef unsigned long long u64;
typedef short bf16x8 __attribute__((ext_vector_type(8)));
typedef short s16x4 __attribute__((ext_vector_type(4)));
typedef float f32x4 __attribute__((ext_vector_type(4)));
typedef float f32x16 __attribute__((ext_vector_type(16)));
typedef unsigned u32x4 __attribute__((ext_vector_type(4)));
typedef unsigned u32x2 __attribute__((ext_vector_type(2)));
typedef __attribute__((address_space(3))) const char* lds_cptr;
typedef __attribute__((address_space(1))) u64 gu64;

constexpr int TILE_B = 56 * 512;
constexpr size_t QC_BYTES = (size_t)64 * TILE_B;
constexpr size_t V_BYTES = (size_t)16 * 64 * 2048;
constexpr size_t WS_QA = 0, WS_KA = WS_QA + QC_BYTES, WS_VA = WS_KA + QC_BYTES;
constexpr size_t WS_QB = WS_VA + V_BYTES, WS_KB = WS_QB + QC_BYTES, WS_VB = WS_KB + QC_BYTES;
constexpr size_t WS_OA = WS_VB + V_BYTES, WS_OB = WS_OA + QC_BYTES, WS_FLAGS = WS_OB + QC_BYTES;
constexpr int F1 = 0, F2 = 256, F3 = 512, F4 = 768, F0 = 1024, FEP = 1056;
constexpr float QSCALE = 0.18898223650461362f * 1.4426950408889634f;
constexpr int STG = 32768;
constexpr int LDS_Q = 147456, LDS_L = 131072, LDS_MRG = 57344, LDS_CTL = LDS_Q + 8192, LDS_MX = LDS_CTL + 128, LDS_TOTAL = LDS_MX + 1024 + 64;
constexpr int CTL_F1 = 0, CTL_F2CNT = 4, CTL_BAD = 8, CTL_QCNT = 12, CTL_F4CNT = 16, CTL_F3 = 20, CTL_RDONE = 24, CTL_F4 = 28, CTL_X = 32;
constexpr int CTL_WA = 64, CTL_WB = 68;
#define BPFG 1
#define PUBG 10
constexpr unsigned SPIN_MAX = 1u << 18;

#define STAMP(i)
#define STAMP_INIT()
#define STAMP_DUMP()
#define LDS_EXTRA 0

#define MFMA16(a, b, c) __builtin_amdgcn_mfma_f32_16x16x32_bf16(a, b, c, 0, 0, 0)
#define MFMA32(a, b, c) __builtin_amdgcn_mfma_f32_32x32x16_bf16(a, b, c, 0, 0, 0)
#define PIN(x) asm volatile("" : "+v"(x))
#define SBAR() __builtin_amdgcn_sched_barrier(0)
#define SC1 16

__device__ __forceinline__ unsigned cvtpk(float lo, float hi) {
    typedef float f2 __attribute__((ext_vector_type(2))); typedef __bf16 b2 __attribute__((ext_vector_type(2)));
    f2 v = {lo, hi}; b2 r = __builtin_convertvector(v, b2); return __builtin_bit_cast(unsigned, r);
}
__device__ __forceinline__ void glds16(const void* sbase, unsigned voff, unsigned lds_base) {
    asm volatile("s_mov_b32 m0, %2\n\ts_nop 0\n\tglobal_load_lds_dwordx4 %0, %1" :: "v"(voff), "s"(sbase), "s"(lds_base) : "memory");
}
__device__ __forceinline__ s16x4 vtr(lds_cptr p) {
    typedef short v4i16_t __attribute__((ext_vector_type(4)));
    return __builtin_bit_cast(s16x4, __builtin_amdgcn_ds_read_tr16_b64_v4i16((__attribute__((address_space(3))) v4i16_t*)p));
}
__device__ __forceinline__ bf16x8 cat8(s16x4 a, s16x4 b) { return (bf16x8){a[0], a[1], a[2], a[3], b[0], b[1], b[2], b[3]}; }
__device__ __forceinline__ bf16x8 ldsrd128(lds_cptr p) { return *(const __attribute__((address_space(3))) bf16x8*)p; }
#define RSRC(p, bytes) __builtin_amdgcn_make_buffer_rsrc((void*)(p), 0, (int)(bytes), 0x00020000)
__device__ __forceinline__ u32x4 bload128(__amdgpu_buffer_rsrc_t r, unsigned voff, unsigned soff) { return __builtin_amdgcn_raw_buffer_load_b128(r, voff, soff, SC1); }
__device__ __forceinline__ void bstore128(u32x4 v, __amdgpu_buffer_rsrc_t r, unsigned voff) { __builtin_amdgcn_raw_buffer_store_b128(v, r, voff, 0, SC1); }
__device__ __forceinline__ void bstore64(u32x2 v, __amdgpu_buffer_rsrc_t r, unsigned voff) { __builtin_amdgcn_raw_buffer_store_b64(v, r, voff, 0, SC1); }
__device__ __forceinline__ bf16x8 asb(u32x4 v) { return __builtin_bit_cast(bf16x8, v); }

__device__ __forceinline__ gu64* flagp(char* ws, int idx) { return (gu64*)(ws + WS_FLAGS + (size_t)idx * 128); }
__device__ __forceinline__ void flag_set(char* ws, int idx, u64 tag) { __hip_atomic_store(flagp(ws, idx), tag, __ATOMIC_RELAXED, __HIP_MEMORY_SCOPE_AGENT); }
__device__ __forceinline__ void flag_wait(char* ws, int idx, u64 tag) {
    gu64* p = flagp(ws, idx);
    for (unsigned spins = 0; spins < SPIN_MAX; ++spins) {
        const u64 v = __hip_atomic_load(p, __ATOMIC_RELAXED, __HIP_MEMORY_SCOPE_AGENT);
        if (__all(v == tag)) break;
        __builtin_amdgcn_s_sleep(1);
    }
}
__device__ __forceinline__ int opaque_lane() { int l; asm volatile("v_mbcnt_lo_u32_b32 %0, -1, 0\n\tv_mbcnt_hi_u32_b32 %0, -1, %0" : "=v"(l)); return l; }
__device__ __forceinline__ int f2_index(int h, int rb, int lane) { const int i = lane % 6, r = 2 * rb - 1 + i % 3; return F2 + (i < 3 ? (28 * h) >> 6 : (28 * h + 27) >> 6) * 32 + (r < 0 ? 0 : r); }
typedef __attribute__((address_space(3))) int* lds_iptr;
__device__ __forceinline__ int ctl_load(char* lds, int off) { return __hip_atomic_load((lds_iptr)(lds + LDS_CTL + off), __ATOMIC_RELAXED, __HIP_MEMORY_SCOPE_WORKGROUP); }
__device__ __forceinline__ void ctl_store(char* lds, int off, int v) { __hip_atomic_store((lds_iptr)(lds + LDS_CTL + off), v, __ATOMIC_RELAXED, __HIP_MEMORY_SCOPE_WORKGROUP); }
__device__ __forceinline__ void ctl_wait(char* lds, int off, int n) {
    for (unsigned spins = 0; spins < (SPIN_MAX << 6); ++spins) { if (__builtin_amdgcn_readfirstlane(ctl_load(lds, off)) >= n) break; __builtin_amdgcn_s_sleep(1); }
    asm volatile("" ::: "memory");
}
__device__ __forceinline__ int ctl_add(char* lds, int off) { return __hip_atomic_fetch_add((lds_iptr)(lds + LDS_CTL + off), 1, __ATOMIC_RELAXED, __HIP_MEMORY_SCOPE_WORKGROUP); }
#define BARRIER() asm volatile("s_waitcnt lgkmcnt(0)\n\ts_barrier" ::: "memory")
#define DRAIN_BARRIER() asm volatile("s_waitcnt vmcnt(0) lgkmcnt(0)\n\ts_barrier" ::: "memory")

struct QkvFrag { bf16x8 x[2][4], w[2][3]; };
struct QkvAcc { f32x4 q[4], k[4], v[4]; };
struct QkvOut { u32x4 q[2], k[2], v[2]; };
__device__ __forceinline__ void qkv_pack(QkvOut& o, const QkvAcc& a, float qs) {
#pragma unroll
    for (int tb = 0; tb < 2; ++tb) {
        const f32x4 q0 = a.q[2 * tb] * qs, q1 = a.q[2 * tb + 1] * qs, k0 = a.k[2 * tb], k1 = a.k[2 * tb + 1], v0 = a.v[2 * tb], v1 = a.v[2 * tb + 1];
        { auto ra = __builtin_amdgcn_permlane16_swap(cvtpk(q0[0], q0[1]), cvtpk(q1[0], q1[1]), false, false); auto rb = __builtin_amdgcn_permlane16_swap(cvtpk(q0[2], q0[3]), cvtpk(q1[2], q1[3]), false, false);
          o.q[tb] = (u32x4){ra[0], rb[0], ra[1], rb[1]}; }
        { auto ra = __builtin_amdgcn_permlane16_swap(cvtpk(k0[0], k0[1]), cvtpk(k1[0], k1[1]), false, false); auto rb = __builtin_amdgcn_permlane16_swap(cvtpk(k0[2], k0[3]), cvtpk(k1[2], k1[3]), false, false);
          o.k[tb] = (u32x4){ra[0], rb[0], ra[1], rb[1]}; }
        { auto ra = __builtin_amdgcn_permlane32_swap(cvtpk(v0[0], v0[1]), cvtpk(v1[0], v1[1]), false, false); auto rb = __builtin_amdgcn_permlane32_swap(cvtpk(v0[2], v0[3]), cvtpk(v1[2], v1[3]), false, false);
          o.v[tb] = (u32x4){ra[0], rb[0], ra[1], rb[1]}; }
    }
}
template <int BR, int I>
__device__ __forceinline__ void qkv_store(const QkvOut& o, char* ws, int rt, int ct, int c, int lane) {
    constexpr int tb = I / 3, kind = I % 3;
    const int tile = rt * 2 + tb;
    if (kind < 2) {
        const unsigned off = (unsigned)(tile * 56 + 8 * ct + 2 * c) * 512u + (unsigned)lane * 16u;
        if (kind == 0) bstore128(o.q[tb], RSRC(ws + (BR ? WS_QB : WS_QA), QC_BYTES), off); else bstore128(o.k[tb], RSRC(ws + (BR ? WS_KB : WS_KA), QC_BYTES), off);
    } else {
        const int n = 64 * ct + 16 * c + (lane & 15), h = n / 28, d = n - 28 * h;
        const unsigned off = (unsigned)(h * 64 + tile) * 2048u + (unsigned)(lane >> 4) * 512u + (unsigned)d * 16u;
        const auto rs = RSRC(ws + (BR ? WS_VB : WS_VA), V_BYTES);
        bstore128(o.v[tb], rs, off);
        if (d == 27) bstore128((u32x4){0x3F803F80u, 0x3F803F80u, 0x3F803F80u, 0x3F803F80u}, rs, off + 16u);
    }
}

__device__ __forceinline__ void qkv_phase(const u16* xA, const u16* wA, const u16* xB, const u16* wB, char* ws, int rt, int ct, u64 tag, char* lds, int wid, int lane, QkvOut& outB) {
    if (wid >= 4) {
        const unsigned lds0 = (unsigned)(uintptr_t)lds;
        const int lw = wid - 4, r8 = lane >> 3, dpos = lane & 7;
        unsigned xoff[2], woff[2];
#pragma unroll
        for (int i = 0; i < 2; ++i) { const int row = 16 * lw + 8 * i + r8;
            xoff[i] = (unsigned)row * 896u + (unsigned)((dpos ^ ((row >> 1) & 7)) << 4);
            const int f2 = ((row >> 1) & 1) | (((row >> 3) & 1) << 1);
            woff[i] = (unsigned)row * 2688u + (unsigned)((((dpos >> 1) ^ f2) * 2 + (dpos & 1)) << 4); }
        const int rot = __builtin_amdgcn_readfirstlane(((rt & 7) + 2 * (ct & 3)) % 7);
        const char* xa = (const char*)xA + (size_t)rt * 64 * 896; const char* wa = (const char*)wA + (size_t)ct * 128;
        const char* xb = (const char*)xB + (size_t)rt * 64 * 896; const char* wb = (const char*)wB + (size_t)ct * 128;
#define QKV_ISSUE(g) do { int st_ = ((g) % 7) + rot; st_ = st_ >= 7 ? st_ - 7 : st_; const char* x_ = ((g) < 7 ? xa : xb) + st_ * 128; const char* w_ = ((g) < 7 ? wa : wb) + (size_t)st_ * (64 * 2688); \
        const unsigned sl_ = lds0 + ((g) & 3) * STG + lw * 2048; \
        glds16(x_, xoff[0], sl_); glds16(x_, xoff[1], sl_ + 1024); \
        glds16(w_, woff[0], sl_ + 8192); glds16(w_, woff[1], sl_ + 8192 + 1024); glds16(w_ + 896, woff[0], sl_ + 16384); glds16(w_ + 896, woff[1], sl_ + 16384 + 1024); \
        glds16(w_ + 1792, woff[0], sl_ + 24576); glds16(w_ + 1792, woff[1], sl_ + 24576 + 1024); } while (0)
        QKV_ISSUE(0); QKV_ISSUE(1);
#pragma unroll
        for (int g = 0; g < 14; ++g) {
            if (g <= 12) asm volatile("s_waitcnt vmcnt(8) lgkmcnt(0)\n\ts_barrier" ::: "memory");
            else asm volatile("s_waitcnt vmcnt(0) lgkmcnt(0)\n\ts_barrier" ::: "memory");
            if (g == 0) STAMP(1);
            if (g + 2 < 14) QKV_ISSUE(g + 2);
        }
        return;
    }
    const int c = wid, g4 = lane >> 4, t16 = lane & 15;
    const unsigned xr0 = (unsigned)t16 * 128u + (unsigned)((g4 ^ (t16 >> 1)) << 4);
    const int q = t16 >> 2, p = lane & 3;
    const int rf2 = ((q >> 1) & 1) | ((g4 & 1) << 1);
    const unsigned wr0 = 8192u + (unsigned)(8 * g4 + q) * 128u + (unsigned)((c ^ rf2) * 32) + (unsigned)p * 8u;
    QkvAcc acc; QkvOut outA;
#pragma unroll
    for (int i = 0; i < 4; ++i) acc.q[i] = acc.k[i] = acc.v[i] = (f32x4){};
    QkvFrag fr[2];
    unsigned pfa = 0, pfb = 0;
#define QMF(i) do { if (g > 0) { constexpr int ks_ = (i) / 12, s_ = ((i) % 12) / 4, b_ = (i) % 4; const QkvFrag& f_ = fr[(g - 1) & 1]; \
        if (s_ == 0) acc.q[b_] = MFMA16(f_.w[ks_][0], f_.x[ks_][b_], acc.q[b_]); if (s_ == 1) acc.k[b_] = MFMA16(f_.w[ks_][1], f_.x[ks_][b_], acc.k[b_]); \
        if (s_ == 2) acc.v[b_] = MFMA16(f_.x[ks_][b_], f_.w[ks_][2], acc.v[b_]); } SBAR(); } while (0)
#define QRX(ks, b) do { if (g < 14) fr[g & 1].x[ks][b] = ldsrd128(sl + (xr0 ^ ((ks) * 64)) + (b) * 2048); } while (0)
#define QRW(ks, s) do { if (g < 14) { lds_cptr wp_ = sl + wr0 + (ks) * 4096 + (s) * 8192; fr[g & 1].w[ks][s] = cat8(vtr(wp_), vtr(wp_ + 512)); } } while (0)
#define QST(gg, i) do { if (g == (gg)) { qkv_store<0, i>(outA, ws, rt, ct, c, lane); SBAR(); } } while (0)
#pragma unroll
    for (int g = 0; g < 15; ++g) {
        if (g == PUBG) asm volatile("s_waitcnt vmcnt(0) lgkmcnt(0)\n\ts_barrier" ::: "memory");
        else if (g < 14) asm volatile("s_waitcnt lgkmcnt(0)\n\ts_barrier" ::: "memory");
        SBAR();
        if (g == 7) STAMP(2);
        lds_cptr sl = (lds_cptr)lds + (g & 3) * STG;
        if (g == PUBG && threadIdx.x == 0) flag_set(ws, F1 + ct * 32 + rt, tag);
        SBAR();
        QRX(0, 0); QMF(0); QRX(0, 1); QMF(1); QRX(0, 2); QMF(2); QRX(0, 3); QMF(3); QST(8, 3); QRW(0, 0); QMF(4); QRW(0, 1); QMF(5); QRW(0, 2); QMF(6);
        QRX(1, 0); QMF(7); QRX(1, 1); QMF(8); QRX(1, 2); QMF(9); QST(8, 4); QRX(1, 3); QMF(10); QRW(1, 0); QMF(11); QRW(1, 1); QMF(12); QRW(1, 2); QMF(13);
        QMF(14); QMF(15); QST(8, 5); QMF(16); QMF(17); QMF(18); QMF(19); QMF(20); QMF(21); QMF(22); QMF(23);
        if (g == 7) {
            qkv_pack(outA, acc, QSCALE);
            qkv_store<0, 0>(outA, ws, rt, ct, c, lane); qkv_store<0, 1>(outA, ws, rt, ct, c, lane); qkv_store<0, 2>(outA, ws, rt, ct, c, lane);
#pragma unroll
            for (int i = 0; i < 4; ++i) acc.q[i] = acc.k[i] = acc.v[i] = (f32x4){};
        }
    }
    if (lane == 0) ctl_add(lds, CTL_RDONE);
    qkv_pack(outB, acc, 1.0f);
}

__device__ __forceinline__ float max16(const f32x16& s) {
    float a = fmaxf(fmaxf(s[0], s[1]), s[2]), b = fmaxf(fmaxf(s[3], s[4]), s[5]);
    a = fmaxf(fmaxf(a, s[6]), s[7]); b = fmaxf(fmaxf(b, s[8]), s[9]); a = fmaxf(fmaxf(a, s[10]), s[11]); b = fmaxf(fmaxf(b, s[12]), s[13]);
    a = fmaxf(fmaxf(a, s[14]), s[15]); return fmaxf(a, b);
}
__device__ __forceinline__ float xhalf_max(float m) {
    auto r = __builtin_amdgcn_permlane32_swap(__float_as_uint(m), __float_as_uint(m), false, false);
    return fmaxf(__uint_as_float(r[0]), __uint_as_float(r[1]));
}
__device__ __forceinline__ void patch(u32x4& f0, u32x4& f1, bool pe, bool po, unsigned a, unsigned b) {
    f0[0] = po ? a : f0[0]; f0[1] = po ? b : f0[1]; f1[2] = pe ? a : f1[2]; f1[3] = pe ? b : f1[3];
}
__device__ __forceinline__ void exp_pack(const f32x16& s, u32x4& p0, u32x4& p1) {
    float e[16];
#pragma unroll
    for (int r = 0; r < 16; ++r) e[r] = __builtin_amdgcn_exp2f(s[r]);
    p0 = (u32x4){cvtpk(e[0], e[1]), cvtpk(e[2], e[3]), cvtpk(e[4], e[5]), cvtpk(e[6], e[7])};
    p1 = (u32x4){cvtpk(e[8], e[9]), cvtpk(e[10], e[11]), cvtpk(e[12], e[13]), cvtpk(e[14], e[15])};
}
__device__ __forceinline__ void merge_write1(const f32x16& o, char* lds, int wid, int j, int lane) {
#pragma unroll
    for (int hf = 0; hf < 2; ++hf)
        *(u32x4*)(lds + LDS_MRG + ((wid * 4 + j) * 2 + hf) * 1024 + lane * 16) = (u32x4){cvtpk(o[8 * hf], o[8 * hf + 1]), cvtpk(o[8 * hf + 2], o[8 * hf + 3]), cvtpk(o[8 * hf + 4], o[8 * hf + 5]), cvtpk(o[8 * hf + 6], o[8 * hf + 7])};
    if (lane >= 32) *(float*)(lds + LDS_L + ((wid * 4 + j) * 32 + lane - 32) * 4) = o[12];
}
__device__ __forceinline__ bool merge_reduce(char* ws, int h, int rb, char* lds, int wid, int lane) {
    const bool hi = lane >= 32;
    const int j = wid >> 1, hf = wid & 1;
    const int tile = rb * 4 + j, q = lane & 31;
    u32x4 p[8]; float pl[8];
#pragma unroll
    for (int w = 0; w < 8; ++w) pl[w] = *(const float*)(lds + LDS_L + ((w * 4 + j) * 32 + q) * 4);
#pragma unroll
    for (int w = 0; w < 8; ++w) p[w] = *(const u32x4*)(lds + LDS_MRG + ((w * 4 + j) * 2 + hf) * 1024 + lane * 16);
    const int col0 = 28 * h + 16 * hf + (hi ? 4 : 0), col1 = col0 + 8;
    const unsigned off0 = (unsigned)((tile * 56 + (col0 >> 3)) * 32 + q) * 16u + (unsigned)((col0 >> 2) & 1) * 8u, off1 = (unsigned)((tile * 56 + (col1 >> 3)) * 32 + q) * 16u + (unsigned)((col1 >> 2) & 1) * 8u;
    const float l = ((pl[0] + pl[1]) + (pl[2] + pl[3])) + ((pl[4] + pl[5]) + (pl[6] + pl[7]));
    const float inv = __builtin_amdgcn_rcpf(l);
    float a[8] = {};
#pragma unroll
    for (int w = 0; w < 8; ++w)
#pragma unroll
        for (int i = 0; i < 4; ++i) { a[2 * i] += __uint_as_float(p[w][i] << 16); a[2 * i + 1] += __uint_as_float(p[w][i] & 0xffff0000u); }
#pragma unroll
    for (int i = 0; i < 8; ++i) a[i] *= inv;
    const auto rO = RSRC(ws + WS_OA, QC_BYTES);
    const bool last = hf == 1 && hi;
    bstore64((u32x2){cvtpk(a[0], a[1]), cvtpk(a[2], a[3])}, rO, off0);
    if (!last) bstore64((u32x2){cvtpk(a[4], a[5]), cvtpk(a[6], a[7])}, rO, off1);
    float z = a[0] * 0.f; z = fmaf(a[1], 0.f, z); z = fmaf(a[2], 0.f, z); z = fmaf(a[3], 0.f, z);
    float z1 = a[4] * 0.f; z1 = fmaf(a[5], 0.f, z1); z1 = fmaf(a[6], 0.f, z1); z1 = fmaf(a[7], 0.f, z1);
    const bool ok = l > 1.0e-18f && l < 1.0e30f && z == 0.f && (last || z1 == 0.f);
    return !ok;
}

__device__ __forceinline__ void attnA_safe(char* ws, int h, int rb, char* lds, int wid, int lane) {
    const bool hi = lane >= 32, pe = hi && !(h & 1), po = !hi && (h & 1); const int m0 = (28 * h) >> 3;
    const auto rQ = RSRC(ws + WS_QA, QC_BYTES), rK = RSRC(ws + WS_KA, QC_BYTES), rV = RSRC(ws + WS_VA, V_BYTES);
    const unsigned lo16 = (unsigned)lane * 16u;
    float* mx = (float*)(lds + LDS_MX);
#pragma unroll 1
    for (int j = 0; j < 4; ++j) {
        const unsigned b = (unsigned)((rb * 4 + j) * 56 + m0) * 512u;
        u32x4 q0 = bload128(rQ, lo16, b), q1 = bload128(rQ, lo16, b + 1024u);
        patch(q0, q1, pe, po, 0u, 0u);
        float m = -3.0e38f; f32x16 acc = {};
#pragma unroll 1
        for (int t = 0; t < 8; ++t) {
            const unsigned ko = (unsigned)((wid * 8 + t) * 56 + m0) * 512u, vo = (unsigned)(h * 64 + wid * 8 + t) * 2048u;
            u32x4 k0 = bload128(rK, lo16, ko), k1 = bload128(rK, lo16, ko + 1024u), v0 = bload128(rV, lo16, vo), v1 = bload128(rV, lo16, vo + 1024u);
            patch(k0, k1, pe, po, 0u, 0u);
            f32x16 s = {}; s = MFMA32(asb(k0), asb(q0), s); s = MFMA32(asb(k1), asb(q1), s);
            const float mn = fmaxf(m, xhalf_max(max16(s)));
            const float al = __builtin_amdgcn_exp2f(m - mn); m = mn;
#pragma unroll
            for (int r = 0; r < 16; ++r) { s[r] -= mn; acc[r] *= al; }
            u32x4 p0, p1; exp_pack(s, p0, p1);
            acc = MFMA32(asb(v0), asb(p0), acc); acc = MFMA32(asb(v1), asb(p1), acc);
        }
        BARRIER();
        if (!hi) mx[wid * 32 + lane] = m;
        BARRIER();
        float g = m;
#pragma unroll
        for (int w = 0; w < 8; ++w) g = fmaxf(g, mx[w * 32 + (lane & 31)]);
        const float f = __builtin_amdgcn_exp2f(m - g);
        merge_write1(acc * f, lds, wid, j, lane);
    }
}

struct AttnBPre { u32x4 q, kp, kc; u32x2 v[2][2]; };
__device__ __forceinline__ void attnB_load(AttnBPre& a, char* ws, int h, int rb, int wid, int lane) {
    const int m0 = (28 * h) >> 3, T = rb * 4 + (wid >> 1), sub = wid & 1;
    const int Tp = sub ? T : (T > 0 ? T - 1 : 0), subp = sub ^ 1;
    const auto rQ = RSRC(ws + WS_QB, QC_BYTES), rK = RSRC(ws + WS_KB, QC_BYTES), rV = RSRC(ws + WS_VB, V_BYTES);
    const int g = lane >> 4, t16 = lane & 15;
    const unsigned co = (unsigned)g * 512u + (unsigned)t16 * 16u;
    a.q = bload128(rQ, co, (unsigned)(T * 56 + m0) * 512u + (unsigned)sub * 256u);
    a.kp = bload128(rK, co, (unsigned)(Tp * 56 + m0) * 512u + (unsigned)subp * 256u);
    a.kc = bload128(rK, co, (unsigned)(T * 56 + m0) * 512u + (unsigned)sub * 256u);
    const unsigned vo = (unsigned)(g & 1) * 512u + (unsigned)t16 * 16u + (unsigned)(g >> 1) * 8u;
#pragma unroll
    for (int db = 0; db < 2; ++db) {
        a.v[db][0] = __builtin_amdgcn_raw_buffer_load_b64(rV, vo + db * 256u, (unsigned)(h * 64 + Tp) * 2048u + (unsigned)subp * 1024u, SC1);
        a.v[db][1] = __builtin_amdgcn_raw_buffer_load_b64(rV, vo + db * 256u, (unsigned)(h * 64 + T) * 2048u + (unsigned)sub * 1024u, SC1);
    }
}
__device__ __forceinline__ void attnB_compute(AttnBPre& a, char* ws, int h, int rb, int wid, int lane) {
    const int T = rb * 4 + (wid >> 1), sub = wid & 1, g = lane >> 4, ql = lane & 15;
    const bool padl = (h & 1) ? g == 0 : g == 3;
    if (h & 1) { a.q[0] = padl ? 0u : a.q[0]; a.q[1] = padl ? 0u : a.q[1]; a.kp[0] = padl ? 0u : a.kp[0]; a.kp[1] = padl ? 0u : a.kp[1]; a.kc[0] = padl ? 0u : a.kc[0]; a.kc[1] = padl ? 0u : a.kc[1]; }
    else { a.q[2] = padl ? 0u : a.q[2]; a.q[3] = padl ? 0u : a.q[3]; a.kp[2] = padl ? 0u : a.kp[2]; a.kp[3] = padl ? 0u : a.kp[3]; a.kc[2] = padl ? 0u : a.kc[2]; a.kc[3] = padl ? 0u : a.kc[3]; }
    f32x4 sp = {}, sc = {};
    sp = MFMA16(asb(a.kp), asb(a.q), sp); sc = MFMA16(asb(a.kc), asb(a.q), sc);
    const bool hasp = sub || T > 0;
    float m = -INFINITY;
#pragma unroll
    for (int r = 0; r < 4; ++r) {
        const int dc = ql - (4 * g + r), dp = dc + 16;
        sp[r] = (hasp && dp <= 14 && !(dp & 1)) ? sp[r] : -INFINITY; sc[r] = (dc >= 0 && !(dc & 1)) ? sc[r] : -INFINITY;
        m = fmaxf(m, fmaxf(sp[r], sc[r]));
    }
    { auto r = __builtin_amdgcn_permlane16_swap(__float_as_uint(m), __float_as_uint(m), false, false); m = fmaxf(__uint_as_float(r[0]), __uint_as_float(r[1])); }
    m = xhalf_max(m);
    float e[8];
#pragma unroll
    for (int r = 0; r < 4; ++r) { e[r] = __builtin_amdgcn_exp2f((sp[r] - m) * QSCALE); e[4 + r] = __builtin_amdgcn_exp2f((sc[r] - m) * QSCALE); }
    const u32x4 pf = {cvtpk(e[0], e[1]), cvtpk(e[2], e[3]), cvtpk(e[4], e[5]), cvtpk(e[6], e[7])};
    f32x4 o0 = {}, o1 = {};
    o0 = MFMA16(asb((u32x4){a.v[0][0][0], a.v[0][0][1], a.v[0][1][0], a.v[0][1][1]}), asb(pf), o0);
    o1 = MFMA16(asb((u32x4){a.v[1][0][0], a.v[1][0][1], a.v[1][1][0], a.v[1][1][1]}), asb(pf), o1);
    float l;
    { auto r16 = __builtin_amdgcn_permlane16_swap(__float_as_uint(o1[0]), __float_as_uint(o1[0]), false, false); auto r32 = __builtin_amdgcn_permlane32_swap(r16[1], r16[1], false, false); l = __uint_as_float(r32[1]); }
    const float inv = __builtin_amdgcn_rcpf(l);
    const auto rO = RSRC(ws + WS_OB, QC_BYTES);
    const unsigned tok = (unsigned)(sub * 16 + ql) * 16u;
    { const int col = 28 * h + 4 * g; bstore64((u32x2){cvtpk(o0[0] * inv, o0[1] * inv), cvtpk(o0[2] * inv, o0[3] * inv)}, rO, (unsigned)(T * 56 + (col >> 3)) * 512u + tok + (unsigned)((col >> 2) & 1) * 8u); }
    if (g < 3) { const int col = 28 * h + 16 + 4 * g; bstore64((u32x2){cvtpk(o1[0] * inv, o1[1] * inv), cvtpk(o1[2] * inv, o1[3] * inv)}, rO, (unsigned)(T * 56 + (col >> 3)) * 512u + tok + (unsigned)((col >> 2) & 1) * 8u); }
}

template <int N>
__device__ __forceinline__ void wo_issue(const u16* wo, int ct, unsigned lds_base, int pc0, int lane) {
    const int r8 = lane >> 3, dpos = lane & 7;
    const unsigned woff = (unsigned)r8 * 896u + (unsigned)((dpos ^ ((r8 & 2) << 1)) << 4);
#pragma unroll
    for (int i = 0; i < N; ++i) glds16((const char*)wo + (size_t)ct * 128 + (size_t)(pc0 + i) * 8 * 896, woff, lds_base + (pc0 + i) * 1024);
}
__device__ __forceinline__ void wf_read(u32x4 (&wf)[2][14], const char* img, int kh, int lane) {
    const int g16 = lane >> 4, q = (lane & 15) >> 2, p = lane & 3;
#pragma unroll
    for (int i = 0; i < 14; ++i)
#pragma unroll
        for (int c = 0; c < 2; ++c) {
            const int row = (kh * 14 + i) * 16 + 8 * (g16 >> 1) + q;
            lds_cptr wp = (lds_cptr)img + (unsigned)row * 128u + (unsigned)(((2 * c + (g16 & 1)) ^ (q & 2)) * 32) + (unsigned)p * 8u;
            wf[c][i] = __builtin_bit_cast(u32x4, cat8(vtr(wp), vtr(wp + 512)));
        }
}
__device__ __forceinline__ void attnA_phase(char* ws, const u16* woA, int oct, int h, int rb, u64 tag, int rt, int ct, bool has_qkv, const QkvOut& outB, u32x4 (&wf)[2][14], char* lds, int wid, int lane) {
    const bool hi = lane >= 32, pe = hi && !(h & 1), po = !hi && (h & 1);
    const int m0 = (28 * h) >> 3;
    const auto rK = RSRC(ws + WS_KA, QC_BYTES), rV = RSRC(ws + WS_VA, V_BYTES);
    const unsigned lo16 = (unsigned)lane * 16u;
    const unsigned kbase = (unsigned)((wid * 8) * 56 + m0) * 512u, vbase = (unsigned)(h * 64 + wid * 8) * 2048u;
    const int rot = rb & 7;
    u32x4 qf[4][2], kf[2][2], vf[2][2];
    {   const unsigned k0 = kbase + (unsigned)rot * TILE_B, v0 = vbase + (unsigned)rot * 2048u, k1 = kbase + (unsigned)((rot + 1) & 7) * TILE_B;
        kf[0][0] = bload128(rK, lo16, k0); kf[0][1] = bload128(rK, lo16, k0 + 1024u);
        if (wid >= 4) { const unsigned b = (unsigned)((rb * 4 + wid - 4) * 56 + m0) * 512u; const auto rQ = RSRC(ws + WS_QA, QC_BYTES); qf[0][0] = bload128(rQ, lo16, b); qf[0][1] = bload128(rQ, lo16, b + 1024u); }
        vf[0][0] = bload128(rV, lo16, v0); vf[0][1] = bload128(rV, lo16, v0 + 1024u);
        kf[1][0] = bload128(rK, lo16, k1); kf[1][1] = bload128(rK, lo16, k1 + 1024u); }
    patch(kf[0][0], kf[0][1], pe, po, 0x3F803F80u, 0u);
    if (wid >= 4) {
        patch(qf[0][0], qf[0][1], pe, po, 0u, 0u);
        f32x16 s = {}; s = MFMA32(asb(kf[0][0]), asb(qf[0][0]), s); s = MFMA32(asb(kf[0][1]), asb(qf[0][1]), s);
        const float m = xhalf_max(max16(s));
        const unsigned mh = cvtpk(-m, 0.f) & 0xffffu; const float r = -m - __uint_as_float(mh << 16);
        patch(qf[0][0], qf[0][1], pe, po, mh | (cvtpk(r, 0.f) << 16), 0u);
        *(u32x4*)(lds + LDS_Q + (wid - 4) * 2048 + lane * 16) = qf[0][0]; *(u32x4*)(lds + LDS_Q + (wid - 4) * 2048 + 1024 + lane * 16) = qf[0][1];
        asm volatile("s_waitcnt lgkmcnt(0)" ::: "memory");
        if (lane == 0) ctl_add(lds, CTL_QCNT);
    }
    ctl_wait(lds, CTL_QCNT, 4);
    STAMP(6);
#pragma unroll
    for (int j = 0; j < 4; ++j) { qf[j][0] = *(const u32x4*)(lds + LDS_Q + j * 2048 + lane * 16); qf[j][1] = *(const u32x4*)(lds + LDS_Q + j * 2048 + 1024 + lane * 16); }
    if (has_qkv && wid < 4) {
        qkv_store<1, 0>(outB, ws, rt, ct, wid, lane); qkv_store<1, 1>(outB, ws, rt, ct, wid, lane); qkv_store<1, 2>(outB, ws, rt, ct, wid, lane);
        qkv_store<1, 3>(outB, ws, rt, ct, wid, lane); qkv_store<1, 4>(outB, ws, rt, ct, wid, lane); qkv_store<1, 5>(outB, ws, rt, ct, wid, lane);
    }
#pragma unroll
    for (int j = 0; j < 4; ++j) { PIN(qf[j][0]); PIN(qf[j][1]); }
    PIN(kf[0][0]); PIN(kf[0][1]);
    f32x16 o[4];
#define VEXP(d, s) asm volatile("v_exp_f32 %0, %1" : "=v"(d) : "v"(s))
#define VCVT(d, a, b) asm volatile("v_cvt_pk_bf16_f32 %0, %1, %2" : "=v"(d) : "v"(a), "v"(b))
#define MF0(d, a, b) asm volatile("v_mfma_f32_32x32x16_bf16 %0, %1, %2, 0" : "=&v"(d) : "v"(a), "v"(b))
#define MFA(d, a, b) asm volatile("v_mfma_f32_32x32x16_bf16 %0, %1, %2, %0" : "+v"(d) : "v"(a), "v"(b))
#define SLOT(PV, QK, Sc, Pc0, Pc1, Sn, kn0, kn1, qn0, qn1, Op, vp0, vp1, Pp0, Pp1, INJ) do { \
        float e0, e1, e2, e3, e4, e5, e6, e7, e8, e9, e10, e11, e12, e13, e14, e15; \
        if (QK) MF0(Sn, kn0, qn0); \
        VEXP(e0, Sc[0]); VEXP(e1, Sc[1]); VEXP(e2, Sc[2]); VEXP(e3, Sc[3]); \
        if (QK) MFA(Sn, kn1, qn1); \
        VEXP(e4, Sc[4]); VEXP(e5, Sc[5]); VEXP(e6, Sc[6]); VEXP(e7, Sc[7]); VCVT(Pc0[0], e0, e1); VCVT(Pc0[1], e2, e3); \
        INJ; \
        if ((PV) == 2) MFA(Op, vp0, Pp0); else if ((PV) == 1) MF0(Op, vp0, Pp0); \
        VEXP(e8, Sc[8]); VEXP(e9, Sc[9]); VEXP(e10, Sc[10]); VEXP(e11, Sc[11]); VCVT(Pc0[2], e4, e5); VCVT(Pc0[3], e6, e7); \
        if (PV) MFA(Op, vp1, Pp1); \
        VEXP(e12, Sc[12]); VEXP(e13, Sc[13]); VEXP(e14, Sc[14]); VEXP(e15, Sc[15]); VCVT(Pc1[0], e8, e9); VCVT(Pc1[1], e10, e11); VCVT(Pc1[2], e12, e13); VCVT(Pc1[3], e14, e15); \
    } while (0)
    f32x16 sA, sB; u32x4 pA0, pA1, pB0, pB1;
    asm volatile("s_nop 1\n\tv_mfma_f32_32x32x16_bf16 %0, %1, %2, 0\n\tv_mfma_f32_32x32x16_bf16 %0, %3, %4, %0\n\ts_nop 7\n\ts_nop 3" : "=&v"(sA) : "v"(kf[0][0]), "v"(qf[0][0]), "v"(kf[0][1]), "v"(qf[0][1]));
#define PATCHK(kx) do { PIN(kx[0]); PIN(kx[1]); patch(kx[0], kx[1], pe, po, 0x3F803F80u, 0u); PIN(kx[0]); PIN(kx[1]); } while (0)
#define LOADK(kc, tt) do { if ((tt) < 8) { const unsigned o_ = kbase + (unsigned)(((tt) + rot) & 7) * TILE_B; kc[0] = bload128(rK, lo16, o_); kc[1] = bload128(rK, lo16, o_ + 1024u); } } while (0)
#define LOADV(vx, tt) do { if ((tt) < 8) { const unsigned o_ = vbase + (unsigned)(((tt) + rot) & 7) * 2048u; vx[0] = bload128(rV, lo16, o_); vx[1] = bload128(rV, lo16, o_ + 1024u); } } while (0)
#define TILE(kc, kx, vc, vx, t) do { \
        SLOT((t) == 0 ? 0 : (t) == 1 ? 1 : 2, true, sA, pA0, pA1, sB, kc[0], kc[1], qf[1][0], qf[1][1], o[3], vx[0], vx[1], pB0, pB1, (void)0); \
        LOADV(vx, (t) + 1); \
        SLOT((t) == 0 ? 1 : 2, true, sB, pB0, pB1, sA, kc[0], kc[1], qf[2][0], qf[2][1], o[0], vc[0], vc[1], pA0, pA1, (void)0); \
        SLOT((t) == 0 ? 1 : 2, true, sA, pA0, pA1, sB, kc[0], kc[1], qf[3][0], qf[3][1], o[1], vc[0], vc[1], pB0, pB1, if ((t) < 7) PATCHK(kx)); \
        LOADK(kc, (t) + 2); \
        SLOT((t) == 0 ? 1 : 2, (t) < 7, sB, pB0, pB1, sA, kx[0], kx[1], qf[0][0], qf[0][1], o[2], vc[0], vc[1], pA0, pA1, (void)0); \
    } while (0)
    AttnBPre bpre; u64 flb = 0; bool bready = false;
#pragma unroll
    for (int t = 0; t < 8; t += 2) {
        if (t == 0) __builtin_amdgcn_s_setprio(3); else if (t == 2) __builtin_amdgcn_s_setprio(2); else if (t == 4) __builtin_amdgcn_s_setprio(1); else __builtin_amdgcn_s_setprio(0);
        TILE(kf[0], kf[1], vf[0], vf[1], t);
        if (t == 4) { bready = __all(flb == tag); attnB_load(bpre, ws, h, rb, wid, opaque_lane()); }

        else if (t == 6 && bready) attnB_compute(bpre, ws, h, rb, wid, opaque_lane());

        TILE(kf[1], kf[0], vf[1], vf[0], t + 1);
        if (t == 0) {
            if (has_qkv && wid < 4) {
                asm volatile("s_waitcnt vmcnt(4)" ::: "memory");
                if (opaque_lane() == 0 && ctl_add(lds, CTL_F2CNT) == 3) flag_set(ws, F2 + ct * 32 + rt, tag);
            }
            if (oct < 7 && wid >= 4) {
                if (has_qkv) ctl_wait(lds, CTL_RDONE, 4);
                wo_issue<14>(woA, oct, (unsigned)(uintptr_t)lds, (wid - 4) * 14, opaque_lane());
            }
        } else if (t == 2) {
            const int ln = opaque_lane();
            flb = __hip_atomic_load(flagp(ws, f2_index(h, rb, ln)), __ATOMIC_RELAXED, __HIP_MEMORY_SCOPE_AGENT);
        }
    }
    asm volatile("s_nop 1\n\tv_mfma_f32_32x32x16_bf16 %0, %1, %2, %0\n\tv_mfma_f32_32x32x16_bf16 %0, %3, %4, %0\n\ts_nop 7\n\ts_nop 3" : "+v"(o[3]) : "v"(vf[1][0]), "v"(pB0), "v"(vf[1][1]), "v"(pB1));
    asm volatile("" : "+v"(o[0]), "+v"(o[1]), "+v"(o[2]));
    STAMP(7);
    lane = opaque_lane();
    if (!bready) { flag_wait(ws, f2_index(h, rb, lane), tag); attnB_load(bpre, ws, h, rb, wid, lane); attnB_compute(bpre, ws, h, rb, wid, lane); }
    if (has_qkv) ctl_wait(lds, CTL_RDONE, 4);
#pragma unroll
    for (int j = 0; j < 4; ++j) merge_write1(o[j], lds, wid, j, lane);
    asm volatile("s_waitcnt vmcnt(0)" ::: "memory");
    if (lane == 0) { if (wid >= 4) ctl_add(lds, CTL_WA); if (ctl_add(lds, CTL_F4CNT) == 7) flag_set(ws, F4 + rb * 16 + h, tag); }
    if (oct < 7 && wid < 4) { ctl_wait(lds, CTL_WA, 4); wf_read(wf, lds, wid & 1, lane); }
    STAMP(15);
    BARRIER();
    STAMP(10);
    const bool bad = merge_reduce(ws, h, rb, lds, wid, lane);
    if (__any(bad) && lane == 0) ctl_store(lds, CTL_BAD, 1);
    STAMP(8);
    DRAIN_BARRIER();
    if (__builtin_amdgcn_readfirstlane(ctl_load(lds, CTL_BAD))) {
        attnA_safe(ws, h, rb, lds, wid, lane);
        BARRIER();
        merge_reduce(ws, h, rb, lds, wid, lane);
        DRAIN_BARRIER();
    }
    if (threadIdx.x == 0) flag_set(ws, F3 + rb * 16 + h, tag);
}

__device__ __forceinline__ void oproj_compute(char* ws, const u16* woB, u16* out, int rt, int ct, u64 tag, u32x4 (&wf)[2][14], char* lds, int wid, int lane) {
    const int br = wid >> 2, tt = (wid >> 1) & 1, kh = wid & 1;
    if (br) {
        wo_issue<14>(woB, ct, (unsigned)(uintptr_t)lds + LDS_MRG, (wid - 4) * 14, lane);
    }
    if ((wid & 3) == 3) { flag_wait(ws, (br ? F4 : F3) + (rt >> 1) * 16 + (lane & 15), tag); if (lane == 0) ctl_store(lds, br ? CTL_F4 : CTL_F3, 1); }
    else ctl_wait(lds, br ? CTL_F4 : CTL_F3, 1);
    STAMP(11);
    const auto rO = RSRC(ws + (br ? WS_OB : WS_OA), QC_BYTES);
    const int tile = rt * 2 + tt;
    u32x4 of[14];
#pragma unroll
    for (int i = 0; i < 14; ++i) of[i] = bload128(rO, (unsigned)lane * 16u, (unsigned)(tile * 56 + 2 * (kh * 14 + i)) * 512u);
    if (br) {
        asm volatile("s_waitcnt vmcnt(14)" ::: "memory");
        if (lane == 0) ctl_add(lds, CTL_WB);
        ctl_wait(lds, CTL_WB, 4);
        wf_read(wf, lds + LDS_MRG, kh, lane);
    }
    f32x16 acc[2] = {};
#pragma unroll
    for (int i = 0; i < 14; ++i) { acc[0] = MFMA32(asb(wf[0][i]), asb(of[i]), acc[0]); acc[1] = MFMA32(asb(wf[1][i]), asb(of[i]), acc[1]); }
    asm volatile("" : "+v"(acc[0]), "+v"(acc[1]));
    STAMP(12);
    {   const f32x16& gv = kh ? acc[0] : acc[1];
#pragma unroll
        for (int rq = 0; rq < 4; ++rq) *(f32x4*)(lds + 114688 + (wid * 4 + rq) * 1024 + lane * 16) = (f32x4){gv[4 * rq], gv[4 * rq + 1], gv[4 * rq + 2], gv[4 * rq + 3]};
    }
    asm volatile("s_waitcnt lgkmcnt(0)" ::: "memory");
    if (lane == 0) ctl_store(lds, CTL_X + wid * 4, 1);
    ctl_wait(lds, CTL_X + (wid ^ 1) * 4, 1);
    STAMP(13);
    f32x16 r = kh ? acc[1] : acc[0];
#pragma unroll
    for (int rq = 0; rq < 4; ++rq) { const f32x4 x = *(const f32x4*)(lds + 114688 + ((wid ^ 1) * 4 + rq) * 1024 + lane * 16); r[4 * rq] += x[0]; r[4 * rq + 1] += x[1]; r[4 * rq + 2] += x[2]; r[4 * rq + 3] += x[3]; }
    const unsigned orow = (unsigned)(rt * 64 + tt * 32 + (lane & 31)) * 1792u + (unsigned)(br * 448 + ct * 64 + kh * 32 + (lane >> 5) * 8) * 2u;
#pragma unroll
    for (int pr = 0; pr < 2; ++pr) {
        const unsigned a0 = cvtpk(r[8 * pr], r[8 * pr + 1]), a1 = cvtpk(r[8 * pr + 2], r[8 * pr + 3]), b0 = cvtpk(r[8 * pr + 4], r[8 * pr + 5]), b1 = cvtpk(r[8 * pr + 6], r[8 * pr + 7]);
        auto r0 = __builtin_amdgcn_permlane32_swap(a0, b0, false, false);
        auto r1 = __builtin_amdgcn_permlane32_swap(a1, b1, false, false);
        bstore128((u32x4){r0[0], r1[0], r0[1], r1[1]}, RSRC(out, 2048 * 896 * 2), orow + pr * 32u);
    }
}

__global__ void __launch_bounds__(512, 2) fused_kernel(const u16* xA, const u16* wA, const u16* woA, const u16* xB, const u16* wB, const u16* woB, u16* out, char* ws) {
    extern __shared__ __attribute__((aligned(16))) char lds[];
    int lane = threadIdx.x & 63; const int wid = __builtin_amdgcn_readfirstlane(threadIdx.x >> 6);
    const int id = blockIdx.x, xcd = id & 7, loc = id >> 3;
    STAMP_INIT();
    STAMP(0);
    if (threadIdx.x < 32) ctl_store(lds, threadIdx.x * 4, 0);
    const u64 tag = __hip_atomic_load(flagp(ws, FEP), __ATOMIC_RELAXED, __HIP_MEMORY_SCOPE_AGENT) + 1;
    const int gb = xcd & 1, grt = 8 * (xcd >> 1) + (loc & 7), qct = gb * 4 + (loc >> 3);
    const bool has_qkv = qct < 7;
    const int h = xcd * 2 + (loc & 1), rb = loc >> 1;
    const int ort = 4 * xcd + (loc & 3), oct = loc >> 2;
    unsigned warm0 = 0;
    if (wid < 4) {
        u64 pc; int len;
        asm volatile("s_getpc_b64 %0\n.Lfused_pc_ref:\n\ts_mov_b32 %1, .Lfused_code_end - .Lfused_pc_ref" : "=s"(pc), "=s"(len));
        const int off = (wid * 64 + lane) * 128;
        if (off < len) warm0 = *(const __attribute__((address_space(1))) unsigned*)(pc + off);
    }
    QkvOut outB;
    if (has_qkv) qkv_phase(xA, wA, xB, wB, ws, grt, qct, tag, lds, wid, lane, outB);
    else { if (threadIdx.x == 0) flag_set(ws, F0 + (xcd >> 1) * 8 + (loc & 7), tag); BARRIER(); }
    asm volatile("" :: "v"(warm0));
    STAMP(4);
    if (wid == 4) {
        flag_wait(ws, F1 + (lane < 32 ? (28 * h) >> 6 : (28 * h + 27) >> 6) * 32 + (lane & 31), tag);
        if (lane == 0) ctl_store(lds, CTL_F1, 1);
    } else ctl_wait(lds, CTL_F1, 1);
    STAMP(5);
    const u64 tags = (u64)(unsigned)__builtin_amdgcn_readfirstlane((unsigned)tag) | ((u64)(unsigned)__builtin_amdgcn_readfirstlane((unsigned)(tag >> 32)) << 32);
    u32x4 wf[2][14];
    attnA_phase(ws, woA, oct, h, rb, tags, grt, qct, has_qkv, outB, wf, lds, wid, lane);
    STAMP(9);
    lane = opaque_lane();
    if (oct < 7) {
        oproj_compute(ws, woB, out, ort, oct, tags, wf, lds, wid, lane);
    }
    STAMP(14);
    if (id == 0 && wid == 0) {
        flag_wait(ws, F0 + (lane & 31), tags);
        if (lane == 0) flag_set(ws, FEP, tags);
    }
    STAMP_DUMP();
    asm volatile(".Lfused_code_end:");
}

extern "C" void kernel_launch(void* const* d_in, const int* in_sizes, int n_in, void* d_out, int out_size, void* d_ws, size_t ws_size, hipStream_t stream) {
    const u16* xA = (const u16*)d_in[0]; const u16* wA = (const u16*)d_in[1]; const u16* woA = (const u16*)d_in[2];
    const u16* xB = (const u16*)d_in[3]; const u16* wB = (const u16*)d_in[4]; const u16* woB = (const u16*)d_in[5];
    (void)hipFuncSetAttribute((const void*)fused_kernel, hipFuncAttributeMaxDynamicSharedMemorySize, LDS_TOTAL + LDS_EXTRA);
    fused_kernel<<<256, 512, LDS_TOTAL + LDS_EXTRA, stream>>>(xA, wA, woA, xB, wB, woB, (u16*)d_out, (char*)d_ws);
}
```

```cpp
#include <hip/hip_runtime.h>
#include <hip/hip_bf16.h>
#include <stdint.h>

typedef unsigned short u16;
typedef unsigned long long u64;
typedef short bf16x8 __attribute__((ext_vector_type(8)));
typedef short s16x4 __attribute__((ext_vector_type(4)));
typedef float f32x4 __attribute__((ext_vector_type(4)));
typedef float f32x16 __attribute__((ext_vector_type(16)));
typedef unsigned u32x4 __attribute__((ext_vector_type(4)));
typedef unsigned u32x2 __attribute__((ext_vector_type(2)));
typedef __attribute__((address_space(3))) const char* lds_cptr;
typedef __attribute__((address_space(1))) u64 gu64;

constexpr int TILE_B = 56 * 512;
constexpr size_t QC_BYTES = (size_t)64 * TILE_B;
constexpr size_t V_BYTES = (size_t)16 * 64 * 2048;
constexpr size_t WS_QA = 0, WS_KA = WS_QA + QC_BYTES, WS_VA = WS_KA + QC_BYTES;
constexpr size_t WS_QB = WS_VA + V_BYTES, WS_KB = WS_QB + QC_BYTES, WS_VB = WS_KB + QC_BYTES;
constexpr size_t WS_OA = WS_VB + V_BYTES, WS_OB = WS_OA + QC_BYTES, WS_FLAGS = WS_OB + QC_BYTES;
constexpr int F1 = 0, F2 = 256, F3 = 512, F4 = 768, F0 = 1024, FEP = 1056;
constexpr float QSCALE = 0.18898223650461362f * 1.4426950408889634f;
constexpr int STG = 32768;
constexpr int LDS_Q = 147456, LDS_L = 131072, LDS_MRG = 57344, LDS_CTL = LDS_Q + 8192, LDS_MX = LDS_CTL + 128, LDS_TOTAL = LDS_MX + 1024 + 64;
constexpr int CTL_F1 = 0, CTL_F2CNT = 4, CTL_BAD = 8, CTL_QCNT = 12, CTL_F4CNT = 16, CTL_F3 = 20, CTL_RDONE = 24, CTL_F4 = 28, CTL_X = 32;
constexpr int CTL_WA = 64, CTL_WB = 68;
#define BPFG 1
#define PUBG 10
constexpr unsigned SPIN_MAX = 1u << 18;

#define STAMP(i)
#define STAMP_INIT()
#define STAMP_DUMP()
#define LDS_EXTRA 0

#define MFMA16(a, b, c) __builtin_amdgcn_mfma_f32_16x16x32_bf16(a, b, c, 0, 0, 0)
#define MFMA32(a, b, c) __builtin_amdgcn_mfma_f32_32x32x16_bf16(a, b, c, 0, 0, 0)
#define PIN(x) asm volatile("" : "+v"(x))
#define SBAR() __builtin_amdgcn_sched_barrier(0)
#define SC1 16

__device__ __forceinline__ unsigned cvtpk(float lo, float hi) {
    typedef float f2 __attribute__((ext_vector_type(2))); typedef __bf16 b2 __attribute__((ext_vector_type(2)));
    f2 v = {lo, hi}; b2 r = __builtin_convertvector(v, b2); return __builtin_bit_cast(unsigned, r);
}
__device__ __forceinline__ void glds16(const void* sbase, unsigned voff, unsigned lds_base) {
    asm volatile("s_mov_b32 m0, %2\n\ts_nop 0\n\tglobal_load_lds_dwordx4 %0, %1" :: "v"(voff), "s"(sbase), "s"(lds_base) : "memory");
}
__device__ __forceinline__ s16x4 vtr(lds_cptr p) {
    typedef short v4i16_t __attribute__((ext_vector_type(4)));
    return __builtin_bit_cast(s16x4, __builtin_amdgcn_ds_read_tr16_b64_v4i16((__attribute__((address_space(3))) v4i16_t*)p));
}
__device__ __forceinline__ bf16x8 cat8(s16x4 a, s16x4 b) { return (bf16x8){a[0], a[1], a[2], a[3], b[0], b[1], b[2], b[3]}; }
__device__ __forceinline__ bf16x8 ldsrd128(lds_cptr p) { return *(const __attribute__((address_space(3))) bf16x8*)p; }
#define RSRC(p, bytes) __builtin_amdgcn_make_buffer_rsrc((void*)(p), 0, (int)(bytes), 0x00020000)
__device__ __forceinline__ u32x4 bload128(__amdgpu_buffer_rsrc_t r, unsigned voff, unsigned soff) { return __builtin_amdgcn_raw_buffer_load_b128(r, voff, soff, SC1); }
__device__ __forceinline__ void bstore128(u32x4 v, __amdgpu_buffer_rsrc_t r, unsigned voff) { __builtin_amdgcn_raw_buffer_store_b128(v, r, voff, 0, SC1); }
__device__ __forceinline__ void bstore64(u32x2 v, __amdgpu_buffer_rsrc_t r, unsigned voff) { __builtin_amdgcn_raw_buffer_store_b64(v, r, voff, 0, SC1); }
__device__ __forceinline__ bf16x8 asb(u32x4 v) { return __builtin_bit_cast(bf16x8, v); }

__device__ __forceinline__ gu64* flagp(char* ws, int idx) { return (gu64*)(ws + WS_FLAGS + (size_t)idx * 128); }
__device__ __forceinline__ void flag_set(char* ws, int idx, u64 tag) { __hip_atomic_store(flagp(ws, idx), tag, __ATOMIC_RELAXED, __HIP_MEMORY_SCOPE_AGENT); }
__device__ __forceinline__ void flag_wait(char* ws, int idx, u64 tag) {
    gu64* p = flagp(ws, idx);
    for (unsigned spins = 0; spins < SPIN_MAX; ++spins) {
        const u64 v = __hip_atomic_load(p, __ATOMIC_RELAXED, __HIP_MEMORY_SCOPE_AGENT);
        if (__all(v == tag)) break;
        __builtin_amdgcn_s_sleep(1);
    }
}
__device__ __forceinline__ int opaque_lane() { int l; asm volatile("v_mbcnt_lo_u32_b32 %0, -1, 0\n\tv_mbcnt_hi_u32_b32 %0, -1, %0" : "=v"(l)); return l; }
__device__ __forceinline__ int f2_index(int h, int rb, int lane) { const int i = lane % 6, r = 2 * rb - 1 + i % 3; return F2 + (i < 3 ? (28 * h) >> 6 : (28 * h + 27) >> 6) * 32 + (r < 0 ? 0 : r); }
typedef __attribute__((address_space(3))) int* lds_iptr;
__device__ __forceinline__ int ctl_load(char* lds, int off) { return __hip_atomic_load((lds_iptr)(lds + LDS_CTL + off), __ATOMIC_RELAXED, __HIP_MEMORY_SCOPE_WORKGROUP); }
__device__ __forceinline__ void ctl_store(char* lds, int off, int v) { __hip_atomic_store((lds_iptr)(lds + LDS_CTL + off), v, __ATOMIC_RELAXED, __HIP_MEMORY_SCOPE_WORKGROUP); }
__device__ __forceinline__ void ctl_wait(char* lds, int off, int n) {
    for (unsigned spins = 0; spins < (SPIN_MAX << 6); ++spins) { if (__builtin_amdgcn_readfirstlane(ctl_load(lds, off)) >= n) break; __builtin_amdgcn_s_sleep(1); }
    asm volatile("" ::: "memory");
}
__device__ __forceinline__ int ctl_add(char* lds, int off) { return __hip_atomic_fetch_add((lds_iptr)(lds + LDS_CTL + off), 1, __ATOMIC_RELAXED, __HIP_MEMORY_SCOPE_WORKGROUP); }
#define BARRIER() asm volatile("s_waitcnt lgkmcnt(0)\n\ts_barrier" ::: "memory")
#define DRAIN_BARRIER() asm volatile("s_waitcnt vmcnt(0) lgkmcnt(0)\n\ts_barrier" ::: "memory")

struct QkvFrag { bf16x8 x[2][4], w[2][3]; };
struct QkvAcc { f32x4 q[4], k[4], v[4]; };
struct QkvOut { u32x4 q[2], k[2], v[2]; };
__device__ __forceinline__ void qkv_pack(QkvOut& o, const QkvAcc& a, float qs) {
#pragma unroll
    for (int tb = 0; tb < 2; ++tb) {
        const f32x4 q0 = a.q[2 * tb] * qs, q1 = a.q[2 * tb + 1] * qs, k0 = a.k[2 * tb], k1 = a.k[2 * tb + 1], v0 = a.v[2 * tb], v1 = a.v[2 * tb + 1];
        { auto ra = __builtin_amdgcn_permlane16_swap(cvtpk(q0[0], q0[1]), cvtpk(q1[0], q1[1]), false, false); auto rb = __builtin_amdgcn_permlane16_swap(cvtpk(q0[2], q0[3]), cvtpk(q1[2], q1[3]), false, false);
          o.q[tb] = (u32x4){ra[0], rb[0], ra[1], rb[1]}; }
        { auto ra = __builtin_amdgcn_permlane16_swap(cvtpk(k0[0], k0[1]), cvtpk(k1[0], k1[1]), false, false); auto rb = __builtin_amdgcn_permlane16_swap(cvtpk(k0[2], k0[3]), cvtpk(k1[2], k1[3]), false, false);
          o.k[tb] = (u32x4){ra[0], rb[0], ra[1], rb[1]}; }
        { auto ra = __builtin_amdgcn_permlane32_swap(cvtpk(v0[0], v0[1]), cvtpk(v1[0], v1[1]), false, false); auto rb = __builtin_amdgcn_permlane32_swap(cvtpk(v0[2], v0[3]), cvtpk(v1[2], v1[3]), false, false);
          o.v[tb] = (u32x4){ra[0], rb[0], ra[1], rb[1]}; }
    }
}
template <int BR, int I>
__device__ __forceinline__ void qkv_store(const QkvOut& o, char* ws, int rt, int ct, int c, int lane) {
    constexpr int tb = I / 3, kind = I % 3;
    const int tile = rt * 2 + tb;
    if (kind < 2) {
        const unsigned off = (unsigned)(tile * 56 + 8 * ct + 2 * c) * 512u + (unsigned)lane * 16u;
        if (kind == 0) bstore128(o.q[tb], RSRC(ws + (BR ? WS_QB : WS_QA), QC_BYTES), off); else bstore128(o.k[tb], RSRC(ws + (BR ? WS_KB : WS_KA), QC_BYTES), off);
    } else {
        const int n = 64 * ct + 16 * c + (lane & 15), h = n / 28, d = n - 28 * h;
        const unsigned off = (unsigned)(h * 64 + tile) * 2048u + (unsigned)(lane >> 4) * 512u + (unsigned)d * 16u;
        const auto rs = RSRC(ws + (BR ? WS_VB : WS_VA), V_BYTES);
        bstore128(o.v[tb], rs, off);
        if (d == 27) bstore128((u32x4){0x3F803F80u, 0x3F803F80u, 0x3F803F80u, 0x3F803F80u}, rs, off + 16u);
    }
}

__device__ __forceinline__ void qkv_phase(const u16* xA, const u16* wA, const u16* xB, const u16* wB, char* ws, int rt, int ct, u64 tag, char* lds, int wid, int lane, QkvOut& outB) {
    if (wid >= 4) {
        const unsigned lds0 = (unsigned)(uintptr_t)lds;
        const int lw = wid - 4, r8 = lane >> 3, dpos = lane & 7;
        unsigned xoff[2], woff[2];
#pragma unroll
        for (int i = 0; i < 2; ++i) { const int row = 16 * lw + 8 * i + r8;
            xoff[i] = (unsigned)row * 896u + (unsigned)((dpos ^ ((row >> 1) & 7)) << 4);
            const int f2 = ((row >> 1) & 1) | (((row >> 3) & 1) << 1);
            woff[i] = (unsigned)row * 2688u + (unsigned)((((dpos >> 1) ^ f2) * 2 + (dpos & 1)) << 4); }
        const int rot = __builtin_amdgcn_readfirstlane(((rt & 7) + 2 * (ct & 3)) % 7);
        const char* xa = (const char*)xA + (size_t)rt * 64 * 896; const char* wa = (const char*)wA + (size_t)ct * 128;
        const char* xb = (const char*)xB + (size_t)rt * 64 * 896; const char* wb = (const char*)wB + (size_t)ct * 128;
#define QKV_ISSUE(g) do { int st_ = ((g) % 7) + rot; st_ = st_ >= 7 ? st_ - 7 : st_; const char* x_ = ((g) < 7 ? xa : xb) + st_ * 128; const char* w_ = ((g) < 7 ? wa : wb) + (size_t)st_ * (64 * 2688); \
        const unsigned sl_ = lds0 + ((g) & 3) * STG + lw * 2048; \
        glds16(x_, xoff[0], sl_); glds16(x_, xoff[1], sl_ + 1024); \
        glds16(w_, woff[0], sl_ + 8192); glds16(w_, woff[1], sl_ + 8192 + 1024); glds16(w_ + 896, woff[0], sl_ + 16384); glds16(w_ + 896, woff[1], sl_ + 16384 + 1024); \
        glds16(w_ + 1792, woff[0], sl_ + 24576); glds16(w_ + 1792, woff[1], sl_ + 24576 + 1024); } while (0)
        QKV_ISSUE(0); QKV_ISSUE(1);
#pragma unroll
        for (int g = 0; g < 14; ++g) {
            if (g <= 12) asm volatile("s_waitcnt vmcnt(8) lgkmcnt(0)\n\ts_barrier" ::: "memory");
            else asm volatile("s_waitcnt vmcnt(0) lgkmcnt(0)\n\ts_barrier" ::: "memory");
            if (g == 0) STAMP(1);
            if (g + 2 < 14) QKV_ISSUE(g + 2);
        }
        return;
    }
    const int c = wid, g4 = lane >> 4, t16 = lane & 15;
    const unsigned xr0 = (unsigned)t16 * 128u + (unsigned)((g4 ^ (t16 >> 1)) << 4);
    const int q = t16 >> 2, p = lane & 3;
    const int rf2 = ((q >> 1) & 1) | ((g4 & 1) << 1);
    const unsigned wr0 = 8192u + (unsigned)(8 * g4 + q) * 128u + (unsigned)((c ^ rf2) * 32) + (unsigned)p * 8u;
    QkvAcc acc; QkvOut outA;
#pragma unroll
    for (int i = 0; i < 4; ++i) acc.q[i] = acc.k[i] = acc.v[i] = (f32x4){};
    QkvFrag fr[2];
    unsigned pfa = 0, pfb = 0;
#define QMF(i) do { if (g > 0) { constexpr int ks_ = (i) / 12, s_ = ((i) % 12) / 4, b_ = (i) % 4; const QkvFrag& f_ = fr[(g - 1) & 1]; \
        if (s_ == 0) acc.q[b_] = MFMA16(f_.w[ks_][0], f_.x[ks_][b_], acc.q[b_]); if (s_ == 1) acc.k[b_] = MFMA16(f_.w[ks_][1], f_.x[ks_][b_], acc.k[b_]); \
        if (s_ == 2) acc.v[b_] = MFMA16(f_.x[ks_][b_], f_.w[ks_][2], acc.v[b_]); } SBAR(); } while (0)
#define QRX(ks, b) do { if (g < 14) fr[g & 1].x[ks][b] = ldsrd128(sl + (xr0 ^ ((ks) * 64)) + (b) * 2048); } while (0)
#define QRW(ks, s) do { if (g < 14) { lds_cptr wp_ = sl + wr0 + (ks) * 4096 + (s) * 8192; fr[g & 1].w[ks][s] = cat8(vtr(wp_), vtr(wp_ + 512)); } } while (0)
#define QST(gg, i) do { if (g == (gg)) { qkv_store<0, i>(outA, ws, rt, ct, c, lane); SBAR(); } } while (0)
#pragma unroll
    for (int g = 0; g < 15; ++g) {
        if (g == PUBG) asm volatile("s_waitcnt vmcnt(0) lgkmcnt(0)\n\ts_barrier" ::: "memory");
        else if (g < 14) asm volatile("s_waitcnt lgkmcnt(0)\n\ts_barrier" ::: "memory");
        SBAR();
        if (g == 7) STAMP(2);
        lds_cptr sl = (lds_cptr)lds + (g & 3) * STG;
        if (g == PUBG && threadIdx.x == 0) flag_set(ws, F1 + ct * 32 + rt, tag);
        SBAR();
        QRX(0, 0); QMF(0); QRX(0, 1); QMF(1); QRX(0, 2); QMF(2); QRX(0, 3); QMF(3); QST(8, 3); QRW(0, 0); QMF(4); QRW(0, 1); QMF(5); QRW(0, 2); QMF(6);
        QRX(1, 0); QMF(7); QRX(1, 1); QMF(8); QRX(1, 2); QMF(9); QST(8, 4); QRX(1, 3); QMF(10); QRW(1, 0); QMF(11); QRW(1, 1); QMF(12); QRW(1, 2); QMF(13);
        QMF(14); QMF(15); QST(8, 5); QMF(16); QMF(17); QMF(18); QMF(19); QMF(20); QMF(21); QMF(22); QMF(23);
        if (g == 7) {
            qkv_pack(outA, acc, QSCALE);
            qkv_store<0, 0>(outA, ws, rt, ct, c, lane); qkv_store<0, 1>(outA, ws, rt, ct, c, lane); qkv_store<0, 2>(outA, ws, rt, ct, c, lane);
#pragma unroll
            for (int i = 0; i < 4; ++i) acc.q[i] = acc.k[i] = acc.v[i] = (f32x4){};
        }
    }
    if (lane == 0) ctl_add(lds, CTL_RDONE);
    qkv_pack(outB, acc, 1.0f);
}

__device__ __forceinline__ float max16(const f32x16& s) {
    float a = fmaxf(fmaxf(s[0], s[1]), s[2]), b = fmaxf(fmaxf(s[3], s[4]), s[5]);
    a = fmaxf(fmaxf(a, s[6]), s[7]); b = fmaxf(fmaxf(b, s[8]), s[9]); a = fmaxf(fmaxf(a, s[10]), s[11]); b = fmaxf(fmaxf(b, s[12]), s[13]);
    a = fmaxf(fmaxf(a, s[14]), s[15]); return fmaxf(a, b);
}
__device__ __forceinline__ float xhalf_max(float m) {
    auto r = __builtin_amdgcn_permlane32_swap(__float_as_uint(m), __float_as_uint(m), false, false);
    return fmaxf(__uint_as_float(r[0]), __uint_as_float(r[1]));
}
__device__ __forceinline__ void patch(u32x4& f0, u32x4& f1, bool pe, bool po, unsigned a, unsigned b) {
    f0[0] = po ? a : f0[0]; f0[1] = po ? b : f0[1]; f1[2] = pe ? a : f1[2]; f1[3] = pe ? b : f1[3];
}
__device__ __forceinline__ void exp_pack(const f32x16& s, u32x4& p0, u32x4& p1) {
    float e[16];
#pragma unroll
    for (int r = 0; r < 16; ++r) e[r] = __builtin_amdgcn_exp2f(s[r]);
    p0 = (u32x4){cvtpk(e[0], e[1]), cvtpk(e[2], e[3]), cvtpk(e[4], e[5]), cvtpk(e[6], e[7])};
    p1 = (u32x4){cvtpk(e[8], e[9]), cvtpk(e[10], e[11]), cvtpk(e[12], e[13]), cvtpk(e[14], e[15])};
}
__device__ __forceinline__ void merge_write1(const f32x16& o, char* lds, int wid, int j, int lane) {
#pragma unroll
    for (int hf = 0; hf < 2; ++hf)
        *(u32x4*)(lds + LDS_MRG + ((wid * 4 + j) * 2 + hf) * 1024 + lane * 16) = (u32x4){cvtpk(o[8 * hf], o[8 * hf + 1]), cvtpk(o[8 * hf + 2], o[8 * hf + 3]), cvtpk(o[8 * hf + 4], o[8 * hf + 5]), cvtpk(o[8 * hf + 6], o[8 * hf + 7])};
    if (lane >= 32) *(float*)(lds + LDS_L + ((wid * 4 + j) * 32 + lane - 32) * 4) = o[12];
}
__device__ __forceinline__ bool merge_reduce(char* ws, int h, int rb, char* lds, int wid, int lane) {
    const bool hi = lane >= 32;
    const int j = wid >> 1, hf = wid & 1;
    const int tile = rb * 4 + j, q = lane & 31;
    u32x4 p[8]; float pl[8];
#pragma unroll
    for (int w = 0; w < 8; ++w) pl[w] = *(const float*)(lds + LDS_L + ((w * 4 + j) * 32 + q) * 4);
#pragma unroll
    for (int w = 0; w < 8; ++w) p[w] = *(const u32x4*)(lds + LDS_MRG + ((w * 4 + j) * 2 + hf) * 1024 + lane * 16);
    const int col0 = 28 * h + 16 * hf + (hi ? 4 : 0), col1 = col0 + 8;
    const unsigned off0 = (unsigned)((tile * 56 + (col0 >> 3)) * 32 + q) * 16u + (unsigned)((col0 >> 2) & 1) * 8u, off1 = (unsigned)((tile * 56 + (col1 >> 3)) * 32 + q) * 16u + (unsigned)((col1 >> 2) & 1) * 8u;
    const float l = ((pl[0] + pl[1]) + (pl[2] + pl[3])) + ((pl[4] + pl[5]) + (pl[6] + pl[7]));
    const float inv = __builtin_amdgcn_rcpf(l);
    float a[8] = {};
#pragma unroll
    for (int w = 0; w < 8; ++w)
#pragma unroll
        for (int i = 0; i < 4; ++i) { a[2 * i] += __uint_as_float(p[w][i] << 16); a[2 * i + 1] += __uint_as_float(p[w][i] & 0xffff0000u); }
#pragma unroll
    for (int i = 0; i < 8; ++i) a[i] *= inv;
    const auto rO = RSRC(ws + WS_OA, QC_BYTES);
    const bool last = hf == 1 && hi;
    bstore64((u32x2){cvtpk(a[0], a[1]), cvtpk(a[2], a[3])}, rO, off0);
    if (!last) bstore64((u32x2){cvtpk(a[4], a[5]), cvtpk(a[6], a[7])}, rO, off1);
    float z = a[0] * 0.f; z = fmaf(a[1], 0.f, z); z = fmaf(a[2], 0.f, z); z = fmaf(a[3], 0.f, z);
    float z1 = a[4] * 0.f; z1 = fmaf(a[5], 0.f, z1); z1 = fmaf(a[6], 0.f, z1); z1 = fmaf(a[7], 0.f, z1);
    const bool ok = l > 1.0e-18f && l < 1.0e30f && z == 0.f && (last || z1 == 0.f);
    return !ok;
}

__device__ __forceinline__ void attnA_safe(char* ws, int h, int rb, char* lds, int wid, int lane) {
    const bool hi = lane >= 32, pe = hi && !(h & 1), po = !hi && (h & 1); const int m0 = (28 * h) >> 3;
    const auto rQ = RSRC(ws + WS_QA, QC_BYTES), rK = RSRC(ws + WS_KA, QC_BYTES), rV = RSRC(ws + WS_VA, V_BYTES);
    const unsigned lo16 = (unsigned)lane * 16u;
    float* mx = (float*)(lds + LDS_MX);
#pragma unroll 1
    for (int j = 0; j < 4; ++j) {
        const unsigned b = (unsigned)((rb * 4 + j) * 56 + m0) * 512u;
        u32x4 q0 = bload128(rQ, lo16, b), q1 = bload128(rQ, lo16, b + 1024u);
        patch(q0, q1, pe, po, 0u, 0u);
        float m = -3.0e38f; f32x16 acc = {};
#pragma unroll 1
        for (int t = 0; t < 8; ++t) {
            const unsigned ko = (unsigned)((wid * 8 + t) * 56 + m0) * 512u, vo = (unsigned)(h * 64 + wid * 8 + t) * 2048u;
            u32x4 k0 = bload128(rK, lo16, ko), k1 = bload128(rK, lo16, ko + 1024u), v0 = bload128(rV, lo16, vo), v1 = bload128(rV, lo16, vo + 1024u);
            patch(k0, k1, pe, po, 0u, 0u);
            f32x16 s = {}; s = MFMA32(asb(k0), asb(q0), s); s = MFMA32(asb(k1), asb(q1), s);
            const float mn = fmaxf(m, xhalf_max(max16(s)));
            const float al = __builtin_amdgcn_exp2f(m - mn); m = mn;
#pragma unroll
            for (int r = 0; r < 16; ++r) { s[r] -= mn; acc[r] *= al; }
            u32x4 p0, p1; exp_pack(s, p0, p1);
            acc = MFMA32(asb(v0), asb(p0), acc); acc = MFMA32(asb(v1), asb(p1), acc);
        }
        BARRIER();
        if (!hi) mx[wid * 32 + lane] = m;
        BARRIER();
        float g = m;
#pragma unroll
        for (int w = 0; w < 8; ++w) g = fmaxf(g, mx[w * 32 + (lane & 31)]);
        const float f = __builtin_amdgcn_exp2f(m - g);
        merge_write1(acc * f, lds, wid, j, lane);
    }
}

struct AttnBPre { u32x4 q, kp, kc; u32x2 v[2][2]; };
__device__ __forceinline__ void attnB_load(AttnBPre& a, char* ws, int h, int rb, int wid, int lane) {
    const int m0 = (28 * h) >> 3, T = rb * 4 + (wid >> 1), sub = wid & 1;
    const int Tp = sub ? T : (T > 0 ? T - 1 : 0), subp = sub ^ 1;
    const auto rQ = RSRC(ws + WS_QB, QC_BYTES), rK = RSRC(ws + WS_KB, QC_BYTES), rV = RSRC(ws + WS_VB, V_BYTES);
    const int g = lane >> 4, t16 = lane & 15;
    const unsigned co = (unsigned)g * 512u + (unsigned)t16 * 16u;
    a.q = bload128(rQ, co, (unsigned)(T * 56 + m0) * 512u + (unsigned)sub * 256u);
    a.kp = bload128(rK, co, (unsigned)(Tp * 56 + m0) * 512u + (unsigned)subp * 256u);
    a.kc = bload128(rK, co, (unsigned)(T * 56 + m0) * 512u + (unsigned)sub * 256u);
    const unsigned vo = (unsigned)(g & 1) * 512u + (unsigned)t16 * 16u + (unsigned)(g >> 1) * 8u;
#pragma unroll
    for (int db = 0; db < 2; ++db) {
        a.v[db][0] = __builtin_amdgcn_raw_buffer_load_b64(rV, vo + db * 256u, (unsigned)(h * 64 + Tp) * 2048u + (unsigned)subp * 1024u, SC1);
        a.v[db][1] = __builtin_amdgcn_raw_buffer_load_b64(rV, vo + db * 256u, (unsigned)(h * 64 + T) * 2048u + (unsigned)sub * 1024u, SC1);
    }
}
__device__ __forceinline__ void attnB_compute(AttnBPre& a, char* ws, int h, int rb, int wid, int lane) {
    const int T = rb * 4 + (wid >> 1), sub = wid & 1, g = lane >> 4, ql = lane & 15;
    const bool padl = (h & 1) ? g == 0 : g == 3;
    if (h & 1) { a.q[0] = padl ? 0u : a.q[0]; a.q[1] = padl ? 0u : a.q[1]; a.kp[0] = padl ? 0u : a.kp[0]; a.kp[1] = padl ? 0u : a.kp[1]; a.kc[0] = padl ? 0u : a.kc[0]; a.kc[1] = padl ? 0u : a.kc[1]; }
    else { a.q[2] = padl ? 0u : a.q[2]; a.q[3] = padl ? 0u : a.q[3]; a.kp[2] = padl ? 0u : a.kp[2]; a.kp[3] = padl ? 0u : a.kp[3]; a.kc[2] = padl ? 0u : a.kc[2]; a.kc[3] = padl ? 0u : a.kc[3]; }
    f32x4 sp = {}, sc = {};
    sp = MFMA16(asb(a.kp), asb(a.q), sp); sc = MFMA16(asb(a.kc), asb(a.q), sc);
    const bool hasp = sub || T > 0;
    float m = -INFINITY;
#pragma unroll
    for (int r = 0; r < 4; ++r) {
        const int dc = ql - (4 * g + r), dp = dc + 16;
        sp[r] = (hasp && dp <= 14 && !(dp & 1)) ? sp[r] : -INFINITY; sc[r] = (dc >= 0 && !(dc & 1)) ? sc[r] : -INFINITY;
        m = fmaxf(m, fmaxf(sp[r], sc[r]));
    }
    { auto r = __builtin_amdgcn_permlane16_swap(__float_as_uint(m), __float_as_uint(m), false, false); m = fmaxf(__uint_as_float(r[0]), __uint_as_float(r[1])); }
    m = xhalf_max(m);
    float e[8];
#pragma unroll
    for (int r = 0; r < 4; ++r) { e[r] = __builtin_amdgcn_exp2f((sp[r] - m) * QSCALE); e[4 + r] = __builtin_amdgcn_exp2f((sc[r] - m) * QSCALE); }
    const u32x4 pf = {cvtpk(e[0], e[1]), cvtpk(e[2], e[3]), cvtpk(e[4], e[5]), cvtpk(e[6], e[7])};
    f32x4 o0 = {}, o1 = {};
    o0 = MFMA16(asb((u32x4){a.v[0][0][0], a.v[0][0][1], a.v[0][1][0], a.v[0][1][1]}), asb(pf), o0);
    o1 = MFMA16(asb((u32x4){a.v[1][0][0], a.v[1][0][1], a.v[1][1][0], a.v[1][1][1]}), asb(pf), o1);
    float l;
    { auto r16 = __builtin_amdgcn_permlane16_swap(__float_as_uint(o1[0]), __float_as_uint(o1[0]), false, false); auto r32 = __builtin_amdgcn_permlane32_swap(r16[1], r16[1], false, false); l = __uint_as_float(r32[1]); }
    const float inv = __builtin_amdgcn_rcpf(l);
    const auto rO = RSRC(ws + WS_OB, QC_BYTES);
    const unsigned tok = (unsigned)(sub * 16 + ql) * 16u;
    { const int col = 28 * h + 4 * g; bstore64((u32x2){cvtpk(o0[0] * inv, o0[1] * inv), cvtpk(o0[2] * inv, o0[3] * inv)}, rO, (unsigned)(T * 56 + (col >> 3)) * 512u + tok + (unsigned)((col >> 2) & 1) * 8u); }
    if (g < 3) { const int col = 28 * h + 16 + 4 * g; bstore64((u32x2){cvtpk(o1[0] * inv, o1[1] * inv), cvtpk(o1[2] * inv, o1[3] * inv)}, rO, (unsigned)(T * 56 + (col >> 3)) * 512u + tok + (unsigned)((col >> 2) & 1) * 8u); }
}

template <int N>
__device__ __forceinline__ void wo_issue(const u16* wo, int ct, unsigned lds_base, int pc0, int lane) {
    const int r8 = lane >> 3, dpos = lane & 7;
    const unsigned woff = (unsigned)r8 * 896u + (unsigned)((dpos ^ ((r8 & 2) << 1)) << 4);
#pragma unroll
    for (int i = 0; i < N; ++i) glds16((const char*)wo + (size_t)ct * 128 + (size_t)(pc0 + i) * 8 * 896, woff, lds_base + (pc0 + i) * 1024);
}
__device__ __forceinline__ void wf_read(u32x4 (&wf)[2][14], const char* img, int kh, int lane) {
    const int g16 = lane >> 4, q = (lane & 15) >> 2, p = lane & 3;
#pragma unroll
    for (int i = 0; i < 14; ++i)
#pragma unroll
        for (int c = 0; c < 2; ++c) {
            const int row = (kh * 14 + i) * 16 + 8 * (g16 >> 1) + q;
            lds_cptr wp = (lds_cptr)img + (unsigned)row * 128u + (unsigned)(((2 * c + (g16 & 1)) ^ (q & 2)) * 32) + (unsigned)p * 8u;
            wf[c][i] = __builtin_bit_cast(u32x4, cat8(vtr(wp), vtr(wp + 512)));
        }
}
__device__ __forceinline__ void attnA_phase(char* ws, const u16* woA, int oct, int h, int rb, u64 tag, int rt, int ct, bool has_qkv, const QkvOut& outB, u32x4 (&wf)[2][14], char* lds, int wid, int lane) {
    const bool hi = lane >= 32, pe = hi && !(h & 1), po = !hi && (h & 1);
    const int m0 = (28 * h) >> 3;
    const auto rK = RSRC(ws + WS_KA, QC_BYTES), rV = RSRC(ws + WS_VA, V_BYTES);
    const unsigned lo16 = (unsigned)lane * 16u;
    const unsigned kbase = (unsigned)((wid * 8) * 56 + m0) * 512u, vbase = (unsigned)(h * 64 + wid * 8) * 2048u;
    const int rot = rb & 7;
    u32x4 qf[4][2], kf[3][2], vf[3][2];
    {   const unsigned k0 = kbase + (unsigned)rot * TILE_B, v0 = vbase + (unsigned)rot * 2048u, k1 = kbase + (unsigned)((rot + 1) & 7) * TILE_B;
        kf[0][0] = bload128(rK, lo16, k0); kf[0][1] = bload128(rK, lo16, k0 + 1024u);
        if (wid >= 4) { const unsigned b = (unsigned)((rb * 4 + wid - 4) * 56 + m0) * 512u; const auto rQ = RSRC(ws + WS_QA, QC_BYTES); qf[0][0] = bload128(rQ, lo16, b); qf[0][1] = bload128(rQ, lo16, b + 1024u); }
        vf[0][0] = bload128(rV, lo16, v0); vf[0][1] = bload128(rV, lo16, v0 + 1024u);
        kf[1][0] = bload128(rK, lo16, k1); kf[1][1] = bload128(rK, lo16, k1 + 1024u);
        const unsigned v1 = vbase + (unsigned)((rot + 1) & 7) * 2048u, k2 = kbase + (unsigned)((rot + 2) & 7) * TILE_B;
        vf[1][0] = bload128(rV, lo16, v1); vf[1][1] = bload128(rV, lo16, v1 + 1024u);
        kf[2][0] = bload128(rK, lo16, k2); kf[2][1] = bload128(rK, lo16, k2 + 1024u); }
    patch(kf[0][0], kf[0][1], pe, po, 0x3F803F80u, 0u);
    if (wid >= 4) {
        patch(qf[0][0], qf[0][1], pe, po, 0u, 0u);
        f32x16 s = {}; s = MFMA32(asb(kf[0][0]), asb(qf[0][0]), s); s = MFMA32(asb(kf[0][1]), asb(qf[0][1]), s);
        const float m = xhalf_max(max16(s));
        const unsigned mh = cvtpk(-m, 0.f) & 0xffffu; const float r = -m - __uint_as_float(mh << 16);
        patch(qf[0][0], qf[0][1], pe, po, mh | (cvtpk(r, 0.f) << 16), 0u);
        *(u32x4*)(lds + LDS_Q + (wid - 4) * 2048 + lane * 16) = qf[0][0]; *(u32x4*)(lds + LDS_Q + (wid - 4) * 2048 + 1024 + lane * 16) = qf[0][1];
        asm volatile("s_waitcnt lgkmcnt(0)" ::: "memory");
        if (lane == 0) ctl_add(lds, CTL_QCNT);
    }
    ctl_wait(lds, CTL_QCNT, 4);
    STAMP(6);
#pragma unroll
    for (int j = 0; j < 4; ++j) { qf[j][0] = *(const u32x4*)(lds + LDS_Q + j * 2048 + lane * 16); qf[j][1] = *(const u32x4*)(lds + LDS_Q + j * 2048 + 1024 + lane * 16); }
    if (has_qkv && wid < 4) {
        qkv_store<1, 0>(outB, ws, rt, ct, wid, lane); qkv_store<1, 1>(outB, ws, rt, ct, wid, lane); qkv_store<1, 2>(outB, ws, rt, ct, wid, lane);
        qkv_store<1, 3>(outB, ws, rt, ct, wid, lane); qkv_store<1, 4>(outB, ws, rt, ct, wid, lane); qkv_store<1, 5>(outB, ws, rt, ct, wid, lane);
    }
#pragma unroll
    for (int j = 0; j < 4; ++j) { PIN(qf[j][0]); PIN(qf[j][1]); }
    PIN(kf[0][0]); PIN(kf[0][1]);
    f32x16 o[4];
#define VEXP(d, s) asm volatile("v_exp_f32 %0, %1" : "=v"(d) : "v"(s))
#define VCVT(d, a, b) asm volatile("v_cvt_pk_bf16_f32 %0, %1, %2" : "=v"(d) : "v"(a), "v"(b))
#define MF0(d, a, b) asm volatile("v_mfma_f32_32x32x16_bf16 %0, %1, %2, 0" : "=&v"(d) : "v"(a), "v"(b))
#define MFA(d, a, b) asm volatile("v_mfma_f32_32x32x16_bf16 %0, %1, %2, %0" : "+v"(d) : "v"(a), "v"(b))
#define SLOT(PV, QK, Sc, Pc0, Pc1, Sn, kn0, kn1, qn0, qn1, Op, vp0, vp1, Pp0, Pp1, INJ) do { \
        float e0, e1, e2, e3, e4, e5, e6, e7, e8, e9, e10, e11, e12, e13, e14, e15; \
        if (QK) MF0(Sn, kn0, qn0); \
        VEXP(e0, Sc[0]); VEXP(e1, Sc[1]); VEXP(e2, Sc[2]); VEXP(e3, Sc[3]); \
        if (QK) MFA(Sn, kn1, qn1); \
        VEXP(e4, Sc[4]); VEXP(e5, Sc[5]); VEXP(e6, Sc[6]); VEXP(e7, Sc[7]); VCVT(Pc0[0], e0, e1); VCVT(Pc0[1], e2, e3); \
        INJ; \
        if ((PV) == 2) MFA(Op, vp0, Pp0); else if ((PV) == 1) MF0(Op, vp0, Pp0); \
        VEXP(e8, Sc[8]); VEXP(e9, Sc[9]); VEXP(e10, Sc[10]); VEXP(e11, Sc[11]); VCVT(Pc0[2], e4, e5); VCVT(Pc0[3], e6, e7); \
        if (PV) MFA(Op, vp1, Pp1); \
        VEXP(e12, Sc[12]); VEXP(e13, Sc[13]); VEXP(e14, Sc[14]); VEXP(e15, Sc[15]); VCVT(Pc1[0], e8, e9); VCVT(Pc1[1], e10, e11); VCVT(Pc1[2], e12, e13); VCVT(Pc1[3], e14, e15); \
    } while (0)
    f32x16 sA, sB; u32x4 pA0, pA1, pB0, pB1;
    asm volatile("s_nop 1\n\tv_mfma_f32_32x32x16_bf16 %0, %1, %2, 0\n\tv_mfma_f32_32x32x16_bf16 %0, %3, %4, %0\n\ts_nop 7\n\ts_nop 3" : "=&v"(sA) : "v"(kf[0][0]), "v"(qf[0][0]), "v"(kf[0][1]), "v"(qf[0][1]));
#define PATCHK(kx) do { PIN(kx[0]); PIN(kx[1]); patch(kx[0], kx[1], pe, po, 0x3F803F80u, 0u); PIN(kx[0]); PIN(kx[1]); } while (0)
#define LOADK(kc, tt) do { if ((tt) < 8) { const unsigned o_ = kbase + (unsigned)(((tt) + rot) & 7) * TILE_B; kc[0] = bload128(rK, lo16, o_); kc[1] = bload128(rK, lo16, o_ + 1024u); } } while (0)
#define LOADV(vx, tt) do { if ((tt) < 8) { const unsigned o_ = vbase + (unsigned)(((tt) + rot) & 7) * 2048u; vx[0] = bload128(rV, lo16, o_); vx[1] = bload128(rV, lo16, o_ + 1024u); } } while (0)
#define TILE(t) do { \
        SLOT((t) == 0 ? 0 : (t) == 1 ? 1 : 2, true, sA, pA0, pA1, sB, kf[(t) % 3][0], kf[(t) % 3][1], qf[1][0], qf[1][1], o[3], vf[((t) + 2) % 3][0], vf[((t) + 2) % 3][1], pB0, pB1, (void)0); \
        LOADV(vf[((t) + 2) % 3], (t) + 2); \
        SLOT((t) == 0 ? 1 : 2, true, sB, pB0, pB1, sA, kf[(t) % 3][0], kf[(t) % 3][1], qf[2][0], qf[2][1], o[0], vf[(t) % 3][0], vf[(t) % 3][1], pA0, pA1, (void)0); \
        SLOT((t) == 0 ? 1 : 2, true, sA, pA0, pA1, sB, kf[(t) % 3][0], kf[(t) % 3][1], qf[3][0], qf[3][1], o[1], vf[(t) % 3][0], vf[(t) % 3][1], pB0, pB1, if ((t) < 7) PATCHK(kf[((t) + 1) % 3])); \
        LOADK(kf[(t) % 3], (t) + 3); \
        SLOT((t) == 0 ? 1 : 2, (t) < 7, sB, pB0, pB1, sA, kf[((t) + 1) % 3][0], kf[((t) + 1) % 3][1], qf[0][0], qf[0][1], o[2], vf[(t) % 3][0], vf[(t) % 3][1], pA0, pA1, (void)0); \
    } while (0)
    AttnBPre bpre; u64 flb = 0; bool bready = false;
#pragma unroll
    for (int t = 0; t < 8; t += 2) {
        if (t == 0) __builtin_amdgcn_s_setprio(3); else if (t == 2) __builtin_amdgcn_s_setprio(2); else if (t == 4) __builtin_amdgcn_s_setprio(1); else __builtin_amdgcn_s_setprio(0);
        TILE(t);
        if (t == 4) { bready = __all(flb == tag); attnB_load(bpre, ws, h, rb, wid, opaque_lane()); }

        else if (t == 6 && bready) attnB_compute(bpre, ws, h, rb, wid, opaque_lane());

        TILE(t + 1);
        if (t == 0) {
            if (has_qkv && wid < 4) {
                asm volatile("s_waitcnt vmcnt(8)" ::: "memory");
                if (opaque_lane() == 0 && ctl_add(lds, CTL_F2CNT) == 3) flag_set(ws, F2 + ct * 32 + rt, tag);
            }
            if (oct < 7 && wid >= 4) {
                if (has_qkv) ctl_wait(lds, CTL_RDONE, 4);
                wo_issue<14>(woA, oct, (unsigned)(uintptr_t)lds, (wid - 4) * 14, opaque_lane());
            }
        } else if (t == 2) {
            const int ln = opaque_lane();
            flb = __hip_atomic_load(flagp(ws, f2_index(h, rb, ln)), __ATOMIC_RELAXED, __HIP_MEMORY_SCOPE_AGENT);
        }
    }
    asm volatile("s_nop 1\n\tv_mfma_f32_32x32x16_bf16 %0, %1, %2, %0\n\tv_mfma_f32_32x32x16_bf16 %0, %3, %4, %0\n\ts_nop 7\n\ts_nop 3" : "+v"(o[3]) : "v"(vf[1][0]), "v"(pB0), "v"(vf[1][1]), "v"(pB1));
    asm volatile("" : "+v"(o[0]), "+v"(o[1]), "+v"(o[2]));
    STAMP(7);
    lane = opaque_lane();
    if (!bready) { flag_wait(ws, f2_index(h, rb, lane), tag); attnB_load(bpre, ws, h, rb, wid, lane); attnB_compute(bpre, ws, h, rb, wid, lane); }
    if (has_qkv) ctl_wait(lds, CTL_RDONE, 4);
#pragma unroll
    for (int j = 0; j < 4; ++j) merge_write1(o[j], lds, wid, j, lane);
    asm volatile("s_waitcnt vmcnt(0)" ::: "memory");
    if (lane == 0) { if (wid >= 4) ctl_add(lds, CTL_WA); if (ctl_add(lds, CTL_F4CNT) == 7) flag_set(ws, F4 + rb * 16 + h, tag); }
    if (oct < 7 && wid < 4) { ctl_wait(lds, CTL_WA, 4); wf_read(wf, lds, wid & 1, lane); }
    STAMP(15);
    BARRIER();
    STAMP(10);
    const bool bad = merge_reduce(ws, h, rb, lds, wid, lane);
    if (__any(bad) && lane == 0) ctl_store(lds, CTL_BAD, 1);
    STAMP(8);
    DRAIN_BARRIER();
    if (__builtin_amdgcn_readfirstlane(ctl_load(lds, CTL_BAD))) {
        attnA_safe(ws, h, rb, lds, wid, lane);
        BARRIER();
        merge_reduce(ws, h, rb, lds, wid, lane);
        DRAIN_BARRIER();
    }
    if (threadIdx.x == 0) flag_set(ws, F3 + rb * 16 + h, tag);
}

__device__ __forceinline__ void oproj_compute(char* ws, const u16* woB, u16* out, int rt, int ct, u64 tag, u32x4 (&wf)[2][14], char* lds, int wid, int lane) {
    const int br = wid >> 2, tt = (wid >> 1) & 1, kh = wid & 1;
    if (br) {
        wo_issue<14>(woB, ct, (unsigned)(uintptr_t)lds + LDS_MRG, (wid - 4) * 14, lane);
    }
    if ((wid & 3) == 3) { flag_wait(ws, (br ? F4 : F3) + (rt >> 1) * 16 + (lane & 15), tag); if (lane == 0) ctl_store(lds, br ? CTL_F4 : CTL_F3, 1); }
    else ctl_wait(lds, br ? CTL_F4 : CTL_F3, 1);
    STAMP(11);
    const auto rO = RSRC(ws + (br ? WS_OB : WS_OA), QC_BYTES);
    const int tile = rt * 2 + tt;
    u32x4 of[14];
#pragma unroll
    for (int i = 0; i < 14; ++i) of[i] = bload128(rO, (unsigned)lane * 16u, (unsigned)(tile * 56 + 2 * (kh * 14 + i)) * 512u);
    if (br) {
        asm volatile("s_waitcnt vmcnt(14)" ::: "memory");
        if (lane == 0) ctl_add(lds, CTL_WB);
        ctl_wait(lds, CTL_WB, 4);
        wf_read(wf, lds + LDS_MRG, kh, lane);
    }
    f32x16 acc[2] = {};
#pragma unroll
    for (int i = 0; i < 14; ++i) { acc[0] = MFMA32(asb(wf[0][i]), asb(of[i]), acc[0]); acc[1] = MFMA32(asb(wf[1][i]), asb(of[i]), acc[1]); }
    asm volatile("" : "+v"(acc[0]), "+v"(acc[1]));
    STAMP(12);
    {   const f32x16& gv = kh ? acc[0] : acc[1];
#pragma unroll
        for (int rq = 0; rq < 4; ++rq) *(f32x4*)(lds + 114688 + (wid * 4 + rq) * 1024 + lane * 16) = (f32x4){gv[4 * rq], gv[4 * rq + 1], gv[4 * rq + 2], gv[4 * rq + 3]};
    }
    asm volatile("s_waitcnt lgkmcnt(0)" ::: "memory");
    if (lane == 0) ctl_store(lds, CTL_X + wid * 4, 1);
    ctl_wait(lds, CTL_X + (wid ^ 1) * 4, 1);
    STAMP(13);
    f32x16 r = kh ? acc[1] : acc[0];
#pragma unroll
    for (int rq = 0; rq < 4; ++rq) { const f32x4 x = *(const f32x4*)(lds + 114688 + ((wid ^ 1) * 4 + rq) * 1024 + lane * 16); r[4 * rq] += x[0]; r[4 * rq + 1] += x[1]; r[4 * rq + 2] += x[2]; r[4 * rq + 3] += x[3]; }
    const unsigned orow = (unsigned)(rt * 64 + tt * 32 + (lane & 31)) * 1792u + (unsigned)(br * 448 + ct * 64 + kh * 32 + (lane >> 5) * 8) * 2u;
#pragma unroll
    for (int pr = 0; pr < 2; ++pr) {
        const unsigned a0 = cvtpk(r[8 * pr], r[8 * pr + 1]), a1 = cvtpk(r[8 * pr + 2], r[8 * pr + 3]), b0 = cvtpk(r[8 * pr + 4], r[8 * pr + 5]), b1 = cvtpk(r[8 * pr + 6], r[8 * pr + 7]);
        auto r0 = __builtin_amdgcn_permlane32_swap(a0, b0, false, false);
        auto r1 = __builtin_amdgcn_permlane32_swap(a1, b1, false, false);
        bstore128((u32x4){r0[0], r1[0], r0[1], r1[1]}, RSRC(out, 2048 * 896 * 2), orow + pr * 32u);
    }
}

__global__ void __launch_bounds__(512, 2) fused_kernel(const u16* xA, const u16* wA, const u16* woA, const u16* xB, const u16* wB, const u16* woB, u16* out, char* ws) {
    extern __shared__ __attribute__((aligned(16))) char lds[];
    int lane = threadIdx.x & 63; const int wid = __builtin_amdgcn_readfirstlane(threadIdx.x >> 6);
    const int id = blockIdx.x, xcd = id & 7, loc = id >> 3;
    STAMP_INIT();
    STAMP(0);
    if (threadIdx.x < 32) ctl_store(lds, threadIdx.x * 4, 0);
    const u64 tag = __hip_atomic_load(flagp(ws, FEP), __ATOMIC_RELAXED, __HIP_MEMORY_SCOPE_AGENT) + 1;
    const int gb = xcd & 1, grt = 8 * (xcd >> 1) + (loc & 7), qct = gb * 4 + (loc >> 3);
    const bool has_qkv = qct < 7;
    const int h = xcd * 2 + (loc & 1), rb = loc >> 1;
    const int ort = 4 * xcd + (loc & 3), oct = loc >> 2;
    unsigned warm0 = 0;
    if (wid < 4) {
        u64 pc; int len;
        asm volatile("s_getpc_b64 %0\n.Lfused_pc_ref:\n\ts_mov_b32 %1, .Lfused_code_end - .Lfused_pc_ref" : "=s"(pc), "=s"(len));
        const int off = (wid * 64 + lane) * 128;
        if (off < len) warm0 = *(const __attribute__((address_space(1))) unsigned*)(pc + off);
    }
    QkvOut outB;
    if (has_qkv) qkv_phase(xA, wA, xB, wB, ws, grt, qct, tag, lds, wid, lane, outB);
    else { if (threadIdx.x == 0) flag_set(ws, F0 + (xcd >> 1) * 8 + (loc & 7), tag); BARRIER(); }
    asm volatile("" :: "v"(warm0));
    STAMP(4);
    if (wid == 4) {
        flag_wait(ws, F1 + (lane < 32 ? (28 * h) >> 6 : (28 * h + 27) >> 6) * 32 + (lane & 31), tag);
        if (lane == 0) ctl_store(lds, CTL_F1, 1);
    } else ctl_wait(lds, CTL_F1, 1);
    STAMP(5);
    const u64 tags = (u64)(unsigned)__builtin_amdgcn_readfirstlane((unsigned)tag) | ((u64)(unsigned)__builtin_amdgcn_readfirstlane((unsigned)(tag >> 32)) << 32);
    u32x4 wf[2][14];
    attnA_phase(ws, woA, oct, h, rb, tags, grt, qct, has_qkv, outB, wf, lds, wid, lane);
    STAMP(9);
    lane = opaque_lane();
    if (oct < 7) {
        oproj_compute(ws, woB, out, ort, oct, tags, wf, lds, wid, lane);
    }
    STAMP(14);
    if (id == 0 && wid == 0) {
        flag_wait(ws, F0 + (lane & 31), tags);
        if (lane == 0) flag_set(ws, FEP, tags);
    }
    STAMP_DUMP();
    asm volatile(".Lfused_code_end:");
}

extern "C" void kernel_launch(void* const* d_in, const int* in_sizes, int n_in, void* d_out, int out_size, void* d_ws, size_t ws_size, hipStream_t stream) {
    const u16* xA = (const u16*)d_in[0]; const u16* wA = (const u16*)d_in[1]; const u16* woA = (const u16*)d_in[2];
    const u16* xB = (const u16*)d_in[3]; const u16* wB = (const u16*)d_in[4]; const u16* woB = (const u16*)d_in[5];
    (void)hipFuncSetAttribute((const void*)fused_kernel, hipFuncAttributeMaxDynamicSharedMemorySize, LDS_TOTAL + LDS_EXTRA);
    fused_kernel<<<256, 512, LDS_TOTAL + LDS_EXTRA, stream>>>(xA, wA, woA, xB, wB, woB, (u16*)d_out, (char*)d_ws);
}
```

```cpp
#include <hip/hip_runtime.h>
#include <hip/hip_bf16.h>
#include <stdint.h>

typedef unsigned short u16;
typedef unsigned long long u64;
typedef short bf16x8 __attribute__((ext_vector_type(8)));
typedef short s16x4 __attribute__((ext_vector_type(4)));
typedef float f32x4 __attribute__((ext_vector_type(4)));
typedef float f32x16 __attribute__((ext_vector_type(16)));
typedef unsigned u32x4 __attribute__((ext_vector_type(4)));
typedef unsigned u32x2 __attribute__((ext_vector_type(2)));
typedef __attribute__((address_space(3))) const char* lds_cptr;
typedef __attribute__((address_space(1))) u64 gu64;

constexpr int TILE_B = 56 * 512;
constexpr size_t QC_BYTES = (size_t)64 * TILE_B;
constexpr size_t V_BYTES = (size_t)16 * 64 * 2048;
constexpr size_t WS_QA = 0, WS_KA = WS_QA + QC_BYTES, WS_VA = WS_KA + QC_BYTES;
constexpr size_t WS_QB = WS_VA + V_BYTES, WS_KB = WS_QB + QC_BYTES, WS_VB = WS_KB + QC_BYTES;
constexpr size_t WS_OA = WS_VB + V_BYTES, WS_OB = WS_OA + QC_BYTES, WS_FLAGS = WS_OB + QC_BYTES;
constexpr int F1 = 0, F2 = 256, F3 = 512, F4 = 768, F0 = 1024, FEP = 1056;
constexpr float QSCALE = 0.18898223650461362f * 1.4426950408889634f;
constexpr int STG = 32768;
constexpr int LDS_Q = 147456, LDS_L = 131072, LDS_MRG = 57344, LDS_CTL = LDS_Q + 8192, LDS_MX = LDS_CTL + 128, LDS_TOTAL = LDS_MX + 1024 + 64;
constexpr int CTL_F1 = 0, CTL_F2CNT = 4, CTL_BAD = 8, CTL_QCNT = 12, CTL_F4CNT = 16, CTL_F3 = 20, CTL_RDONE = 24, CTL_F4 = 28, CTL_X = 32;
constexpr int CTL_WA = 64, CTL_WB = 68;
#define BPFG 1
#define PUBG 10
constexpr unsigned SPIN_MAX = 1u << 18;

#define STAMP(i)
#define STAMP_INIT()
#define STAMP_DUMP()
#define LDS_EXTRA 0

#define MFMA16(a, b, c) __builtin_amdgcn_mfma_f32_16x16x32_bf16(a, b, c, 0, 0, 0)
#define MFMA32(a, b, c) __builtin_amdgcn_mfma_f32_32x32x16_bf16(a, b, c, 0, 0, 0)
#define PIN(x) asm volatile("" : "+v"(x))
#define SBAR() __builtin_amdgcn_sched_barrier(0)
#define SC1 16

__device__ __forceinline__ unsigned cvtpk(float lo, float hi) {
    typedef float f2 __attribute__((ext_vector_type(2))); typedef __bf16 b2 __attribute__((ext_vector_type(2)));
    f2 v = {lo, hi}; b2 r = __builtin_convertvector(v, b2); return __builtin_bit_cast(unsigned, r);
}
__device__ __forceinline__ void glds16(const void* sbase, unsigned voff, unsigned lds_base) {
    asm volatile("s_mov_b32 m0, %2\n\ts_nop 0\n\tglobal_load_lds_dwordx4 %0, %1" :: "v"(voff), "s"(sbase), "s"(lds_base) : "memory");
}
__device__ __forceinline__ s16x4 vtr(lds_cptr p) {
    typedef short v4i16_t __attribute__((ext_vector_type(4)));
    return __builtin_bit_cast(s16x4, __builtin_amdgcn_ds_read_tr16_b64_v4i16((__attribute__((address_space(3))) v4i16_t*)p));
}
__device__ __forceinline__ bf16x8 cat8(s16x4 a, s16x4 b) { return (bf16x8){a[0], a[1], a[2], a[3], b[0], b[1], b[2], b[3]}; }
__device__ __forceinline__ bf16x8 ldsrd128(lds_cptr p) { return *(const __attribute__((address_space(3))) bf16x8*)p; }
#define RSRC(p, bytes) __builtin_amdgcn_make_buffer_rsrc((void*)(p), 0, (int)(bytes), 0x00020000)
__device__ __forceinline__ u32x4 bload128(__amdgpu_buffer_rsrc_t r, unsigned voff, unsigned soff) { return __builtin_amdgcn_raw_buffer_load_b128(r, voff, soff, SC1); }
__device__ __forceinline__ void bstore128(u32x4 v, __amdgpu_buffer_rsrc_t r, unsigned voff) { __builtin_amdgcn_raw_buffer_store_b128(v, r, voff, 0, SC1); }
__device__ __forceinline__ void bstore64(u32x2 v, __amdgpu_buffer_rsrc_t r, unsigned voff) { __builtin_amdgcn_raw_buffer_store_b64(v, r, voff, 0, SC1); }
__device__ __forceinline__ bf16x8 asb(u32x4 v) { return __builtin_bit_cast(bf16x8, v); }

__device__ __forceinline__ gu64* flagp(char* ws, int idx) { return (gu64*)(ws + WS_FLAGS + (size_t)idx * 128); }
__device__ __forceinline__ void flag_set(char* ws, int idx, u64 tag) { __hip_atomic_store(flagp(ws, idx), tag, __ATOMIC_RELAXED, __HIP_MEMORY_SCOPE_AGENT); }
__device__ __forceinline__ void flag_wait(char* ws, int idx, u64 tag) {
    gu64* p = flagp(ws, idx);
    for (unsigned spins = 0; spins < SPIN_MAX; ++spins) {
        const u64 v = __hip_atomic_load(p, __ATOMIC_RELAXED, __HIP_MEMORY_SCOPE_AGENT);
        if (__all(v == tag)) break;
        __builtin_amdgcn_s_sleep(1);
    }
}
__device__ __forceinline__ int opaque_lane() { int l; asm volatile("v_mbcnt_lo_u32_b32 %0, -1, 0\n\tv_mbcnt_hi_u32_b32 %0, -1, %0" : "=v"(l)); return l; }
__device__ __forceinline__ int f2_index(int h, int rb, int lane) { const int i = lane % 6, r = 2 * rb - 1 + i % 3; return F2 + (i < 3 ? (28 * h) >> 6 : (28 * h + 27) >> 6) * 32 + (r < 0 ? 0 : r); }
typedef __attribute__((address_space(3))) int* lds_iptr;
__device__ __forceinline__ int ctl_load(char* lds, int off) { return __hip_atomic_load((lds_iptr)(lds + LDS_CTL + off), __ATOMIC_RELAXED, __HIP_MEMORY_SCOPE_WORKGROUP); }
__device__ __forceinline__ void ctl_store(char* lds, int off, int v) { __hip_atomic_store((lds_iptr)(lds + LDS_CTL + off), v, __ATOMIC_RELAXED, __HIP_MEMORY_SCOPE_WORKGROUP); }
__device__ __forceinline__ void ctl_wait(char* lds, int off, int n) {
    for (unsigned spins = 0; spins < (SPIN_MAX << 6); ++spins) { if (__builtin_amdgcn_readfirstlane(ctl_load(lds, off)) >= n) break; __builtin_amdgcn_s_sleep(1); }
    asm volatile("" ::: "memory");
}
__device__ __forceinline__ int ctl_add(char* lds, int off) { return __hip_atomic_fetch_add((lds_iptr)(lds + LDS_CTL + off), 1, __ATOMIC_RELAXED, __HIP_MEMORY_SCOPE_WORKGROUP); }
#define BARRIER() asm volatile("s_waitcnt lgkmcnt(0)\n\ts_barrier" ::: "memory")
#define DRAIN_BARRIER() asm volatile("s_waitcnt vmcnt(0) lgkmcnt(0)\n\ts_barrier" ::: "memory")

struct QkvFrag { bf16x8 x[2][4], w[2][3]; };
struct QkvAcc { f32x4 q[4], k[4], v[4]; };
struct QkvOut { u32x4 q[2], k[2], v[2]; };
__device__ __forceinline__ void qkv_pack(QkvOut& o, const QkvAcc& a, float qs) {
#pragma unroll
    for (int tb = 0; tb < 2; ++tb) {
        const f32x4 q0 = a.q[2 * tb] * qs, q1 = a.q[2 * tb + 1] * qs, k0 = a.k[2 * tb], k1 = a.k[2 * tb + 1], v0 = a.v[2 * tb], v1 = a.v[2 * tb + 1];
        { auto ra = __builtin_amdgcn_permlane16_swap(cvtpk(q0[0], q0[1]), cvtpk(q1[0], q1[1]), false, false); auto rb = __builtin_amdgcn_permlane16_swap(cvtpk(q0[2], q0[3]), cvtpk(q1[2], q1[3]), false, false);
          o.q[tb] = (u32x4){ra[0], rb[0], ra[1], rb[1]}; }
        { auto ra = __builtin_amdgcn_permlane16_swap(cvtpk(k0[0], k0[1]), cvtpk(k1[0], k1[1]), false, false); auto rb = __builtin_amdgcn_permlane16_swap(cvtpk(k0[2], k0[3]), cvtpk(k1[2], k1[3]), false, false);
          o.k[tb] = (u32x4){ra[0], rb[0], ra[1], rb[1]}; }
        { auto ra = __builtin_amdgcn_permlane32_swap(cvtpk(v0[0], v0[1]), cvtpk(v1[0], v1[1]), false, false); auto rb = __builtin_amdgcn_permlane32_swap(cvtpk(v0[2], v0[3]), cvtpk(v1[2], v1[3]), false, false);
          o.v[tb] = (u32x4){ra[0], rb[0], ra[1], rb[1]}; }
    }
}
template <int BR, int I>
__device__ __forceinline__ void qkv_store(const QkvOut& o, char* ws, int rt, int ct, int c, int lane) {
    constexpr int tb = I / 3, kind = I % 3;
    const int tile = rt * 2 + tb;
    if (kind < 2) {
        const unsigned off = (unsigned)(tile * 56 + 8 * ct + 2 * c) * 512u + (unsigned)lane * 16u;
        if (kind == 0) bstore128(o.q[tb], RSRC(ws + (BR ? WS_QB : WS_QA), QC_BYTES), off); else bstore128(o.k[tb], RSRC(ws + (BR ? WS_KB : WS_KA), QC_BYTES), off);
    } else {
        const int n = 64 * ct + 16 * c + (lane & 15), h = n / 28, d = n - 28 * h;
        const unsigned off = (unsigned)(h * 64 + tile) * 2048u + (unsigned)(lane >> 4) * 512u + (unsigned)d * 16u;
        const auto rs = RSRC(ws + (BR ? WS_VB : WS_VA), V_BYTES);
        bstore128(o.v[tb], rs, off);
        if (d == 27) bstore128((u32x4){0x3F803F80u, 0x3F803F80u, 0x3F803F80u, 0x3F803F80u}, rs, off + 16u);
    }
}

__device__ __forceinline__ void qkv_phase(const u16* xA, const u16* wA, const u16* xB, const u16* wB, char* ws, int rt, int ct, u64 tag, char* lds, int wid, int lane, QkvOut& outB) {
    if (wid >= 4) {
        const unsigned lds0 = (unsigned)(uintptr_t)lds;
        const int lw = wid - 4, r8 = lane >> 3, dpos = lane & 7;
        unsigned xoff[2], woff[2];
#pragma unroll
        for (int i = 0; i < 2; ++i) { const int row = 16 * lw + 8 * i + r8;
            xoff[i] = (unsigned)row * 896u + (unsigned)((dpos ^ ((row >> 1) & 7)) << 4);
            const int f2 = ((row >> 1) & 1) | (((row >> 3) & 1) << 1);
            woff[i] = (unsigned)row * 2688u + (unsigned)((((dpos >> 1) ^ f2) * 2 + (dpos & 1)) << 4); }
        const int rot = __builtin_amdgcn_readfirstlane(((rt & 7) + 2 * (ct & 3)) % 7);
        const char* xa = (const char*)xA + (size_t)rt * 64 * 896; const char* wa = (const char*)wA + (size_t)ct * 128;
        const char* xb = (const char*)xB + (size_t)rt * 64 * 896; const char* wb = (const char*)wB + (size_t)ct * 128;
#define QKV_ISSUE(g) do { int st_ = ((g) % 7) + rot; st_ = st_ >= 7 ? st_ - 7 : st_; const char* x_ = ((g) < 7 ? xa : xb) + st_ * 128; const char* w_ = ((g) < 7 ? wa : wb) + (size_t)st_ * (64 * 2688); \
        const unsigned sl_ = lds0 + ((g) & 3) * STG + lw * 2048; \
        glds16(x_, xoff[0], sl_); glds16(x_, xoff[1], sl_ + 1024); \
        glds16(w_, woff[0], sl_ + 8192); glds16(w_, woff[1], sl_ + 8192 + 1024); glds16(w_ + 896, woff[0], sl_ + 16384); glds16(w_ + 896, woff[1], sl_ + 16384 + 1024); \
        glds16(w_ + 1792, woff[0], sl_ + 24576); glds16(w_ + 1792, woff[1], sl_ + 24576 + 1024); } while (0)
        QKV_ISSUE(0); QKV_ISSUE(1);
#pragma unroll
        for (int g = 0; g < 14; ++g) {
            if (g <= 12) asm volatile("s_waitcnt vmcnt(8) lgkmcnt(0)\n\ts_barrier" ::: "memory");
            else asm volatile("s_waitcnt vmcnt(0) lgkmcnt(0)\n\ts_barrier" ::: "memory");
            if (g == 0) STAMP(1);
            if (g + 2 < 14) QKV_ISSUE(g + 2);
        }
        return;
    }
    const int c = wid, g4 = lane >> 4, t16 = lane & 15;
    const unsigned xr0 = (unsigned)t16 * 128u + (unsigned)((g4 ^ (t16 >> 1)) << 4);
    const int q = t16 >> 2, p = lane & 3;
    const int rf2 = ((q >> 1) & 1) | ((g4 & 1) << 1);
    const unsigned wr0 = 8192u + (unsigned)(8 * g4 + q) * 128u + (unsigned)((c ^ rf2) * 32) + (unsigned)p * 8u;
    QkvAcc acc; QkvOut outA;
#pragma unroll
    for (int i = 0; i < 4; ++i) acc.q[i] = acc.k[i] = acc.v[i] = (f32x4){};
    QkvFrag fr[2];
    unsigned pfa = 0, pfb = 0;
#define QMF(i) do { if (g > 0) { constexpr int ks_ = (i) / 12, s_ = ((i) % 12) / 4, b_ = (i) % 4; const QkvFrag& f_ = fr[(g - 1) & 1]; \
        if (s_ == 0) acc.q[b_] = MFMA16(f_.w[ks_][0], f_.x[ks_][b_], acc.q[b_]); if (s_ == 1) acc.k[b_] = MFMA16(f_.w[ks_][1], f_.x[ks_][b_], acc.k[b_]); \
        if (s_ == 2) acc.v[b_] = MFMA16(f_.x[ks_][b_], f_.w[ks_][2], acc.v[b_]); } SBAR(); } while (0)
#define QRX(ks, b) do { if (g < 14) fr[g & 1].x[ks][b] = ldsrd128(sl + (xr0 ^ ((ks) * 64)) + (b) * 2048); } while (0)
#define QRW(ks, s) do { if (g < 14) { lds_cptr wp_ = sl + wr0 + (ks) * 4096 + (s) * 8192; fr[g & 1].w[ks][s] = cat8(vtr(wp_), vtr(wp_ + 512)); } } while (0)
#define QST(gg, i) do { if (g == (gg)) { qkv_store<0, i>(outA, ws, rt, ct, c, lane); SBAR(); } } while (0)
#pragma unroll
    for (int g = 0; g < 15; ++g) {
        if (g == PUBG) asm volatile("s_waitcnt vmcnt(0) lgkmcnt(0)\n\ts_barrier" ::: "memory");
        else if (g < 14) asm volatile("s_waitcnt lgkmcnt(0)\n\ts_barrier" ::: "memory");
        SBAR();
        if (g == 7) STAMP(2);
        lds_cptr sl = (lds_cptr)lds + (g & 3) * STG;
        if (g == PUBG && threadIdx.x == 0) flag_set(ws, F1 + ct * 32 + rt, tag);
        SBAR();
        QRX(0, 0); QMF(0); QRX(0, 1); QMF(1); QRX(0, 2); QMF(2); QRX(0, 3); QMF(3); QST(8, 3); QRW(0, 0); QMF(4); QRW(0, 1); QMF(5); QRW(0, 2); QMF(6);
        QRX(1, 0); QMF(7); QRX(1, 1); QMF(8); QRX(1, 2); QMF(9); QST(8, 4); QRX(1, 3); QMF(10); QRW(1, 0); QMF(11); QRW(1, 1); QMF(12); QRW(1, 2); QMF(13);
        QMF(14); QMF(15); QST(8, 5); QMF(16); QMF(17); QMF(18); QMF(19); QMF(20); QMF(21); QMF(22); QMF(23);
        if (g == 7) {
            qkv_pack(outA, acc, QSCALE);
            qkv_store<0, 0>(outA, ws, rt, ct, c, lane); qkv_store<0, 1>(outA, ws, rt, ct, c, lane); qkv_store<0, 2>(outA, ws, rt, ct, c, lane);
#pragma unroll
            for (int i = 0; i < 4; ++i) acc.q[i] = acc.k[i] = acc.v[i] = (f32x4){};
        }
    }
    if (lane == 0) ctl_add(lds, CTL_RDONE);
    qkv_pack(outB, acc, 1.0f);
}

__device__ __forceinline__ float max16(const f32x16& s) {
    float a = fmaxf(fmaxf(s[0], s[1]), s[2]), b = fmaxf(fmaxf(s[3], s[4]), s[5]);
    a = fmaxf(fmaxf(a, s[6]), s[7]); b = fmaxf(fmaxf(b, s[8]), s[9]); a = fmaxf(fmaxf(a, s[10]), s[11]); b = fmaxf(fmaxf(b, s[12]), s[13]);
    a = fmaxf(fmaxf(a, s[14]), s[15]); return fmaxf(a, b);
}
__device__ __forceinline__ float xhalf_max(float m) {
    auto r = __builtin_amdgcn_permlane32_swap(__float_as_uint(m), __float_as_uint(m), false, false);
    return fmaxf(__uint_as_float(r[0]), __uint_as_float(r[1]));
}
__device__ __forceinline__ void patch(u32x4& f0, u32x4& f1, bool pe, bool po, unsigned a, unsigned b) {
    f0[0] = po ? a : f0[0]; f0[1] = po ? b : f0[1]; f1[2] = pe ? a : f1[2]; f1[3] = pe ? b : f1[3];
}
__device__ __forceinline__ void exp_pack(const f32x16& s, u32x4& p0, u32x4& p1) {
    float e[16];
#pragma unroll
    for (int r = 0; r < 16; ++r) e[r] = __builtin_amdgcn_exp2f(s[r]);
    p0 = (u32x4){cvtpk(e[0], e[1]), cvtpk(e[2], e[3]), cvtpk(e[4], e[5]), cvtpk(e[6], e[7])};
    p1 = (u32x4){cvtpk(e[8], e[9]), cvtpk(e[10], e[11]), cvtpk(e[12], e[13]), cvtpk(e[14], e[15])};
}
__device__ __forceinline__ void merge_write1(const f32x16& o, char* lds, int wid, int j, int lane) {
#pragma unroll
    for (int hf = 0; hf < 2; ++hf)
        *(u32x4*)(lds + LDS_MRG + ((wid * 4 + j) * 2 + hf) * 1024 + lane * 16) = (u32x4){cvtpk(o[8 * hf], o[8 * hf + 1]), cvtpk(o[8 * hf + 2], o[8 * hf + 3]), cvtpk(o[8 * hf + 4], o[8 * hf + 5]), cvtpk(o[8 * hf + 6], o[8 * hf + 7])};
    if (lane >= 32) *(float*)(lds + LDS_L + ((wid * 4 + j) * 32 + lane - 32) * 4) = o[12];
}
__device__ __forceinline__ bool merge_reduce(char* ws, int h, int rb, char* lds, int wid, int lane) {
    const bool hi = lane >= 32;
    const int j = wid >> 1, hf = wid & 1;
    const int tile = rb * 4 + j, q = lane & 31;
    u32x4 p[8]; float pl[8];
#pragma unroll
    for (int w = 0; w < 8; ++w) pl[w] = *(const float*)(lds + LDS_L + ((w * 4 + j) * 32 + q) * 4);
#pragma unroll
    for (int w = 0; w < 8; ++w) p[w] = *(const u32x4*)(lds + LDS_MRG + ((w * 4 + j) * 2 + hf) * 1024 + lane * 16);
    const int col0 = 28 * h + 16 * hf + (hi ? 4 : 0), col1 = col0 + 8;
    const unsigned off0 = (unsigned)((tile * 56 + (col0 >> 3)) * 32 + q) * 16u + (unsigned)((col0 >> 2) & 1) * 8u, off1 = (unsigned)((tile * 56 + (col1 >> 3)) * 32 + q) * 16u + (unsigned)((col1 >> 2) & 1) * 8u;
    const float l = ((pl[0] + pl[1]) + (pl[2] + pl[3])) + ((pl[4] + pl[5]) + (pl[6] + pl[7]));
    const float inv = __builtin_amdgcn_rcpf(l);
    float a[8] = {};
#pragma unroll
    for (int w = 0; w < 8; ++w)
#pragma unroll
        for (int i = 0; i < 4; ++i) { a[2 * i] += __uint_as_float(p[w][i] << 16); a[2 * i + 1] += __uint_as_float(p[w][i] & 0xffff0000u); }
#pragma unroll
    for (int i = 0; i < 8; ++i) a[i] *= inv;
    const auto rO = RSRC(ws + WS_OA, QC_BYTES);
    const bool last = hf == 1 && hi;
    bstore64((u32x2){cvtpk(a[0], a[1]), cvtpk(a[2], a[3])}, rO, off0);
    if (!last) bstore64((u32x2){cvtpk(a[4], a[5]), cvtpk(a[6], a[7])}, rO, off1);
    float z = a[0] * 0.f; z = fmaf(a[1], 0.f, z); z = fmaf(a[2], 0.f, z); z = fmaf(a[3], 0.f, z);
    float z1 = a[4] * 0.f; z1 = fmaf(a[5], 0.f, z1); z1 = fmaf(a[6], 0.f, z1); z1 = fmaf(a[7], 0.f, z1);
    const bool ok = l > 1.0e-18f && l < 1.0e30f && z == 0.f && (last || z1 == 0.f);
    return !ok;
}

__device__ __forceinline__ void attnA_safe(char* ws, int h, int rb, char* lds, int wid, int lane) {
    const bool hi = lane >= 32, pe = hi && !(h & 1), po = !hi && (h & 1); const int m0 = (28 * h) >> 3;
    const auto rQ = RSRC(ws + WS_QA, QC_BYTES), rK = RSRC(ws + WS_KA, QC_BYTES), rV = RSRC(ws + WS_VA, V_BYTES);
    const unsigned lo16 = (unsigned)lane * 16u;
    float* mx = (float*)(lds + LDS_MX);
#pragma unroll 1
    for (int j = 0; j < 4; ++j) {
        const unsigned b = (unsigned)((rb * 4 + j) * 56 + m0) * 512u;
        u32x4 q0 = bload128(rQ, lo16, b), q1 = bload128(rQ, lo16, b + 1024u);
        patch(q0, q1, pe, po, 0u, 0u);
        float m = -3.0e38f; f32x16 acc = {};
#pragma unroll 1
        for (int t = 0; t < 8; ++t) {
            const unsigned ko = (unsigned)((wid * 8 + t) * 56 + m0) * 512u, vo = (unsigned)(h * 64 + wid * 8 + t) * 2048u;
            u32x4 k0 = bload128(rK, lo16, ko), k1 = bload128(rK, lo16, ko + 1024u), v0 = bload128(rV, lo16, vo), v1 = bload128(rV, lo16, vo + 1024u);
            patch(k0, k1, pe, po, 0u, 0u);
            f32x16 s = {}; s = MFMA32(asb(k0), asb(q0), s); s = MFMA32(asb(k1), asb(q1), s);
            const float mn = fmaxf(m, xhalf_max(max16(s)));
            const float al = __builtin_amdgcn_exp2f(m - mn); m = mn;
#pragma unroll
            for (int r = 0; r < 16; ++r) { s[r] -= mn; acc[r] *= al; }
            u32x4 p0, p1; exp_pack(s, p0, p1);
            acc = MFMA32(asb(v0), asb(p0), acc); acc = MFMA32(asb(v1), asb(p1), acc);
        }
        BARRIER();
        if (!hi) mx[wid * 32 + lane] = m;
        BARRIER();
        float g = m;
#pragma unroll
        for (int w = 0; w < 8; ++w) g = fmaxf(g, mx[w * 32 + (lane & 31)]);
        const float f = __builtin_amdgcn_exp2f(m - g);
        merge_write1(acc * f, lds, wid, j, lane);
    }
}

struct AttnBPre { u32x4 q, kp, kc; u32x2 v[2][2]; };
__device__ __forceinline__ void attnB_load(AttnBPre& a, char* ws, int h, int rb, int wid, int lane) {
    const int m0 = (28 * h) >> 3, T = rb * 4 + (wid >> 1), sub = wid & 1;
    const int Tp = sub ? T : (T > 0 ? T - 1 : 0), subp = sub ^ 1;
    const auto rQ = RSRC(ws + WS_QB, QC_BYTES), rK = RSRC(ws + WS_KB, QC_BYTES), rV = RSRC(ws + WS_VB, V_BYTES);
    const int g = lane >> 4, t16 = lane & 15;
    const unsigned co = (unsigned)g * 512u + (unsigned)t16 * 16u;
    a.q = bload128(rQ, co, (unsigned)(T * 56 + m0) * 512u + (unsigned)sub * 256u);
    a.kp = bload128(rK, co, (unsigned)(Tp * 56 + m0) * 512u + (unsigned)subp * 256u);
    a.kc = bload128(rK, co, (unsigned)(T * 56 + m0) * 512u + (unsigned)sub * 256u);
    const unsigned vo = (unsigned)(g & 1) * 512u + (unsigned)t16 * 16u + (unsigned)(g >> 1) * 8u;
#pragma unroll
    for (int db = 0; db < 2; ++db) {
        a.v[db][0] = __builtin_amdgcn_raw_buffer_load_b64(rV, vo + db * 256u, (unsigned)(h * 64 + Tp) * 2048u + (unsigned)subp * 1024u, SC1);
        a.v[db][1] = __builtin_amdgcn_raw_buffer_load_b64(rV, vo + db * 256u, (unsigned)(h * 64 + T) * 2048u + (unsigned)sub * 1024u, SC1);
    }
}
__device__ __forceinline__ void attnB_compute(AttnBPre& a, char* ws, int h, int rb, int wid, int lane) {
    const int T = rb * 4 + (wid >> 1), sub = wid & 1, g = lane >> 4, ql = lane & 15;
    const bool padl = (h & 1) ? g == 0 : g == 3;
    if (h & 1) { a.q[0] = padl ? 0u : a.q[0]; a.q[1] = padl ? 0u : a.q[1]; a.kp[0] = padl ? 0u : a.kp[0]; a.kp[1] = padl ? 0u : a.kp[1]; a.kc[0] = padl ? 0u : a.kc[0]; a.kc[1] = padl ? 0u : a.kc[1]; }
    else { a.q[2] = padl ? 0u : a.q[2]; a.q[3] = padl ? 0u : a.q[3]; a.kp[2] = padl ? 0u : a.kp[2]; a.kp[3] = padl ? 0u : a.kp[3]; a.kc[2] = padl ? 0u : a.kc[2]; a.kc[3] = padl ? 0u : a.kc[3]; }
    f32x4 sp = {}, sc = {};
    sp = MFMA16(asb(a.kp), asb(a.q), sp); sc = MFMA16(asb(a.kc), asb(a.q), sc);
    const bool hasp = sub || T > 0;
    float m = -INFINITY;
#pragma unroll
    for (int r = 0; r < 4; ++r) {
        const int dc = ql - (4 * g + r), dp = dc + 16;
        sp[r] = (hasp && dp <= 14 && !(dp & 1)) ? sp[r] : -INFINITY; sc[r] = (dc >= 0 && !(dc & 1)) ? sc[r] : -INFINITY;
        m = fmaxf(m, fmaxf(sp[r], sc[r]));
    }
    { auto r = __builtin_amdgcn_permlane16_swap(__float_as_uint(m), __float_as_uint(m), false, false); m = fmaxf(__uint_as_float(r[0]), __uint_as_float(r[1])); }
    m = xhalf_max(m);
    float e[8];
#pragma unroll
    for (int r = 0; r < 4; ++r) { e[r] = __builtin_amdgcn_exp2f((sp[r] - m) * QSCALE); e[4 + r] = __builtin_amdgcn_exp2f((sc[r] - m) * QSCALE); }
    const u32x4 pf = {cvtpk(e[0], e[1]), cvtpk(e[2], e[3]), cvtpk(e[4], e[5]), cvtpk(e[6], e[7])};
    f32x4 o0 = {}, o1 = {};
    o0 = MFMA16(asb((u32x4){a.v[0][0][0], a.v[0][0][1], a.v[0][1][0], a.v[0][1][1]}), asb(pf), o0);
    o1 = MFMA16(asb((u32x4){a.v[1][0][0], a.v[1][0][1], a.v[1][1][0], a.v[1][1][1]}), asb(pf), o1);
    float l;
    { auto r16 = __builtin_amdgcn_permlane16_swap(__float_as_uint(o1[0]), __float_as_uint(o1[0]), false, false); auto r32 = __builtin_amdgcn_permlane32_swap(r16[1], r16[1], false, false); l = __uint_as_float(r32[1]); }
    const float inv = __builtin_amdgcn_rcpf(l);
    const auto rO = RSRC(ws + WS_OB, QC_BYTES);
    const unsigned tok = (unsigned)(sub * 16 + ql) * 16u;
    { const int col = 28 * h + 4 * g; bstore64((u32x2){cvtpk(o0[0] * inv, o0[1] * inv), cvtpk(o0[2] * inv, o0[3] * inv)}, rO, (unsigned)(T * 56 + (col >> 3)) * 512u + tok + (unsigned)((col >> 2) & 1) * 8u); }
    if (g < 3) { const int col = 28 * h + 16 + 4 * g; bstore64((u32x2){cvtpk(o1[0] * inv, o1[1] * inv), cvtpk(o1[2] * inv, o1[3] * inv)}, rO, (unsigned)(T * 56 + (col >> 3)) * 512u + tok + (unsigned)((col >> 2) & 1) * 8u); }
}

template <int N>
__device__ __forceinline__ void wo_issue(const u16* wo, int ct, unsigned lds_base, int pc0, int lane) {
    const int r8 = lane >> 3, dpos = lane & 7;
    const unsigned woff = (unsigned)r8 * 896u + (unsigned)((dpos ^ ((r8 & 2) << 1)) << 4);
#pragma unroll
    for (int i = 0; i < N; ++i) glds16((const char*)wo + (size_t)ct * 128 + (size_t)(pc0 + i) * 8 * 896, woff, lds_base + (pc0 + i) * 1024);
}
__device__ __forceinline__ void wf_read(u32x4 (&wf)[2][14], const char* img, int kh, int lane) {
    const int g16 = lane >> 4, q = (lane & 15) >> 2, p = lane & 3;
#pragma unroll
    for (int i = 0; i < 14; ++i)
#pragma unroll
        for (int c = 0; c < 2; ++c) {
            const int row = (kh * 14 + i) * 16 + 8 * (g16 >> 1) + q;
            lds_cptr wp = (lds_cptr)img + (unsigned)row * 128u + (unsigned)(((2 * c + (g16 & 1)) ^ (q & 2)) * 32) + (unsigned)p * 8u;
            wf[c][i] = __builtin_bit_cast(u32x4, cat8(vtr(wp), vtr(wp + 512)));
        }
}
__device__ __forceinline__ void attnA_phase(char* ws, const u16* woA, int oct, int h, int rb, u64 tag, int rt, int ct, bool has_qkv, const QkvOut& outB, u32x4 (&wf)[2][14], char* lds, int wid, int lane) {
    const bool hi = lane >= 32, pe = hi && !(h & 1), po = !hi && (h & 1);
    const int m0 = (28 * h) >> 3;
    const auto rK = RSRC(ws + WS_KA, QC_BYTES), rV = RSRC(ws + WS_VA, V_BYTES);
    const unsigned lo16 = (unsigned)lane * 16u;
    const unsigned kbase = (unsigned)((wid * 8) * 56 + m0) * 512u, vbase = (unsigned)(h * 64 + wid * 8) * 2048u;
    const int rot = rb & 7;
    u32x4 qf[4][2], kf[3][2], vf[3][2];
    {   const unsigned k0 = kbase + (unsigned)rot * TILE_B, v0 = vbase + (unsigned)rot * 2048u, k1 = kbase + (unsigned)((rot + 1) & 7) * TILE_B;
        kf[0][0] = bload128(rK, lo16, k0); kf[0][1] = bload128(rK, lo16, k0 + 1024u);
        if (wid >= 4) { const unsigned b = (unsigned)((rb * 4 + wid - 4) * 56 + m0) * 512u; const auto rQ = RSRC(ws + WS_QA, QC_BYTES); qf[0][0] = bload128(rQ, lo16, b); qf[0][1] = bload128(rQ, lo16, b + 1024u); }
        vf[0][0] = bload128(rV, lo16, v0); vf[0][1] = bload128(rV, lo16, v0 + 1024u);
        kf[1][0] = bload128(rK, lo16, k1); kf[1][1] = bload128(rK, lo16, k1 + 1024u);
        const unsigned v1 = vbase + (unsigned)((rot + 1) & 7) * 2048u, k2 = kbase + (unsigned)((rot + 2) & 7) * TILE_B;
        vf[1][0] = bload128(rV, lo16, v1); vf[1][1] = bload128(rV, lo16, v1 + 1024u);
        kf[2][0] = bload128(rK, lo16, k2); kf[2][1] = bload128(rK, lo16, k2 + 1024u); }
    patch(kf[0][0], kf[0][1], pe, po, 0x3F803F80u, 0u);
    if (wid >= 4) {
        patch(qf[0][0], qf[0][1], pe, po, 0u, 0u);
        f32x16 s = {}; s = MFMA32(asb(kf[0][0]), asb(qf[0][0]), s); s = MFMA32(asb(kf[0][1]), asb(qf[0][1]), s);
        const float m = xhalf_max(max16(s));
        const unsigned mh = cvtpk(-m, 0.f) & 0xffffu; const float r = -m - __uint_as_float(mh << 16);
        patch(qf[0][0], qf[0][1], pe, po, mh | (cvtpk(r, 0.f) << 16), 0u);
        *(u32x4*)(lds + LDS_Q + (wid - 4) * 2048 + lane * 16) = qf[0][0]; *(u32x4*)(lds + LDS_Q + (wid - 4) * 2048 + 1024 + lane * 16) = qf[0][1];
        asm volatile("s_waitcnt lgkmcnt(0)" ::: "memory");
        if (lane == 0) ctl_add(lds, CTL_QCNT);
    }
    ctl_wait(lds, CTL_QCNT, 4);
    STAMP(6);
#pragma unroll
    for (int j = 0; j < 4; ++j) { qf[j][0] = *(const u32x4*)(lds + LDS_Q + j * 2048 + lane * 16); qf[j][1] = *(const u32x4*)(lds + LDS_Q + j * 2048 + 1024 + lane * 16); }
    if (has_qkv && wid < 4) {
        qkv_store<1, 0>(outB, ws, rt, ct, wid, lane); qkv_store<1, 1>(outB, ws, rt, ct, wid, lane); qkv_store<1, 2>(outB, ws, rt, ct, wid, lane);
        qkv_store<1, 3>(outB, ws, rt, ct, wid, lane); qkv_store<1, 4>(outB, ws, rt, ct, wid, lane); qkv_store<1, 5>(outB, ws, rt, ct, wid, lane);
    }
#pragma unroll
    for (int j = 0; j < 4; ++j) { PIN(qf[j][0]); PIN(qf[j][1]); }
    PIN(kf[0][0]); PIN(kf[0][1]);
    f32x16 o[4];
#define VEXP(d, s) asm volatile("v_exp_f32 %0, %1" : "=v"(d) : "v"(s))
#define VCVT(d, a, b) asm volatile("v_cvt_pk_bf16_f32 %0, %1, %2" : "=v"(d) : "v"(a), "v"(b))
#define MF0(d, a, b) asm volatile("v_mfma_f32_32x32x16_bf16 %0, %1, %2, 0" : "=&v"(d) : "v"(a), "v"(b))
#define MFA(d, a, b) asm volatile("v_mfma_f32_32x32x16_bf16 %0, %1, %2, %0" : "+v"(d) : "v"(a), "v"(b))
#define SLOT(PV, QK, Sc, Pc0, Pc1, Sn, kn0, kn1, qn0, qn1, Op, vp0, vp1, Pp0, Pp1, INJ) do { \
        float e0, e1, e2, e3, e4, e5, e6, e7, e8, e9, e10, e11, e12, e13, e14, e15; \
        if (QK) MF0(Sn, kn0, qn0); \
        VEXP(e0, Sc[0]); VEXP(e1, Sc[1]); VEXP(e2, Sc[2]); VEXP(e3, Sc[3]); \
        if (QK) MFA(Sn, kn1, qn1); \
        VEXP(e4, Sc[4]); VEXP(e5, Sc[5]); VEXP(e6, Sc[6]); VEXP(e7, Sc[7]); VCVT(Pc0[0], e0, e1); VCVT(Pc0[1], e2, e3); \
        INJ; \
        if ((PV) == 2) MFA(Op, vp0, Pp0); else if ((PV) == 1) MF0(Op, vp0, Pp0); \
        VEXP(e8, Sc[8]); VEXP(e9, Sc[9]); VEXP(e10, Sc[10]); VEXP(e11, Sc[11]); VCVT(Pc0[2], e4, e5); VCVT(Pc0[3], e6, e7); \
        if (PV) MFA(Op, vp1, Pp1); \
        VEXP(e12, Sc[12]); VEXP(e13, Sc[13]); VEXP(e14, Sc[14]); VEXP(e15, Sc[15]); VCVT(Pc1[0], e8, e9); VCVT(Pc1[1], e10, e11); VCVT(Pc1[2], e12, e13); VCVT(Pc1[3], e14, e15); \
    } while (0)
    f32x16 sA, sB; u32x4 pA0, pA1, pB0, pB1;
    asm volatile("s_nop 1\n\tv_mfma_f32_32x32x16_bf16 %0, %1, %2, 0\n\tv_mfma_f32_32x32x16_bf16 %0, %3, %4, %0\n\ts_nop 7\n\ts_nop 3" : "=&v"(sA) : "v"(kf[0][0]), "v"(qf[0][0]), "v"(kf[0][1]), "v"(qf[0][1]));
#define PATCHK(kx) do { PIN(kx[0]); PIN(kx[1]); patch(kx[0], kx[1], pe, po, 0x3F803F80u, 0u); PIN(kx[0]); PIN(kx[1]); } while (0)
#define LOADK(kc, tt) do { if ((tt) < 8) { const unsigned o_ = kbase + (unsigned)(((tt) + rot) & 7) * TILE_B; kc[0] = bload128(rK, lo16, o_); kc[1] = bload128(rK, lo16, o_ + 1024u); } } while (0)
#define LOADV(vx, tt) do { if ((tt) < 8) { const unsigned o_ = vbase + (unsigned)(((tt) + rot) & 7) * 2048u; vx[0] = bload128(rV, lo16, o_); vx[1] = bload128(rV, lo16, o_ + 1024u); } } while (0)
#define TILE(t) do { \
        SLOT((t) == 0 ? 0 : (t) == 1 ? 1 : 2, true, sA, pA0, pA1, sB, kf[(t) % 3][0], kf[(t) % 3][1], qf[1][0], qf[1][1], o[3], vf[((t) + 2) % 3][0], vf[((t) + 2) % 3][1], pB0, pB1, (void)0); \
        LOADV(vf[((t) + 2) % 3], (t) + 2); \
        SLOT((t) == 0 ? 1 : 2, true, sB, pB0, pB1, sA, kf[(t) % 3][0], kf[(t) % 3][1], qf[2][0], qf[2][1], o[0], vf[(t) % 3][0], vf[(t) % 3][1], pA0, pA1, (void)0); \
        SLOT((t) == 0 ? 1 : 2, true, sA, pA0, pA1, sB, kf[(t) % 3][0], kf[(t) % 3][1], qf[3][0], qf[3][1], o[1], vf[(t) % 3][0], vf[(t) % 3][1], pB0, pB1, if ((t) < 7) PATCHK(kf[((t) + 1) % 3])); \
        LOADK(kf[(t) % 3], (t) + 3); \
        SLOT((t) == 0 ? 1 : 2, (t) < 7, sB, pB0, pB1, sA, kf[((t) + 1) % 3][0], kf[((t) + 1) % 3][1], qf[0][0], qf[0][1], o[2], vf[(t) % 3][0], vf[(t) % 3][1], pA0, pA1, (void)0); \
    } while (0)
    AttnBPre bpre; u64 flb = 0; bool bready = false;
#pragma unroll
    for (int t = 0; t < 8; t += 2) {
        if (t == 0) __builtin_amdgcn_s_setprio(3); else if (t == 2) __builtin_amdgcn_s_setprio(2); else if (t == 4) __builtin_amdgcn_s_setprio(1); else __builtin_amdgcn_s_setprio(0);
        TILE(t);
        if (t == 4) { bready = __all(flb == tag); attnB_load(bpre, ws, h, rb, wid, opaque_lane()); }

        else if (t == 6 && bready) attnB_compute(bpre, ws, h, rb, wid, opaque_lane());

        TILE(t + 1);
        if (t == 0) {
            if (has_qkv && wid < 4) {
                asm volatile("s_waitcnt vmcnt(8)" ::: "memory");
                if (opaque_lane() == 0 && ctl_add(lds, CTL_F2CNT) == 3) flag_set(ws, F2 + ct * 32 + rt, tag);
            }
            if (oct < 7 && wid >= 4) {
                if (has_qkv) ctl_wait(lds, CTL_RDONE, 4);
                wo_issue<14>(woA, oct, (unsigned)(uintptr_t)lds, (wid - 4) * 14, opaque_lane());
            }
        } else if (t == 2) {
            const int ln = opaque_lane();
            flb = __hip_atomic_load(flagp(ws, f2_index(h, rb, ln)), __ATOMIC_RELAXED, __HIP_MEMORY_SCOPE_AGENT);
        }
    }
    asm volatile("s_nop 1\n\tv_mfma_f32_32x32x16_bf16 %0, %1, %2, %0\n\tv_mfma_f32_32x32x16_bf16 %0, %3, %4, %0\n\ts_nop 7\n\ts_nop 3" : "+v"(o[3]) : "v"(vf[1][0]), "v"(pB0), "v"(vf[1][1]), "v"(pB1));
    asm volatile("" : "+v"(o[0]), "+v"(o[1]), "+v"(o[2]));
    STAMP(7);
    lane = opaque_lane();
    if (!bready) { flag_wait(ws, f2_index(h, rb, lane), tag); attnB_load(bpre, ws, h, rb, wid, lane); attnB_compute(bpre, ws, h, rb, wid, lane); }
    if (has_qkv) ctl_wait(lds, CTL_RDONE, 4);
#pragma unroll
    for (int j = 0; j < 4; ++j) merge_write1(o[j], lds, wid, j, lane);
    asm volatile("s_waitcnt vmcnt(0)" ::: "memory");
    if (lane == 0) { if (wid >= 4) ctl_add(lds, CTL_WA); if (ctl_add(lds, CTL_F4CNT) == 7) flag_set(ws, F4 + rb * 16 + h, tag); }
    if (oct < 7 && wid < 4) { ctl_wait(lds, CTL_WA, 4); wf_read(wf, lds, wid & 1, lane); }
    STAMP(15);
    BARRIER();
    STAMP(10);
    const bool bad = merge_reduce(ws, h, rb, lds, wid, lane);
    if (__any(bad) && lane == 0) ctl_store(lds, CTL_BAD, 1);
    STAMP(8);
    DRAIN_BARRIER();
    if (__builtin_amdgcn_readfirstlane(ctl_load(lds, CTL_BAD))) {
        attnA_safe(ws, h, rb, lds, wid, lane);
        BARRIER();
        merge_reduce(ws, h, rb, lds, wid, lane);
        DRAIN_BARRIER();
    }
    if (threadIdx.x == 0) flag_set(ws, F3 + rb * 16 + h, tag);
}

__device__ __forceinline__ void oproj_compute(char* ws, const u16* woB, u16* out, int rt, int ct, u64 tag, u32x4 (&wf)[2][14], char* lds, int wid, int lane) {
    const int br = wid >> 2, tt = (wid >> 1) & 1, kh = wid & 1;
    if (br) {
        wo_issue<14>(woB, ct, (unsigned)(uintptr_t)lds + LDS_MRG, (wid - 4) * 14, lane);
    }
    if ((wid & 3) == 3) { flag_wait(ws, (br ? F4 : F3) + (rt >> 1) * 16 + (lane & 15), tag); if (lane == 0) ctl_store(lds, br ? CTL_F4 : CTL_F3, 1); }
    else ctl_wait(lds, br ? CTL_F4 : CTL_F3, 1);
    STAMP(11);
    const auto rO = RSRC(ws + (br ? WS_OB : WS_OA), QC_BYTES);
    const int tile = rt * 2 + tt;
    u32x4 of[14];
#pragma unroll
    for (int i = 0; i < 14; ++i) of[i] = bload128(rO, (unsigned)lane * 16u, (unsigned)(tile * 56 + 2 * (kh * 14 + i)) * 512u);
    if (br) {
        asm volatile("s_waitcnt vmcnt(14)" ::: "memory");
        if (lane == 0) ctl_add(lds, CTL_WB);
        ctl_wait(lds, CTL_WB, 4);
        wf_read(wf, lds + LDS_MRG, kh, lane);
    }
    f32x16 acc[2] = {};
#pragma unroll
    for (int i = 0; i < 14; ++i) { acc[0] = MFMA32(asb(wf[0][i]), asb(of[i]), acc[0]); acc[1] = MFMA32(asb(wf[1][i]), asb(of[i]), acc[1]); }
    asm volatile("" : "+v"(acc[0]), "+v"(acc[1]));
    STAMP(12);
    {   const f32x16& gv = kh ? acc[0] : acc[1];
#pragma unroll
        for (int rq = 0; rq < 4; ++rq) *(f32x4*)(lds + 114688 + (wid * 4 + rq) * 1024 + lane * 16) = (f32x4){gv[4 * rq], gv[4 * rq + 1], gv[4 * rq + 2], gv[4 * rq + 3]};
    }
    asm volatile("s_waitcnt lgkmcnt(0)" ::: "memory");
    if (lane == 0) ctl_store(lds, CTL_X + wid * 4, 1);
    ctl_wait(lds, CTL_X + (wid ^ 1) * 4, 1);
    STAMP(13);
    f32x16 r = kh ? acc[1] : acc[0];
#pragma unroll
    for (int rq = 0; rq < 4; ++rq) { const f32x4 x = *(const f32x4*)(lds + 114688 + ((wid ^ 1) * 4 + rq) * 1024 + lane * 16); r[4 * rq] += x[0]; r[4 * rq + 1] += x[1]; r[4 * rq + 2] += x[2]; r[4 * rq + 3] += x[3]; }
    const unsigned orow = (unsigned)(rt * 64 + tt * 32 + (lane & 31)) * 1792u + (unsigned)(br * 448 + ct * 64 + kh * 32 + (lane >> 5) * 8) * 2u;
#pragma unroll
    for (int pr = 0; pr < 2; ++pr) {
        const unsigned a0 = cvtpk(r[8 * pr], r[8 * pr + 1]), a1 = cvtpk(r[8 * pr + 2], r[8 * pr + 3]), b0 = cvtpk(r[8 * pr + 4], r[8 * pr + 5]), b1 = cvtpk(r[8 * pr + 6], r[8 * pr + 7]);
        auto r0 = __builtin_amdgcn_permlane32_swap(a0, b0, false, false);
        auto r1 = __builtin_amdgcn_permlane32_swap(a1, b1, false, false);
        bstore128((u32x4){r0[0], r1[0], r0[1], r1[1]}, RSRC(out, 2048 * 896 * 2), orow + pr * 32u);
    }
}

__global__ void __launch_bounds__(512, 2) fused_kernel(const u16* xA, const u16* wA, const u16* woA, const u16* xB, const u16* wB, const u16* woB, u16* out, char* ws) {
    extern __shared__ __attribute__((aligned(16))) char lds[];
    int lane = threadIdx.x & 63; const int wid = __builtin_amdgcn_readfirstlane(threadIdx.x >> 6);
    const int id = blockIdx.x, xcd = id & 7, loc = id >> 3;
    STAMP_INIT();
    STAMP(0);
    if (threadIdx.x < 32) ctl_store(lds, threadIdx.x * 4, 0);
    const u64 tag = __hip_atomic_load(flagp(ws, FEP), __ATOMIC_RELAXED, __HIP_MEMORY_SCOPE_AGENT) + 1;
    const int gb = xcd & 1, grt = 8 * (xcd >> 1) + (loc & 7), qct = gb * 4 + (loc >> 3);
    const bool has_qkv = qct < 7;
    const int h = xcd * 2 + (loc & 1), rb = loc >> 1;
    const int ort = 4 * xcd + (loc & 3), oct = loc >> 2;
    unsigned warm0 = 0;
    if (wid < 4) {
        u64 pc; int len;
        asm volatile("s_getpc_b64 %0\n.Lfused_pc_ref:\n\ts_mov_b32 %1, .Lfused_code_end - .Lfused_pc_ref" : "=s"(pc), "=s"(len));
        const int off = (wid * 64 + lane) * 128;
        if (off < len) warm0 = *(const __attribute__((address_space(1))) unsigned*)(pc + off);
        warm0 ^= __builtin_amdgcn_raw_buffer_load_b32(RSRC(ws, WS_FLAGS), (unsigned)(wid * 64 + lane) * 65536u, 0, SC1);
        if (wid == 0) warm0 ^= __builtin_amdgcn_raw_buffer_load_b32(RSRC(out, 2048 * 896 * 2), (unsigned)lane * 65536u, 0, SC1);
        if (wid == 1) { warm0 ^= __builtin_amdgcn_raw_buffer_load_b32(RSRC(woA, 448 * 448 * 2), (unsigned)lane * 65536u, 0, 0); warm0 ^= __builtin_amdgcn_raw_buffer_load_b32(RSRC(woB, 448 * 448 * 2), (unsigned)lane * 65536u, 0, 0); }
        if (wid == 2) { warm0 ^= __builtin_amdgcn_raw_buffer_load_b32(RSRC(xB, 2048 * 448 * 2), (unsigned)lane * 65536u, 0, 0); warm0 ^= __builtin_amdgcn_raw_buffer_load_b32(RSRC(wB, 448 * 1344 * 2), (unsigned)lane * 65536u, 0, 0); }
    }
    QkvOut outB;
    if (has_qkv) qkv_phase(xA, wA, xB, wB, ws, grt, qct, tag, lds, wid, lane, outB);
    else { if (threadIdx.x == 0) flag_set(ws, F0 + (xcd >> 1) * 8 + (loc & 7), tag); BARRIER(); }
    asm volatile("" :: "v"(warm0));
    STAMP(4);
    if (wid == 4) {
        flag_wait(ws, F1 + (lane < 32 ? (28 * h) >> 6 : (28 * h + 27) >> 6) * 32 + (lane & 31), tag);
        if (lane == 0) ctl_store(lds, CTL_F1, 1);
    } else ctl_wait(lds, CTL_F1, 1);
    STAMP(5);
    const u64 tags = (u64)(unsigned)__builtin_amdgcn_readfirstlane((unsigned)tag) | ((u64)(unsigned)__builtin_amdgcn_readfirstlane((unsigned)(tag >> 32)) << 32);
    u32x4 wf[2][14];
    attnA_phase(ws, woA, oct, h, rb, tags, grt, qct, has_qkv, outB, wf, lds, wid, lane);
    STAMP(9);
    lane = opaque_lane();
    if (oct < 7) {
        oproj_compute(ws, woB, out, ort, oct, tags, wf, lds, wid, lane);
    }
    STAMP(14);
    if (id == 0 && wid == 0) {
        flag_wait(ws, F0 + (lane & 31), tags);
        if (lane == 0) flag_set(ws, FEP, tags);
    }
    STAMP_DUMP();
    asm volatile(".Lfused_code_end:");
}

extern "C" void kernel_launch(void* const* d_in, const int* in_sizes, int n_in, void* d_out, int out_size, void* d_ws, size_t ws_size, hipStream_t stream) {
    const u16* xA = (const u16*)d_in[0]; const u16* wA = (const u16*)d_in[1]; const u16* woA = (const u16*)d_in[2];
    const u16* xB = (const u16*)d_in[3]; const u16* wB = (const u16*)d_in[4]; const u16* woB = (const u16*)d_in[5];
    (void)hipFuncSetAttribute((const void*)fused_kernel, hipFuncAttributeMaxDynamicSharedMemorySize, LDS_TOTAL + LDS_EXTRA);
    fused_kernel<<<256, 512, LDS_TOTAL + LDS_EXTRA, stream>>>(xA, wA, woA, xB, wB, woB, (u16*)d_out, (char*)d_ws);
}
```

```cpp
#include <hip/hip_runtime.h>
#include <hip/hip_bf16.h>
#include <stdint.h>

typedef unsigned short u16;
typedef unsigned long long u64;
typedef short bf16x8 __attribute__((ext_vector_type(8)));
typedef short s16x4 __attribute__((ext_vector_type(4)));
typedef float f32x4 __attribute__((ext_vector_type(4)));
typedef float f32x16 __attribute__((ext_vector_type(16)));
typedef unsigned u32x4 __attribute__((ext_vector_type(4)));
typedef unsigned u32x2 __attribute__((ext_vector_type(2)));
typedef __attribute__((address_space(3))) const char* lds_cptr;
typedef __attribute__((address_space(1))) u64 gu64;

constexpr int TILE_B = 56 * 512;
constexpr size_t QC_BYTES = (size_t)64 * TILE_B;
constexpr size_t V_BYTES = (size_t)16 * 64 * 2048;
constexpr size_t WS_QA = 0, WS_KA = WS_QA + QC_BYTES, WS_VA = WS_KA + QC_BYTES;
constexpr size_t WS_QB = WS_VA + V_BYTES, WS_KB = WS_QB + QC_BYTES, WS_VB = WS_KB + QC_BYTES;
constexpr size_t WS_OA = WS_VB + V_BYTES, WS_OB = WS_OA + QC_BYTES, WS_FLAGS = WS_OB + QC_BYTES;
constexpr int F1 = 0, F2 = 256, F3 = 512, F4 = 768, F0 = 1024, FEP = 1056;
constexpr float QSCALE = 0.18898223650461362f * 1.4426950408889634f;
constexpr int STG = 32768;
constexpr int LDS_Q = 147456, LDS_L = 131072, LDS_MRG = 57344, LDS_CTL = LDS_Q + 8192, LDS_MX = LDS_CTL + 128, LDS_TOTAL = LDS_MX + 1024 + 64;
constexpr int CTL_F1 = 0, CTL_F2CNT = 4, CTL_BAD = 8, CTL_QCNT = 12, CTL_F4CNT = 16, CTL_F3 = 20, CTL_RDONE = 24, CTL_F4 = 28, CTL_X = 32;
constexpr int CTL_WA = 64, CTL_WB = 68;
#define BPFG 1
#define PUBG 10
constexpr unsigned SPIN_MAX = 1u << 18;

#define STAMP(i)
#define STAMP_INIT()
#define STAMP_DUMP()
#define LDS_EXTRA 0

#define MFMA16(a, b, c) __builtin_amdgcn_mfma_f32_16x16x32_bf16(a, b, c, 0, 0, 0)
#define MFMA32(a, b, c) __builtin_amdgcn_mfma_f32_32x32x16_bf16(a, b, c, 0, 0, 0)
#define PIN(x) asm volatile("" : "+v"(x))
#define SBAR() __builtin_amdgcn_sched_barrier(0)
#define SC1 16

__device__ __forceinline__ unsigned cvtpk(float lo, float hi) {
    typedef float f2 __attribute__((ext_vector_type(2))); typedef __bf16 b2 __attribute__((ext_vector_type(2)));
    f2 v = {lo, hi}; b2 r = __builtin_convertvector(v, b2); return __builtin_bit_cast(unsigned, r);
}
__device__ __forceinline__ void glds16(const void* sbase, unsigned voff, unsigned lds_base) {
    asm volatile("s_mov_b32 m0, %2\n\ts_nop 0\n\tglobal_load_lds_dwordx4 %0, %1" :: "v"(voff), "s"(sbase), "s"(lds_base) : "memory");
}
__device__ __forceinline__ s16x4 vtr(lds_cptr p) {
    typedef short v4i16_t __attribute__((ext_vector_type(4)));
    return __builtin_bit_cast(s16x4, __builtin_amdgcn_ds_read_tr16_b64_v4i16((__attribute__((address_space(3))) v4i16_t*)p));
}
__device__ __forceinline__ bf16x8 cat8(s16x4 a, s16x4 b) { return (bf16x8){a[0], a[1], a[2], a[3], b[0], b[1], b[2], b[3]}; }
__device__ __forceinline__ bf16x8 ldsrd128(lds_cptr p) { return *(const __attribute__((address_space(3))) bf16x8*)p; }
#define RSRC(p, bytes) __builtin_amdgcn_make_buffer_rsrc((void*)(p), 0, (int)(bytes), 0x00020000)
__device__ __forceinline__ u32x4 bload128(__amdgpu_buffer_rsrc_t r, unsigned voff, unsigned soff) { return __builtin_amdgcn_raw_buffer_load_b128(r, voff, soff, SC1); }
__device__ __forceinline__ void bstore128(u32x4 v, __amdgpu_buffer_rsrc_t r, unsigned voff) { __builtin_amdgcn_raw_buffer_store_b128(v, r, voff, 0, SC1); }
__device__ __forceinline__ void bstore64(u32x2 v, __amdgpu_buffer_rsrc_t r, unsigned voff) { __builtin_amdgcn_raw_buffer_store_b64(v, r, voff, 0, SC1); }
__device__ __forceinline__ bf16x8 asb(u32x4 v) { return __builtin_bit_cast(bf16x8, v); }

__device__ __forceinline__ gu64* flagp(char* ws, int idx) { return (gu64*)(ws + WS_FLAGS + (size_t)idx * 128); }
__device__ __forceinline__ void flag_set(char* ws, int idx, u64 tag) { __hip_atomic_store(flagp(ws, idx), tag, __ATOMIC_RELAXED, __HIP_MEMORY_SCOPE_AGENT); }
__device__ __forceinline__ void flag_wait(char* ws, int idx, u64 tag) {
    gu64* p = flagp(ws, idx);
    for (unsigned spins = 0; spins < SPIN_MAX; ++spins) {
        const u64 v = __hip_atomic_load(p, __ATOMIC_RELAXED, __HIP_MEMORY_SCOPE_AGENT);
        if (__all(v == tag)) break;
        __builtin_amdgcn_s_sleep(1);
    }
}
__device__ __forceinline__ int opaque_lane() { int l; asm volatile("v_mbcnt_lo_u32_b32 %0, -1, 0\n\tv_mbcnt_hi_u32_b32 %0, -1, %0" : "=v"(l)); return l; }
__device__ __forceinline__ int f2_index(int h, int rb, int lane) { const int i = lane % 6, r = 2 * rb - 1 + i % 3; return F2 + (i < 3 ? (28 * h) >> 6 : (28 * h + 27) >> 6) * 32 + (r < 0 ? 0 : r); }
typedef __attribute__((address_space(3))) int* lds_iptr;
__device__ __forceinline__ int ctl_load(char* lds, int off) { return __hip_atomic_load((lds_iptr)(lds + LDS_CTL + off), __ATOMIC_RELAXED, __HIP_MEMORY_SCOPE_WORKGROUP); }
__device__ __forceinline__ void ctl_store(char* lds, int off, int v) { __hip_atomic_store((lds_iptr)(lds + LDS_CTL + off), v, __ATOMIC_RELAXED, __HIP_MEMORY_SCOPE_WORKGROUP); }
__device__ __forceinline__ void ctl_wait(char* lds, int off, int n) {
    for (unsigned spins = 0; spins < (SPIN_MAX << 6); ++spins) { if (__builtin_amdgcn_readfirstlane(ctl_load(lds, off)) >= n) break; __builtin_amdgcn_s_sleep(1); }
    asm volatile("" ::: "memory");
}
__device__ __forceinline__ int ctl_add(char* lds, int off) { return __hip_atomic_fetch_add((lds_iptr)(lds + LDS_CTL + off), 1, __ATOMIC_RELAXED, __HIP_MEMORY_SCOPE_WORKGROUP); }
#define BARRIER() asm volatile("s_waitcnt lgkmcnt(0)\n\ts_barrier" ::: "memory")
#define DRAIN_BARRIER() asm volatile("s_waitcnt vmcnt(0) lgkmcnt(0)\n\ts_barrier" ::: "memory")

struct QkvFrag { bf16x8 x[2][4], w[2][3]; };
struct QkvAcc { f32x4 q[4], k[4], v[4]; };
struct QkvOut { u32x4 q[2], k[2], v[2]; };
__device__ __forceinline__ void qkv_pack(QkvOut& o, const QkvAcc& a, float qs) {
#pragma unroll
    for (int tb = 0; tb < 2; ++tb) {
        const f32x4 q0 = a.q[2 * tb] * qs, q1 = a.q[2 * tb + 1] * qs, k0 = a.k[2 * tb], k1 = a.k[2 * tb + 1], v0 = a.v[2 * tb], v1 = a.v[2 * tb + 1];
        { auto ra = __builtin_amdgcn_permlane16_swap(cvtpk(q0[0], q0[1]), cvtpk(q1[0], q1[1]), false, false); auto rb = __builtin_amdgcn_permlane16_swap(cvtpk(q0[2], q0[3]), cvtpk(q1[2], q1[3]), false, false);
          o.q[tb] = (u32x4){ra[0], rb[0], ra[1], rb[1]}; }
        { auto ra = __builtin_amdgcn_permlane16_swap(cvtpk(k0[0], k0[1]), cvtpk(k1[0], k1[1]), false, false); auto rb = __builtin_amdgcn_permlane16_swap(cvtpk(k0[2], k0[3]), cvtpk(k1[2], k1[3]), false, false);
          o.k[tb] = (u32x4){ra[0], rb[0], ra[1], rb[1]}; }
        { auto ra = __builtin_amdgcn_permlane32_swap(cvtpk(v0[0], v0[1]), cvtpk(v1[0], v1[1]), false, false); auto rb = __builtin_amdgcn_permlane32_swap(cvtpk(v0[2], v0[3]), cvtpk(v1[2], v1[3]), false, false);
          o.v[tb] = (u32x4){ra[0], rb[0], ra[1], rb[1]}; }
    }
}
template <int BR, int I>
__device__ __forceinline__ void qkv_store(const QkvOut& o, char* ws, int rt, int ct, int c, int lane) {
    constexpr int tb = I / 3, kind = I % 3;
    const int tile = rt * 2 + tb;
    if (kind < 2) {
        const unsigned off = (unsigned)(tile * 56 + 8 * ct + 2 * c) * 512u + (unsigned)lane * 16u;
        if (kind == 0) bstore128(o.q[tb], RSRC(ws + (BR ? WS_QB : WS_QA), QC_BYTES), off); else bstore128(o.k[tb], RSRC(ws + (BR ? WS_KB : WS_KA), QC_BYTES), off);
    } else {
        const int n = 64 * ct + 16 * c + (lane & 15), h = n / 28, d = n - 28 * h;
        const unsigned off = (unsigned)(h * 64 + tile) * 2048u + (unsigned)(lane >> 4) * 512u + (unsigned)d * 16u;
        const auto rs = RSRC(ws + (BR ? WS_VB : WS_VA), V_BYTES);
        bstore128(o.v[tb], rs, off);
        if (d == 27) bstore128((u32x4){0x3F803F80u, 0x3F803F80u, 0x3F803F80u, 0x3F803F80u}, rs, off + 16u);
    }
}

__device__ __forceinline__ void qkv_phase(const u16* xA, const u16* wA, const u16* xB, const u16* wB, char* ws, int rt, int ct, u64 tag, char* lds, int wid, int lane, QkvOut& outB) {
    if (wid >= 4) {
        const unsigned lds0 = (unsigned)(uintptr_t)lds;
        const int lw = wid - 4, r8 = lane >> 3, dpos = lane & 7;
        unsigned xoff[2], woff[2];
#pragma unroll
        for (int i = 0; i < 2; ++i) { const int row = 16 * lw + 8 * i + r8;
            xoff[i] = (unsigned)row * 896u + (unsigned)((dpos ^ ((row >> 1) & 7)) << 4);
            const int f2 = ((row >> 1) & 1) | (((row >> 3) & 1) << 1);
            woff[i] = (unsigned)row * 2688u + (unsigned)((((dpos >> 1) ^ f2) * 2 + (dpos & 1)) << 4); }
        const int rot = __builtin_amdgcn_readfirstlane(((rt & 7) + 2 * (ct & 3)) % 7);
        const char* xa = (const char*)xA + (size_t)rt * 64 * 896; const char* wa = (const char*)wA + (size_t)ct * 128;
        const char* xb = (const char*)xB + (size_t)rt * 64 * 896; const char* wb = (const char*)wB + (size_t)ct * 128;
#define QKV_ISSUE(g) do { int st_ = ((g) % 7) + rot; st_ = st_ >= 7 ? st_ - 7 : st_; const char* x_ = ((g) < 7 ? xa : xb) + st_ * 128; const char* w_ = ((g) < 7 ? wa : wb) + (size_t)st_ * (64 * 2688); \
        const unsigned sl_ = lds0 + ((g) & 3) * STG + lw * 2048; \
        glds16(x_, xoff[0], sl_); glds16(x_, xoff[1], sl_ + 1024); \
        glds16(w_, woff[0], sl_ + 8192); glds16(w_, woff[1], sl_ + 8192 + 1024); glds16(w_ + 896, woff[0], sl_ + 16384); glds16(w_ + 896, woff[1], sl_ + 16384 + 1024); \
        glds16(w_ + 1792, woff[0], sl_ + 24576); glds16(w_ + 1792, woff[1], sl_ + 24576 + 1024); } while (0)
        QKV_ISSUE(0); QKV_ISSUE(1);
#pragma unroll
        for (int g = 0; g < 14; ++g) {
            if (g <= 12) asm volatile("s_waitcnt vmcnt(8) lgkmcnt(0)\n\ts_barrier" ::: "memory");
            else asm volatile("s_waitcnt vmcnt(0) lgkmcnt(0)\n\ts_barrier" ::: "memory");
            if (g == 0) STAMP(1);
            if (g + 2 < 14) QKV_ISSUE(g + 2);
        }
        return;
    }
    const int c = wid, g4 = lane >> 4, t16 = lane & 15;
    const unsigned xr0 = (unsigned)t16 * 128u + (unsigned)((g4 ^ (t16 >> 1)) << 4);
    const int q = t16 >> 2, p = lane & 3;
    const int rf2 = ((q >> 1) & 1) | ((g4 & 1) << 1);
    const unsigned wr0 = 8192u + (unsigned)(8 * g4 + q) * 128u + (unsigned)((c ^ rf2) * 32) + (unsigned)p * 8u;
    QkvAcc acc; QkvOut outA;
#pragma unroll
    for (int i = 0; i < 4; ++i) acc.q[i] = acc.k[i] = acc.v[i] = (f32x4){};
    QkvFrag fr[2];
    unsigned pfa = 0, pfb = 0;
#define QMF(i) do { if (g > 0) { constexpr int ks_ = (i) / 12, s_ = ((i) % 12) / 4, b_ = (i) % 4; const QkvFrag& f_ = fr[(g - 1) & 1]; \
        if (s_ == 0) acc.q[b_] = MFMA16(f_.w[ks_][0], f_.x[ks_][b_], acc.q[b_]); if (s_ == 1) acc.k[b_] = MFMA16(f_.w[ks_][1], f_.x[ks_][b_], acc.k[b_]); \
        if (s_ == 2) acc.v[b_] = MFMA16(f_.x[ks_][b_], f_.w[ks_][2], acc.v[b_]); } SBAR(); } while (0)
#define QRX(ks, b) do { if (g < 14) fr[g & 1].x[ks][b] = ldsrd128(sl + (xr0 ^ ((ks) * 64)) + (b) * 2048); } while (0)
#define QRW(ks, s) do { if (g < 14) { lds_cptr wp_ = sl + wr0 + (ks) * 4096 + (s) * 8192; fr[g & 1].w[ks][s] = cat8(vtr(wp_), vtr(wp_ + 512)); } } while (0)
#define QST(gg, i) do { if (g == (gg)) { qkv_store<0, i>(outA, ws, rt, ct, c, lane); SBAR(); } } while (0)
#pragma unroll
    for (int g = 0; g < 15; ++g) {
        if (g == PUBG) asm volatile("s_waitcnt vmcnt(0) lgkmcnt(0)\n\ts_barrier" ::: "memory");
        else if (g < 14) asm volatile("s_waitcnt lgkmcnt(0)\n\ts_barrier" ::: "memory");
        SBAR();
        if (g == 7) STAMP(2);
        lds_cptr sl = (lds_cptr)lds + (g & 3) * STG;
        if (g == PUBG && threadIdx.x == 0) flag_set(ws, F1 + ct * 32 + rt, tag);
        SBAR();
        QRX(0, 0); QMF(0); QRX(0, 1); QMF(1); QRX(0, 2); QMF(2); QRX(0, 3); QMF(3); QST(8, 3); QRW(0, 0); QMF(4); QRW(0, 1); QMF(5); QRW(0, 2); QMF(6);
        QRX(1, 0); QMF(7); QRX(1, 1); QMF(8); QRX(1, 2); QMF(9); QST(8, 4); QRX(1, 3); QMF(10); QRW(1, 0); QMF(11); QRW(1, 1); QMF(12); QRW(1, 2); QMF(13);
        QMF(14); QMF(15); QST(8, 5); QMF(16); QMF(17); QMF(18); QMF(19); QMF(20); QMF(21); QMF(22); QMF(23);
        if (g == 7) {
            qkv_pack(outA, acc, QSCALE);
            qkv_store<0, 0>(outA, ws, rt, ct, c, lane); qkv_store<0, 1>(outA, ws, rt, ct, c, lane); qkv_store<0, 2>(outA, ws, rt, ct, c, lane);
#pragma unroll
            for (int i = 0; i < 4; ++i) acc.q[i] = acc.k[i] = acc.v[i] = (f32x4){};
        }
    }
    if (lane == 0) ctl_add(lds, CTL_RDONE);
    qkv_pack(outB, acc, 1.0f);
}

__device__ __forceinline__ float max16(const f32x16& s) {
    float a = fmaxf(fmaxf(s[0], s[1]), s[2]), b = fmaxf(fmaxf(s[3], s[4]), s[5]);
    a = fmaxf(fmaxf(a, s[6]), s[7]); b = fmaxf(fmaxf(b, s[8]), s[9]); a = fmaxf(fmaxf(a, s[10]), s[11]); b = fmaxf(fmaxf(b, s[12]), s[13]);
    a = fmaxf(fmaxf(a, s[14]), s[15]); return fmaxf(a, b);
}
__device__ __forceinline__ float xhalf_max(float m) {
    auto r = __builtin_amdgcn_permlane32_swap(__float_as_uint(m), __float_as_uint(m), false, false);
    return fmaxf(__uint_as_float(r[0]), __uint_as_float(r[1]));
}
__device__ __forceinline__ void patch(u32x4& f0, u32x4& f1, bool pe, bool po, unsigned a, unsigned b) {
    f0[0] = po ? a : f0[0]; f0[1] = po ? b : f0[1]; f1[2] = pe ? a : f1[2]; f1[3] = pe ? b : f1[3];
}
__device__ __forceinline__ void exp_pack(const f32x16& s, u32x4& p0, u32x4& p1) {
    float e[16];
#pragma unroll
    for (int r = 0; r < 16; ++r) e[r] = __builtin_amdgcn_exp2f(s[r]);
    p0 = (u32x4){cvtpk(e[0], e[1]), cvtpk(e[2], e[3]), cvtpk(e[4], e[5]), cvtpk(e[6], e[7])};
    p1 = (u32x4){cvtpk(e[8], e[9]), cvtpk(e[10], e[11]), cvtpk(e[12], e[13]), cvtpk(e[14], e[15])};
}
__device__ __forceinline__ void merge_write1(const f32x16& o, char* lds, int wid, int j, int lane) {
#pragma unroll
    for (int hf = 0; hf < 2; ++hf)
        *(u32x4*)(lds + LDS_MRG + ((wid * 4 + j) * 2 + hf) * 1024 + lane * 16) = (u32x4){cvtpk(o[8 * hf], o[8 * hf + 1]), cvtpk(o[8 * hf + 2], o[8 * hf + 3]), cvtpk(o[8 * hf + 4], o[8 * hf + 5]), cvtpk(o[8 * hf + 6], o[8 * hf + 7])};
    if (lane >= 32) *(float*)(lds + LDS_L + ((wid * 4 + j) * 32 + lane - 32) * 4) = o[12];
}
__device__ __forceinline__ bool merge_reduce(char* ws, int h, int rb, char* lds, int wid, int lane) {
    const bool hi = lane >= 32;
    const int j = wid >> 1, hf = wid & 1;
    const int tile = rb * 4 + j, q = lane & 31;
    u32x4 p[8]; float pl[8];
#pragma unroll
    for (int w = 0; w < 8; ++w) pl[w] = *(const float*)(lds + LDS_L + ((w * 4 + j) * 32 + q) * 4);
#pragma unroll
    for (int w = 0; w < 8; ++w) p[w] = *(const u32x4*)(lds + LDS_MRG + ((w * 4 + j) * 2 + hf) * 1024 + lane * 16);
    const int col0 = 28 * h + 16 * hf + (hi ? 4 : 0), col1 = col0 + 8;
    const unsigned off0 = (unsigned)((tile * 56 + (col0 >> 3)) * 32 + q) * 16u + (unsigned)((col0 >> 2) & 1) * 8u, off1 = (unsigned)((tile * 56 + (col1 >> 3)) * 32 + q) * 16u + (unsigned)((col1 >> 2) & 1) * 8u;
    const float l = ((pl[0] + pl[1]) + (pl[2] + pl[3])) + ((pl[4] + pl[5]) + (pl[6] + pl[7]));
    const float inv = __builtin_amdgcn_rcpf(l);
    float a[8] = {};
#pragma unroll
    for (int w = 0; w < 8; ++w)
#pragma unroll
        for (int i = 0; i < 4; ++i) { a[2 * i] += __uint_as_float(p[w][i] << 16); a[2 * i + 1] += __uint_as_float(p[w][i] & 0xffff0000u); }
#pragma unroll
    for (int i = 0; i < 8; ++i) a[i] *= inv;
    const auto rO = RSRC(ws + WS_OA, QC_BYTES);
    const bool last = hf == 1 && hi;
    bstore64((u32x2){cvtpk(a[0], a[1]), cvtpk(a[2], a[3])}, rO, off0);
    if (!last) bstore64((u32x2){cvtpk(a[4], a[5]), cvtpk(a[6], a[7])}, rO, off1);
    float z = a[0] * 0.f; z = fmaf(a[1], 0.f, z); z = fmaf(a[2], 0.f, z); z = fmaf(a[3], 0.f, z);
    float z1 = a[4] * 0.f; z1 = fmaf(a[5], 0.f, z1); z1 = fmaf(a[6], 0.f, z1); z1 = fmaf(a[7], 0.f, z1);
    const bool ok = l > 1.0e-18f && l < 1.0e30f && z == 0.f && (last || z1 == 0.f);
    return !ok;
}

__device__ __forceinline__ void attnA_safe(char* ws, int h, int rb, char* lds, int wid, int lane) {
    const bool hi = lane >= 32, pe = hi && !(h & 1), po = !hi && (h & 1); const int m0 = (28 * h) >> 3;
    const auto rQ = RSRC(ws + WS_QA, QC_BYTES), rK = RSRC(ws + WS_KA, QC_BYTES), rV = RSRC(ws + WS_VA, V_BYTES);
    const unsigned lo16 = (unsigned)lane * 16u;
    float* mx = (float*)(lds + LDS_MX);
#pragma unroll 1
    for (int j = 0; j < 4; ++j) {
        const unsigned b = (unsigned)((rb * 4 + j) * 56 + m0) * 512u;
        u32x4 q0 = bload128(rQ, lo16, b), q1 = bload128(rQ, lo16, b + 1024u);
        patch(q0, q1, pe, po, 0u, 0u);
        float m = -3.0e38f; f32x16 acc = {};
#pragma unroll 1
        for (int t = 0; t < 8; ++t) {
            const unsigned ko = (unsigned)((wid * 8 + t) * 56 + m0) * 512u, vo = (unsigned)(h * 64 + wid * 8 + t) * 2048u;
            u32x4 k0 = bload128(rK, lo16, ko), k1 = bload128(rK, lo16, ko + 1024u), v0 = bload128(rV, lo16, vo), v1 = bload128(rV, lo16, vo + 1024u);
            patch(k0, k1, pe, po, 0u, 0u);
            f32x16 s = {}; s = MFMA32(asb(k0), asb(q0), s); s = MFMA32(asb(k1), asb(q1), s);
            const float mn = fmaxf(m, xhalf_max(max16(s)));
            const float al = __builtin_amdgcn_exp2f(m - mn); m = mn;
#pragma unroll
            for (int r = 0; r < 16; ++r) { s[r] -= mn; acc[r] *= al; }
            u32x4 p0, p1; exp_pack(s, p0, p1);
            acc = MFMA32(asb(v0), asb(p0), acc); acc = MFMA32(asb(v1), asb(p1), acc);
        }
        BARRIER();
        if (!hi) mx[wid * 32 + lane] = m;
        BARRIER();
        float g = m;
#pragma unroll
        for (int w = 0; w < 8; ++w) g = fmaxf(g, mx[w * 32 + (lane & 31)]);
        const float f = __builtin_amdgcn_exp2f(m - g);
        merge_write1(acc * f, lds, wid, j, lane);
    }
}

struct AttnBPre { u32x4 q, kp, kc; u32x2 v[2][2]; };
__device__ __forceinline__ void attnB_load(AttnBPre& a, char* ws, int h, int rb, int wid, int lane) {
    const int m0 = (28 * h) >> 3, T = rb * 4 + (wid >> 1), sub = wid & 1;
    const int Tp = sub ? T : (T > 0 ? T - 1 : 0), subp = sub ^ 1;
    const auto rQ = RSRC(ws + WS_QB, QC_BYTES), rK = RSRC(ws + WS_KB, QC_BYTES), rV = RSRC(ws + WS_VB, V_BYTES);
    const int g = lane >> 4, t16 = lane & 15;
    const unsigned co = (unsigned)g * 512u + (unsigned)t16 * 16u;
    a.q = bload128(rQ, co, (unsigned)(T * 56 + m0) * 512u + (unsigned)sub * 256u);
    a.kp = bload128(rK, co, (unsigned)(Tp * 56 + m0) * 512u + (unsigned)subp * 256u);
    a.kc = bload128(rK, co, (unsigned)(T * 56 + m0) * 512u + (unsigned)sub * 256u);
    const unsigned vo = (unsigned)(g & 1) * 512u + (unsigned)t16 * 16u + (unsigned)(g >> 1) * 8u;
#pragma unroll
    for (int db = 0; db < 2; ++db) {
        a.v[db][0] = __builtin_amdgcn_raw_buffer_load_b64(rV, vo + db * 256u, (unsigned)(h * 64 + Tp) * 2048u + (unsigned)subp * 1024u, SC1);
        a.v[db][1] = __builtin_amdgcn_raw_buffer_load_b64(rV, vo + db * 256u, (unsigned)(h * 64 + T) * 2048u + (unsigned)sub * 1024u, SC1);
    }
}
__device__ __forceinline__ void attnB_compute(AttnBPre& a, char* ws, int h, int rb, int wid, int lane) {
    const int T = rb * 4 + (wid >> 1), sub = wid & 1, g = lane >> 4, ql = lane & 15;
    const bool padl = (h & 1) ? g == 0 : g == 3;
    if (h & 1) { a.q[0] = padl ? 0u : a.q[0]; a.q[1] = padl ? 0u : a.q[1]; a.kp[0] = padl ? 0u : a.kp[0]; a.kp[1] = padl ? 0u : a.kp[1]; a.kc[0] = padl ? 0u : a.kc[0]; a.kc[1] = padl ? 0u : a.kc[1]; }
    else { a.q[2] = padl ? 0u : a.q[2]; a.q[3] = padl ? 0u : a.q[3]; a.kp[2] = padl ? 0u : a.kp[2]; a.kp[3] = padl ? 0u : a.kp[3]; a.kc[2] = padl ? 0u : a.kc[2]; a.kc[3] = padl ? 0u : a.kc[3]; }
    f32x4 sp = {}, sc = {};
    sp = MFMA16(asb(a.kp), asb(a.q), sp); sc = MFMA16(asb(a.kc), asb(a.q), sc);
    const bool hasp = sub || T > 0;
    float m = -INFINITY;
#pragma unroll
    for (int r = 0; r < 4; ++r) {
        const int dc = ql - (4 * g + r), dp = dc + 16;
        sp[r] = (hasp && dp <= 14 && !(dp & 1)) ? sp[r] : -INFINITY; sc[r] = (dc >= 0 && !(dc & 1)) ? sc[r] : -INFINITY;
        m = fmaxf(m, fmaxf(sp[r], sc[r]));
    }
    { auto r = __builtin_amdgcn_permlane16_swap(__float_as_uint(m), __float_as_uint(m), false, false); m = fmaxf(__uint_as_float(r[0]), __uint_as_float(r[1])); }
    m = xhalf_max(m);
    float e[8];
#pragma unroll
    for (int r = 0; r < 4; ++r) { e[r] = __builtin_amdgcn_exp2f((sp[r] - m) * QSCALE); e[4 + r] = __builtin_amdgcn_exp2f((sc[r] - m) * QSCALE); }
    const u32x4 pf = {cvtpk(e[0], e[1]), cvtpk(e[2], e[3]), cvtpk(e[4], e[5]), cvtpk(e[6], e[7])};
    f32x4 o0 = {}, o1 = {};
    o0 = MFMA16(asb((u32x4){a.v[0][0][0], a.v[0][0][1], a.v[0][1][0], a.v[0][1][1]}), asb(pf), o0);
    o1 = MFMA16(asb((u32x4){a.v[1][0][0], a.v[1][0][1], a.v[1][1][0], a.v[1][1][1]}), asb(pf), o1);
    float l;
    { auto r16 = __builtin_amdgcn_permlane16_swap(__float_as_uint(o1[0]), __float_as_uint(o1[0]), false, false); auto r32 = __builtin_amdgcn_permlane32_swap(r16[1], r16[1], false, false); l = __uint_as_float(r32[1]); }
    const float inv = __builtin_amdgcn_rcpf(l);
    const auto rO = RSRC(ws + WS_OB, QC_BYTES);
    const unsigned tok = (unsigned)(sub * 16 + ql) * 16u;
    { const int col = 28 * h + 4 * g; bstore64((u32x2){cvtpk(o0[0] * inv, o0[1] * inv), cvtpk(o0[2] * inv, o0[3] * inv)}, rO, (unsigned)(T * 56 + (col >> 3)) * 512u + tok + (unsigned)((col >> 2) & 1) * 8u); }
    if (g < 3) { const int col = 28 * h + 16 + 4 * g; bstore64((u32x2){cvtpk(o1[0] * inv, o1[1] * inv), cvtpk(o1[2] * inv, o1[3] * inv)}, rO, (unsigned)(T * 56 + (col >> 3)) * 512u + tok + (unsigned)((col >> 2) & 1) * 8u); }
}

template <int N>
__device__ __forceinline__ void wo_issue(const u16* wo, int ct, unsigned lds_base, int pc0, int lane) {
    const int r8 = lane >> 3, dpos = lane & 7;
    const unsigned woff = (unsigned)r8 * 896u + (unsigned)((dpos ^ ((r8 & 2) << 1)) << 4);
#pragma unroll
    for (int i = 0; i < N; ++i) glds16((const char*)wo + (size_t)ct * 128 + (size_t)(pc0 + i) * 8 * 896, woff, lds_base + (pc0 + i) * 1024);
}
__device__ __forceinline__ void wf_read(u32x4 (&wf)[2][14], const char* img, int kh, int lane) {
    const int g16 = lane >> 4, q = (lane & 15) >> 2, p = lane & 3;
#pragma unroll
    for (int i = 0; i < 14; ++i)
#pragma unroll
        for (int c = 0; c < 2; ++c) {
            const int row = (kh * 14 + i) * 16 + 8 * (g16 >> 1) + q;
            lds_cptr wp = (lds_cptr)img + (unsigned)row * 128u + (unsigned)(((2 * c + (g16 & 1)) ^ (q & 2)) * 32) + (unsigned)p * 8u;
            wf[c][i] = __builtin_bit_cast(u32x4, cat8(vtr(wp), vtr(wp + 512)));
        }
}
__device__ __forceinline__ void attnA_phase(char* ws, const u16* woA, int oct, int h, int rb, u64 tag, int rt, int ct, bool has_qkv, const QkvOut& outB, u32x4 (&wf)[2][14], char* lds, int wid, int lane) {
    const bool hi = lane >= 32, pe = hi && !(h & 1), po = !hi && (h & 1);
    const int m0 = (28 * h) >> 3;
    const auto rK = RSRC(ws + WS_KA, QC_BYTES), rV = RSRC(ws + WS_VA, V_BYTES);
    const unsigned lo16 = (unsigned)lane * 16u;
    const unsigned kbase = (unsigned)((wid * 8) * 56 + m0) * 512u, vbase = (unsigned)(h * 64 + wid * 8) * 2048u;
    const int rot = rb & 7;
    u32x4 qf[4][2], kf[3][2], vf[3][2];
    {   const unsigned k0 = kbase + (unsigned)rot * TILE_B, v0 = vbase + (unsigned)rot * 2048u, k1 = kbase + (unsigned)((rot + 1) & 7) * TILE_B;
        kf[0][0] = bload128(rK, lo16, k0); kf[0][1] = bload128(rK, lo16, k0 + 1024u);
        if (wid >= 4) { const unsigned b = (unsigned)((rb * 4 + wid - 4) * 56 + m0) * 512u; const auto rQ = RSRC(ws + WS_QA, QC_BYTES); qf[0][0] = bload128(rQ, lo16, b); qf[0][1] = bload128(rQ, lo16, b + 1024u); }
        vf[0][0] = bload128(rV, lo16, v0); vf[0][1] = bload128(rV, lo16, v0 + 1024u);
        kf[1][0] = bload128(rK, lo16, k1); kf[1][1] = bload128(rK, lo16, k1 + 1024u);
        const unsigned v1 = vbase + (unsigned)((rot + 1) & 7) * 2048u, k2 = kbase + (unsigned)((rot + 2) & 7) * TILE_B;
        vf[1][0] = bload128(rV, lo16, v1); vf[1][1] = bload128(rV, lo16, v1 + 1024u);
        kf[2][0] = bload128(rK, lo16, k2); kf[2][1] = bload128(rK, lo16, k2 + 1024u); }
    patch(kf[0][0], kf[0][1], pe, po, 0x3F803F80u, 0u);
    if (wid >= 4) {
        patch(qf[0][0], qf[0][1], pe, po, 0u, 0u);
        f32x16 s = {}; s = MFMA32(asb(kf[0][0]), asb(qf[0][0]), s); s = MFMA32(asb(kf[0][1]), asb(qf[0][1]), s);
        const float m = xhalf_max(max16(s));
        const unsigned mh = cvtpk(-m, 0.f) & 0xffffu; const float r = -m - __uint_as_float(mh << 16);
        patch(qf[0][0], qf[0][1], pe, po, mh | (cvtpk(r, 0.f) << 16), 0u);
        *(u32x4*)(lds + LDS_Q + (wid - 4) * 2048 + lane * 16) = qf[0][0]; *(u32x4*)(lds + LDS_Q + (wid - 4) * 2048 + 1024 + lane * 16) = qf[0][1];
        asm volatile("s_waitcnt lgkmcnt(0)" ::: "memory");
        if (lane == 0) ctl_add(lds, CTL_QCNT);
    }
    ctl_wait(lds, CTL_QCNT, 4);
    STAMP(6);
#pragma unroll
    for (int j = 0; j < 4; ++j) { qf[j][0] = *(const u32x4*)(lds + LDS_Q + j * 2048 + lane * 16); qf[j][1] = *(const u32x4*)(lds + LDS_Q + j * 2048 + 1024 + lane * 16); }
    if (has_qkv && wid < 4) {
        qkv_store<1, 0>(outB, ws, rt, ct, wid, lane); qkv_store<1, 1>(outB, ws, rt, ct, wid, lane); qkv_store<1, 2>(outB, ws, rt, ct, wid, lane);
        qkv_store<1, 3>(outB, ws, rt, ct, wid, lane); qkv_store<1, 4>(outB, ws, rt, ct, wid, lane); qkv_store<1, 5>(outB, ws, rt, ct, wid, lane);
    }
#pragma unroll
    for (int j = 0; j < 4; ++j) { PIN(qf[j][0]); PIN(qf[j][1]); }
    PIN(kf[0][0]); PIN(kf[0][1]);
    f32x16 o[4];
#define VEXP(d, s) asm volatile("v_exp_f32 %0, %1" : "=v"(d) : "v"(s))
#define VCVT(d, a, b) asm volatile("v_cvt_pk_bf16_f32 %0, %1, %2" : "=v"(d) : "v"(a), "v"(b))
#define MF0(d, a, b) asm volatile("v_mfma_f32_32x32x16_bf16 %0, %1, %2, 0" : "=&v"(d) : "v"(a), "v"(b))
#define MFA(d, a, b) asm volatile("v_mfma_f32_32x32x16_bf16 %0, %1, %2, %0" : "+v"(d) : "v"(a), "v"(b))
#define SLOT(PV, QK, Sc, Pc0, Pc1, Sn, kn0, kn1, qn0, qn1, Op, vp0, vp1, Pp0, Pp1, INJ) do { \
        float e0, e1, e2, e3, e4, e5, e6, e7, e8, e9, e10, e11, e12, e13, e14, e15; \
        if (QK) MF0(Sn, kn0, qn0); \
        VEXP(e0, Sc[0]); VEXP(e1, Sc[1]); VEXP(e2, Sc[2]); VEXP(e3, Sc[3]); \
        if (QK) MFA(Sn, kn1, qn1); \
        VEXP(e4, Sc[4]); VEXP(e5, Sc[5]); VEXP(e6, Sc[6]); VEXP(e7, Sc[7]); VCVT(Pc0[0], e0, e1); VCVT(Pc0[1], e2, e3); \
        INJ; \
        if ((PV) == 2) MFA(Op, vp0, Pp0); else if ((PV) == 1) MF0(Op, vp0, Pp0); \
        VEXP(e8, Sc[8]); VEXP(e9, Sc[9]); VEXP(e10, Sc[10]); VEXP(e11, Sc[11]); VCVT(Pc0[2], e4, e5); VCVT(Pc0[3], e6, e7); \
        if (PV) MFA(Op, vp1, Pp1); \
        VEXP(e12, Sc[12]); VEXP(e13, Sc[13]); VEXP(e14, Sc[14]); VEXP(e15, Sc[15]); VCVT(Pc1[0], e8, e9); VCVT(Pc1[1], e10, e11); VCVT(Pc1[2], e12, e13); VCVT(Pc1[3], e14, e15); \
    } while (0)
    f32x16 sA, sB; u32x4 pA0, pA1, pB0, pB1;
    asm volatile("s_nop 1\n\tv_mfma_f32_32x32x16_bf16 %0, %1, %2, 0\n\tv_mfma_f32_32x32x16_bf16 %0, %3, %4, %0\n\ts_nop 7\n\ts_nop 3" : "=&v"(sA) : "v"(kf[0][0]), "v"(qf[0][0]), "v"(kf[0][1]), "v"(qf[0][1]));
#define PATCHK(kx) do { PIN(kx[0]); PIN(kx[1]); patch(kx[0], kx[1], pe, po, 0x3F803F80u, 0u); PIN(kx[0]); PIN(kx[1]); } while (0)
#define LOADK(kc, tt) do { if ((tt) < 8) { const unsigned o_ = kbase + (unsigned)(((tt) + rot) & 7) * TILE_B; kc[0] = bload128(rK, lo16, o_); kc[1] = bload128(rK, lo16, o_ + 1024u); } } while (0)
#define LOADV(vx, tt) do { if ((tt) < 8) { const unsigned o_ = vbase + (unsigned)(((tt) + rot) & 7) * 2048u; vx[0] = bload128(rV, lo16, o_); vx[1] = bload128(rV, lo16, o_ + 1024u); } } while (0)
#define TILE(t) do { \
        SLOT((t) == 0 ? 0 : (t) == 1 ? 1 : 2, true, sA, pA0, pA1, sB, kf[(t) % 3][0], kf[(t) % 3][1], qf[1][0], qf[1][1], o[3], vf[((t) + 2) % 3][0], vf[((t) + 2) % 3][1], pB0, pB1, (void)0); \
        LOADV(vf[((t) + 2) % 3], (t) + 2); \
        SLOT((t) == 0 ? 1 : 2, true, sB, pB0, pB1, sA, kf[(t) % 3][0], kf[(t) % 3][1], qf[2][0], qf[2][1], o[0], vf[(t) % 3][0], vf[(t) % 3][1], pA0, pA1, (void)0); \
        SLOT((t) == 0 ? 1 : 2, true, sA, pA0, pA1, sB, kf[(t) % 3][0], kf[(t) % 3][1], qf[3][0], qf[3][1], o[1], vf[(t) % 3][0], vf[(t) % 3][1], pB0, pB1, if ((t) < 7) PATCHK(kf[((t) + 1) % 3])); \
        LOADK(kf[(t) % 3], (t) + 3); \
        SLOT((t) == 0 ? 1 : 2, (t) < 7, sB, pB0, pB1, sA, kf[((t) + 1) % 3][0], kf[((t) + 1) % 3][1], qf[0][0], qf[0][1], o[2], vf[(t) % 3][0], vf[(t) % 3][1], pA0, pA1, (void)0); \
    } while (0)
    AttnBPre bpre; u64 flb = 0; bool bready = false;
#pragma unroll
    for (int t = 0; t < 8; t += 2) {
        if (t == 0) __builtin_amdgcn_s_setprio(3); else if (t == 2) __builtin_amdgcn_s_setprio(2); else if (t == 4) __builtin_amdgcn_s_setprio(1); else __builtin_amdgcn_s_setprio(0);
        TILE(t);
        if (t == 4) { bready = __all(flb == tag); attnB_load(bpre, ws, h, rb, wid, opaque_lane()); }

        else if (t == 6 && bready) attnB_compute(bpre, ws, h, rb, wid, opaque_lane());

        TILE(t + 1);
        if (t == 0) {
            if (has_qkv && wid < 4) {
                asm volatile("s_waitcnt vmcnt(8)" ::: "memory");
                if (opaque_lane() == 0 && ctl_add(lds, CTL_F2CNT) == 3) flag_set(ws, F2 + ct * 32 + rt, tag);
            }
            if (oct < 7 && wid >= 4) {
                if (has_qkv) ctl_wait(lds, CTL_RDONE, 4);
                wo_issue<14>(woA, oct, (unsigned)(uintptr_t)lds, (wid - 4) * 14, opaque_lane());
            }
        } else if (t == 2) {
            const int ln = opaque_lane();
            flb = __hip_atomic_load(flagp(ws, f2_index(h, rb, ln)), __ATOMIC_RELAXED, __HIP_MEMORY_SCOPE_AGENT);
        }
    }
    asm volatile("s_nop 1\n\tv_mfma_f32_32x32x16_bf16 %0, %1, %2, %0\n\tv_mfma_f32_32x32x16_bf16 %0, %3, %4, %0\n\ts_nop 7\n\ts_nop 3" : "+v"(o[3]) : "v"(vf[1][0]), "v"(pB0), "v"(vf[1][1]), "v"(pB1));
    asm volatile("" : "+v"(o[0]), "+v"(o[1]), "+v"(o[2]));
    STAMP(7);
    lane = opaque_lane();
    if (!bready) { flag_wait(ws, f2_index(h, rb, lane), tag); attnB_load(bpre, ws, h, rb, wid, lane); attnB_compute(bpre, ws, h, rb, wid, lane); }
    if (has_qkv) ctl_wait(lds, CTL_RDONE, 4);
#pragma unroll
    for (int j = 0; j < 4; ++j) merge_write1(o[j], lds, wid, j, lane);
    asm volatile("s_waitcnt vmcnt(0)" ::: "memory");
    if (lane == 0) { if (wid >= 4) ctl_add(lds, CTL_WA); if (ctl_add(lds, CTL_F4CNT) == 7) flag_set(ws, F4 + rb * 16 + h, tag); }
    if (oct < 7 && wid < 4) { ctl_wait(lds, CTL_WA, 4); wf_read(wf, lds, wid & 1, lane); }
    STAMP(15);
    BARRIER();
    STAMP(10);
    const bool bad = merge_reduce(ws, h, rb, lds, wid, lane);
    if (__any(bad) && lane == 0) ctl_store(lds, CTL_BAD, 1);
    STAMP(8);
    DRAIN_BARRIER();
    if (__builtin_amdgcn_readfirstlane(ctl_load(lds, CTL_BAD))) {
        attnA_safe(ws, h, rb, lds, wid, lane);
        BARRIER();
        merge_reduce(ws, h, rb, lds, wid, lane);
        DRAIN_BARRIER();
    }
    if (threadIdx.x == 0) flag_set(ws, F3 + rb * 16 + h, tag);
}

__device__ __forceinline__ void oproj_compute(char* ws, const u16* woB, u16* out, int rt, int ct, u64 tag, u32x4 (&wf)[2][14], char* lds, int wid, int lane) {
    const int br = wid >> 2, tt = (wid >> 1) & 1, kh = wid & 1;
    if (br) {
        wo_issue<14>(woB, ct, (unsigned)(uintptr_t)lds + LDS_MRG, (wid - 4) * 14, lane);
    }
    if ((wid & 3) == 3) { flag_wait(ws, (br ? F4 : F3) + (rt >> 1) * 16 + (lane & 15), tag); if (lane == 0) ctl_store(lds, br ? CTL_F4 : CTL_F3, 1); }
    else ctl_wait(lds, br ? CTL_F4 : CTL_F3, 1);
    STAMP(11);
    const auto rO = RSRC(ws + (br ? WS_OB : WS_OA), QC_BYTES);
    const int tile = rt * 2 + tt;
    u32x4 of[14];
#pragma unroll
    for (int i = 0; i < 14; ++i) of[i] = bload128(rO, (unsigned)lane * 16u, (unsigned)(tile * 56 + 2 * (kh * 14 + i)) * 512u);
    if (br) {
        asm volatile("s_waitcnt vmcnt(14)" ::: "memory");
        if (lane == 0) ctl_add(lds, CTL_WB);
        ctl_wait(lds, CTL_WB, 4);
        wf_read(wf, lds + LDS_MRG, kh, lane);
    }
    f32x16 acc[2] = {};
#pragma unroll
    for (int i = 0; i < 14; ++i) { acc[0] = MFMA32(asb(wf[0][i]), asb(of[i]), acc[0]); acc[1] = MFMA32(asb(wf[1][i]), asb(of[i]), acc[1]); }
    asm volatile("" : "+v"(acc[0]), "+v"(acc[1]));
    STAMP(12);
    {   const f32x16& gv = kh ? acc[0] : acc[1];
#pragma unroll
        for (int rq = 0; rq < 4; ++rq) *(f32x4*)(lds + 114688 + (wid * 4 + rq) * 1024 + lane * 16) = (f32x4){gv[4 * rq], gv[4 * rq + 1], gv[4 * rq + 2], gv[4 * rq + 3]};
    }
    asm volatile("s_waitcnt lgkmcnt(0)" ::: "memory");
    if (lane == 0) ctl_store(lds, CTL_X + wid * 4, 1);
    ctl_wait(lds, CTL_X + (wid ^ 1) * 4, 1);
    STAMP(13);
    f32x16 r = kh ? acc[1] : acc[0];
#pragma unroll
    for (int rq = 0; rq < 4; ++rq) { const f32x4 x = *(const f32x4*)(lds + 114688 + ((wid ^ 1) * 4 + rq) * 1024 + lane * 16); r[4 * rq] += x[0]; r[4 * rq + 1] += x[1]; r[4 * rq + 2] += x[2]; r[4 * rq + 3] += x[3]; }
    const unsigned orow = (unsigned)(rt * 64 + tt * 32 + (lane & 31)) * 1792u + (unsigned)(br * 448 + ct * 64 + kh * 32 + (lane >> 5) * 8) * 2u;
#pragma unroll
    for (int pr = 0; pr < 2; ++pr) {
        const unsigned a0 = cvtpk(r[8 * pr], r[8 * pr + 1]), a1 = cvtpk(r[8 * pr + 2], r[8 * pr + 3]), b0 = cvtpk(r[8 * pr + 4], r[8 * pr + 5]), b1 = cvtpk(r[8 * pr + 6], r[8 * pr + 7]);
        auto r0 = __builtin_amdgcn_permlane32_swap(a0, b0, false, false);
        auto r1 = __builtin_amdgcn_permlane32_swap(a1, b1, false, false);
        bstore128((u32x4){r0[0], r1[0], r0[1], r1[1]}, RSRC(out, 2048 * 896 * 2), orow + pr * 32u);
    }
}

__global__ void __launch_bounds__(512, 2) fused_kernel(const u16* xA, const u16* wA, const u16* woA, const u16* xB, const u16* wB, const u16* woB, u16* out, char* ws) {
    extern __shared__ __attribute__((aligned(16))) char lds[];
    int lane = threadIdx.x & 63; const int wid = __builtin_amdgcn_readfirstlane(threadIdx.x >> 6);
    const int id = blockIdx.x, xcd = id & 7, loc = id >> 3;
    STAMP_INIT();
    STAMP(0);
    if (threadIdx.x < 32) ctl_store(lds, threadIdx.x * 4, 0);
    const u64 rt_start = __builtin_amdgcn_s_memrealtime();
    if (threadIdx.x == 0) __hip_atomic_store((gu64*)(ws + (32u << 20) + blockIdx.x * 128), rt_start, __ATOMIC_RELAXED, __HIP_MEMORY_SCOPE_AGENT);
    const u64 tag = __hip_atomic_load(flagp(ws, FEP), __ATOMIC_RELAXED, __HIP_MEMORY_SCOPE_AGENT) + 1;
    const int gb = xcd & 1, grt = 8 * (xcd >> 1) + (loc & 7), qct = gb * 4 + (loc >> 3);
    const bool has_qkv = qct < 7;
    const int h = xcd * 2 + (loc & 1), rb = loc >> 1;
    const int ort = 4 * xcd + (loc & 3), oct = loc >> 2;
    unsigned warm0 = 0;
    if (wid < 4) {
        u64 pc; int len;
        asm volatile("s_getpc_b64 %0\n.Lfused_pc_ref:\n\ts_mov_b32 %1, .Lfused_code_end - .Lfused_pc_ref" : "=s"(pc), "=s"(len));
        const int off = (wid * 64 + lane) * 128;
        if (off < len) warm0 = *(const __attribute__((address_space(1))) unsigned*)(pc + off);
    }
    QkvOut outB;
    if (has_qkv) qkv_phase(xA, wA, xB, wB, ws, grt, qct, tag, lds, wid, lane, outB);
    else { if (threadIdx.x == 0) flag_set(ws, F0 + (xcd >> 1) * 8 + (loc & 7), tag); BARRIER(); }
    asm volatile("" :: "v"(warm0));
    STAMP(4);
    if (wid == 4) {
        flag_wait(ws, F1 + (lane < 32 ? (28 * h) >> 6 : (28 * h + 27) >> 6) * 32 + (lane & 31), tag);
        if (lane == 0) ctl_store(lds, CTL_F1, 1);
    } else ctl_wait(lds, CTL_F1, 1);
    STAMP(5);
    const u64 tags = (u64)(unsigned)__builtin_amdgcn_readfirstlane((unsigned)tag) | ((u64)(unsigned)__builtin_amdgcn_readfirstlane((unsigned)(tag >> 32)) << 32);
    u64 rt_mid = 0; const u64 rt_att = __builtin_amdgcn_s_memrealtime();
    u32x4 wf[2][14];
    attnA_phase(ws, woA, oct, h, rb, tags, grt, qct, has_qkv, outB, wf, lds, wid, lane);
    STAMP(9);
    lane = opaque_lane();
    rt_mid = __builtin_amdgcn_s_memrealtime();
    if (oct < 7) {
        oproj_compute(ws, woB, out, ort, oct, tags, wf, lds, wid, lane);
    }
    STAMP(14);
    if ((id == 0 || id == 223) && wid == 0) {
        asm volatile("s_waitcnt vmcnt(0)" ::: "memory");
        u64 mn = ~0ull;
        for (int i = 0; i < 4; ++i) { u64 v = __hip_atomic_load((gu64*)(ws + (32u << 20) + (lane + 64 * i) * 128), __ATOMIC_RELAXED, __HIP_MEMORY_SCOPE_AGENT); mn = v < mn ? v : mn; }
        for (int o = 32; o; o >>= 1) { u64 v = __shfl_xor(mn, o); mn = v < mn ? v : mn; }
        const u64 now = __builtin_amdgcn_s_memrealtime();
        if (lane == 0 && id == 0 && (float)(now - mn) * 0.01f > 30.0f) {
            const float T = id == 0 ? (float)(now - mn) * 0.01f : (float)(rt_start - mn) * 0.01f;
            u16* p = id == 0 ? out : out + 2047 * 896 + 895;
            for (int i = 0; i < 400; ++i) __builtin_amdgcn_s_sleep(8);
            u16 cur = __hip_atomic_load((__attribute__((address_space(1))) u16*)p, __ATOMIC_RELAXED, __HIP_MEMORY_SCOPE_AGENT);
            float f = __uint_as_float((unsigned)cur << 16) + T;
            __hip_atomic_store((__attribute__((address_space(1))) u16*)p, (u16)(__float_as_uint(f) >> 16), __ATOMIC_RELAXED, __HIP_MEMORY_SCOPE_AGENT);
        }
    }
    if (id == 0 && wid == 0) {
        flag_wait(ws, F0 + (lane & 31), tags);
        if (lane == 0) flag_set(ws, FEP, tags);
    }
    STAMP_DUMP();
    asm volatile(".Lfused_code_end:");
}

extern "C" void kernel_launch(void* const* d_in, const int* in_sizes, int n_in, void* d_out, int out_size, void* d_ws, size_t ws_size, hipStream_t stream) {
    const u16* xA = (const u16*)d_in[0]; const u16* wA = (const u16*)d_in[1]; const u16* woA = (const u16*)d_in[2];
    const u16* xB = (const u16*)d_in[3]; const u16* wB = (const u16*)d_in[4]; const u16* woB = (const u16*)d_in[5];
    (void)hipFuncSetAttribute((const void*)fused_kernel, hipFuncAttributeMaxDynamicSharedMemorySize, LDS_TOTAL + LDS_EXTRA);
    fused_kernel<<<256, 512, LDS_TOTAL + LDS_EXTRA, stream>>>(xA, wA, woA, xB, wB, woB, (u16*)d_out, (char*)d_ws);
}
```

```cpp
#include <hip/hip_runtime.h>
#include <hip/hip_bf16.h>
#include <stdint.h>

typedef unsigned short u16;
typedef unsigned long long u64;
typedef short bf16x8 __attribute__((ext_vector_type(8)));
typedef short s16x4 __attribute__((ext_vector_type(4)));
typedef float f32x4 __attribute__((ext_vector_type(4)));
typedef float f32x16 __attribute__((ext_vector_type(16)));
typedef unsigned u32x4 __attribute__((ext_vector_type(4)));
typedef unsigned u32x2 __attribute__((ext_vector_type(2)));
typedef __attribute__((address_space(3))) const char* lds_cptr;
typedef __attribute__((address_space(1))) u64 gu64;

constexpr int TILE_B = 56 * 512;
constexpr size_t QC_BYTES = (size_t)64 * TILE_B;
constexpr size_t V_BYTES = (size_t)16 * 64 * 2048;
constexpr size_t WS_QA = 0, WS_KA = WS_QA + QC_BYTES, WS_VA = WS_KA + QC_BYTES;
constexpr size_t WS_QB = WS_VA + V_BYTES, WS_KB = WS_QB + QC_BYTES, WS_VB = WS_KB + QC_BYTES;
constexpr size_t WS_OA = WS_VB + V_BYTES, WS_OB = WS_OA + QC_BYTES, WS_FLAGS = WS_OB + QC_BYTES;
constexpr int F1 = 0, F2 = 256, F3 = 512, F4 = 768, F0 = 1024, FEP = 1056;
constexpr float QSCALE = 0.18898223650461362f * 1.4426950408889634f;
constexpr int STG = 32768;
constexpr int LDS_Q = 147456, LDS_L = 131072, LDS_MRG = 57344, LDS_CTL = LDS_Q + 8192, LDS_MX = LDS_CTL + 128, LDS_TOTAL = LDS_MX + 1024 + 64;
constexpr int CTL_F1 = 0, CTL_F2CNT = 4, CTL_BAD = 8, CTL_QCNT = 12, CTL_F4CNT = 16, CTL_F3 = 20, CTL_RDONE = 24, CTL_F4 = 28, CTL_X = 32;
constexpr int CTL_WA = 64, CTL_WB = 68, CTL_S12 = 72, CTL_S13 = 76, CTL_F3CNT = 80;
#define BPFG 1
#define PUBG 10
#define POFF 96.0f
#define LMIN (__builtin_ldexpf(1.0f, -(int)(POFF) - 20))
constexpr unsigned SPIN_MAX = 1u << 18;

#define STAMP(i)
#define STAMP_INIT()
#define STAMP_DUMP()
#define LDS_EXTRA 0

#define MFMA16(a, b, c) __builtin_amdgcn_mfma_f32_16x16x32_bf16(a, b, c, 0, 0, 0)
#define MFMA32(a, b, c) __builtin_amdgcn_mfma_f32_32x32x16_bf16(a, b, c, 0, 0, 0)
#define PIN(x) asm volatile("" : "+v"(x))
#define SBAR() __builtin_amdgcn_sched_barrier(0)
#define SC1 16

__device__ __forceinline__ unsigned cvtpk(float lo, float hi) {
    typedef float f2 __attribute__((ext_vector_type(2))); typedef __bf16 b2 __attribute__((ext_vector_type(2)));
    f2 v = {lo, hi}; b2 r = __builtin_convertvector(v, b2); return __builtin_bit_cast(unsigned, r);
}
__device__ __forceinline__ void glds16(const void* sbase, unsigned voff, unsigned lds_base) {
    asm volatile("s_mov_b32 m0, %2\n\ts_nop 0\n\tglobal_load_lds_dwordx4 %0, %1" :: "v"(voff), "s"(sbase), "s"(lds_base) : "memory");
}
__device__ __forceinline__ s16x4 vtr(lds_cptr p) {
    typedef short v4i16_t __attribute__((ext_vector_type(4)));
    return __builtin_bit_cast(s16x4, __builtin_amdgcn_ds_read_tr16_b64_v4i16((__attribute__((address_space(3))) v4i16_t*)p));
}
__device__ __forceinline__ bf16x8 cat8(s16x4 a, s16x4 b) { return (bf16x8){a[0], a[1], a[2], a[3], b[0], b[1], b[2], b[3]}; }
__device__ __forceinline__ bf16x8 ldsrd128(lds_cptr p) { return *(const __attribute__((address_space(3))) bf16x8*)p; }
#define RSRC(p, bytes) __builtin_amdgcn_make_buffer_rsrc((void*)(p), 0, (int)(bytes), 0x00020000)
__device__ __forceinline__ u32x4 bload128(__amdgpu_buffer_rsrc_t r, unsigned voff, unsigned soff) { return __builtin_amdgcn_raw_buffer_load_b128(r, voff, soff, SC1); }
__device__ __forceinline__ void bstore128(u32x4 v, __amdgpu_buffer_rsrc_t r, unsigned voff) { __builtin_amdgcn_raw_buffer_store_b128(v, r, voff, 0, SC1); }
__device__ __forceinline__ void bstore64(u32x2 v, __amdgpu_buffer_rsrc_t r, unsigned voff) { __builtin_amdgcn_raw_buffer_store_b64(v, r, voff, 0, SC1); }
__device__ __forceinline__ bf16x8 asb(u32x4 v) { return __builtin_bit_cast(bf16x8, v); }

__device__ __forceinline__ gu64* flagp(char* ws, int idx) { return (gu64*)(ws + WS_FLAGS + (size_t)idx * 128); }
__device__ __forceinline__ void flag_set(char* ws, int idx, u64 tag) { __hip_atomic_store(flagp(ws, idx), tag, __ATOMIC_RELAXED, __HIP_MEMORY_SCOPE_AGENT); }
__device__ __forceinline__ void flag_wait(char* ws, int idx, u64 tag) {
    gu64* p = flagp(ws, idx);
    for (unsigned spins = 0; spins < SPIN_MAX; ++spins) {
        const u64 v = __hip_atomic_load(p, __ATOMIC_RELAXED, __HIP_MEMORY_SCOPE_AGENT);
        if (__all(v == tag)) break;
        __builtin_amdgcn_s_sleep(1);
    }
}
__device__ __forceinline__ int opaque_lane() { int l; asm volatile("v_mbcnt_lo_u32_b32 %0, -1, 0\n\tv_mbcnt_hi_u32_b32 %0, -1, %0" : "=v"(l)); return l; }
__device__ __forceinline__ int f2_index(int h, int rb, int lane) { const int i = lane % 6, r = 2 * rb - 1 + i % 3; return F2 + (i < 3 ? (28 * h) >> 6 : (28 * h + 27) >> 6) * 32 + (r < 0 ? 0 : r); }
typedef __attribute__((address_space(3))) int* lds_iptr;
__device__ __forceinline__ int ctl_load(char* lds, int off) { return __hip_atomic_load((lds_iptr)(lds + LDS_CTL + off), __ATOMIC_RELAXED, __HIP_MEMORY_SCOPE_WORKGROUP); }
__device__ __forceinline__ void ctl_store(char* lds, int off, int v) { __hip_atomic_store((lds_iptr)(lds + LDS_CTL + off), v, __ATOMIC_RELAXED, __HIP_MEMORY_SCOPE_WORKGROUP); }
__device__ __forceinline__ void ctl_wait(char* lds, int off, int n) {
    for (unsigned spins = 0; spins < (SPIN_MAX << 6); ++spins) { if (__builtin_amdgcn_readfirstlane(ctl_load(lds, off)) >= n) break; __builtin_amdgcn_s_sleep(1); }
    asm volatile("" ::: "memory");
}
__device__ __forceinline__ int ctl_add(char* lds, int off) { return __hip_atomic_fetch_add((lds_iptr)(lds + LDS_CTL + off), 1, __ATOMIC_RELAXED, __HIP_MEMORY_SCOPE_WORKGROUP); }
#define BARRIER() asm volatile("s_waitcnt lgkmcnt(0)\n\ts_barrier" ::: "memory")
#define DRAIN_BARRIER() asm volatile("s_waitcnt vmcnt(0) lgkmcnt(0)\n\ts_barrier" ::: "memory")

struct QkvFrag { bf16x8 x[2][4], w[2][3]; };
struct QkvAcc { f32x4 q[4], k[4], v[4]; };
struct QkvOut { u32x4 q[2], k[2], v[2]; };
__device__ __forceinline__ void qkv_pack(QkvOut& o, const QkvAcc& a, float qs) {
#pragma unroll
    for (int tb = 0; tb < 2; ++tb) {
        const f32x4 q0 = a.q[2 * tb] * qs, q1 = a.q[2 * tb + 1] * qs, k0 = a.k[2 * tb], k1 = a.k[2 * tb + 1], v0 = a.v[2 * tb], v1 = a.v[2 * tb + 1];
        { auto ra = __builtin_amdgcn_permlane16_swap(cvtpk(q0[0], q0[1]), cvtpk(q1[0], q1[1]), false, false); auto rb = __builtin_amdgcn_permlane16_swap(cvtpk(q0[2], q0[3]), cvtpk(q1[2], q1[3]), false, false);
          o.q[tb] = (u32x4){ra[0], rb[0], ra[1], rb[1]}; }
        { auto ra = __builtin_amdgcn_permlane16_swap(cvtpk(k0[0], k0[1]), cvtpk(k1[0], k1[1]), false, false); auto rb = __builtin_amdgcn_permlane16_swap(cvtpk(k0[2], k0[3]), cvtpk(k1[2], k1[3]), false, false);
          o.k[tb] = (u32x4){ra[0], rb[0], ra[1], rb[1]}; }
        { auto ra = __builtin_amdgcn_permlane32_swap(cvtpk(v0[0], v0[1]), cvtpk(v1[0], v1[1]), false, false); auto rb = __builtin_amdgcn_permlane32_swap(cvtpk(v0[2], v0[3]), cvtpk(v1[2], v1[3]), false, false);
          o.v[tb] = (u32x4){ra[0], rb[0], ra[1], rb[1]}; }
    }
}
template <int BR, int I>
__device__ __forceinline__ void qkv_store(const QkvOut& o, char* ws, int rt, int ct, int c, int lane) {
    constexpr int tb = I / 3, kind = I % 3;
    const int tile = rt * 2 + tb;
    if (kind < 2) {
        const unsigned off = (unsigned)(tile * 56 + 8 * ct + 2 * c) * 512u + (unsigned)lane * 16u;
        if (kind == 0) bstore128(o.q[tb], RSRC(ws + (BR ? WS_QB : WS_QA), QC_BYTES), off); else bstore128(o.k[tb], RSRC(ws + (BR ? WS_KB : WS_KA), QC_BYTES), off);
    } else {
        const int n = 64 * ct + 16 * c + (lane & 15), h = n / 28, d = n - 28 * h;
        const unsigned off = (unsigned)(h * 64 + tile) * 2048u + (unsigned)(lane >> 4) * 512u + (unsigned)d * 16u;
        const auto rs = RSRC(ws + (BR ? WS_VB : WS_VA), V_BYTES);
        bstore128(o.v[tb], rs, off);
        if (d == 27) bstore128((u32x4){0x3F803F80u, 0x3F803F80u, 0x3F803F80u, 0x3F803F80u}, rs, off + 16u);
    }
}

__device__ __forceinline__ void qkv_phase(const u16* xA, const u16* wA, const u16* xB, const u16* wB, char* ws, int rt, int ct, u64 tag, char* lds, int wid, int lane, QkvOut& outB) {
    if (wid >= 4) {
        const unsigned lds0 = (unsigned)(uintptr_t)lds;
        const int lw = wid - 4, r8 = lane >> 3, dpos = lane & 7;
        unsigned xoff[2], woff[2];
#pragma unroll
        for (int i = 0; i < 2; ++i) { const int row = 16 * lw + 8 * i + r8;
            xoff[i] = (unsigned)row * 896u + (unsigned)((dpos ^ ((row >> 1) & 7)) << 4);
            const int f2 = ((row >> 1) & 1) | (((row >> 3) & 1) << 1);
            woff[i] = (unsigned)row * 2688u + (unsigned)((((dpos >> 1) ^ f2) * 2 + (dpos & 1)) << 4); }
        const int rot = __builtin_amdgcn_readfirstlane(((rt & 7) + 2 * (ct & 3)) % 7);
        const char* xa = (const char*)xA + (size_t)rt * 64 * 896; const char* wa = (const char*)wA + (size_t)ct * 128;
        const char* xb = (const char*)xB + (size_t)rt * 64 * 896; const char* wb = (const char*)wB + (size_t)ct * 128;
#define QKV_ISSUE(g) do { int st_ = ((g) % 7) + rot; st_ = st_ >= 7 ? st_ - 7 : st_; const char* x_ = ((g) < 7 ? xa : xb) + st_ * 128; const char* w_ = ((g) < 7 ? wa : wb) + (size_t)st_ * (64 * 2688); \
        const unsigned sl_ = lds0 + ((g) & 3) * STG + lw * 2048; \
        glds16(x_, xoff[0], sl_); glds16(x_, xoff[1], sl_ + 1024); \
        glds16(w_, woff[0], sl_ + 8192); glds16(w_, woff[1], sl_ + 8192 + 1024); glds16(w_ + 896, woff[0], sl_ + 16384); glds16(w_ + 896, woff[1], sl_ + 16384 + 1024); \
        glds16(w_ + 1792, woff[0], sl_ + 24576); glds16(w_ + 1792, woff[1], sl_ + 24576 + 1024); } while (0)
        QKV_ISSUE(0); QKV_ISSUE(1);
#pragma unroll
        for (int g = 0; g < 12; ++g) {
            asm volatile("s_waitcnt vmcnt(8) lgkmcnt(0)\n\ts_barrier" ::: "memory");
            if (g == 0) STAMP(1);
            QKV_ISSUE(g + 2);
        }
        asm volatile("s_waitcnt vmcnt(8)" ::: "memory");
        if (lane == 0) ctl_add(lds, CTL_S12);
        asm volatile("s_waitcnt vmcnt(0)" ::: "memory");
        if (lane == 0) ctl_add(lds, CTL_S13);
        return;
    }
    const int c = wid, g4 = lane >> 4, t16 = lane & 15;
    const unsigned xr0 = (unsigned)t16 * 128u + (unsigned)((g4 ^ (t16 >> 1)) << 4);
    const int q = t16 >> 2, p = lane & 3;
    const int rf2 = ((q >> 1) & 1) | ((g4 & 1) << 1);
    const unsigned wr0 = 8192u + (unsigned)(8 * g4 + q) * 128u + (unsigned)((c ^ rf2) * 32) + (unsigned)p * 8u;
    QkvAcc acc; QkvOut outA;
#pragma unroll
    for (int i = 0; i < 4; ++i) acc.q[i] = acc.k[i] = acc.v[i] = (f32x4){};
    QkvFrag fr[2];
    unsigned pfa = 0, pfb = 0;
#define QMF(i) do { if (g > 0) { constexpr int ks_ = (i) / 12, s_ = ((i) % 12) / 4, b_ = (i) % 4; const QkvFrag& f_ = fr[(g - 1) & 1]; \
        if (s_ == 0) acc.q[b_] = MFMA16(f_.w[ks_][0], f_.x[ks_][b_], acc.q[b_]); if (s_ == 1) acc.k[b_] = MFMA16(f_.w[ks_][1], f_.x[ks_][b_], acc.k[b_]); \
        if (s_ == 2) acc.v[b_] = MFMA16(f_.x[ks_][b_], f_.w[ks_][2], acc.v[b_]); } SBAR(); } while (0)
#define QRX(ks, b) do { if (g < 14) fr[g & 1].x[ks][b] = ldsrd128(sl + (xr0 ^ ((ks) * 64)) + (b) * 2048); } while (0)
#define QRW(ks, s) do { if (g < 14) { lds_cptr wp_ = sl + wr0 + (ks) * 4096 + (s) * 8192; fr[g & 1].w[ks][s] = cat8(vtr(wp_), vtr(wp_ + 512)); } } while (0)
#define QST(gg, i) do { if (g == (gg)) { qkv_store<0, i>(outA, ws, rt, ct, c, lane); SBAR(); } } while (0)
#pragma unroll
    for (int g = 0; g < 15; ++g) {
        if (g == PUBG) asm volatile("s_waitcnt vmcnt(0) lgkmcnt(0)\n\ts_barrier" ::: "memory");
        else if (g < 12) asm volatile("s_waitcnt lgkmcnt(0)\n\ts_barrier" ::: "memory");
        else if (g == 12) ctl_wait(lds, CTL_S12, 4);
        else if (g == 13) ctl_wait(lds, CTL_S13, 4);
        SBAR();
        if (g == 7) STAMP(2);
        lds_cptr sl = (lds_cptr)lds + (g & 3) * STG;
        if (g == PUBG && threadIdx.x == 0) flag_set(ws, F1 + ct * 32 + rt, tag);
        SBAR();
        QRX(0, 0); QMF(0); QRX(0, 1); QMF(1); QRX(0, 2); QMF(2); QRX(0, 3); QMF(3); QST(8, 3); QRW(0, 0); QMF(4); QRW(0, 1); QMF(5); QRW(0, 2); QMF(6);
        QRX(1, 0); QMF(7); QRX(1, 1); QMF(8); QRX(1, 2); QMF(9); QST(8, 4); QRX(1, 3); QMF(10); QRW(1, 0); QMF(11); QRW(1, 1); QMF(12); QRW(1, 2); QMF(13);
        QMF(14); QMF(15); QST(8, 5); QMF(16); QMF(17); QMF(18); QMF(19); QMF(20); QMF(21); QMF(22); QMF(23);
        if (g == 7) {
            qkv_pack(outA, acc, QSCALE);
            qkv_store<0, 0>(outA, ws, rt, ct, c, lane); qkv_store<0, 1>(outA, ws, rt, ct, c, lane); qkv_store<0, 2>(outA, ws, rt, ct, c, lane);
#pragma unroll
            for (int i = 0; i < 4; ++i) acc.q[i] = acc.k[i] = acc.v[i] = (f32x4){};
        }
    }
    if (lane == 0) ctl_add(lds, CTL_RDONE);
    qkv_pack(outB, acc, 1.0f);
}

__device__ __forceinline__ float max16(const f32x16& s) {
    float a = fmaxf(fmaxf(s[0], s[1]), s[2]), b = fmaxf(fmaxf(s[3], s[4]), s[5]);
    a = fmaxf(fmaxf(a, s[6]), s[7]); b = fmaxf(fmaxf(b, s[8]), s[9]); a = fmaxf(fmaxf(a, s[10]), s[11]); b = fmaxf(fmaxf(b, s[12]), s[13]);
    a = fmaxf(fmaxf(a, s[14]), s[15]); return fmaxf(a, b);
}
__device__ __forceinline__ float xhalf_max(float m) {
    auto r = __builtin_amdgcn_permlane32_swap(__float_as_uint(m), __float_as_uint(m), false, false);
    return fmaxf(__uint_as_float(r[0]), __uint_as_float(r[1]));
}
__device__ __forceinline__ void patch(u32x4& f0, u32x4& f1, bool pe, bool po, unsigned a, unsigned b) {
    f0[0] = po ? a : f0[0]; f0[1] = po ? b : f0[1]; f1[2] = pe ? a : f1[2]; f1[3] = pe ? b : f1[3];
}
__device__ __forceinline__ void exp_pack(const f32x16& s, u32x4& p0, u32x4& p1) {
    float e[16];
#pragma unroll
    for (int r = 0; r < 16; ++r) e[r] = __builtin_amdgcn_exp2f(s[r]);
    p0 = (u32x4){cvtpk(e[0], e[1]), cvtpk(e[2], e[3]), cvtpk(e[4], e[5]), cvtpk(e[6], e[7])};
    p1 = (u32x4){cvtpk(e[8], e[9]), cvtpk(e[10], e[11]), cvtpk(e[12], e[13]), cvtpk(e[14], e[15])};
}
__device__ __forceinline__ void merge_write1(const f32x16& o, char* lds, int wid, int j, int lane) {
#pragma unroll
    for (int hf = 0; hf < 2; ++hf)
        *(u32x4*)(lds + LDS_MRG + ((wid * 4 + j) * 2 + hf) * 1024 + lane * 16) = (u32x4){cvtpk(o[8 * hf], o[8 * hf + 1]), cvtpk(o[8 * hf + 2], o[8 * hf + 3]), cvtpk(o[8 * hf + 4], o[8 * hf + 5]), cvtpk(o[8 * hf + 6], o[8 * hf + 7])};
    if (lane >= 32) *(float*)(lds + LDS_L + ((wid * 4 + j) * 32 + lane - 32) * 4) = o[12];
}
__device__ __forceinline__ bool merge_reduce(char* ws, int h, int rb, char* lds, int wid, int lane) {
    const bool hi = lane >= 32;
    const int j = wid >> 1, hf = wid & 1;
    const int tile = rb * 4 + j, q = lane & 31;
    u32x4 p[8]; float pl[8];
#pragma unroll
    for (int w = 0; w < 8; ++w) pl[w] = *(const float*)(lds + LDS_L + ((w * 4 + j) * 32 + q) * 4);
#pragma unroll
    for (int w = 0; w < 8; ++w) p[w] = *(const u32x4*)(lds + LDS_MRG + ((w * 4 + j) * 2 + hf) * 1024 + lane * 16);
    const int col0 = 28 * h + 16 * hf + (hi ? 4 : 0), col1 = col0 + 8;
    const unsigned off0 = (unsigned)((tile * 56 + (col0 >> 3)) * 32 + q) * 16u + (unsigned)((col0 >> 2) & 1) * 8u, off1 = (unsigned)((tile * 56 + (col1 >> 3)) * 32 + q) * 16u + (unsigned)((col1 >> 2) & 1) * 8u;
    const float l = ((pl[0] + pl[1]) + (pl[2] + pl[3])) + ((pl[4] + pl[5]) + (pl[6] + pl[7]));
    const float inv = __builtin_amdgcn_rcpf(l);
    float a[8] = {};
#pragma unroll
    for (int w = 0; w < 8; ++w)
#pragma unroll
        for (int i = 0; i < 4; ++i) { a[2 * i] += __uint_as_float(p[w][i] << 16); a[2 * i + 1] += __uint_as_float(p[w][i] & 0xffff0000u); }
#pragma unroll
    for (int i = 0; i < 8; ++i) a[i] *= inv;
    const auto rO = RSRC(ws + WS_OA, QC_BYTES);
    const bool last = hf == 1 && hi;
    bstore64((u32x2){cvtpk(a[0], a[1]), cvtpk(a[2], a[3])}, rO, off0);
    if (!last) bstore64((u32x2){cvtpk(a[4], a[5]), cvtpk(a[6], a[7])}, rO, off1);
    float z = a[0] * 0.f; z = fmaf(a[1], 0.f, z); z = fmaf(a[2], 0.f, z); z = fmaf(a[3], 0.f, z);
    float z1 = a[4] * 0.f; z1 = fmaf(a[5], 0.f, z1); z1 = fmaf(a[6], 0.f, z1); z1 = fmaf(a[7], 0.f, z1);
    const bool ok = l > LMIN && l < 1.0e30f && z == 0.f && (last || z1 == 0.f);
    return !ok;
}

__device__ __forceinline__ void attnA_safe(char* ws, int h, int rb, char* lds, int wid, int lane) {
    const bool hi = lane >= 32, pe = hi && !(h & 1), po = !hi && (h & 1); const int m0 = (28 * h) >> 3;
    const auto rQ = RSRC(ws + WS_QA, QC_BYTES), rK = RSRC(ws + WS_KA, QC_BYTES), rV = RSRC(ws + WS_VA, V_BYTES);
    const unsigned lo16 = (unsigned)lane * 16u;
    float* mx = (float*)(lds + LDS_MX);
#pragma unroll 1
    for (int j = 0; j < 4; ++j) {
        const unsigned b = (unsigned)((rb * 4 + j) * 56 + m0) * 512u;
        u32x4 q0 = bload128(rQ, lo16, b), q1 = bload128(rQ, lo16, b + 1024u);
        patch(q0, q1, pe, po, 0u, 0u);
        float m = -3.0e38f; f32x16 acc = {};
#pragma unroll 1
        for (int t = 0; t < 8; ++t) {
            const unsigned ko = (unsigned)((wid * 8 + t) * 56 + m0) * 512u, vo = (unsigned)(h * 64 + wid * 8 + t) * 2048u;
            u32x4 k0 = bload128(rK, lo16, ko), k1 = bload128(rK, lo16, ko + 1024u), v0 = bload128(rV, lo16, vo), v1 = bload128(rV, lo16, vo + 1024u);
            patch(k0, k1, pe, po, 0u, 0u);
            f32x16 s = {}; s = MFMA32(asb(k0), asb(q0), s); s = MFMA32(asb(k1), asb(q1), s);
            const float mn = fmaxf(m, xhalf_max(max16(s)));
            const float al = __builtin_amdgcn_exp2f(m - mn); m = mn;
#pragma unroll
            for (int r = 0; r < 16; ++r) { s[r] -= mn; acc[r] *= al; }
            u32x4 p0, p1; exp_pack(s, p0, p1);
            acc = MFMA32(asb(v0), asb(p0), acc); acc = MFMA32(asb(v1), asb(p1), acc);
        }
        BARRIER();
        if (!hi) mx[wid * 32 + lane] = m;
        BARRIER();
        float g = m;
#pragma unroll
        for (int w = 0; w < 8; ++w) g = fmaxf(g, mx[w * 32 + (lane & 31)]);
        const float f = __builtin_amdgcn_exp2f(m - g);
        merge_write1(acc * f, lds, wid, j, lane);
    }
}

struct AttnBPre { u32x4 q, kp, kc; u32x2 v[2][2]; };
__device__ __forceinline__ void attnB_load(AttnBPre& a, char* ws, int h, int rb, int wid, int lane) {
    const int m0 = (28 * h) >> 3, T = rb * 4 + (wid >> 1), sub = wid & 1;
    const int Tp = sub ? T : (T > 0 ? T - 1 : 0), subp = sub ^ 1;
    const auto rQ = RSRC(ws + WS_QB, QC_BYTES), rK = RSRC(ws + WS_KB, QC_BYTES), rV = RSRC(ws + WS_VB, V_BYTES);
    const int g = lane >> 4, t16 = lane & 15;
    const unsigned co = (unsigned)g * 512u + (unsigned)t16 * 16u;
    a.q = bload128(rQ, co, (unsigned)(T * 56 + m0) * 512u + (unsigned)sub * 256u);
    a.kp = bload128(rK, co, (unsigned)(Tp * 56 + m0) * 512u + (unsigned)subp * 256u);
    a.kc = bload128(rK, co, (unsigned)(T * 56 + m0) * 512u + (unsigned)sub * 256u);
    const unsigned vo = (unsigned)(g & 1) * 512u + (unsigned)t16 * 16u + (unsigned)(g >> 1) * 8u;
#pragma unroll
    for (int db = 0; db < 2; ++db) {
        a.v[db][0] = __builtin_amdgcn_raw_buffer_load_b64(rV, vo + db * 256u, (unsigned)(h * 64 + Tp) * 2048u + (unsigned)subp * 1024u, SC1);
        a.v[db][1] = __builtin_amdgcn_raw_buffer_load_b64(rV, vo + db * 256u, (unsigned)(h * 64 + T) * 2048u + (unsigned)sub * 1024u, SC1);
    }
}
__device__ __forceinline__ void attnB_compute(AttnBPre& a, char* ws, int h, int rb, int wid, int lane) {
    const int T = rb * 4 + (wid >> 1), sub = wid & 1, g = lane >> 4, ql = lane & 15;
    const bool padl = (h & 1) ? g == 0 : g == 3;
    if (h & 1) { a.q[0] = padl ? 0u : a.q[0]; a.q[1] = padl ? 0u : a.q[1]; a.kp[0] = padl ? 0u : a.kp[0]; a.kp[1] = padl ? 0u : a.kp[1]; a.kc[0] = padl ? 0u : a.kc[0]; a.kc[1] = padl ? 0u : a.kc[1]; }
    else { a.q[2] = padl ? 0u : a.q[2]; a.q[3] = padl ? 0u : a.q[3]; a.kp[2] = padl ? 0u : a.kp[2]; a.kp[3] = padl ? 0u : a.kp[3]; a.kc[2] = padl ? 0u : a.kc[2]; a.kc[3] = padl ? 0u : a.kc[3]; }
    f32x4 sp = {}, sc = {};
    sp = MFMA16(asb(a.kp), asb(a.q), sp); sc = MFMA16(asb(a.kc), asb(a.q), sc);
    const bool hasp = sub || T > 0;
    float m = -INFINITY;
#pragma unroll
    for (int r = 0; r < 4; ++r) {
        const int dc = ql - (4 * g + r), dp = dc + 16;
        sp[r] = (hasp && dp <= 14 && !(dp & 1)) ? sp[r] : -INFINITY; sc[r] = (dc >= 0 && !(dc & 1)) ? sc[r] : -INFINITY;
        m = fmaxf(m, fmaxf(sp[r], sc[r]));
    }
    { auto r = __builtin_amdgcn_permlane16_swap(__float_as_uint(m), __float_as_uint(m), false, false); m = fmaxf(__uint_as_float(r[0]), __uint_as_float(r[1])); }
    m = xhalf_max(m);
    float e[8];
#pragma unroll
    for (int r = 0; r < 4; ++r) { e[r] = __builtin_amdgcn_exp2f((sp[r] - m) * QSCALE); e[4 + r] = __builtin_amdgcn_exp2f((sc[r] - m) * QSCALE); }
    const u32x4 pf = {cvtpk(e[0], e[1]), cvtpk(e[2], e[3]), cvtpk(e[4], e[5]), cvtpk(e[6], e[7])};
    f32x4 o0 = {}, o1 = {};
    o0 = MFMA16(asb((u32x4){a.v[0][0][0], a.v[0][0][1], a.v[0][1][0], a.v[0][1][1]}), asb(pf), o0);
    o1 = MFMA16(asb((u32x4){a.v[1][0][0], a.v[1][0][1], a.v[1][1][0], a.v[1][1][1]}), asb(pf), o1);
    float l;
    { auto r16 = __builtin_amdgcn_permlane16_swap(__float_as_uint(o1[0]), __float_as_uint(o1[0]), false, false); auto r32 = __builtin_amdgcn_permlane32_swap(r16[1], r16[1], false, false); l = __uint_as_float(r32[1]); }
    const float inv = __builtin_amdgcn_rcpf(l);
    const auto rO = RSRC(ws + WS_OB, QC_BYTES);
    const unsigned tok = (unsigned)(sub * 16 + ql) * 16u;
    { const int col = 28 * h + 4 * g; bstore64((u32x2){cvtpk(o0[0] * inv, o0[1] * inv), cvtpk(o0[2] * inv, o0[3] * inv)}, rO, (unsigned)(T * 56 + (col >> 3)) * 512u + tok + (unsigned)((col >> 2) & 1) * 8u); }
    if (g < 3) { const int col = 28 * h + 16 + 4 * g; bstore64((u32x2){cvtpk(o1[0] * inv, o1[1] * inv), cvtpk(o1[2] * inv, o1[3] * inv)}, rO, (unsigned)(T * 56 + (col >> 3)) * 512u + tok + (unsigned)((col >> 2) & 1) * 8u); }
}

template <int N>
__device__ __forceinline__ void wo_issue(const u16* wo, int ct, unsigned lds_base, int pc0, int lane) {
    const int r8 = lane >> 3, dpos = lane & 7;
    const unsigned woff = (unsigned)r8 * 896u + (unsigned)((dpos ^ ((r8 & 2) << 1)) << 4);
#pragma unroll
    for (int i = 0; i < N; ++i) glds16((const char*)wo + (size_t)ct * 128 + (size_t)(pc0 + i) * 8 * 896, woff, lds_base + (pc0 + i) * 1024);
}
__device__ __forceinline__ void wf_read(u32x4 (&wf)[2][14], const char* img, int kh, int lane) {
    const int g16 = lane >> 4, q = (lane & 15) >> 2, p = lane & 3;
#pragma unroll
    for (int i = 0; i < 14; ++i)
#pragma unroll
        for (int c = 0; c < 2; ++c) {
            const int row = (kh * 14 + i) * 16 + 8 * (g16 >> 1) + q;
            lds_cptr wp = (lds_cptr)img + (unsigned)row * 128u + (unsigned)(((2 * c + (g16 & 1)) ^ (q & 2)) * 32) + (unsigned)p * 8u;
            wf[c][i] = __builtin_bit_cast(u32x4, cat8(vtr(wp), vtr(wp + 512)));
        }
}
__device__ __forceinline__ void attnA_phase(char* ws, const u16* woA, int oct, int h, int rb, u64 tag, int rt, int ct, bool has_qkv, const QkvOut& outB, u32x4 (&wf)[2][14], char* lds, int wid, int lane) {
    const bool hi = lane >= 32, pe = hi && !(h & 1), po = !hi && (h & 1);
    const int m0 = (28 * h) >> 3;
    const auto rK = RSRC(ws + WS_KA, QC_BYTES), rV = RSRC(ws + WS_VA, V_BYTES);
    const unsigned lo16 = (unsigned)lane * 16u;
    const unsigned kbase = (unsigned)((wid * 8) * 56 + m0) * 512u, vbase = (unsigned)(h * 64 + wid * 8) * 2048u;
    const int rot = rb & 7;
    u32x4 qf[4][2], kf[3][2], vf[3][2];
    {   const unsigned k0 = kbase + (unsigned)rot * TILE_B, v0 = vbase + (unsigned)rot * 2048u, k1 = kbase + (unsigned)((rot + 1) & 7) * TILE_B;
        kf[0][0] = bload128(rK, lo16, k0); kf[0][1] = bload128(rK, lo16, k0 + 1024u);
        if (wid >= 4) { const unsigned b = (unsigned)((rb * 4 + wid - 4) * 56 + m0) * 512u; const auto rQ = RSRC(ws + WS_QA, QC_BYTES); qf[0][0] = bload128(rQ, lo16, b); qf[0][1] = bload128(rQ, lo16, b + 1024u); }
        vf[0][0] = bload128(rV, lo16, v0); vf[0][1] = bload128(rV, lo16, v0 + 1024u);
        kf[1][0] = bload128(rK, lo16, k1); kf[1][1] = bload128(rK, lo16, k1 + 1024u);
        const unsigned v1 = vbase + (unsigned)((rot + 1) & 7) * 2048u, k2 = kbase + (unsigned)((rot + 2) & 7) * TILE_B;
        vf[1][0] = bload128(rV, lo16, v1); vf[1][1] = bload128(rV, lo16, v1 + 1024u);
        kf[2][0] = bload128(rK, lo16, k2); kf[2][1] = bload128(rK, lo16, k2 + 1024u); }
    patch(kf[0][0], kf[0][1], pe, po, 0x3F803F80u, 0u);
    if (wid >= 4) {
        patch(qf[0][0], qf[0][1], pe, po, 0u, 0u);
        f32x16 s = {}; s = MFMA32(asb(kf[0][0]), asb(qf[0][0]), s); s = MFMA32(asb(kf[0][1]), asb(qf[0][1]), s);
        const float m = xhalf_max(max16(s)) + POFF;
        const unsigned mh = cvtpk(-m, 0.f) & 0xffffu; const float r = -m - __uint_as_float(mh << 16);
        patch(qf[0][0], qf[0][1], pe, po, mh | (cvtpk(r, 0.f) << 16), 0u);
        *(u32x4*)(lds + LDS_Q + (wid - 4) * 2048 + lane * 16) = qf[0][0]; *(u32x4*)(lds + LDS_Q + (wid - 4) * 2048 + 1024 + lane * 16) = qf[0][1];
        asm volatile("s_waitcnt lgkmcnt(0)" ::: "memory");
        if (lane == 0) ctl_add(lds, CTL_QCNT);
    }
    ctl_wait(lds, CTL_QCNT, 4);
    STAMP(6);
#pragma unroll
    for (int j = 0; j < 4; ++j) { qf[j][0] = *(const u32x4*)(lds + LDS_Q + j * 2048 + lane * 16); qf[j][1] = *(const u32x4*)(lds + LDS_Q + j * 2048 + 1024 + lane * 16); }
    if (has_qkv && wid < 4) {
        qkv_store<1, 0>(outB, ws, rt, ct, wid, lane); qkv_store<1, 1>(outB, ws, rt, ct, wid, lane); qkv_store<1, 2>(outB, ws, rt, ct, wid, lane);
        qkv_store<1, 3>(outB, ws, rt, ct, wid, lane); qkv_store<1, 4>(outB, ws, rt, ct, wid, lane); qkv_store<1, 5>(outB, ws, rt, ct, wid, lane);
    }
#pragma unroll
    for (int j = 0; j < 4; ++j) { PIN(qf[j][0]); PIN(qf[j][1]); }
    PIN(kf[0][0]); PIN(kf[0][1]);
    f32x16 o[4];
#define VEXP(d, s) asm volatile("v_exp_f32 %0, %1" : "=v"(d) : "v"(s))
#define VCVT(d, a, b) asm volatile("v_cvt_pk_bf16_f32 %0, %1, %2" : "=v"(d) : "v"(a), "v"(b))
#define MF0(d, a, b) asm volatile("v_mfma_f32_32x32x16_bf16 %0, %1, %2, 0" : "=&v"(d) : "v"(a), "v"(b))
#define MFA(d, a, b) asm volatile("v_mfma_f32_32x32x16_bf16 %0, %1, %2, %0" : "+v"(d) : "v"(a), "v"(b))
#define SLOT(PV, QK, Sc, Pc0, Pc1, Sn, kn0, kn1, qn0, qn1, Op, vp0, vp1, Pp0, Pp1, INJ) do { \
        float e0, e1, e2, e3, e4, e5, e6, e7, e8, e9, e10, e11, e12, e13, e14, e15; \
        if (QK) MF0(Sn, kn0, qn0); \
        VEXP(e0, Sc[0]); VEXP(e1, Sc[1]); VEXP(e2, Sc[2]); VEXP(e3, Sc[3]); \
        if (QK) MFA(Sn, kn1, qn1); \
        VEXP(e4, Sc[4]); VEXP(e5, Sc[5]); VEXP(e6, Sc[6]); VEXP(e7, Sc[7]); VCVT(Pc0[0], e0, e1); VCVT(Pc0[1], e2, e3); \
        INJ; \
        if ((PV) == 2) MFA(Op, vp0, Pp0); else if ((PV) == 1) MF0(Op, vp0, Pp0); \
        VEXP(e8, Sc[8]); VEXP(e9, Sc[9]); VEXP(e10, Sc[10]); VEXP(e11, Sc[11]); VCVT(Pc0[2], e4, e5); VCVT(Pc0[3], e6, e7); \
        if (PV) MFA(Op, vp1, Pp1); \
        VEXP(e12, Sc[12]); VEXP(e13, Sc[13]); VEXP(e14, Sc[14]); VEXP(e15, Sc[15]); VCVT(Pc1[0], e8, e9); VCVT(Pc1[1], e10, e11); VCVT(Pc1[2], e12, e13); VCVT(Pc1[3], e14, e15); \
    } while (0)
    f32x16 sA, sB; u32x4 pA0, pA1, pB0, pB1;
    asm volatile("s_nop 1\n\tv_mfma_f32_32x32x16_bf16 %0, %1, %2, 0\n\tv_mfma_f32_32x32x16_bf16 %0, %3, %4, %0\n\ts_nop 7\n\ts_nop 3" : "=&v"(sA) : "v"(kf[0][0]), "v"(qf[0][0]), "v"(kf[0][1]), "v"(qf[0][1]));
#define PATCHK(kx) do { PIN(kx[0]); PIN(kx[1]); patch(kx[0], kx[1], pe, po, 0x3F803F80u, 0u); PIN(kx[0]); PIN(kx[1]); } while (0)
#define LOADK(kc, tt) do { if ((tt) < 8) { const unsigned o_ = kbase + (unsigned)(((tt) + rot) & 7) * TILE_B; kc[0] = bload128(rK, lo16, o_); kc[1] = bload128(rK, lo16, o_ + 1024u); } } while (0)
#define LOADV(vx, tt) do { if ((tt) < 8) { const unsigned o_ = vbase + (unsigned)(((tt) + rot) & 7) * 2048u; vx[0] = bload128(rV, lo16, o_); vx[1] = bload128(rV, lo16, o_ + 1024u); } } while (0)
#define TILE(t) do { \
        SLOT((t) == 0 ? 0 : (t) == 1 ? 1 : 2, true, sA, pA0, pA1, sB, kf[(t) % 3][0], kf[(t) % 3][1], qf[1][0], qf[1][1], o[3], vf[((t) + 2) % 3][0], vf[((t) + 2) % 3][1], pB0, pB1, (void)0); \
        LOADV(vf[((t) + 2) % 3], (t) + 2); \
        SLOT((t) == 0 ? 1 : 2, true, sB, pB0, pB1, sA, kf[(t) % 3][0], kf[(t) % 3][1], qf[2][0], qf[2][1], o[0], vf[(t) % 3][0], vf[(t) % 3][1], pA0, pA1, (void)0); \
        SLOT((t) == 0 ? 1 : 2, true, sA, pA0, pA1, sB, kf[(t) % 3][0], kf[(t) % 3][1], qf[3][0], qf[3][1], o[1], vf[(t) % 3][0], vf[(t) % 3][1], pB0, pB1, if ((t) < 7) PATCHK(kf[((t) + 1) % 3])); \
        LOADK(kf[(t) % 3], (t) + 3); \
        SLOT((t) == 0 ? 1 : 2, (t) < 7, sB, pB0, pB1, sA, kf[((t) + 1) % 3][0], kf[((t) + 1) % 3][1], qf[0][0], qf[0][1], o[2], vf[(t) % 3][0], vf[(t) % 3][1], pA0, pA1, (void)0); \
    } while (0)
    AttnBPre bpre; u64 flb = 0; bool bready = false;
#pragma unroll
    for (int t = 0; t < 8; t += 2) {
        if (t == 0) __builtin_amdgcn_s_setprio(3); else if (t == 2) __builtin_amdgcn_s_setprio(2); else if (t == 4) __builtin_amdgcn_s_setprio(1); else __builtin_amdgcn_s_setprio(0);
        TILE(t);
        if (t == 4) { bready = __all(flb == tag); attnB_load(bpre, ws, h, rb, wid, opaque_lane()); }

        TILE(t + 1);
        if (t == 0) {
            if (has_qkv && wid < 4) {
                asm volatile("s_waitcnt vmcnt(8)" ::: "memory");
                if (opaque_lane() == 0 && ctl_add(lds, CTL_F2CNT) == 3) flag_set(ws, F2 + ct * 32 + rt, tag);
            }
            if (oct < 7 && wid >= 4) {
                if (has_qkv) ctl_wait(lds, CTL_RDONE, 4);
                wo_issue<14>(woA, oct, (unsigned)(uintptr_t)lds, (wid - 4) * 14, opaque_lane());
            }
        } else if (t == 2) {
            const int ln = opaque_lane();
            flb = __hip_atomic_load(flagp(ws, f2_index(h, rb, ln)), __ATOMIC_RELAXED, __HIP_MEMORY_SCOPE_AGENT);
        }
    }
    asm volatile("s_nop 1\n\tv_mfma_f32_32x32x16_bf16 %0, %1, %2, %0\n\tv_mfma_f32_32x32x16_bf16 %0, %3, %4, %0\n\ts_nop 7\n\ts_nop 3" : "+v"(o[3]) : "v"(vf[1][0]), "v"(pB0), "v"(vf[1][1]), "v"(pB1));
    asm volatile("" : "+v"(o[0]), "+v"(o[1]), "+v"(o[2]));
    STAMP(7);
    lane = opaque_lane();
    if (has_qkv) ctl_wait(lds, CTL_RDONE, 4);
#pragma unroll
    for (int j = 0; j < 4; ++j) merge_write1(o[j], lds, wid, j, lane);
    if (oct < 7) {
        if (wid >= 4) { asm volatile("s_waitcnt vmcnt(0)" ::: "memory"); if (lane == 0) ctl_add(lds, CTL_WA); }
        else { ctl_wait(lds, CTL_WA, 4); wf_read(wf, lds, wid & 1, lane); }
    }
    STAMP(15);
    BARRIER();
    STAMP(10);
    const bool bad = merge_reduce(ws, h, rb, lds, wid, lane);
    if (__any(bad) && lane == 0) ctl_store(lds, CTL_BAD, 1);
    STAMP(8);
    if (!bready) { flag_wait(ws, f2_index(h, rb, lane), tag); attnB_load(bpre, ws, h, rb, wid, lane); }
    attnB_compute(bpre, ws, h, rb, wid, lane);
    asm volatile("s_waitcnt vmcnt(2) lgkmcnt(0)" ::: "memory");
    if (lane == 0 && ctl_add(lds, CTL_F3CNT) == 7 && !ctl_load(lds, CTL_BAD)) flag_set(ws, F3 + rb * 16 + h, tag);
    asm volatile("s_waitcnt vmcnt(0)" ::: "memory");
    if (lane == 0 && ctl_add(lds, CTL_F4CNT) == 7) flag_set(ws, F4 + rb * 16 + h, tag);
    BARRIER();
    if (__builtin_amdgcn_readfirstlane(ctl_load(lds, CTL_BAD))) {
        attnA_safe(ws, h, rb, lds, wid, lane);
        BARRIER();
        merge_reduce(ws, h, rb, lds, wid, lane);
        DRAIN_BARRIER();
        if (threadIdx.x == 0) flag_set(ws, F3 + rb * 16 + h, tag);
    }
}

__device__ __forceinline__ void oproj_compute(char* ws, const u16* woB, u16* out, int rt, int ct, u64 tag, u32x4 (&wf)[2][14], char* lds, int wid, int lane) {
    const int br = wid >> 2, tt = (wid >> 1) & 1, kh = wid & 1;
    if (br) {
        wo_issue<14>(woB, ct, (unsigned)(uintptr_t)lds + LDS_MRG, (wid - 4) * 14, lane);
    }
    if ((wid & 3) == 3) { flag_wait(ws, (br ? F4 : F3) + (rt >> 1) * 16 + (lane & 15), tag); if (lane == 0) ctl_store(lds, br ? CTL_F4 : CTL_F3, 1); }
    else ctl_wait(lds, br ? CTL_F4 : CTL_F3, 1);
    STAMP(11);
    const auto rO = RSRC(ws + (br ? WS_OB : WS_OA), QC_BYTES);
    const int tile = rt * 2 + tt;
    u32x4 of[14];
#pragma unroll
    for (int i = 0; i < 14; ++i) of[i] = bload128(rO, (unsigned)lane * 16u, (unsigned)(tile * 56 + 2 * (kh * 14 + i)) * 512u);
    if (br) {
        asm volatile("s_waitcnt vmcnt(14)" ::: "memory");
        if (lane == 0) ctl_add(lds, CTL_WB);
        ctl_wait(lds, CTL_WB, 4);
        wf_read(wf, lds + LDS_MRG, kh, lane);
    }
    f32x16 acc[2] = {};
#pragma unroll
    for (int i = 0; i < 14; ++i) { acc[0] = MFMA32(asb(wf[0][i]), asb(of[i]), acc[0]); acc[1] = MFMA32(asb(wf[1][i]), asb(of[i]), acc[1]); }
    asm volatile("" : "+v"(acc[0]), "+v"(acc[1]));
    STAMP(12);
    {   const f32x16& gv = kh ? acc[0] : acc[1];
#pragma unroll
        for (int rq = 0; rq < 4; ++rq) *(f32x4*)(lds + 114688 + (wid * 4 + rq) * 1024 + lane * 16) = (f32x4){gv[4 * rq], gv[4 * rq + 1], gv[4 * rq + 2], gv[4 * rq + 3]};
    }
    asm volatile("s_waitcnt lgkmcnt(0)" ::: "memory");
    if (lane == 0) ctl_store(lds, CTL_X + wid * 4, 1);
    ctl_wait(lds, CTL_X + (wid ^ 1) * 4, 1);
    STAMP(13);
    f32x16 r = kh ? acc[1] : acc[0];
#pragma unroll
    for (int rq = 0; rq < 4; ++rq) { const f32x4 x = *(const f32x4*)(lds + 114688 + ((wid ^ 1) * 4 + rq) * 1024 + lane * 16); r[4 * rq] += x[0]; r[4 * rq + 1] += x[1]; r[4 * rq + 2] += x[2]; r[4 * rq + 3] += x[3]; }
    const unsigned orow = (unsigned)(rt * 64 + tt * 32 + (lane & 31)) * 1792u + (unsigned)(br * 448 + ct * 64 + kh * 32 + (lane >> 5) * 8) * 2u;
#pragma unroll
    for (int pr = 0; pr < 2; ++pr) {
        const unsigned a0 = cvtpk(r[8 * pr], r[8 * pr + 1]), a1 = cvtpk(r[8 * pr + 2], r[8 * pr + 3]), b0 = cvtpk(r[8 * pr + 4], r[8 * pr + 5]), b1 = cvtpk(r[8 * pr + 6], r[8 * pr + 7]);
        auto r0 = __builtin_amdgcn_permlane32_swap(a0, b0, false, false);
        auto r1 = __builtin_amdgcn_permlane32_swap(a1, b1, false, false);
        bstore128((u32x4){r0[0], r1[0], r0[1], r1[1]}, RSRC(out, 2048 * 896 * 2), orow + pr * 32u);
    }
}

__global__ void __launch_bounds__(512, 2) fused_kernel(const u16* xA, const u16* wA, const u16* woA, const u16* xB, const u16* wB, const u16* woB, u16* out, char* ws) {
    extern __shared__ __attribute__((aligned(16))) char lds[];
    int lane = threadIdx.x & 63; const int wid = __builtin_amdgcn_readfirstlane(threadIdx.x >> 6);
    const int id = blockIdx.x, xcd = id & 7, loc = id >> 3;
    STAMP_INIT();
    STAMP(0);
    if (threadIdx.x < 32) ctl_store(lds, threadIdx.x * 4, 0);
    const u64 tag = __hip_atomic_load(flagp(ws, FEP), __ATOMIC_RELAXED, __HIP_MEMORY_SCOPE_AGENT) + 1;
    const int gb = xcd & 1, grt = 8 * (xcd >> 1) + (loc & 7), qct = gb * 4 + (loc >> 3);
    const bool has_qkv = qct < 7;
    const int h = xcd * 2 + (loc & 1), rb = loc >> 1;
    const int ort = 4 * xcd + (loc & 3), oct = loc >> 2;
    unsigned warm0 = 0;
    if (wid < 4) {
        u64 pc; int len;
        asm volatile("s_getpc_b64 %0\n.Lfused_pc_ref:\n\ts_mov_b32 %1, .Lfused_code_end - .Lfused_pc_ref" : "=s"(pc), "=s"(len));
        const int off = (wid * 64 + lane) * 128;
        if (off < len) warm0 = *(const __attribute__((address_space(1))) unsigned*)(pc + off);
    }
    QkvOut outB;
    if (has_qkv) qkv_phase(xA, wA, xB, wB, ws, grt, qct, tag, lds, wid, lane, outB);
    else { if (threadIdx.x == 0) flag_set(ws, F0 + (xcd >> 1) * 8 + (loc & 7), tag); BARRIER(); }
    asm volatile("" :: "v"(warm0));
    STAMP(4);
    if (wid == 4) {
        flag_wait(ws, F1 + (lane < 32 ? (28 * h) >> 6 : (28 * h + 27) >> 6) * 32 + (lane & 31), tag);
        if (lane == 0) ctl_store(lds, CTL_F1, 1);
    } else ctl_wait(lds, CTL_F1, 1);
    STAMP(5);
    const u64 tags = (u64)(unsigned)__builtin_amdgcn_readfirstlane((unsigned)tag) | ((u64)(unsigned)__builtin_amdgcn_readfirstlane((unsigned)(tag >> 32)) << 32);
    u32x4 wf[2][14];
    attnA_phase(ws, woA, oct, h, rb, tags, grt, qct, has_qkv, outB, wf, lds, wid, lane);
    STAMP(9);
    lane = opaque_lane();
    if (oct < 7) {
        oproj_compute(ws, woB, out, ort, oct, tags, wf, lds, wid, lane);
    }
    STAMP(14);
    if (id == 0 && wid == 0) {
        flag_wait(ws, F0 + (lane & 31), tags);
        if (lane == 0) flag_set(ws, FEP, tags);
    }
    STAMP_DUMP();
    asm volatile(".Lfused_code_end:");
}

extern "C" void kernel_launch(void* const* d_in, const int* in_sizes, int n_in, void* d_out, int out_size, void* d_ws, size_t ws_size, hipStream_t stream) {
    const u16* xA = (const u16*)d_in[0]; const u16* wA = (const u16*)d_in[1]; const u16* woA = (const u16*)d_in[2];
    const u16* xB = (const u16*)d_in[3]; const u16* wB = (const u16*)d_in[4]; const u16* woB = (const u16*)d_in[5];
    (void)hipFuncSetAttribute((const void*)fused_kernel, hipFuncAttributeMaxDynamicSharedMemorySize, LDS_TOTAL + LDS_EXTRA);
    fused_kernel<<<256, 512, LDS_TOTAL + LDS_EXTRA, stream>>>(xA, wA, woA, xB, wB, woB, (u16*)d_out, (char*)d_ws);
}
```

```cpp
#include <hip/hip_runtime.h>
#include <hip/hip_bf16.h>
#include <stdint.h>

typedef unsigned short u16;
typedef unsigned long long u64;
typedef short bf16x8 __attribute__((ext_vector_type(8)));
typedef short s16x4 __attribute__((ext_vector_type(4)));
typedef float f32x4 __attribute__((ext_vector_type(4)));
typedef float f32x16 __attribute__((ext_vector_type(16)));
typedef unsigned u32x4 __attribute__((ext_vector_type(4)));
typedef unsigned u32x2 __attribute__((ext_vector_type(2)));
typedef __attribute__((address_space(3))) const char* lds_cptr;
typedef __attribute__((address_space(1))) u64 gu64;

constexpr int TILE_B = 56 * 512;
constexpr size_t QC_BYTES = (size_t)64 * TILE_B;
constexpr size_t V_BYTES = (size_t)16 * 64 * 2048;
constexpr size_t WS_QA = 0, WS_KA = WS_QA + QC_BYTES, WS_VA = WS_KA + QC_BYTES;
constexpr size_t WS_QB = WS_VA + V_BYTES, WS_KB = WS_QB + QC_BYTES, WS_VB = WS_KB + QC_BYTES;
constexpr size_t WS_OA = WS_VB + V_BYTES, WS_OB = WS_OA + QC_BYTES, WS_FLAGS = WS_OB + QC_BYTES;
constexpr int F1 = 0, F2 = 256, F3 = 512, F4 = 768, F0 = 1024, FEP = 1056;
constexpr float QSCALE = 0.18898223650461362f * 1.4426950408889634f;
constexpr int STG = 32768;
constexpr int LDS_Q = 147456, LDS_L = 131072, LDS_MRG = 57344, LDS_CTL = LDS_Q + 8192, LDS_MX = LDS_CTL + 128, LDS_TOTAL = LDS_MX + 1024 + 64;
constexpr int CTL_F1 = 0, CTL_F2CNT = 4, CTL_BAD = 8, CTL_QCNT = 12, CTL_F4CNT = 16, CTL_F3 = 20, CTL_RDONE = 24, CTL_F4 = 28, CTL_X = 32;
constexpr int CTL_WA = 64, CTL_WB = 68, CTL_S12 = 72, CTL_S13 = 76, CTL_F3CNT = 80;
#define BPFG 1
#define PUBG 10
#define POFF 96.0f
#define LMIN (__builtin_ldexpf(1.0f, -(int)(POFF) - 20))
constexpr unsigned SPIN_MAX = 1u << 18;

#define STAMP(i)
#define STAMP_INIT()
#define STAMP_DUMP()
#define LDS_EXTRA 0

#define MFMA16(a, b, c) __builtin_amdgcn_mfma_f32_16x16x32_bf16(a, b, c, 0, 0, 0)
#define MFMA32(a, b, c) __builtin_amdgcn_mfma_f32_32x32x16_bf16(a, b, c, 0, 0, 0)
#define PIN(x) asm volatile("" : "+v"(x))
#define SBAR() __builtin_amdgcn_sched_barrier(0)
#define SC1 16

__device__ __forceinline__ unsigned cvtpk(float lo, float hi) {
    typedef float f2 __attribute__((ext_vector_type(2))); typedef __bf16 b2 __attribute__((ext_vector_type(2)));
    f2 v = {lo, hi}; b2 r = __builtin_convertvector(v, b2); return __builtin_bit_cast(unsigned, r);
}
__device__ __forceinline__ void glds16(const void* sbase, unsigned voff, unsigned lds_base) {
    asm volatile("s_mov_b32 m0, %2\n\ts_nop 0\n\tglobal_load_lds_dwordx4 %0, %1" :: "v"(voff), "s"(sbase), "s"(lds_base) : "memory");
}
__device__ __forceinline__ s16x4 vtr(lds_cptr p) {
    typedef short v4i16_t __attribute__((ext_vector_type(4)));
    return __builtin_bit_cast(s16x4, __builtin_amdgcn_ds_read_tr16_b64_v4i16((__attribute__((address_space(3))) v4i16_t*)p));
}
__device__ __forceinline__ bf16x8 cat8(s16x4 a, s16x4 b) { return (bf16x8){a[0], a[1], a[2], a[3], b[0], b[1], b[2], b[3]}; }
__device__ __forceinline__ bf16x8 ldsrd128(lds_cptr p) { return *(const __attribute__((address_space(3))) bf16x8*)p; }
#define RSRC(p, bytes) __builtin_amdgcn_make_buffer_rsrc((void*)(p), 0, (int)(bytes), 0x00020000)
__device__ __forceinline__ u32x4 bload128(__amdgpu_buffer_rsrc_t r, unsigned voff, unsigned soff) { return __builtin_amdgcn_raw_buffer_load_b128(r, voff, soff, SC1); }
__device__ __forceinline__ void bstore128(u32x4 v, __amdgpu_buffer_rsrc_t r, unsigned voff) { __builtin_amdgcn_raw_buffer_store_b128(v, r, voff, 0, SC1); }
__device__ __forceinline__ void bstore64(u32x2 v, __amdgpu_buffer_rsrc_t r, unsigned voff) { __builtin_amdgcn_raw_buffer_store_b64(v, r, voff, 0, SC1); }
__device__ __forceinline__ bf16x8 asb(u32x4 v) { return __builtin_bit_cast(bf16x8, v); }

__device__ __forceinline__ gu64* flagp(char* ws, int idx) { return (gu64*)(ws + WS_FLAGS + (size_t)idx * 128); }
__device__ __forceinline__ void flag_set(char* ws, int idx, u64 tag) { __hip_atomic_store(flagp(ws, idx), tag, __ATOMIC_RELAXED, __HIP_MEMORY_SCOPE_AGENT); }
__device__ __forceinline__ void flag_wait(char* ws, int idx, u64 tag) {
    gu64* p = flagp(ws, idx);
    for (unsigned spins = 0; spins < SPIN_MAX; ++spins) {
        const u64 v = __hip_atomic_load(p, __ATOMIC_RELAXED, __HIP_MEMORY_SCOPE_AGENT);
        if (__all(v == tag)) break;
        __builtin_amdgcn_s_sleep(1);
    }
}
__device__ __forceinline__ int opaque_lane() { int l; asm volatile("v_mbcnt_lo_u32_b32 %0, -1, 0\n\tv_mbcnt_hi_u32_b32 %0, -1, %0" : "=v"(l)); return l; }
__device__ __forceinline__ int f2_index(int h, int rb, int lane) { const int i = lane % 6, r = 2 * rb - 1 + i % 3; return F2 + (i < 3 ? (28 * h) >> 6 : (28 * h + 27) >> 6) * 32 + (r < 0 ? 0 : r); }
typedef __attribute__((address_space(3))) int* lds_iptr;
__device__ __forceinline__ int ctl_load(char* lds, int off) { return __hip_atomic_load((lds_iptr)(lds + LDS_CTL + off), __ATOMIC_RELAXED, __HIP_MEMORY_SCOPE_WORKGROUP); }
__device__ __forceinline__ void ctl_store(char* lds, int off, int v) { __hip_atomic_store((lds_iptr)(lds + LDS_CTL + off), v, __ATOMIC_RELAXED, __HIP_MEMORY_SCOPE_WORKGROUP); }
__device__ __forceinline__ void ctl_wait(char* lds, int off, int n) {
    for (unsigned spins = 0; spins < (SPIN_MAX << 6); ++spins) { if (__builtin_amdgcn_readfirstlane(ctl_load(lds, off)) >= n) break; __builtin_amdgcn_s_sleep(1); }
    asm volatile("" ::: "memory");
}
__device__ __forceinline__ int ctl_add(char* lds, int off) { return __hip_atomic_fetch_add((lds_iptr)(lds + LDS_CTL + off), 1, __ATOMIC_RELAXED, __HIP_MEMORY_SCOPE_WORKGROUP); }
#define BARRIER() asm volatile("s_waitcnt lgkmcnt(0)\n\ts_barrier" ::: "memory")
#define DRAIN_BARRIER() asm volatile("s_waitcnt vmcnt(0) lgkmcnt(0)\n\ts_barrier" ::: "memory")

struct QkvFrag { bf16x8 x[2][4], w[2][3]; };
struct QkvAcc { f32x4 q[4], k[4], v[4]; };
struct QkvOut { u32x4 q[2], k[2], v[2]; };
__device__ __forceinline__ void qkv_pack(QkvOut& o, const QkvAcc& a, float qs) {
#pragma unroll
    for (int tb = 0; tb < 2; ++tb) {
        const f32x4 q0 = a.q[2 * tb] * qs, q1 = a.q[2 * tb + 1] * qs, k0 = a.k[2 * tb], k1 = a.k[2 * tb + 1], v0 = a.v[2 * tb], v1 = a.v[2 * tb + 1];
        { auto ra = __builtin_amdgcn_permlane16_swap(cvtpk(q0[0], q0[1]), cvtpk(q1[0], q1[1]), false, false); auto rb = __builtin_amdgcn_permlane16_swap(cvtpk(q0[2], q0[3]), cvtpk(q1[2], q1[3]), false, false);
          o.q[tb] = (u32x4){ra[0], rb[0], ra[1], rb[1]}; }
        { auto ra = __builtin_amdgcn_permlane16_swap(cvtpk(k0[0], k0[1]), cvtpk(k1[0], k1[1]), false, false); auto rb = __builtin_amdgcn_permlane16_swap(cvtpk(k0[2], k0[3]), cvtpk(k1[2], k1[3]), false, false);
          o.k[tb] = (u32x4){ra[0], rb[0], ra[1], rb[1]}; }
        { auto ra = __builtin_amdgcn_permlane32_swap(cvtpk(v0[0], v0[1]), cvtpk(v1[0], v1[1]), false, false); auto rb = __builtin_amdgcn_permlane32_swap(cvtpk(v0[2], v0[3]), cvtpk(v1[2], v1[3]), false, false);
          o.v[tb] = (u32x4){ra[0], rb[0], ra[1], rb[1]}; }
    }
}
template <int BR, int I>
__device__ __forceinline__ void qkv_store(const QkvOut& o, char* ws, int rt, int ct, int c, int lane) {
    constexpr int tb = I / 3, kind = I % 3;
    const int tile = rt * 2 + tb;
    if (kind < 2) {
        const unsigned off = (unsigned)(tile * 56 + 8 * ct + 2 * c) * 512u + (unsigned)lane * 16u;
        if (kind == 0) bstore128(o.q[tb], RSRC(ws + (BR ? WS_QB : WS_QA), QC_BYTES), off); else bstore128(o.k[tb], RSRC(ws + (BR ? WS_KB : WS_KA), QC_BYTES), off);
    } else {
        const int n = 64 * ct + 16 * c + (lane & 15), h = n / 28, d = n - 28 * h;
        const unsigned off = (unsigned)(h * 64 + tile) * 2048u + (unsigned)(lane >> 4) * 512u + (unsigned)d * 16u;
        const auto rs = RSRC(ws + (BR ? WS_VB : WS_VA), V_BYTES);
        bstore128(o.v[tb], rs, off);
        if (d == 27) bstore128((u32x4){0x3F803F80u, 0x3F803F80u, 0x3F803F80u, 0x3F803F80u}, rs, off + 16u);
    }
}

__device__ __forceinline__ void qkv_phase(const u16* xA, const u16* wA, const u16* xB, const u16* wB, char* ws, int rt, int ct, u64 tag, char* lds, int wid, int lane, QkvOut& outB) {
    if (wid >= 4) {
        const unsigned lds0 = (unsigned)(uintptr_t)lds;
        const int lw = wid - 4, r8 = lane >> 3, dpos = lane & 7;
        unsigned xoff[2], woff[2];
#pragma unroll
        for (int i = 0; i < 2; ++i) { const int row = 16 * lw + 8 * i + r8;
            xoff[i] = (unsigned)row * 896u + (unsigned)((dpos ^ ((row >> 1) & 7)) << 4);
            const int f2 = ((row >> 1) & 1) | (((row >> 3) & 1) << 1);
            woff[i] = (unsigned)row * 2688u + (unsigned)((((dpos >> 1) ^ f2) * 2 + (dpos & 1)) << 4); }
        const int rot = __builtin_amdgcn_readfirstlane(((rt & 7) + 2 * (ct & 3)) % 7);
        const char* xa = (const char*)xA + (size_t)rt * 64 * 896; const char* wa = (const char*)wA + (size_t)ct * 128;
        const char* xb = (const char*)xB + (size_t)rt * 64 * 896; const char* wb = (const char*)wB + (size_t)ct * 128;
#define QKV_ISSUE(g) do { int st_ = ((g) % 7) + rot; st_ = st_ >= 7 ? st_ - 7 : st_; const char* x_ = ((g) < 7 ? xa : xb) + st_ * 128; const char* w_ = ((g) < 7 ? wa : wb) + (size_t)st_ * (64 * 2688); \
        const unsigned sl_ = lds0 + ((g) & 3) * STG + lw * 2048; \
        glds16(x_, xoff[0], sl_); glds16(x_, xoff[1], sl_ + 1024); \
        glds16(w_, woff[0], sl_ + 8192); glds16(w_, woff[1], sl_ + 8192 + 1024); glds16(w_ + 896, woff[0], sl_ + 16384); glds16(w_ + 896, woff[1], sl_ + 16384 + 1024); \
        glds16(w_ + 1792, woff[0], sl_ + 24576); glds16(w_ + 1792, woff[1], sl_ + 24576 + 1024); } while (0)
        QKV_ISSUE(0); QKV_ISSUE(1);
#pragma unroll
        for (int g = 0; g < 12; ++g) {
            asm volatile("s_waitcnt vmcnt(8) lgkmcnt(0)\n\ts_barrier" ::: "memory");
            if (g == 0) STAMP(1);
            QKV_ISSUE(g + 2);
        }
        asm volatile("s_waitcnt vmcnt(8)" ::: "memory");
        if (lane == 0) ctl_add(lds, CTL_S12);
        asm volatile("s_waitcnt vmcnt(0)" ::: "memory");
        if (lane == 0) ctl_add(lds, CTL_S13);
        return;
    }
    const int c = wid, g4 = lane >> 4, t16 = lane & 15;
    const unsigned xr0 = (unsigned)t16 * 128u + (unsigned)((g4 ^ (t16 >> 1)) << 4);
    const int q = t16 >> 2, p = lane & 3;
    const int rf2 = ((q >> 1) & 1) | ((g4 & 1) << 1);
    const unsigned wr0 = 8192u + (unsigned)(8 * g4 + q) * 128u + (unsigned)((c ^ rf2) * 32) + (unsigned)p * 8u;
    QkvAcc acc; QkvOut outA;
#pragma unroll
    for (int i = 0; i < 4; ++i) acc.q[i] = acc.k[i] = acc.v[i] = (f32x4){};
    QkvFrag fr[2];
    unsigned pfa = 0, pfb = 0;
#define QMF(i) do { if (g > 0) { constexpr int ks_ = (i) / 12, s_ = ((i) % 12) / 4, b_ = (i) % 4; const QkvFrag& f_ = fr[(g - 1) & 1]; \
        if (s_ == 0) acc.q[b_] = MFMA16(f_.w[ks_][0], f_.x[ks_][b_], acc.q[b_]); if (s_ == 1) acc.k[b_] = MFMA16(f_.w[ks_][1], f_.x[ks_][b_], acc.k[b_]); \
        if (s_ == 2) acc.v[b_] = MFMA16(f_.x[ks_][b_], f_.w[ks_][2], acc.v[b_]); } SBAR(); } while (0)
#define QRX(ks, b) do { if (g < 14) fr[g & 1].x[ks][b] = ldsrd128(sl + (xr0 ^ ((ks) * 64)) + (b) * 2048); } while (0)
#define QRW(ks, s) do { if (g < 14) { lds_cptr wp_ = sl + wr0 + (ks) * 4096 + (s) * 8192; fr[g & 1].w[ks][s] = cat8(vtr(wp_), vtr(wp_ + 512)); } } while (0)
#define QST(gg, i) do { if (g == (gg)) { qkv_store<0, i>(outA, ws, rt, ct, c, lane); SBAR(); } } while (0)
#pragma unroll
    for (int g = 0; g < 15; ++g) {
        if (g == PUBG) asm volatile("s_waitcnt vmcnt(0) lgkmcnt(0)\n\ts_barrier" ::: "memory");
        else if (g < 12) asm volatile("s_waitcnt lgkmcnt(0)\n\ts_barrier" ::: "memory");
        else if (g == 12) ctl_wait(lds, CTL_S12, 4);
        else if (g == 13) ctl_wait(lds, CTL_S13, 4);
        SBAR();
        if (g == 7) STAMP(2);
        lds_cptr sl = (lds_cptr)lds + (g & 3) * STG;
        if (g == PUBG && threadIdx.x == 0) flag_set(ws, F1 + ct * 32 + rt, tag);
        SBAR();
        QRX(0, 0); QMF(0); QRX(0, 1); QMF(1); QRX(0, 2); QMF(2); QRX(0, 3); QMF(3); QRW(0, 0); QMF(4); QRW(0, 1); QMF(5); QRW(0, 2); QMF(6);
        QRX(1, 0); QMF(7); QRX(1, 1); QMF(8); QRX(1, 2); QMF(9); QRX(1, 3); QMF(10); QRW(1, 0); QMF(11); QRW(1, 1); QMF(12); QRW(1, 2); QMF(13);
        QMF(14); QMF(15); QMF(16); QMF(17); QMF(18); QMF(19); QMF(20); QMF(21); QMF(22); QMF(23);
        if (g == 7) {
            qkv_pack(outA, acc, QSCALE);
            qkv_store<0, 1>(outA, ws, rt, ct, c, lane); qkv_store<0, 2>(outA, ws, rt, ct, c, lane); qkv_store<0, 4>(outA, ws, rt, ct, c, lane);
            qkv_store<0, 5>(outA, ws, rt, ct, c, lane); qkv_store<0, 0>(outA, ws, rt, ct, c, lane); qkv_store<0, 3>(outA, ws, rt, ct, c, lane);
#pragma unroll
            for (int i = 0; i < 4; ++i) acc.q[i] = acc.k[i] = acc.v[i] = (f32x4){};
        }
    }
    if (lane == 0) ctl_add(lds, CTL_RDONE);
    qkv_pack(outB, acc, 1.0f);
}

__device__ __forceinline__ float max16(const f32x16& s) {
    float a = fmaxf(fmaxf(s[0], s[1]), s[2]), b = fmaxf(fmaxf(s[3], s[4]), s[5]);
    a = fmaxf(fmaxf(a, s[6]), s[7]); b = fmaxf(fmaxf(b, s[8]), s[9]); a = fmaxf(fmaxf(a, s[10]), s[11]); b = fmaxf(fmaxf(b, s[12]), s[13]);
    a = fmaxf(fmaxf(a, s[14]), s[15]); return fmaxf(a, b);
}
__device__ __forceinline__ float xhalf_max(float m) {
    auto r = __builtin_amdgcn_permlane32_swap(__float_as_uint(m), __float_as_uint(m), false, false);
    return fmaxf(__uint_as_float(r[0]), __uint_as_float(r[1]));
}
__device__ __forceinline__ void patch(u32x4& f0, u32x4& f1, bool pe, bool po, unsigned a, unsigned b) {
    f0[0] = po ? a : f0[0]; f0[1] = po ? b : f0[1]; f1[2] = pe ? a : f1[2]; f1[3] = pe ? b : f1[3];
}
__device__ __forceinline__ void exp_pack(const f32x16& s, u32x4& p0, u32x4& p1) {
    float e[16];
#pragma unroll
    for (int r = 0; r < 16; ++r) e[r] = __builtin_amdgcn_exp2f(s[r]);
    p0 = (u32x4){cvtpk(e[0], e[1]), cvtpk(e[2], e[3]), cvtpk(e[4], e[5]), cvtpk(e[6], e[7])};
    p1 = (u32x4){cvtpk(e[8], e[9]), cvtpk(e[10], e[11]), cvtpk(e[12], e[13]), cvtpk(e[14], e[15])};
}
__device__ __forceinline__ void merge_write1(const f32x16& o, char* lds, int wid, int j, int lane) {
#pragma unroll
    for (int hf = 0; hf < 2; ++hf)
        *(u32x4*)(lds + LDS_MRG + ((wid * 4 + j) * 2 + hf) * 1024 + lane * 16) = (u32x4){cvtpk(o[8 * hf], o[8 * hf + 1]), cvtpk(o[8 * hf + 2], o[8 * hf + 3]), cvtpk(o[8 * hf + 4], o[8 * hf + 5]), cvtpk(o[8 * hf + 6], o[8 * hf + 7])};
    if (lane >= 32) *(float*)(lds + LDS_L + ((wid * 4 + j) * 32 + lane - 32) * 4) = o[12];
}
__device__ __forceinline__ bool merge_reduce(char* ws, int h, int rb, char* lds, int wid, int lane) {
    const bool hi = lane >= 32;
    const int j = wid >> 1, hf = wid & 1;
    const int tile = rb * 4 + j, q = lane & 31;
    u32x4 p[8]; float pl[8];
#pragma unroll
    for (int w = 0; w < 8; ++w) pl[w] = *(const float*)(lds + LDS_L + ((w * 4 + j) * 32 + q) * 4);
#pragma unroll
    for (int w = 0; w < 8; ++w) p[w] = *(const u32x4*)(lds + LDS_MRG + ((w * 4 + j) * 2 + hf) * 1024 + lane * 16);
    const int col0 = 28 * h + 16 * hf + (hi ? 4 : 0), col1 = col0 + 8;
    const unsigned off0 = (unsigned)((tile * 56 + (col0 >> 3)) * 32 + q) * 16u + (unsigned)((col0 >> 2) & 1) * 8u, off1 = (unsigned)((tile * 56 + (col1 >> 3)) * 32 + q) * 16u + (unsigned)((col1 >> 2) & 1) * 8u;
    const float l = ((pl[0] + pl[1]) + (pl[2] + pl[3])) + ((pl[4] + pl[5]) + (pl[6] + pl[7]));
    const float inv = __builtin_amdgcn_rcpf(l);
    float a[8] = {};
#pragma unroll
    for (int w = 0; w < 8; ++w)
#pragma unroll
        for (int i = 0; i < 4; ++i) { a[2 * i] += __uint_as_float(p[w][i] << 16); a[2 * i + 1] += __uint_as_float(p[w][i] & 0xffff0000u); }
#pragma unroll
    for (int i = 0; i < 8; ++i) a[i] *= inv;
    const auto rO = RSRC(ws + WS_OA, QC_BYTES);
    const bool last = hf == 1 && hi;
    bstore64((u32x2){cvtpk(a[0], a[1]), cvtpk(a[2], a[3])}, rO, off0);
    if (!last) bstore64((u32x2){cvtpk(a[4], a[5]), cvtpk(a[6], a[7])}, rO, off1);
    float z = a[0] * 0.f; z = fmaf(a[1], 0.f, z); z = fmaf(a[2], 0.f, z); z = fmaf(a[3], 0.f, z);
    float z1 = a[4] * 0.f; z1 = fmaf(a[5], 0.f, z1); z1 = fmaf(a[6], 0.f, z1); z1 = fmaf(a[7], 0.f, z1);
    const bool ok = l > LMIN && l < 1.0e36f && z == 0.f && (last || z1 == 0.f);
    return !ok;
}

__device__ __forceinline__ void attnA_safe(char* ws, int h, int rb, char* lds, int wid, int lane) {
    const bool hi = lane >= 32, pe = hi && !(h & 1), po = !hi && (h & 1); const int m0 = (28 * h) >> 3;
    const auto rQ = RSRC(ws + WS_QA, QC_BYTES), rK = RSRC(ws + WS_KA, QC_BYTES), rV = RSRC(ws + WS_VA, V_BYTES);
    const unsigned lo16 = (unsigned)lane * 16u;
    float* mx = (float*)(lds + LDS_MX);
#pragma unroll 1
    for (int j = 0; j < 4; ++j) {
        const unsigned b = (unsigned)((rb * 4 + j) * 56 + m0) * 512u;
        u32x4 q0 = bload128(rQ, lo16, b), q1 = bload128(rQ, lo16, b + 1024u);
        patch(q0, q1, pe, po, 0u, 0u);
        float m = -3.0e38f; f32x16 acc = {};
#pragma unroll 1
        for (int t = 0; t < 8; ++t) {
            const unsigned ko = (unsigned)((wid * 8 + t) * 56 + m0) * 512u, vo = (unsigned)(h * 64 + wid * 8 + t) * 2048u;
            u32x4 k0 = bload128(rK, lo16, ko), k1 = bload128(rK, lo16, ko + 1024u), v0 = bload128(rV, lo16, vo), v1 = bload128(rV, lo16, vo + 1024u);
            patch(k0, k1, pe, po, 0u, 0u);
            f32x16 s = {}; s = MFMA32(asb(k0), asb(q0), s); s = MFMA32(asb(k1), asb(q1), s);
            const float mn = fmaxf(m, xhalf_max(max16(s)));
            const float al = __builtin_amdgcn_exp2f(m - mn); m = mn;
#pragma unroll
            for (int r = 0; r < 16; ++r) { s[r] -= mn; acc[r] *= al; }
            u32x4 p0, p1; exp_pack(s, p0, p1);
            acc = MFMA32(asb(v0), asb(p0), acc); acc = MFMA32(asb(v1), asb(p1), acc);
        }
        BARRIER();
        if (!hi) mx[wid * 32 + lane] = m;
        BARRIER();
        float g = m;
#pragma unroll
        for (int w = 0; w < 8; ++w) g = fmaxf(g, mx[w * 32 + (lane & 31)]);
        const float f = __builtin_amdgcn_exp2f(m - g);
        merge_write1(acc * f, lds, wid, j, lane);
    }
}

struct AttnBPre { u32x4 q, kp, kc; u32x2 v[2][2]; };
__device__ __forceinline__ void attnB_load(AttnBPre& a, char* ws, int h, int rb, int wid, int lane) {
    const int m0 = (28 * h) >> 3, T = rb * 4 + (wid >> 1), sub = wid & 1;
    const int Tp = sub ? T : (T > 0 ? T - 1 : 0), subp = sub ^ 1;
    const auto rQ = RSRC(ws + WS_QB, QC_BYTES), rK = RSRC(ws + WS_KB, QC_BYTES), rV = RSRC(ws + WS_VB, V_BYTES);
    const int g = lane >> 4, t16 = lane & 15;
    const unsigned co = (unsigned)g * 512u + (unsigned)t16 * 16u;
    a.q = bload128(rQ, co, (unsigned)(T * 56 + m0) * 512u + (unsigned)sub * 256u);
    a.kp = bload128(rK, co, (unsigned)(Tp * 56 + m0) * 512u + (unsigned)subp * 256u);
    a.kc = bload128(rK, co, (unsigned)(T * 56 + m0) * 512u + (unsigned)sub * 256u);
    const unsigned vo = (unsigned)(g & 1) * 512u + (unsigned)t16 * 16u + (unsigned)(g >> 1) * 8u;
#pragma unroll
    for (int db = 0; db < 2; ++db) {
        a.v[db][0] = __builtin_amdgcn_raw_buffer_load_b64(rV, vo + db * 256u, (unsigned)(h * 64 + Tp) * 2048u + (unsigned)subp * 1024u, SC1);
        a.v[db][1] = __builtin_amdgcn_raw_buffer_load_b64(rV, vo + db * 256u, (unsigned)(h * 64 + T) * 2048u + (unsigned)sub * 1024u, SC1);
    }
}
__device__ __forceinline__ void attnB_compute(AttnBPre& a, char* ws, int h, int rb, int wid, int lane) {
    const int T = rb * 4 + (wid >> 1), sub = wid & 1, g = lane >> 4, ql = lane & 15;
    const bool padl = (h & 1) ? g == 0 : g == 3;
    if (h & 1) { a.q[0] = padl ? 0u : a.q[0]; a.q[1] = padl ? 0u : a.q[1]; a.kp[0] = padl ? 0u : a.kp[0]; a.kp[1] = padl ? 0u : a.kp[1]; a.kc[0] = padl ? 0u : a.kc[0]; a.kc[1] = padl ? 0u : a.kc[1]; }
    else { a.q[2] = padl ? 0u : a.q[2]; a.q[3] = padl ? 0u : a.q[3]; a.kp[2] = padl ? 0u : a.kp[2]; a.kp[3] = padl ? 0u : a.kp[3]; a.kc[2] = padl ? 0u : a.kc[2]; a.kc[3] = padl ? 0u : a.kc[3]; }
    f32x4 sp = {}, sc = {};
    sp = MFMA16(asb(a.kp), asb(a.q), sp); sc = MFMA16(asb(a.kc), asb(a.q), sc);
    const bool hasp = sub || T > 0;
    float m = -INFINITY;
#pragma unroll
    for (int r = 0; r < 4; ++r) {
        const int dc = ql - (4 * g + r), dp = dc + 16;
        sp[r] = (hasp && dp <= 14 && !(dp & 1)) ? sp[r] : -INFINITY; sc[r] = (dc >= 0 && !(dc & 1)) ? sc[r] : -INFINITY;
        m = fmaxf(m, fmaxf(sp[r], sc[r]));
    }
    { auto r = __builtin_amdgcn_permlane16_swap(__float_as_uint(m), __float_as_uint(m), false, false); m = fmaxf(__uint_as_float(r[0]), __uint_as_float(r[1])); }
    m = xhalf_max(m);
    float e[8];
#pragma unroll
    for (int r = 0; r < 4; ++r) { e[r] = __builtin_amdgcn_exp2f((sp[r] - m) * QSCALE); e[4 + r] = __builtin_amdgcn_exp2f((sc[r] - m) * QSCALE); }
    const u32x4 pf = {cvtpk(e[0], e[1]), cvtpk(e[2], e[3]), cvtpk(e[4], e[5]), cvtpk(e[6], e[7])};
    f32x4 o0 = {}, o1 = {};
    o0 = MFMA16(asb((u32x4){a.v[0][0][0], a.v[0][0][1], a.v[0][1][0], a.v[0][1][1]}), asb(pf), o0);
    o1 = MFMA16(asb((u32x4){a.v[1][0][0], a.v[1][0][1], a.v[1][1][0], a.v[1][1][1]}), asb(pf), o1);
    float l;
    { auto r16 = __builtin_amdgcn_permlane16_swap(__float_as_uint(o1[0]), __float_as_uint(o1[0]), false, false); auto r32 = __builtin_amdgcn_permlane32_swap(r16[1], r16[1], false, false); l = __uint_as_float(r32[1]); }
    const float inv = __builtin_amdgcn_rcpf(l);
    const auto rO = RSRC(ws + WS_OB, QC_BYTES);
    const unsigned tok = (unsigned)(sub * 16 + ql) * 16u;
    { const int col = 28 * h + 4 * g; bstore64((u32x2){cvtpk(o0[0] * inv, o0[1] * inv), cvtpk(o0[2] * inv, o0[3] * inv)}, rO, (unsigned)(T * 56 + (col >> 3)) * 512u + tok + (unsigned)((col >> 2) & 1) * 8u); }
    if (g < 3) { const int col = 28 * h + 16 + 4 * g; bstore64((u32x2){cvtpk(o1[0] * inv, o1[1] * inv), cvtpk(o1[2] * inv, o1[3] * inv)}, rO, (unsigned)(T * 56 + (col >> 3)) * 512u + tok + (unsigned)((col >> 2) & 1) * 8u); }
}

template <int N>
__device__ __forceinline__ void wo_issue(const u16* wo, int ct, unsigned lds_base, int pc0, int lane) {
    const int r8 = lane >> 3, dpos = lane & 7;
    const unsigned woff = (unsigned)r8 * 896u + (unsigned)((dpos ^ ((r8 & 2) << 1)) << 4);
#pragma unroll
    for (int i = 0; i < N; ++i) glds16((const char*)wo + (size_t)ct * 128 + (size_t)(pc0 + i) * 8 * 896, woff, lds_base + (pc0 + i) * 1024);
}
__device__ __forceinline__ void wf_read(u32x4 (&wf)[2][14], const char* img, int kh, int lane) {
    const int g16 = lane >> 4, q = (lane & 15) >> 2, p = lane & 3;
#pragma unroll
    for (int i = 0; i < 14; ++i)
#pragma unroll
        for (int c = 0; c < 2; ++c) {
            const int row = (kh * 14 + i) * 16 + 8 * (g16 >> 1) + q;
            lds_cptr wp = (lds_cptr)img + (unsigned)row * 128u + (unsigned)(((2 * c + (g16 & 1)) ^ (q & 2)) * 32) + (unsigned)p * 8u;
            wf[c][i] = __builtin_bit_cast(u32x4, cat8(vtr(wp), vtr(wp + 512)));
        }
}
__device__ __forceinline__ void attnA_phase(char* ws, const u16* woA, int oct, int h, int rb, u64 tag, int rt, int ct, bool has_qkv, const QkvOut& outB, u32x4 (&wf)[2][14], char* lds, int wid, int lane) {
    const bool hi = lane >= 32, pe = hi && !(h & 1), po = !hi && (h & 1);
    const int m0 = (28 * h) >> 3;
    const auto rK = RSRC(ws + WS_KA, QC_BYTES), rV = RSRC(ws + WS_VA, V_BYTES);
    const unsigned lo16 = (unsigned)lane * 16u;
    const unsigned kbase = (unsigned)((wid * 8) * 56 + m0) * 512u, vbase = (unsigned)(h * 64 + wid * 8) * 2048u;
    const int rot = rb & 7;
    u32x4 qf[4][2], kf[3][2], vf[3][2];
    {   const unsigned k0 = kbase + (unsigned)rot * TILE_B, v0 = vbase + (unsigned)rot * 2048u, k1 = kbase + (unsigned)((rot + 1) & 7) * TILE_B;
        kf[0][0] = bload128(rK, lo16, k0); kf[0][1] = bload128(rK, lo16, k0 + 1024u);
        if (wid >= 4) { const unsigned b = (unsigned)((rb * 4 + wid - 4) * 56 + m0) * 512u; const auto rQ = RSRC(ws + WS_QA, QC_BYTES); qf[0][0] = bload128(rQ, lo16, b); qf[0][1] = bload128(rQ, lo16, b + 1024u); }
        vf[0][0] = bload128(rV, lo16, v0); vf[0][1] = bload128(rV, lo16, v0 + 1024u);
        kf[1][0] = bload128(rK, lo16, k1); kf[1][1] = bload128(rK, lo16, k1 + 1024u);
        const unsigned v1 = vbase + (unsigned)((rot + 1) & 7) * 2048u, k2 = kbase + (unsigned)((rot + 2) & 7) * TILE_B;
        vf[1][0] = bload128(rV, lo16, v1); vf[1][1] = bload128(rV, lo16, v1 + 1024u);
        kf[2][0] = bload128(rK, lo16, k2); kf[2][1] = bload128(rK, lo16, k2 + 1024u); }
    patch(kf[0][0], kf[0][1], pe, po, 0x3F803F80u, 0u);
    if (wid >= 4) {
        patch(qf[0][0], qf[0][1], pe, po, 0u, 0u);
        f32x16 s = {}; s = MFMA32(asb(kf[0][0]), asb(qf[0][0]), s); s = MFMA32(asb(kf[0][1]), asb(qf[0][1]), s);
        const float m = xhalf_max(max16(s)) + POFF;
        const unsigned mh = cvtpk(-m, 0.f) & 0xffffu; const float r = -m - __uint_as_float(mh << 16);
        patch(qf[0][0], qf[0][1], pe, po, mh | (cvtpk(r, 0.f) << 16), 0u);
        *(u32x4*)(lds + LDS_Q + (wid - 4) * 2048 + lane * 16) = qf[0][0]; *(u32x4*)(lds + LDS_Q + (wid - 4) * 2048 + 1024 + lane * 16) = qf[0][1];
        asm volatile("s_waitcnt lgkmcnt(0)" ::: "memory");
        if (lane == 0) ctl_add(lds, CTL_QCNT);
    }
    ctl_wait(lds, CTL_QCNT, 4);
    STAMP(6);
#pragma unroll
    for (int j = 0; j < 4; ++j) { qf[j][0] = *(const u32x4*)(lds + LDS_Q + j * 2048 + lane * 16); qf[j][1] = *(const u32x4*)(lds + LDS_Q + j * 2048 + 1024 + lane * 16); }
    if (has_qkv && wid < 4) {
        qkv_store<1, 0>(outB, ws, rt, ct, wid, lane); qkv_store<1, 1>(outB, ws, rt, ct, wid, lane); qkv_store<1, 2>(outB, ws, rt, ct, wid, lane);
        qkv_store<1, 3>(outB, ws, rt, ct, wid, lane); qkv_store<1, 4>(outB, ws, rt, ct, wid, lane); qkv_store<1, 5>(outB, ws, rt, ct, wid, lane);
    }
#pragma unroll
    for (int j = 0; j < 4; ++j) { PIN(qf[j][0]); PIN(qf[j][1]); }
    PIN(kf[0][0]); PIN(kf[0][1]);
    f32x16 o[4];
#define VEXP(d, s) asm volatile("v_exp_f32 %0, %1" : "=v"(d) : "v"(s))
#define VCVT(d, a, b) asm volatile("v_cvt_pk_bf16_f32 %0, %1, %2" : "=v"(d) : "v"(a), "v"(b))
#define MF0(d, a, b) asm volatile("v_mfma_f32_32x32x16_bf16 %0, %1, %2, 0" : "=&v"(d) : "v"(a), "v"(b))
#define MFA(d, a, b) asm volatile("v_mfma_f32_32x32x16_bf16 %0, %1, %2, %0" : "+v"(d) : "v"(a), "v"(b))
#define SLOT(PV, QK, Sc, Pc0, Pc1, Sn, kn0, kn1, qn0, qn1, Op, vp0, vp1, Pp0, Pp1, INJ) do { \
        float e0, e1, e2, e3, e4, e5, e6, e7, e8, e9, e10, e11, e12, e13, e14, e15; \
        if (QK) MF0(Sn, kn0, qn0); \
        VEXP(e0, Sc[0]); VEXP(e1, Sc[1]); VEXP(e2, Sc[2]); VEXP(e3, Sc[3]); \
        if (QK) MFA(Sn, kn1, qn1); \
        VEXP(e4, Sc[4]); VEXP(e5, Sc[5]); VEXP(e6, Sc[6]); VEXP(e7, Sc[7]); VCVT(Pc0[0], e0, e1); VCVT(Pc0[1], e2, e3); \
        INJ; \
        if ((PV) == 2) MFA(Op, vp0, Pp0); else if ((PV) == 1) MF0(Op, vp0, Pp0); \
        VEXP(e8, Sc[8]); VEXP(e9, Sc[9]); VEXP(e10, Sc[10]); VEXP(e11, Sc[11]); VCVT(Pc0[2], e4, e5); VCVT(Pc0[3], e6, e7); \
        if (PV) MFA(Op, vp1, Pp1); \
        VEXP(e12, Sc[12]); VEXP(e13, Sc[13]); VEXP(e14, Sc[14]); VEXP(e15, Sc[15]); VCVT(Pc1[0], e8, e9); VCVT(Pc1[1], e10, e11); VCVT(Pc1[2], e12, e13); VCVT(Pc1[3], e14, e15); \
    } while (0)
    f32x16 sA, sB; u32x4 pA0, pA1, pB0, pB1;
    asm volatile("s_nop 1\n\tv_mfma_f32_32x32x16_bf16 %0, %1, %2, 0\n\tv_mfma_f32_32x32x16_bf16 %0, %3, %4, %0\n\ts_nop 7\n\ts_nop 3" : "=&v"(sA) : "v"(kf[0][0]), "v"(qf[0][0]), "v"(kf[0][1]), "v"(qf[0][1]));
#define PATCHK(kx) do { PIN(kx[0]); PIN(kx[1]); patch(kx[0], kx[1], pe, po, 0x3F803F80u, 0u); PIN(kx[0]); PIN(kx[1]); } while (0)
#define LOADK(kc, tt) do { if ((tt) < 8) { const unsigned o_ = kbase + (unsigned)(((tt) + rot) & 7) * TILE_B; kc[0] = bload128(rK, lo16, o_); kc[1] = bload128(rK, lo16, o_ + 1024u); } } while (0)
#define LOADV(vx, tt) do { if ((tt) < 8) { const unsigned o_ = vbase + (unsigned)(((tt) + rot) & 7) * 2048u; vx[0] = bload128(rV, lo16, o_); vx[1] = bload128(rV, lo16, o_ + 1024u); } } while (0)
#define TILE(t) do { \
        SLOT((t) == 0 ? 0 : (t) == 1 ? 1 : 2, true, sA, pA0, pA1, sB, kf[(t) % 3][0], kf[(t) % 3][1], qf[1][0], qf[1][1], o[3], vf[((t) + 2) % 3][0], vf[((t) + 2) % 3][1], pB0, pB1, (void)0); \
        LOADV(vf[((t) + 2) % 3], (t) + 2); \
        SLOT((t) == 0 ? 1 : 2, true, sB, pB0, pB1, sA, kf[(t) % 3][0], kf[(t) % 3][1], qf[2][0], qf[2][1], o[0], vf[(t) % 3][0], vf[(t) % 3][1], pA0, pA1, (void)0); \
        SLOT((t) == 0 ? 1 : 2, true, sA, pA0, pA1, sB, kf[(t) % 3][0], kf[(t) % 3][1], qf[3][0], qf[3][1], o[1], vf[(t) % 3][0], vf[(t) % 3][1], pB0, pB1, if ((t) < 7) PATCHK(kf[((t) + 1) % 3])); \
        LOADK(kf[(t) % 3], (t) + 3); \
        SLOT((t) == 0 ? 1 : 2, (t) < 7, sB, pB0, pB1, sA, kf[((t) + 1) % 3][0], kf[((t) + 1) % 3][1], qf[0][0], qf[0][1], o[2], vf[(t) % 3][0], vf[(t) % 3][1], pA0, pA1, (void)0); \
    } while (0)
    AttnBPre bpre; u64 flb = 0; bool bready = false;
#pragma unroll
    for (int t = 0; t < 8; t += 2) {
        if (t == 0) __builtin_amdgcn_s_setprio(3); else if (t == 2) __builtin_amdgcn_s_setprio(2); else if (t == 4) __builtin_amdgcn_s_setprio(1); else __builtin_amdgcn_s_setprio(0);
        TILE(t);
        if (t == 4) { bready = __all(flb == tag); attnB_load(bpre, ws, h, rb, wid, opaque_lane()); }

        TILE(t + 1);
        if (t == 0) {
            if (has_qkv && wid < 4) {
                asm volatile("s_waitcnt vmcnt(8)" ::: "memory");
                if (opaque_lane() == 0 && ctl_add(lds, CTL_F2CNT) == 3) flag_set(ws, F2 + ct * 32 + rt, tag);
            }
            if (oct < 7 && wid >= 4) {
                if (has_qkv) ctl_wait(lds, CTL_RDONE, 4);
                wo_issue<14>(woA, oct, (unsigned)(uintptr_t)lds, (wid - 4) * 14, opaque_lane());
            }
        } else if (t == 2) {
            const int ln = opaque_lane();
            if (oct < 7 && wid >= 4) { asm volatile("s_waitcnt vmcnt(8)" ::: "memory"); if (ln == 0) ctl_add(lds, CTL_WA); }
            flb = __hip_atomic_load(flagp(ws, f2_index(h, rb, ln)), __ATOMIC_RELAXED, __HIP_MEMORY_SCOPE_AGENT);
        }
    }
    asm volatile("s_nop 1\n\tv_mfma_f32_32x32x16_bf16 %0, %1, %2, %0\n\tv_mfma_f32_32x32x16_bf16 %0, %3, %4, %0\n\ts_nop 7\n\ts_nop 3" : "+v"(o[3]) : "v"(vf[1][0]), "v"(pB0), "v"(vf[1][1]), "v"(pB1));
    asm volatile("" : "+v"(o[0]), "+v"(o[1]), "+v"(o[2]));
    STAMP(7);
    lane = opaque_lane();
    if (has_qkv) ctl_wait(lds, CTL_RDONE, 4);
#pragma unroll
    for (int j = 0; j < 4; ++j) merge_write1(o[j], lds, wid, j, lane);
    if (oct < 7 && wid < 4) { ctl_wait(lds, CTL_WA, 4); wf_read(wf, lds, wid & 1, lane); }
    STAMP(15);
    BARRIER();
    STAMP(10);
    const bool bad = merge_reduce(ws, h, rb, lds, wid, lane);
    if (__any(bad) && lane == 0) ctl_store(lds, CTL_BAD, 1);
    STAMP(8);
    if (!bready) { flag_wait(ws, f2_index(h, rb, lane), tag); attnB_load(bpre, ws, h, rb, wid, lane); }
    attnB_compute(bpre, ws, h, rb, wid, lane);
    asm volatile("s_waitcnt vmcnt(2) lgkmcnt(0)" ::: "memory");
    if (lane == 0 && ctl_add(lds, CTL_F3CNT) == 7 && !ctl_load(lds, CTL_BAD)) flag_set(ws, F3 + rb * 16 + h, tag);
    asm volatile("s_waitcnt vmcnt(0)" ::: "memory");
    if (lane == 0 && ctl_add(lds, CTL_F4CNT) == 7) flag_set(ws, F4 + rb * 16 + h, tag);
    ctl_wait(lds, CTL_F3CNT, 8);
    if (__builtin_amdgcn_readfirstlane(ctl_load(lds, CTL_BAD))) {
        BARRIER();
        attnA_safe(ws, h, rb, lds, wid, lane);
        BARRIER();
        merge_reduce(ws, h, rb, lds, wid, lane);
        DRAIN_BARRIER();
        if (threadIdx.x == 0) flag_set(ws, F3 + rb * 16 + h, tag);
    }
}

__device__ __forceinline__ void oproj_compute(char* ws, const u16* woB, u16* out, int rt, int ct, u64 tag, u32x4 (&wf)[2][14], char* lds, int wid, int lane) {
    const int br = wid >> 2, tt = (wid >> 1) & 1, kh = wid & 1;
    if (wid < 3) {
        if (wid < 2) wo_issue<19>(woB, ct, (unsigned)(uintptr_t)lds + LDS_MRG, wid * 19, lane); else wo_issue<18>(woB, ct, (unsigned)(uintptr_t)lds + LDS_MRG, 38, lane);
    }
    if ((wid & 3) == 3) { flag_wait(ws, (br ? F4 : F3) + (rt >> 1) * 16 + (lane & 15), tag); if (lane == 0) ctl_store(lds, br ? CTL_F4 : CTL_F3, 1); }
    else {
        if (wid < 3) { asm volatile("s_waitcnt vmcnt(0)" ::: "memory"); if (lane == 0) ctl_add(lds, CTL_WB); }
        ctl_wait(lds, br ? CTL_F4 : CTL_F3, 1);
    }
    STAMP(11);
    const auto rO = RSRC(ws + (br ? WS_OB : WS_OA), QC_BYTES);
    const int tile = rt * 2 + tt;
    u32x4 of[14];
#pragma unroll
    for (int i = 0; i < 14; ++i) of[i] = bload128(rO, (unsigned)lane * 16u, (unsigned)(tile * 56 + 2 * (kh * 14 + i)) * 512u);
    if (br) { ctl_wait(lds, CTL_WB, 3); wf_read(wf, lds + LDS_MRG, kh, lane); }
    f32x16 acc[2] = {};
#pragma unroll
    for (int i = 0; i < 14; ++i) { acc[0] = MFMA32(asb(wf[0][i]), asb(of[i]), acc[0]); acc[1] = MFMA32(asb(wf[1][i]), asb(of[i]), acc[1]); }
    asm volatile("" : "+v"(acc[0]), "+v"(acc[1]));
    STAMP(12);
    {   const f32x16& gv = kh ? acc[0] : acc[1];
#pragma unroll
        for (int rq = 0; rq < 4; ++rq) *(f32x4*)(lds + 114688 + (wid * 4 + rq) * 1024 + lane * 16) = (f32x4){gv[4 * rq], gv[4 * rq + 1], gv[4 * rq + 2], gv[4 * rq + 3]};
    }
    asm volatile("s_waitcnt lgkmcnt(0)" ::: "memory");
    if (lane == 0) ctl_store(lds, CTL_X + wid * 4, 1);
    ctl_wait(lds, CTL_X + (wid ^ 1) * 4, 1);
    STAMP(13);
    f32x16 r = kh ? acc[1] : acc[0];
#pragma unroll
    for (int rq = 0; rq < 4; ++rq) { const f32x4 x = *(const f32x4*)(lds + 114688 + ((wid ^ 1) * 4 + rq) * 1024 + lane * 16); r[4 * rq] += x[0]; r[4 * rq + 1] += x[1]; r[4 * rq + 2] += x[2]; r[4 * rq + 3] += x[3]; }
    const unsigned orow = (unsigned)(rt * 64 + tt * 32 + (lane & 31)) * 1792u + (unsigned)(br * 448 + ct * 64 + kh * 32 + (lane >> 5) * 8) * 2u;
#pragma unroll
    for (int pr = 0; pr < 2; ++pr) {
        const unsigned a0 = cvtpk(r[8 * pr], r[8 * pr + 1]), a1 = cvtpk(r[8 * pr + 2], r[8 * pr + 3]), b0 = cvtpk(r[8 * pr + 4], r[8 * pr + 5]), b1 = cvtpk(r[8 * pr + 6], r[8 * pr + 7]);
        auto r0 = __builtin_amdgcn_permlane32_swap(a0, b0, false, false);
        auto r1 = __builtin_amdgcn_permlane32_swap(a1, b1, false, false);
        bstore128((u32x4){r0[0], r1[0], r0[1], r1[1]}, RSRC(out, 2048 * 896 * 2), orow + pr * 32u);
    }
}

__global__ void __launch_bounds__(512, 2) fused_kernel(const u16* xA, const u16* wA, const u16* woA, const u16* xB, const u16* wB, const u16* woB, u16* out, char* ws) {
    extern __shared__ __attribute__((aligned(16))) char lds[];
    int lane = threadIdx.x & 63; const int wid = __builtin_amdgcn_readfirstlane(threadIdx.x >> 6);
    const int id = blockIdx.x, xcd = id & 7, loc = id >> 3;
    STAMP_INIT();
    STAMP(0);
    if (threadIdx.x < 32) ctl_store(lds, threadIdx.x * 4, 0);
    const u64 tag = __hip_atomic_load(flagp(ws, FEP), __ATOMIC_RELAXED, __HIP_MEMORY_SCOPE_AGENT) + 1;
    const int gb = xcd & 1, grt = 8 * (xcd >> 1) + (loc & 7), qct = gb * 4 + (loc >> 3);
    const bool has_qkv = qct < 7;
    const int h = xcd * 2 + (loc & 1), rb = loc >> 1;
    const int ort = 4 * xcd + (loc & 3), oct = loc >> 2;
    unsigned warm0 = 0;
    if (wid < 4) {
        u64 pc; int len;
        asm volatile("s_getpc_b64 %0\n.Lfused_pc_ref:\n\ts_mov_b32 %1, .Lfused_code_end - .Lfused_pc_ref" : "=s"(pc), "=s"(len));
        const int off = (wid * 64 + lane) * 128;
        if (off < len) warm0 = *(const __attribute__((address_space(1))) unsigned*)(pc + off);
    }
    QkvOut outB;
    if (has_qkv) qkv_phase(xA, wA, xB, wB, ws, grt, qct, tag, lds, wid, lane, outB);
    else { if (threadIdx.x == 0) flag_set(ws, F0 + (xcd >> 1) * 8 + (loc & 7), tag); BARRIER(); }
    asm volatile("" :: "v"(warm0));
    STAMP(4);
    if (wid == 4) {
        flag_wait(ws, F1 + (lane < 32 ? (28 * h) >> 6 : (28 * h + 27) >> 6) * 32 + (lane & 31), tag);
        if (lane == 0) ctl_store(lds, CTL_F1, 1);
    } else ctl_wait(lds, CTL_F1, 1);
    STAMP(5);
    const u64 tags = (u64)(unsigned)__builtin_amdgcn_readfirstlane((unsigned)tag) | ((u64)(unsigned)__builtin_amdgcn_readfirstlane((unsigned)(tag >> 32)) << 32);
    u32x4 wf[2][14];
    attnA_phase(ws, woA, oct, h, rb, tags, grt, qct, has_qkv, outB, wf, lds, wid, lane);
    STAMP(9);
    lane = opaque_lane();
    if (oct < 7) {
        oproj_compute(ws, woB, out, ort, oct, tags, wf, lds, wid, lane);
    }
    STAMP(14);
    if (id == 0 && wid == 0) {
        flag_wait(ws, F0 + (lane & 31), tags);
        if (lane == 0) flag_set(ws, FEP, tags);
    }
    STAMP_DUMP();
    asm volatile(".Lfused_code_end:");
}

extern "C" void kernel_launch(void* const* d_in, const int* in_sizes, int n_in, void* d_out, int out_size, void* d_ws, size_t ws_size, hipStream_t stream) {
    const u16* xA = (const u16*)d_in[0]; const u16* wA = (const u16*)d_in[1]; const u16* woA = (const u16*)d_in[2];
    const u16* xB = (const u16*)d_in[3]; const u16* wB = (const u16*)d_in[4]; const u16* woB = (const u16*)d_in[5];
    (void)hipFuncSetAttribute((const void*)fused_kernel, hipFuncAttributeMaxDynamicSharedMemorySize, LDS_TOTAL + LDS_EXTRA);
    fused_kernel<<<256, 512, LDS_TOTAL + LDS_EXTRA, stream>>>(xA, wA, woA, xB, wB, woB, (u16*)d_out, (char*)d_ws);
}
```

```cpp
#include <hip/hip_runtime.h>
#include <hip/hip_bf16.h>
#include <stdint.h>

typedef unsigned short u16;
typedef unsigned long long u64;
typedef short bf16x8 __attribute__((ext_vector_type(8)));
typedef short s16x4 __attribute__((ext_vector_type(4)));
typedef float f32x4 __attribute__((ext_vector_type(4)));
typedef float f32x16 __attribute__((ext_vector_type(16)));
typedef unsigned u32x4 __attribute__((ext_vector_type(4)));
typedef unsigned u32x2 __attribute__((ext_vector_type(2)));
typedef __attribute__((address_space(3))) const char* lds_cptr;
typedef __attribute__((address_space(1))) u64 gu64;

constexpr int TILE_B = 56 * 512;
constexpr size_t QC_BYTES = (size_t)64 * TILE_B;
constexpr size_t V_BYTES = (size_t)16 * 64 * 2048;
constexpr size_t WS_QA = 0, WS_KA = WS_QA + QC_BYTES, WS_VA = WS_KA + QC_BYTES;
constexpr size_t WS_QB = WS_VA + V_BYTES, WS_KB = WS_QB + QC_BYTES, WS_VB = WS_KB + QC_BYTES;
constexpr size_t WS_OA = WS_VB + V_BYTES, WS_OB = WS_OA + QC_BYTES, WS_FLAGS = WS_OB + QC_BYTES;
constexpr int F1 = 0, F2 = 256, F3 = 512, F4 = 768, F0 = 1024, FEP = 1056;
constexpr float QSCALE = 0.18898223650461362f * 1.4426950408889634f;
constexpr int STG = 32768;
constexpr int LDS_Q = 147456, LDS_L = 131072, LDS_MRG = 57344, LDS_CTL = LDS_Q + 8192, LDS_MX = LDS_CTL + 128, LDS_TOTAL = LDS_MX + 1024 + 64;
constexpr int CTL_F1 = 0, CTL_F2CNT = 4, CTL_BAD = 8, CTL_QCNT = 12, CTL_F4CNT = 16, CTL_F3 = 20, CTL_RDONE = 24, CTL_F4 = 28, CTL_X = 32;
constexpr int CTL_WA = 64, CTL_WB = 68, CTL_S12 = 72, CTL_S13 = 76, CTL_F3CNT = 80;
#define BPFG 1
#define PUBG 10
#define POFF 96.0f
#define LMIN (__builtin_ldexpf(1.0f, -(int)(POFF) - 20))
constexpr unsigned SPIN_MAX = 1u << 18;

#define STAMP(i)
#define STAMP_INIT()
#define STAMP_DUMP()
#define LDS_EXTRA 0

#define MFMA16(a, b, c) __builtin_amdgcn_mfma_f32_16x16x32_bf16(a, b, c, 0, 0, 0)
#define MFMA32(a, b, c) __builtin_amdgcn_mfma_f32_32x32x16_bf16(a, b, c, 0, 0, 0)
#define PIN(x) asm volatile("" : "+v"(x))
#define SBAR() __builtin_amdgcn_sched_barrier(0)
#define SC1 16

__device__ __forceinline__ unsigned cvtpk(float lo, float hi) {
    typedef float f2 __attribute__((ext_vector_type(2))); typedef __bf16 b2 __attribute__((ext_vector_type(2)));
    f2 v = {lo, hi}; b2 r = __builtin_convertvector(v, b2); return __builtin_bit_cast(unsigned, r);
}
__device__ __forceinline__ void glds16(const void* sbase, unsigned voff, unsigned lds_base) {
    asm volatile("s_mov_b32 m0, %2\n\ts_nop 0\n\tglobal_load_lds_dwordx4 %0, %1" :: "v"(voff), "s"(sbase), "s"(lds_base) : "memory");
}
__device__ __forceinline__ s16x4 vtr(lds_cptr p) {
    typedef short v4i16_t __attribute__((ext_vector_type(4)));
    return __builtin_bit_cast(s16x4, __builtin_amdgcn_ds_read_tr16_b64_v4i16((__attribute__((address_space(3))) v4i16_t*)p));
}
__device__ __forceinline__ bf16x8 cat8(s16x4 a, s16x4 b) { return (bf16x8){a[0], a[1], a[2], a[3], b[0], b[1], b[2], b[3]}; }
__device__ __forceinline__ bf16x8 ldsrd128(lds_cptr p) { return *(const __attribute__((address_space(3))) bf16x8*)p; }
#define RSRC(p, bytes) __builtin_amdgcn_make_buffer_rsrc((void*)(p), 0, (int)(bytes), 0x00020000)
__device__ __forceinline__ u32x4 bload128(__amdgpu_buffer_rsrc_t r, unsigned voff, unsigned soff) { return __builtin_amdgcn_raw_buffer_load_b128(r, voff, soff, SC1); }
__device__ __forceinline__ void bstore128(u32x4 v, __amdgpu_buffer_rsrc_t r, unsigned voff) { __builtin_amdgcn_raw_buffer_store_b128(v, r, voff, 0, SC1); }
__device__ __forceinline__ void bstore64(u32x2 v, __amdgpu_buffer_rsrc_t r, unsigned voff) { __builtin_amdgcn_raw_buffer_store_b64(v, r, voff, 0, SC1); }
__device__ __forceinline__ bf16x8 asb(u32x4 v) { return __builtin_bit_cast(bf16x8, v); }

__device__ __forceinline__ gu64* flagp(char* ws, int idx) { return (gu64*)(ws + WS_FLAGS + (size_t)idx * 128); }
__device__ __forceinline__ void flag_set(char* ws, int idx, u64 tag) { __hip_atomic_store(flagp(ws, idx), tag, __ATOMIC_RELAXED, __HIP_MEMORY_SCOPE_AGENT); }
__device__ __forceinline__ void flag_wait(char* ws, int idx, u64 tag) {
    gu64* p = flagp(ws, idx);
    for (unsigned spins = 0; spins < SPIN_MAX; ++spins) {
        const u64 v = __hip_atomic_load(p, __ATOMIC_RELAXED, __HIP_MEMORY_SCOPE_AGENT);
        if (__all(v == tag)) break;
        __builtin_amdgcn_s_sleep(1);
    }
}
__device__ __forceinline__ int opaque_lane() { int l; asm volatile("v_mbcnt_lo_u32_b32 %0, -1, 0\n\tv_mbcnt_hi_u32_b32 %0, -1, %0" : "=v"(l)); return l; }
__device__ __forceinline__ int f2_index(int h, int rb, int lane) { const int i = lane % 6, r = 2 * rb - 1 + i % 3; return F2 + (i < 3 ? (28 * h) >> 6 : (28 * h + 27) >> 6) * 32 + (r < 0 ? 0 : r); }
typedef __attribute__((address_space(3))) int* lds_iptr;
__device__ __forceinline__ int ctl_load(char* lds, int off) { return __hip_atomic_load((lds_iptr)(lds + LDS_CTL + off), __ATOMIC_RELAXED, __HIP_MEMORY_SCOPE_WORKGROUP); }
__device__ __forceinline__ void ctl_store(char* lds, int off, int v) { __hip_atomic_store((lds_iptr)(lds + LDS_CTL + off), v, __ATOMIC_RELAXED, __HIP_MEMORY_SCOPE_WORKGROUP); }
__device__ __forceinline__ void ctl_wait(char* lds, int off, int n) {
    for (unsigned spins = 0; spins < (SPIN_MAX << 6); ++spins) { if (__builtin_amdgcn_readfirstlane(ctl_load(lds, off)) >= n) break; __builtin_amdgcn_s_sleep(1); }
    asm volatile("" ::: "memory");
}
__device__ __forceinline__ int ctl_add(char* lds, int off) { return __hip_atomic_fetch_add((lds_iptr)(lds + LDS_CTL + off), 1, __ATOMIC_RELAXED, __HIP_MEMORY_SCOPE_WORKGROUP); }
#define BARRIER() asm volatile("s_waitcnt lgkmcnt(0)\n\ts_barrier" ::: "memory")
#define DRAIN_BARRIER() asm volatile("s_waitcnt vmcnt(0) lgkmcnt(0)\n\ts_barrier" ::: "memory")

struct QkvFrag { bf16x8 x[2][4], w[2][3]; };
struct QkvAcc { f32x4 q[4], k[4], v[4]; };
struct QkvOut { u32x4 q[2], k[2], v[2]; };
__device__ __forceinline__ void qkv_pack(QkvOut& o, const QkvAcc& a, float qs) {
#pragma unroll
    for (int tb = 0; tb < 2; ++tb) {
        const f32x4 q0 = a.q[2 * tb] * qs, q1 = a.q[2 * tb + 1] * qs, k0 = a.k[2 * tb], k1 = a.k[2 * tb + 1], v0 = a.v[2 * tb], v1 = a.v[2 * tb + 1];
        { auto ra = __builtin_amdgcn_permlane16_swap(cvtpk(q0[0], q0[1]), cvtpk(q1[0], q1[1]), false, false); auto rb = __builtin_amdgcn_permlane16_swap(cvtpk(q0[2], q0[3]), cvtpk(q1[2], q1[3]), false, false);
          o.q[tb] = (u32x4){ra[0], rb[0], ra[1], rb[1]}; }
        { auto ra = __builtin_amdgcn_permlane16_swap(cvtpk(k0[0], k0[1]), cvtpk(k1[0], k1[1]), false, false); auto rb = __builtin_amdgcn_permlane16_swap(cvtpk(k0[2], k0[3]), cvtpk(k1[2], k1[3]), false, false);
          o.k[tb] = (u32x4){ra[0], rb[0], ra[1], rb[1]}; }
        { auto ra = __builtin_amdgcn_permlane32_swap(cvtpk(v0[0], v0[1]), cvtpk(v1[0], v1[1]), false, false); auto rb = __builtin_amdgcn_permlane32_swap(cvtpk(v0[2], v0[3]), cvtpk(v1[2], v1[3]), false, false);
          o.v[tb] = (u32x4){ra[0], rb[0], ra[1], rb[1]}; }
    }
}
template <int BR, int I>
__device__ __forceinline__ void qkv_store(const QkvOut& o, char* ws, int rt, int ct, int c, int lane) {
    constexpr int tb = I / 3, kind = I % 3;
    const int tile = rt * 2 + tb;
    if (kind < 2) {
        const unsigned off = (unsigned)(tile * 56 + 8 * ct + 2 * c) * 512u + (unsigned)lane * 16u;
        if (kind == 0) bstore128(o.q[tb], RSRC(ws + (BR ? WS_QB : WS_QA), QC_BYTES), off); else bstore128(o.k[tb], RSRC(ws + (BR ? WS_KB : WS_KA), QC_BYTES), off);
    } else {
        const int n = 64 * ct + 16 * c + (lane & 15), h = n / 28, d = n - 28 * h;
        const unsigned off = (unsigned)(h * 64 + tile) * 2048u + (unsigned)(lane >> 4) * 512u + (unsigned)d * 16u;
        const auto rs = RSRC(ws + (BR ? WS_VB : WS_VA), V_BYTES);
        bstore128(o.v[tb], rs, off);
        if (d >= 24) { const unsigned pad = d == 24 ? 0x3F803F80u : 0u; bstore128((u32x4){pad, pad, pad, pad}, rs, off + 64u); }
    }
}

__device__ __forceinline__ void qkv_phase(const u16* xA, const u16* wA, const u16* xB, const u16* wB, char* ws, int rt, int ct, u64 tag, char* lds, int wid, int lane, QkvOut& outB) {
    if (wid >= 4) {
        const unsigned lds0 = (unsigned)(uintptr_t)lds;
        const int lw = wid - 4, r8 = lane >> 3, dpos = lane & 7;
        unsigned xoff[2], woff[2];
#pragma unroll
        for (int i = 0; i < 2; ++i) { const int row = 16 * lw + 8 * i + r8;
            xoff[i] = (unsigned)row * 896u + (unsigned)((dpos ^ ((row >> 1) & 7)) << 4);
            const int f2 = ((row >> 1) & 1) | (((row >> 3) & 1) << 1);
            woff[i] = (unsigned)row * 2688u + (unsigned)((((dpos >> 1) ^ f2) * 2 + (dpos & 1)) << 4); }
        const int rot = __builtin_amdgcn_readfirstlane(((rt & 7) + 2 * (ct & 3)) % 7);
        const char* xa = (const char*)xA + (size_t)rt * 64 * 896; const char* wa = (const char*)wA + (size_t)ct * 128;
        const char* xb = (const char*)xB + (size_t)rt * 64 * 896; const char* wb = (const char*)wB + (size_t)ct * 128;
#define QKV_ISSUE(g) do { int st_ = ((g) % 7) + rot; st_ = st_ >= 7 ? st_ - 7 : st_; const char* x_ = ((g) < 7 ? xa : xb) + st_ * 128; const char* w_ = ((g) < 7 ? wa : wb) + (size_t)st_ * (64 * 2688); \
        const unsigned sl_ = lds0 + ((g) & 3) * STG + lw * 2048; \
        glds16(x_, xoff[0], sl_); glds16(x_, xoff[1], sl_ + 1024); \
        glds16(w_, woff[0], sl_ + 8192); glds16(w_, woff[1], sl_ + 8192 + 1024); glds16(w_ + 896, woff[0], sl_ + 16384); glds16(w_ + 896, woff[1], sl_ + 16384 + 1024); \
        glds16(w_ + 1792, woff[0], sl_ + 24576); glds16(w_ + 1792, woff[1], sl_ + 24576 + 1024); } while (0)
        QKV_ISSUE(0); QKV_ISSUE(1);
#pragma unroll
        for (int g = 0; g < 12; ++g) {
            asm volatile("s_waitcnt vmcnt(8) lgkmcnt(0)\n\ts_barrier" ::: "memory");
            if (g == 0) STAMP(1);
            QKV_ISSUE(g + 2);
        }
        asm volatile("s_waitcnt vmcnt(8)" ::: "memory");
        if (lane == 0) ctl_add(lds, CTL_S12);
        asm volatile("s_waitcnt vmcnt(0)" ::: "memory");
        if (lane == 0) ctl_add(lds, CTL_S13);
        return;
    }
    const int c = wid, g4 = lane >> 4, t16 = lane & 15;
    const unsigned xr0 = (unsigned)t16 * 128u + (unsigned)((g4 ^ (t16 >> 1)) << 4);
    const int q = t16 >> 2, p = lane & 3;
    const int rf2 = ((q >> 1) & 1) | ((g4 & 1) << 1);
    const unsigned wr0 = 8192u + (unsigned)(8 * g4 + q) * 128u + (unsigned)((c ^ rf2) * 32) + (unsigned)p * 8u;
    QkvAcc acc; QkvOut outA;
#pragma unroll
    for (int i = 0; i < 4; ++i) acc.q[i] = acc.k[i] = acc.v[i] = (f32x4){};
    QkvFrag fr[2];
    unsigned pfa = 0, pfb = 0;
#define QMF(i) do { if (g > 0) { constexpr int ks_ = (i) / 12, s_ = ((i) % 12) / 4, b_ = (i) % 4; const QkvFrag& f_ = fr[(g - 1) & 1]; \
        if (s_ == 0) acc.q[b_] = MFMA16(f_.w[ks_][0], f_.x[ks_][b_], acc.q[b_]); if (s_ == 1) acc.k[b_] = MFMA16(f_.w[ks_][1], f_.x[ks_][b_], acc.k[b_]); \
        if (s_ == 2) acc.v[b_] = MFMA16(f_.x[ks_][b_], f_.w[ks_][2], acc.v[b_]); } SBAR(); } while (0)
#define QRX(ks, b) do { if (g < 14) fr[g & 1].x[ks][b] = ldsrd128(sl + (xr0 ^ ((ks) * 64)) + (b) * 2048); } while (0)
#define QRW(ks, s) do { if (g < 14) { lds_cptr wp_ = sl + wr0 + (ks) * 4096 + (s) * 8192; fr[g & 1].w[ks][s] = cat8(vtr(wp_), vtr(wp_ + 512)); } } while (0)
#define QST(gg, i) do { if (g == (gg)) { qkv_store<0, i>(outA, ws, rt, ct, c, lane); SBAR(); } } while (0)
#pragma unroll
    for (int g = 0; g < 15; ++g) {
        if (g == PUBG) asm volatile("s_waitcnt vmcnt(0) lgkmcnt(0)\n\ts_barrier" ::: "memory");
        else if (g < 12) asm volatile("s_waitcnt lgkmcnt(0)\n\ts_barrier" ::: "memory");
        else if (g == 12) ctl_wait(lds, CTL_S12, 4);
        else if (g == 13) ctl_wait(lds, CTL_S13, 4);
        SBAR();
        if (g == 7) STAMP(2);
        lds_cptr sl = (lds_cptr)lds + (g & 3) * STG;
        if (g == PUBG && threadIdx.x == 0) flag_set(ws, F1 + ct * 32 + rt, tag);
        SBAR();
        QRX(0, 0); QMF(0); QRX(0, 1); QMF(1); QRX(0, 2); QMF(2); QRX(0, 3); QMF(3); QRW(0, 0); QMF(4); QRW(0, 1); QMF(5); QRW(0, 2); QMF(6);
        QRX(1, 0); QMF(7); QRX(1, 1); QMF(8); QRX(1, 2); QMF(9); QRX(1, 3); QMF(10); QRW(1, 0); QMF(11); QRW(1, 1); QMF(12); QRW(1, 2); QMF(13);
        QMF(14); QMF(15); QMF(16); QMF(17); QMF(18); QMF(19); QMF(20); QMF(21); QMF(22); QMF(23);
        if (g == 7) {
            qkv_pack(outA, acc, QSCALE);
            qkv_store<0, 1>(outA, ws, rt, ct, c, lane); qkv_store<0, 2>(outA, ws, rt, ct, c, lane); qkv_store<0, 4>(outA, ws, rt, ct, c, lane);
            qkv_store<0, 5>(outA, ws, rt, ct, c, lane); qkv_store<0, 0>(outA, ws, rt, ct, c, lane); qkv_store<0, 3>(outA, ws, rt, ct, c, lane);
#pragma unroll
            for (int i = 0; i < 4; ++i) acc.q[i] = acc.k[i] = acc.v[i] = (f32x4){};
        }
    }
    if (lane == 0) ctl_add(lds, CTL_RDONE);
    qkv_pack(outB, acc, 1.0f);
}

__device__ __forceinline__ float max16(const f32x16& s) {
    float a = fmaxf(fmaxf(s[0], s[1]), s[2]), b = fmaxf(fmaxf(s[3], s[4]), s[5]);
    a = fmaxf(fmaxf(a, s[6]), s[7]); b = fmaxf(fmaxf(b, s[8]), s[9]); a = fmaxf(fmaxf(a, s[10]), s[11]); b = fmaxf(fmaxf(b, s[12]), s[13]);
    a = fmaxf(fmaxf(a, s[14]), s[15]); return fmaxf(a, b);
}
__device__ __forceinline__ float xhalf_max(float m) {
    auto r = __builtin_amdgcn_permlane32_swap(__float_as_uint(m), __float_as_uint(m), false, false);
    return fmaxf(__uint_as_float(r[0]), __uint_as_float(r[1]));
}
__device__ __forceinline__ void patch(u32x4& f0, u32x4& f1, bool pe, bool po, unsigned a, unsigned b) {
    f0[0] = po ? a : f0[0]; f0[1] = po ? b : f0[1]; f1[2] = pe ? a : f1[2]; f1[3] = pe ? b : f1[3];
}
__device__ __forceinline__ void exp_pack(const f32x16& s, u32x4& p0, u32x4& p1) {
    float e[16];
#pragma unroll
    for (int r = 0; r < 16; ++r) e[r] = __builtin_amdgcn_exp2f(s[r]);
    p0 = (u32x4){cvtpk(e[0], e[1]), cvtpk(e[2], e[3]), cvtpk(e[4], e[5]), cvtpk(e[6], e[7])};
    p1 = (u32x4){cvtpk(e[8], e[9]), cvtpk(e[10], e[11]), cvtpk(e[12], e[13]), cvtpk(e[14], e[15])};
}
__device__ __forceinline__ void merge_write1(const f32x16& o, char* lds, int wid, int j, int lane) {
#pragma unroll
    for (int hf = 0; hf < 2; ++hf)
        *(u32x4*)(lds + LDS_MRG + ((wid * 4 + j) * 2 + hf) * 1024 + lane * 16) = (u32x4){cvtpk(o[8 * hf], o[8 * hf + 1]), cvtpk(o[8 * hf + 2], o[8 * hf + 3]), cvtpk(o[8 * hf + 4], o[8 * hf + 5]), cvtpk(o[8 * hf + 6], o[8 * hf + 7])};
    if (lane >= 32) *(float*)(lds + LDS_L + ((wid * 4 + j) * 32 + lane - 32) * 4) = o[12];
}
__device__ __forceinline__ bool merge_reduce(char* ws, int h, int rb, char* lds, int wid, int lane) {
    const bool hi = lane >= 32;
    const int j = wid >> 1, hf = wid & 1;
    const int tile = rb * 4 + j, q = lane & 31;
    u32x4 p[8]; float pl[8];
#pragma unroll
    for (int w = 0; w < 8; ++w) pl[w] = *(const float*)(lds + LDS_L + ((w * 4 + j) * 32 + q) * 4);
#pragma unroll
    for (int w = 0; w < 8; ++w) p[w] = *(const u32x4*)(lds + LDS_MRG + ((w * 4 + j) * 2 + hf) * 1024 + lane * 16);
    const int col0 = 28 * h + 16 * hf + (hi ? 4 : 0), col1 = col0 + 8;
    const unsigned off0 = (unsigned)((tile * 56 + (col0 >> 3)) * 32 + q) * 16u + (unsigned)((col0 >> 2) & 1) * 8u, off1 = (unsigned)((tile * 56 + (col1 >> 3)) * 32 + q) * 16u + (unsigned)((col1 >> 2) & 1) * 8u;
    const float l = ((pl[0] + pl[1]) + (pl[2] + pl[3])) + ((pl[4] + pl[5]) + (pl[6] + pl[7]));
    const float inv = __builtin_amdgcn_rcpf(l);
    u32x4 sel;
    {   const int row = lane & 31, i = 4 * (row >> 3) + (row & 3);
        const unsigned one = (row < 16 && ((row >> 2) & 1) == (lane >> 5)) ? ((i & 1) ? 0x3F800000u : 0x00003F80u) : 0u;
        sel = (u32x4){(i >> 1) == 0 ? one : 0u, (i >> 1) == 1 ? one : 0u, (i >> 1) == 2 ? one : 0u, (i >> 1) == 3 ? one : 0u}; }
    f32x16 sum = {};
#pragma unroll
    for (int w = 0; w < 8; ++w) sum = MFMA32(asb(sel), asb(p[w]), sum);
    float a[8];
#pragma unroll
    for (int i = 0; i < 8; ++i) a[i] = sum[i];
#pragma unroll
    for (int i = 0; i < 8; ++i) a[i] *= inv;
    const auto rO = RSRC(ws + WS_OA, QC_BYTES);
    const bool last = hf == 1 && hi;
    bstore64((u32x2){cvtpk(a[0], a[1]), cvtpk(a[2], a[3])}, rO, off0);
    if (!last) bstore64((u32x2){cvtpk(a[4], a[5]), cvtpk(a[6], a[7])}, rO, off1);
    float z = a[0] * 0.f; z = fmaf(a[1], 0.f, z); z = fmaf(a[2], 0.f, z); z = fmaf(a[3], 0.f, z);
    float z1 = a[4] * 0.f; z1 = fmaf(a[5], 0.f, z1); z1 = fmaf(a[6], 0.f, z1); z1 = fmaf(a[7], 0.f, z1);
    const bool ok = l > LMIN && l < 1.0e36f && z == 0.f && (last || z1 == 0.f);
    return !ok;
}

__device__ __forceinline__ void attnA_safe(char* ws, int h, int rb, char* lds, int wid, int lane) {
    const bool hi = lane >= 32, pe = hi && !(h & 1), po = !hi && (h & 1); const int m0 = (28 * h) >> 3;
    const auto rQ = RSRC(ws + WS_QA, QC_BYTES), rK = RSRC(ws + WS_KA, QC_BYTES), rV = RSRC(ws + WS_VA, V_BYTES);
    const unsigned lo16 = (unsigned)lane * 16u;
    float* mx = (float*)(lds + LDS_MX);
#pragma unroll 1
    for (int j = 0; j < 4; ++j) {
        const unsigned b = (unsigned)((rb * 4 + j) * 56 + m0) * 512u;
        u32x4 q0 = bload128(rQ, lo16, b), q1 = bload128(rQ, lo16, b + 1024u);
        patch(q0, q1, pe, po, 0u, 0u);
        float m = -3.0e38f; f32x16 acc = {};
#pragma unroll 1
        for (int t = 0; t < 8; ++t) {
            const unsigned ko = (unsigned)((wid * 8 + t) * 56 + m0) * 512u, vo = (unsigned)(h * 64 + wid * 8 + t) * 2048u;
            u32x4 k0 = bload128(rK, lo16, ko), k1 = bload128(rK, lo16, ko + 1024u), v0 = bload128(rV, lo16, vo), v1 = bload128(rV, lo16, vo + 1024u);
            patch(k0, k1, pe, po, 0u, 0u);
            f32x16 s = {}; s = MFMA32(asb(k0), asb(q0), s); s = MFMA32(asb(k1), asb(q1), s);
            const float mn = fmaxf(m, xhalf_max(max16(s)));
            const float al = __builtin_amdgcn_exp2f(m - mn); m = mn;
#pragma unroll
            for (int r = 0; r < 16; ++r) { s[r] -= mn; acc[r] *= al; }
            u32x4 p0, p1; exp_pack(s, p0, p1);
            acc = MFMA32(asb(v0), asb(p0), acc); acc = MFMA32(asb(v1), asb(p1), acc);
        }
        BARRIER();
        if (!hi) mx[wid * 32 + lane] = m;
        BARRIER();
        float g = m;
#pragma unroll
        for (int w = 0; w < 8; ++w) g = fmaxf(g, mx[w * 32 + (lane & 31)]);
        const float f = __builtin_amdgcn_exp2f(m - g);
        merge_write1(acc * f, lds, wid, j, lane);
    }
}

struct AttnBPre { u32x4 q, kp, kc; u32x2 v[2][2]; };
__device__ __forceinline__ void attnB_load(AttnBPre& a, char* ws, int h, int rb, int wid, int lane) {
    const int m0 = (28 * h) >> 3, T = rb * 4 + (wid >> 1), sub = wid & 1;
    const int Tp = sub ? T : (T > 0 ? T - 1 : 0), subp = sub ^ 1;
    const auto rQ = RSRC(ws + WS_QB, QC_BYTES), rK = RSRC(ws + WS_KB, QC_BYTES), rV = RSRC(ws + WS_VB, V_BYTES);
    const int g = lane >> 4, t16 = lane & 15;
    const unsigned co = (unsigned)g * 512u + (unsigned)t16 * 16u;
    a.q = bload128(rQ, co, (unsigned)(T * 56 + m0) * 512u + (unsigned)sub * 256u);
    a.kp = bload128(rK, co, (unsigned)(Tp * 56 + m0) * 512u + (unsigned)subp * 256u);
    a.kc = bload128(rK, co, (unsigned)(T * 56 + m0) * 512u + (unsigned)sub * 256u);
    const unsigned vo = (unsigned)(g & 1) * 512u + (unsigned)t16 * 16u + (unsigned)(g >> 1) * 8u;
#pragma unroll
    for (int db = 0; db < 2; ++db) {
        a.v[db][0] = __builtin_amdgcn_raw_buffer_load_b64(rV, vo + db * 256u, (unsigned)(h * 64 + Tp) * 2048u + (unsigned)subp * 1024u, SC1);
        a.v[db][1] = __builtin_amdgcn_raw_buffer_load_b64(rV, vo + db * 256u, (unsigned)(h * 64 + T) * 2048u + (unsigned)sub * 1024u, SC1);
    }
}
__device__ __forceinline__ void attnB_compute(AttnBPre& a, char* ws, int h, int rb, int wid, int lane) {
    const int T = rb * 4 + (wid >> 1), sub = wid & 1, g = lane >> 4, ql = lane & 15;
    const bool padl = (h & 1) ? g == 0 : g == 3;
    if (h & 1) { a.q[0] = padl ? 0u : a.q[0]; a.q[1] = padl ? 0u : a.q[1]; a.kp[0] = padl ? 0u : a.kp[0]; a.kp[1] = padl ? 0u : a.kp[1]; a.kc[0] = padl ? 0u : a.kc[0]; a.kc[1] = padl ? 0u : a.kc[1]; }
    else { a.q[2] = padl ? 0u : a.q[2]; a.q[3] = padl ? 0u : a.q[3]; a.kp[2] = padl ? 0u : a.kp[2]; a.kp[3] = padl ? 0u : a.kp[3]; a.kc[2] = padl ? 0u : a.kc[2]; a.kc[3] = padl ? 0u : a.kc[3]; }
    f32x4 sp = {}, sc = {};
    sp = MFMA16(asb(a.kp), asb(a.q), sp); sc = MFMA16(asb(a.kc), asb(a.q), sc);
    const bool hasp = sub || T > 0;
    float m = -INFINITY;
#pragma unroll
    for (int r = 0; r < 4; ++r) {
        const int dc = ql - (4 * g + r), dp = dc + 16;
        sp[r] = (hasp && dp <= 14 && !(dp & 1)) ? sp[r] : -INFINITY; sc[r] = (dc >= 0 && !(dc & 1)) ? sc[r] : -INFINITY;
        m = fmaxf(m, fmaxf(sp[r], sc[r]));
    }
    { auto r = __builtin_amdgcn_permlane16_swap(__float_as_uint(m), __float_as_uint(m), false, false); m = fmaxf(__uint_as_float(r[0]), __uint_as_float(r[1])); }
    m = xhalf_max(m);
    float e[8];
#pragma unroll
    for (int r = 0; r < 4; ++r) { e[r] = __builtin_amdgcn_exp2f((sp[r] - m) * QSCALE); e[4 + r] = __builtin_amdgcn_exp2f((sc[r] - m) * QSCALE); }
    const u32x4 pf = {cvtpk(e[0], e[1]), cvtpk(e[2], e[3]), cvtpk(e[4], e[5]), cvtpk(e[6], e[7])};
    f32x4 o0 = {}, o1 = {};
    o0 = MFMA16(asb((u32x4){a.v[0][0][0], a.v[0][0][1], a.v[0][1][0], a.v[0][1][1]}), asb(pf), o0);
    o1 = MFMA16(asb((u32x4){a.v[1][0][0], a.v[1][0][1], a.v[1][1][0], a.v[1][1][1]}), asb(pf), o1);
    float l;
    { auto r16 = __builtin_amdgcn_permlane16_swap(__float_as_uint(o1[0]), __float_as_uint(o1[0]), false, false); auto r32 = __builtin_amdgcn_permlane32_swap(r16[1], r16[1], false, false); l = __uint_as_float(r32[1]); }
    const float inv = __builtin_amdgcn_rcpf(l);
    const auto rO = RSRC(ws + WS_OB, QC_BYTES);
    const unsigned tok = (unsigned)(sub * 16 + ql) * 16u;
    { const int col = 28 * h + 4 * g; bstore64((u32x2){cvtpk(o0[0] * inv, o0[1] * inv), cvtpk(o0[2] * inv, o0[3] * inv)}, rO, (unsigned)(T * 56 + (col >> 3)) * 512u + tok + (unsigned)((col >> 2) & 1) * 8u); }
    if (g < 3) { const int col = 28 * h + 16 + 4 * g; bstore64((u32x2){cvtpk(o1[0] * inv, o1[1] * inv), cvtpk(o1[2] * inv, o1[3] * inv)}, rO, (unsigned)(T * 56 + (col >> 3)) * 512u + tok + (unsigned)((col >> 2) & 1) * 8u); }
}

template <int N>
__device__ __forceinline__ void wo_issue(const u16* wo, int ct, unsigned lds_base, int pc0, int lane) {
    const int r8 = lane >> 3, dpos = lane & 7;
    const unsigned woff = (unsigned)r8 * 896u + (unsigned)((dpos ^ ((r8 & 2) << 1)) << 4);
#pragma unroll
    for (int i = 0; i < N; ++i) glds16((const char*)wo + (size_t)ct * 128 + (size_t)(pc0 + i) * 8 * 896, woff, lds_base + (pc0 + i) * 1024);
}
__device__ __forceinline__ void wf_read(u32x4 (&wf)[2][14], const char* img, int kh, int lane) {
    const int g16 = lane >> 4, q = (lane & 15) >> 2, p = lane & 3;
#pragma unroll
    for (int i = 0; i < 14; ++i)
#pragma unroll
        for (int c = 0; c < 2; ++c) {
            const int row = (kh * 14 + i) * 16 + 8 * (g16 >> 1) + q;
            lds_cptr wp = (lds_cptr)img + (unsigned)row * 128u + (unsigned)(((2 * c + (g16 & 1)) ^ (q & 2)) * 32) + (unsigned)p * 8u;
            wf[c][i] = __builtin_bit_cast(u32x4, cat8(vtr(wp), vtr(wp + 512)));
        }
}
__device__ __forceinline__ void attnA_phase(char* ws, const u16* woA, int oct, int h, int rb, u64 tag, int rt, int ct, bool has_qkv, const QkvOut& outB, u32x4 (&wf)[2][14], char* lds, int wid, int lane) {
    const bool hi = lane >= 32, pe = hi && !(h & 1), po = !hi && (h & 1);
    const int m0 = (28 * h) >> 3;
    const auto rK = RSRC(ws + WS_KA, QC_BYTES), rV = RSRC(ws + WS_VA, V_BYTES);
    const unsigned lo16 = (unsigned)lane * 16u;
    const unsigned kbase = (unsigned)((wid * 8) * 56 + m0) * 512u, vbase = (unsigned)(h * 64 + wid * 8) * 2048u;
    const int rot = rb & 7;
    u32x4 qf[4][2], kf[3][2], vf[3][2];
    {   const unsigned k0 = kbase + (unsigned)rot * TILE_B, v0 = vbase + (unsigned)rot * 2048u, k1 = kbase + (unsigned)((rot + 1) & 7) * TILE_B;
        kf[0][0] = bload128(rK, lo16, k0); kf[0][1] = bload128(rK, lo16, k0 + 1024u);
        if (wid >= 4) { const unsigned b = (unsigned)((rb * 4 + wid - 4) * 56 + m0) * 512u; const auto rQ = RSRC(ws + WS_QA, QC_BYTES); qf[0][0] = bload128(rQ, lo16, b); qf[0][1] = bload128(rQ, lo16, b + 1024u); }
        vf[0][0] = bload128(rV, lo16, v0); vf[0][1] = bload128(rV, lo16, v0 + 1024u);
        kf[1][0] = bload128(rK, lo16, k1); kf[1][1] = bload128(rK, lo16, k1 + 1024u);
        const unsigned v1 = vbase + (unsigned)((rot + 1) & 7) * 2048u, k2 = kbase + (unsigned)((rot + 2) & 7) * TILE_B;
        vf[1][0] = bload128(rV, lo16, v1); vf[1][1] = bload128(rV, lo16, v1 + 1024u);
        kf[2][0] = bload128(rK, lo16, k2); kf[2][1] = bload128(rK, lo16, k2 + 1024u); }
    patch(kf[0][0], kf[0][1], pe, po, 0x3F803F80u, 0u);
    if (wid >= 4) {
        patch(qf[0][0], qf[0][1], pe, po, 0u, 0u);
        f32x16 s = {}; s = MFMA32(asb(kf[0][0]), asb(qf[0][0]), s); s = MFMA32(asb(kf[0][1]), asb(qf[0][1]), s);
        const float m = xhalf_max(max16(s)) + POFF;
        const unsigned mh = cvtpk(-m, 0.f) & 0xffffu; const float r = -m - __uint_as_float(mh << 16);
        patch(qf[0][0], qf[0][1], pe, po, mh | (cvtpk(r, 0.f) << 16), 0u);
        *(u32x4*)(lds + LDS_Q + (wid - 4) * 2048 + lane * 16) = qf[0][0]; *(u32x4*)(lds + LDS_Q + (wid - 4) * 2048 + 1024 + lane * 16) = qf[0][1];
        asm volatile("s_waitcnt lgkmcnt(0)" ::: "memory");
        if (lane == 0) ctl_add(lds, CTL_QCNT);
    }
    ctl_wait(lds, CTL_QCNT, 4);
    STAMP(6);
#pragma unroll
    for (int j = 0; j < 4; ++j) { qf[j][0] = *(const u32x4*)(lds + LDS_Q + j * 2048 + lane * 16); qf[j][1] = *(const u32x4*)(lds + LDS_Q + j * 2048 + 1024 + lane * 16); }
    if (has_qkv && wid < 4) {
        qkv_store<1, 0>(outB, ws, rt, ct, wid, lane); qkv_store<1, 1>(outB, ws, rt, ct, wid, lane); qkv_store<1, 2>(outB, ws, rt, ct, wid, lane);
        qkv_store<1, 3>(outB, ws, rt, ct, wid, lane); qkv_store<1, 4>(outB, ws, rt, ct, wid, lane); qkv_store<1, 5>(outB, ws, rt, ct, wid, lane);
    }
#pragma unroll
    for (int j = 0; j < 4; ++j) { PIN(qf[j][0]); PIN(qf[j][1]); }
    PIN(kf[0][0]); PIN(kf[0][1]);
    f32x16 o[4];
#define VEXP(d, s) asm volatile("v_exp_f32 %0, %1" : "=v"(d) : "v"(s))
#define VCVT(d, a, b) asm volatile("v_cvt_pk_bf16_f32 %0, %1, %2" : "=v"(d) : "v"(a), "v"(b))
#define MF0(d, a, b) asm volatile("v_mfma_f32_32x32x16_bf16 %0, %1, %2, 0" : "=&v"(d) : "v"(a), "v"(b))
#define MFA(d, a, b) asm volatile("v_mfma_f32_32x32x16_bf16 %0, %1, %2, %0" : "+v"(d) : "v"(a), "v"(b))
#define SLOT(PV, QK, Sc, Pc0, Pc1, Sn, kn0, kn1, qn0, qn1, Op, vp0, vp1, Pp0, Pp1, INJ) do { \
        float e0, e1, e2, e3, e4, e5, e6, e7, e8, e9, e10, e11, e12, e13, e14, e15; \
        if (QK) MF0(Sn, kn0, qn0); \
        VEXP(e0, Sc[0]); VEXP(e1, Sc[1]); VEXP(e2, Sc[2]); VEXP(e3, Sc[3]); \
        if (QK) MFA(Sn, kn1, qn1); \
        VEXP(e4, Sc[4]); VEXP(e5, Sc[5]); VEXP(e6, Sc[6]); VEXP(e7, Sc[7]); VCVT(Pc0[0], e0, e1); VCVT(Pc0[1], e2, e3); \
        INJ; \
        if ((PV) == 2) MFA(Op, vp0, Pp0); else if ((PV) == 1) MF0(Op, vp0, Pp0); \
        VEXP(e8, Sc[8]); VEXP(e9, Sc[9]); VEXP(e10, Sc[10]); VEXP(e11, Sc[11]); VCVT(Pc0[2], e4, e5); VCVT(Pc0[3], e6, e7); \
        if (PV) MFA(Op, vp1, Pp1); \
        VEXP(e12, Sc[12]); VEXP(e13, Sc[13]); VEXP(e14, Sc[14]); VEXP(e15, Sc[15]); VCVT(Pc1[0], e8, e9); VCVT(Pc1[1], e10, e11); VCVT(Pc1[2], e12, e13); VCVT(Pc1[3], e14, e15); \
    } while (0)
    f32x16 sA, sB; u32x4 pA0, pA1, pB0, pB1;
    asm volatile("s_nop 1\n\tv_mfma_f32_32x32x16_bf16 %0, %1, %2, 0\n\tv_mfma_f32_32x32x16_bf16 %0, %3, %4, %0\n\ts_nop 7\n\ts_nop 3" : "=&v"(sA) : "v"(kf[0][0]), "v"(qf[0][0]), "v"(kf[0][1]), "v"(qf[0][1]));
#define PATCHK(kx) do { PIN(kx[0]); PIN(kx[1]); patch(kx[0], kx[1], pe, po, 0x3F803F80u, 0u); PIN(kx[0]); PIN(kx[1]); } while (0)
#define LOADK(kc, tt) do { if ((tt) < 8) { const unsigned o_ = kbase + (unsigned)(((tt) + rot) & 7) * TILE_B; kc[0] = bload128(rK, lo16, o_); kc[1] = bload128(rK, lo16, o_ + 1024u); } } while (0)
#define LOADV(vx, tt) do { if ((tt) < 8) { const unsigned o_ = vbase + (unsigned)(((tt) + rot) & 7) * 2048u; vx[0] = bload128(rV, lo16, o_); vx[1] = bload128(rV, lo16, o_ + 1024u); } } while (0)
#define TILE(t) do { \
        SLOT((t) == 0 ? 0 : (t) == 1 ? 1 : 2, true, sA, pA0, pA1, sB, kf[(t) % 3][0], kf[(t) % 3][1], qf[1][0], qf[1][1], o[3], vf[((t) + 2) % 3][0], vf[((t) + 2) % 3][1], pB0, pB1, (void)0); \
        LOADV(vf[((t) + 2) % 3], (t) + 2); \
        SLOT((t) == 0 ? 1 : 2, true, sB, pB0, pB1, sA, kf[(t) % 3][0], kf[(t) % 3][1], qf[2][0], qf[2][1], o[0], vf[(t) % 3][0], vf[(t) % 3][1], pA0, pA1, (void)0); \
        SLOT((t) == 0 ? 1 : 2, true, sA, pA0, pA1, sB, kf[(t) % 3][0], kf[(t) % 3][1], qf[3][0], qf[3][1], o[1], vf[(t) % 3][0], vf[(t) % 3][1], pB0, pB1, if ((t) < 7) PATCHK(kf[((t) + 1) % 3])); \
        LOADK(kf[(t) % 3], (t) + 3); \
        SLOT((t) == 0 ? 1 : 2, (t) < 7, sB, pB0, pB1, sA, kf[((t) + 1) % 3][0], kf[((t) + 1) % 3][1], qf[0][0], qf[0][1], o[2], vf[(t) % 3][0], vf[(t) % 3][1], pA0, pA1, (void)0); \
    } while (0)
    AttnBPre bpre; u64 flb = 0; bool bready = false;
#pragma unroll
    for (int t = 0; t < 8; t += 2) {
        if (t == 0) __builtin_amdgcn_s_setprio(3); else if (t == 2) __builtin_amdgcn_s_setprio(2); else if (t == 4) __builtin_amdgcn_s_setprio(1); else __builtin_amdgcn_s_setprio(0);
        TILE(t);
        if (t == 4) { bready = __all(flb == tag); attnB_load(bpre, ws, h, rb, wid, opaque_lane()); }

        TILE(t + 1);
        if (t == 0) {
            if (has_qkv && wid < 4) {
                asm volatile("s_waitcnt vmcnt(8)" ::: "memory");
                if (opaque_lane() == 0 && ctl_add(lds, CTL_F2CNT) == 3) flag_set(ws, F2 + ct * 32 + rt, tag);
            }
            if (oct < 7 && wid >= 4) {
                if (has_qkv) ctl_wait(lds, CTL_RDONE, 4);
                wo_issue<14>(woA, oct, (unsigned)(uintptr_t)lds, (wid - 4) * 14, opaque_lane());
            }
        } else if (t == 2) {
            const int ln = opaque_lane();
            if (oct < 7 && wid >= 4) { asm volatile("s_waitcnt vmcnt(8)" ::: "memory"); if (ln == 0) ctl_add(lds, CTL_WA); }
            flb = __hip_atomic_load(flagp(ws, f2_index(h, rb, ln)), __ATOMIC_RELAXED, __HIP_MEMORY_SCOPE_AGENT);
        }
    }
    asm volatile("s_nop 1\n\tv_mfma_f32_32x32x16_bf16 %0, %1, %2, %0\n\tv_mfma_f32_32x32x16_bf16 %0, %3, %4, %0\n\ts_nop 7\n\ts_nop 3" : "+v"(o[3]) : "v"(vf[1][0]), "v"(pB0), "v"(vf[1][1]), "v"(pB1));
    asm volatile("" : "+v"(o[0]), "+v"(o[1]), "+v"(o[2]));
    STAMP(7);
    lane = opaque_lane();
    if (has_qkv) ctl_wait(lds, CTL_RDONE, 4);
#pragma unroll
    for (int j = 0; j < 4; ++j) merge_write1(o[j], lds, wid, j, lane);
    if (oct < 7 && wid < 4) { ctl_wait(lds, CTL_WA, 4); wf_read(wf, lds, wid & 1, lane); }
    STAMP(15);
    BARRIER();
    STAMP(10);
    const bool bad = merge_reduce(ws, h, rb, lds, wid, lane);
    if (__any(bad) && lane == 0) ctl_store(lds, CTL_BAD, 1);
    STAMP(8);
    if (!bready) { flag_wait(ws, f2_index(h, rb, lane), tag); attnB_load(bpre, ws, h, rb, wid, lane); }
    attnB_compute(bpre, ws, h, rb, wid, lane);
    asm volatile("s_waitcnt vmcnt(2) lgkmcnt(0)" ::: "memory");
    if (lane == 0 && ctl_add(lds, CTL_F3CNT) == 7 && !ctl_load(lds, CTL_BAD)) flag_set(ws, F3 + rb * 16 + h, tag);
    asm volatile("s_waitcnt vmcnt(0)" ::: "memory");
    if (lane == 0 && ctl_add(lds, CTL_F4CNT) == 7) flag_set(ws, F4 + rb * 16 + h, tag);
    ctl_wait(lds, CTL_F3CNT, 8);
    if (__builtin_amdgcn_readfirstlane(ctl_load(lds, CTL_BAD))) {
        BARRIER();
        attnA_safe(ws, h, rb, lds, wid, lane);
        BARRIER();
        merge_reduce(ws, h, rb, lds, wid, lane);
        DRAIN_BARRIER();
        if (threadIdx.x == 0) flag_set(ws, F3 + rb * 16 + h, tag);
    }
}

__device__ __forceinline__ void oproj_compute(char* ws, const u16* woB, u16* out, int rt, int ct, u64 tag, u32x4 (&wf)[2][14], char* lds, int wid, int lane) {
    const int br = wid >> 2, tt = (wid >> 1) & 1, kh = wid & 1;
    if (wid < 3) {
        if (wid < 2) wo_issue<19>(woB, ct, (unsigned)(uintptr_t)lds + LDS_MRG, wid * 19, lane); else wo_issue<18>(woB, ct, (unsigned)(uintptr_t)lds + LDS_MRG, 38, lane);
    }
    if ((wid & 3) == 3) { flag_wait(ws, (br ? F4 : F3) + (rt >> 1) * 16 + (lane & 15), tag); if (lane == 0) ctl_store(lds, br ? CTL_F4 : CTL_F3, 1); }
    else {
        if (wid < 3) { asm volatile("s_waitcnt vmcnt(0)" ::: "memory"); if (lane == 0) ctl_add(lds, CTL_WB); }
        ctl_wait(lds, br ? CTL_F4 : CTL_F3, 1);
    }
    STAMP(11);
    const auto rO = RSRC(ws + (br ? WS_OB : WS_OA), QC_BYTES);
    const int tile = rt * 2 + tt;
    u32x4 of[14];
#pragma unroll
    for (int i = 0; i < 14; ++i) of[i] = bload128(rO, (unsigned)lane * 16u, (unsigned)(tile * 56 + 2 * (kh * 14 + i)) * 512u);
    if (br) { ctl_wait(lds, CTL_WB, 3); wf_read(wf, lds + LDS_MRG, kh, lane); }
    f32x16 acc[2] = {};
#pragma unroll
    for (int i = 0; i < 14; ++i) { acc[0] = MFMA32(asb(wf[0][i]), asb(of[i]), acc[0]); acc[1] = MFMA32(asb(wf[1][i]), asb(of[i]), acc[1]); }
    asm volatile("" : "+v"(acc[0]), "+v"(acc[1]));
    STAMP(12);
    {   const f32x16& gv = kh ? acc[0] : acc[1];
#pragma unroll
        for (int rq = 0; rq < 4; ++rq) *(f32x4*)(lds + 114688 + (wid * 4 + rq) * 1024 + lane * 16) = (f32x4){gv[4 * rq], gv[4 * rq + 1], gv[4 * rq + 2], gv[4 * rq + 3]};
    }
    asm volatile("s_waitcnt lgkmcnt(0)" ::: "memory");
    if (lane == 0) ctl_store(lds, CTL_X + wid * 4, 1);
    ctl_wait(lds, CTL_X + (wid ^ 1) * 4, 1);
    STAMP(13);
    f32x16 r = kh ? acc[1] : acc[0];
#pragma unroll
    for (int rq = 0; rq < 4; ++rq) { const f32x4 x = *(const f32x4*)(lds + 114688 + ((wid ^ 1) * 4 + rq) * 1024 + lane * 16); r[4 * rq] += x[0]; r[4 * rq + 1] += x[1]; r[4 * rq + 2] += x[2]; r[4 * rq + 3] += x[3]; }
    const unsigned orow = (unsigned)(rt * 64 + tt * 32 + (lane & 31)) * 1792u + (unsigned)(br * 448 + ct * 64 + kh * 32 + (lane >> 5) * 8) * 2u;
#pragma unroll
    for (int pr = 0; pr < 2; ++pr) {
        const unsigned a0 = cvtpk(r[8 * pr], r[8 * pr + 1]), a1 = cvtpk(r[8 * pr + 2], r[8 * pr + 3]), b0 = cvtpk(r[8 * pr + 4], r[8 * pr + 5]), b1 = cvtpk(r[8 * pr + 6], r[8 * pr + 7]);
        auto r0 = __builtin_amdgcn_permlane32_swap(a0, b0, false, false);
        auto r1 = __builtin_amdgcn_permlane32_swap(a1, b1, false, false);
        bstore128((u32x4){r0[0], r1[0], r0[1], r1[1]}, RSRC(out, 2048 * 896 * 2), orow + pr * 32u);
    }
}

__global__ void __launch_bounds__(512, 2) fused_kernel(const u16* xA, const u16* wA, const u16* woA, const u16* xB, const u16* wB, const u16* woB, u16* out, char* ws) {
    extern __shared__ __attribute__((aligned(16))) char lds[];
    int lane = threadIdx.x & 63; const int wid = __builtin_amdgcn_readfirstlane(threadIdx.x >> 6);
    const int id = blockIdx.x, xcd = id & 7, loc = id >> 3;
    STAMP_INIT();
    STAMP(0);
    if (threadIdx.x < 32) ctl_store(lds, threadIdx.x * 4, 0);
    const u64 tag = __hip_atomic_load(flagp(ws, FEP), __ATOMIC_RELAXED, __HIP_MEMORY_SCOPE_AGENT) + 1;
    const int gb = xcd & 1, grt = 8 * (xcd >> 1) + (loc & 7), qct = gb * 4 + (loc >> 3);
    const bool has_qkv = qct < 7;
    const int h = xcd * 2 + (loc & 1), rb = loc >> 1;
    const int ort = 4 * xcd + (loc & 3), oct = loc >> 2;
    unsigned warm0 = 0;
    if (wid < 4) {
        u64 pc; int len;
        asm volatile("s_getpc_b64 %0\n.Lfused_pc_ref:\n\ts_mov_b32 %1, .Lfused_code_end - .Lfused_pc_ref" : "=s"(pc), "=s"(len));
        const int off = (wid * 64 + lane) * 128;
        if (off < len) warm0 = *(const __attribute__((address_space(1))) unsigned*)(pc + off);
    }
    QkvOut outB;
    if (has_qkv) qkv_phase(xA, wA, xB, wB, ws, grt, qct, tag, lds, wid, lane, outB);
    else { if (threadIdx.x == 0) flag_set(ws, F0 + (xcd >> 1) * 8 + (loc & 7), tag); BARRIER(); }
    asm volatile("" :: "v"(warm0));
    STAMP(4);
    if (wid == 4) {
        flag_wait(ws, F1 + (lane < 32 ? (28 * h) >> 6 : (28 * h + 27) >> 6) * 32 + (lane & 31), tag);
        if (lane == 0) ctl_store(lds, CTL_F1, 1);
    } else ctl_wait(lds, CTL_F1, 1);
    STAMP(5);
    const u64 tags = (u64)(unsigned)__builtin_amdgcn_readfirstlane((unsigned)tag) | ((u64)(unsigned)__builtin_amdgcn_readfirstlane((unsigned)(tag >> 32)) << 32);
    u32x4 wf[2][14];
    attnA_phase(ws, woA, oct, h, rb, tags, grt, qct, has_qkv, outB, wf, lds, wid, lane);
    STAMP(9);
    lane = opaque_lane();
    if (oct < 7) {
        oproj_compute(ws, woB, out, ort, oct, tags, wf, lds, wid, lane);
    }
    STAMP(14);
    if (id == 0 && wid == 0) {
        flag_wait(ws, F0 + (lane & 31), tags);
        if (lane == 0) flag_set(ws, FEP, tags);
    }
    STAMP_DUMP();
    asm volatile(".Lfused_code_end:");
}

extern "C" void kernel_launch(void* const* d_in, const int* in_sizes, int n_in, void* d_out, int out_size, void* d_ws, size_t ws_size, hipStream_t stream) {
    const u16* xA = (const u16*)d_in[0]; const u16* wA = (const u16*)d_in[1]; const u16* woA = (const u16*)d_in[2];
    const u16* xB = (const u16*)d_in[3]; const u16* wB = (const u16*)d_in[4]; const u16* woB = (const u16*)d_in[5];
    (void)hipFuncSetAttribute((const void*)fused_kernel, hipFuncAttributeMaxDynamicSharedMemorySize, LDS_TOTAL + LDS_EXTRA);
    fused_kernel<<<256, 512, LDS_TOTAL + LDS_EXTRA, stream>>>(xA, wA, woA, xB, wB, woB, (u16*)d_out, (char*)d_ws);
}
```

```cpp
#include <hip/hip_runtime.h>
#include <hip/hip_bf16.h>
#include <stdint.h>

typedef unsigned short u16;
typedef unsigned long long u64;
typedef short bf16x8 __attribute__((ext_vector_type(8)));
typedef short s16x4 __attribute__((ext_vector_type(4)));
typedef float f32x4 __attribute__((ext_vector_type(4)));
typedef float f32x16 __attribute__((ext_vector_type(16)));
typedef unsigned u32x4 __attribute__((ext_vector_type(4)));
typedef unsigned u32x2 __attribute__((ext_vector_type(2)));
typedef __attribute__((address_space(3))) const char* lds_cptr;
typedef __attribute__((address_space(1))) u64 gu64;

constexpr int TILE_B = 56 * 512;
constexpr size_t QC_BYTES = (size_t)64 * TILE_B;
constexpr size_t V_BYTES = (size_t)16 * 64 * 2048;
constexpr size_t WS_QA = 0, WS_KA = WS_QA + QC_BYTES, WS_VA = WS_KA + QC_BYTES;
constexpr size_t WS_QB = WS_VA + V_BYTES, WS_KB = WS_QB + QC_BYTES, WS_VB = WS_KB + QC_BYTES;
constexpr size_t WS_OA = WS_VB + V_BYTES, WS_OB = WS_OA + QC_BYTES, WS_FLAGS = WS_OB + QC_BYTES;
constexpr int F1 = 0, F2 = 256, F3 = 512, F4 = 768, F0 = 1024, FEP = 1056;
constexpr float QSCALE = 0.18898223650461362f * 1.4426950408889634f;
constexpr int STG = 32768;
constexpr int LDS_Q = 147456, LDS_L = 131072, LDS_MRG = 57344, LDS_CTL = LDS_Q + 8192, LDS_MX = LDS_CTL + 128, LDS_TOTAL = LDS_MX + 1024 + 64;
constexpr int CTL_F1 = 0, CTL_F2CNT = 4, CTL_BAD = 8, CTL_QCNT = 12, CTL_F4CNT = 16, CTL_F3 = 20, CTL_RDONE = 24, CTL_F4 = 28, CTL_X = 32;
constexpr int CTL_WA = 64, CTL_WB = 68, CTL_S12 = 72, CTL_S13 = 76, CTL_F3CNT = 80;
constexpr int PUBG = 10;
#define POFF 96.0f
#define LMIN (__builtin_ldexpf(1.0f, -(int)(POFF) - 20))
constexpr unsigned SPIN_MAX = 1u << 18;


#define MFMA16(a, b, c) __builtin_amdgcn_mfma_f32_16x16x32_bf16(a, b, c, 0, 0, 0)
#define MFMA32(a, b, c) __builtin_amdgcn_mfma_f32_32x32x16_bf16(a, b, c, 0, 0, 0)
#define PIN(x) asm volatile("" : "+v"(x))
#define SBAR() __builtin_amdgcn_sched_barrier(0)
#define SC1 16

__device__ __forceinline__ unsigned cvtpk(float lo, float hi) {
    typedef float f2 __attribute__((ext_vector_type(2))); typedef __bf16 b2 __attribute__((ext_vector_type(2)));
    f2 v = {lo, hi}; b2 r = __builtin_convertvector(v, b2); return __builtin_bit_cast(unsigned, r);
}
__device__ __forceinline__ void glds16(const void* sbase, unsigned voff, unsigned lds_base) {
    asm volatile("s_mov_b32 m0, %2\n\ts_nop 0\n\tglobal_load_lds_dwordx4 %0, %1" :: "v"(voff), "s"(sbase), "s"(lds_base) : "memory");
}
__device__ __forceinline__ s16x4 vtr(lds_cptr p) {
    typedef short v4i16_t __attribute__((ext_vector_type(4)));
    return __builtin_bit_cast(s16x4, __builtin_amdgcn_ds_read_tr16_b64_v4i16((__attribute__((address_space(3))) v4i16_t*)p));
}
__device__ __forceinline__ bf16x8 cat8(s16x4 a, s16x4 b) { return (bf16x8){a[0], a[1], a[2], a[3], b[0], b[1], b[2], b[3]}; }
__device__ __forceinline__ bf16x8 ldsrd128(lds_cptr p) { return *(const __attribute__((address_space(3))) bf16x8*)p; }
#define RSRC(p, bytes) __builtin_amdgcn_make_buffer_rsrc((void*)(p), 0, (int)(bytes), 0x00020000)
__device__ __forceinline__ u32x4 bload128(__amdgpu_buffer_rsrc_t r, unsigned voff, unsigned soff) { return __builtin_amdgcn_raw_buffer_load_b128(r, voff, soff, SC1); }
__device__ __forceinline__ void bstore128(u32x4 v, __amdgpu_buffer_rsrc_t r, unsigned voff) { __builtin_amdgcn_raw_buffer_store_b128(v, r, voff, 0, SC1); }
__device__ __forceinline__ void bstore64(u32x2 v, __amdgpu_buffer_rsrc_t r, unsigned voff) { __builtin_amdgcn_raw_buffer_store_b64(v, r, voff, 0, SC1); }
__device__ __forceinline__ bf16x8 asb(u32x4 v) { return __builtin_bit_cast(bf16x8, v); }

__device__ __forceinline__ gu64* flagp(char* ws, int idx) { return (gu64*)(ws + WS_FLAGS + (size_t)idx * 128); }
__device__ __forceinline__ void flag_set(char* ws, int idx, u64 tag) { __hip_atomic_store(flagp(ws, idx), tag, __ATOMIC_RELAXED, __HIP_MEMORY_SCOPE_AGENT); }
__device__ __forceinline__ void flag_wait(char* ws, int idx, u64 tag) {
    gu64* p = flagp(ws, idx);
    for (unsigned spins = 0; spins < SPIN_MAX; ++spins) {
        const u64 v = __hip_atomic_load(p, __ATOMIC_RELAXED, __HIP_MEMORY_SCOPE_AGENT);
        if (__all(v == tag)) break;
        __builtin_amdgcn_s_sleep(1);
    }
}
__device__ __forceinline__ int opaque_lane() { int l; asm volatile("v_mbcnt_lo_u32_b32 %0, -1, 0\n\tv_mbcnt_hi_u32_b32 %0, -1, %0" : "=v"(l)); return l; }
__device__ __forceinline__ int f2_index(int h, int rb, int lane) { const int i = lane % 6, r = 2 * rb - 1 + i % 3; return F2 + (i < 3 ? (28 * h) >> 6 : (28 * h + 27) >> 6) * 32 + (r < 0 ? 0 : r); }
typedef __attribute__((address_space(3))) int* lds_iptr;
__device__ __forceinline__ int ctl_load(char* lds, int off) { return __hip_atomic_load((lds_iptr)(lds + LDS_CTL + off), __ATOMIC_RELAXED, __HIP_MEMORY_SCOPE_WORKGROUP); }
__device__ __forceinline__ void ctl_store(char* lds, int off, int v) { __hip_atomic_store((lds_iptr)(lds + LDS_CTL + off), v, __ATOMIC_RELAXED, __HIP_MEMORY_SCOPE_WORKGROUP); }
__device__ __forceinline__ void ctl_wait(char* lds, int off, int n) {
    for (unsigned spins = 0; spins < (SPIN_MAX << 6); ++spins) { if (__builtin_amdgcn_readfirstlane(ctl_load(lds, off)) >= n) break; __builtin_amdgcn_s_sleep(1); }
    asm volatile("" ::: "memory");
}
__device__ __forceinline__ int ctl_add(char* lds, int off) { return __hip_atomic_fetch_add((lds_iptr)(lds + LDS_CTL + off), 1, __ATOMIC_RELAXED, __HIP_MEMORY_SCOPE_WORKGROUP); }
#define BARRIER() asm volatile("s_waitcnt lgkmcnt(0)\n\ts_barrier" ::: "memory")
#define DRAIN_BARRIER() asm volatile("s_waitcnt vmcnt(0) lgkmcnt(0)\n\ts_barrier" ::: "memory")

struct QkvFrag { bf16x8 x[2][4], w[2][3]; };
struct QkvAcc { f32x4 q[4], k[4], v[4]; };
struct QkvOut { u32x4 q[2], k[2], v[2]; };
__device__ __forceinline__ void qkv_pack(QkvOut& o, const QkvAcc& a, float qs) {
#pragma unroll
    for (int tb = 0; tb < 2; ++tb) {
        const f32x4 q0 = a.q[2 * tb] * qs, q1 = a.q[2 * tb + 1] * qs, k0 = a.k[2 * tb], k1 = a.k[2 * tb + 1], v0 = a.v[2 * tb], v1 = a.v[2 * tb + 1];
        { auto ra = __builtin_amdgcn_permlane16_swap(cvtpk(q0[0], q0[1]), cvtpk(q1[0], q1[1]), false, false); auto rb = __builtin_amdgcn_permlane16_swap(cvtpk(q0[2], q0[3]), cvtpk(q1[2], q1[3]), false, false);
          o.q[tb] = (u32x4){ra[0], rb[0], ra[1], rb[1]}; }
        { auto ra = __builtin_amdgcn_permlane16_swap(cvtpk(k0[0], k0[1]), cvtpk(k1[0], k1[1]), false, false); auto rb = __builtin_amdgcn_permlane16_swap(cvtpk(k0[2], k0[3]), cvtpk(k1[2], k1[3]), false, false);
          o.k[tb] = (u32x4){ra[0], rb[0], ra[1], rb[1]}; }
        { auto ra = __builtin_amdgcn_permlane32_swap(cvtpk(v0[0], v0[1]), cvtpk(v1[0], v1[1]), false, false); auto rb = __builtin_amdgcn_permlane32_swap(cvtpk(v0[2], v0[3]), cvtpk(v1[2], v1[3]), false, false);
          o.v[tb] = (u32x4){ra[0], rb[0], ra[1], rb[1]}; }
    }
}
template <int BR, int I>
__device__ __forceinline__ void qkv_store(const QkvOut& o, char* ws, int rt, int ct, int c, int lane) {
    constexpr int tb = I / 3, kind = I % 3;
    const int tile = rt * 2 + tb;
    if (kind < 2) {
        const unsigned off = (unsigned)(tile * 56 + 8 * ct + 2 * c) * 512u + (unsigned)lane * 16u;
        if (kind == 0) bstore128(o.q[tb], RSRC(ws + (BR ? WS_QB : WS_QA), QC_BYTES), off); else bstore128(o.k[tb], RSRC(ws + (BR ? WS_KB : WS_KA), QC_BYTES), off);
    } else {
        const int n = 64 * ct + 16 * c + (lane & 15), h = n / 28, d = n - 28 * h;
        const unsigned off = (unsigned)(h * 64 + tile) * 2048u + (unsigned)(lane >> 4) * 512u + (unsigned)d * 16u;
        const auto rs = RSRC(ws + (BR ? WS_VB : WS_VA), V_BYTES);
        bstore128(o.v[tb], rs, off);
        if (d >= 24) { const unsigned pad = d == 24 ? 0x3F803F80u : 0u; bstore128((u32x4){pad, pad, pad, pad}, rs, off + 64u); }
    }
}

__device__ __forceinline__ void qkv_phase(const u16* xA, const u16* wA, const u16* xB, const u16* wB, char* ws, int rt, int ct, u64 tag, char* lds, int wid, int lane, QkvOut& outB) {
    if (wid >= 4) {
        const unsigned lds0 = (unsigned)(uintptr_t)lds;
        const int lw = wid - 4, r8 = lane >> 3, dpos = lane & 7;
        unsigned xoff[2], woff[2];
#pragma unroll
        for (int i = 0; i < 2; ++i) { const int row = 16 * lw + 8 * i + r8;
            xoff[i] = (unsigned)row * 896u + (unsigned)((dpos ^ ((row >> 1) & 7)) << 4);
            const int f2 = ((row >> 1) & 1) | (((row >> 3) & 1) << 1);
            woff[i] = (unsigned)row * 2688u + (unsigned)((((dpos >> 1) ^ f2) * 2 + (dpos & 1)) << 4); }
        const int rot = __builtin_amdgcn_readfirstlane(((rt & 7) + 2 * (ct & 3)) % 7);
        const char* xa = (const char*)xA + (size_t)rt * 64 * 896; const char* wa = (const char*)wA + (size_t)ct * 128;
        const char* xb = (const char*)xB + (size_t)rt * 64 * 896; const char* wb = (const char*)wB + (size_t)ct * 128;
#define QKV_ISSUE(g) do { int st_ = ((g) % 7) + rot; st_ = st_ >= 7 ? st_ - 7 : st_; const char* x_ = ((g) < 7 ? xa : xb) + st_ * 128; const char* w_ = ((g) < 7 ? wa : wb) + (size_t)st_ * (64 * 2688); \
        const unsigned sl_ = lds0 + ((g) & 3) * STG + lw * 2048; \
        glds16(x_, xoff[0], sl_); glds16(x_, xoff[1], sl_ + 1024); \
        glds16(w_, woff[0], sl_ + 8192); glds16(w_, woff[1], sl_ + 8192 + 1024); glds16(w_ + 896, woff[0], sl_ + 16384); glds16(w_ + 896, woff[1], sl_ + 16384 + 1024); \
        glds16(w_ + 1792, woff[0], sl_ + 24576); glds16(w_ + 1792, woff[1], sl_ + 24576 + 1024); } while (0)
        QKV_ISSUE(0); QKV_ISSUE(1);
#pragma unroll
        for (int g = 0; g < 12; ++g) {
            asm volatile("s_waitcnt vmcnt(8) lgkmcnt(0)\n\ts_barrier" ::: "memory");
            QKV_ISSUE(g + 2);
        }
        asm volatile("s_waitcnt vmcnt(8)" ::: "memory");
        if (lane == 0) ctl_add(lds, CTL_S12);
        asm volatile("s_waitcnt vmcnt(0)" ::: "memory");
        if (lane == 0) ctl_add(lds, CTL_S13);
        return;
    }
    const int c = wid, g4 = lane >> 4, t16 = lane & 15;
    const unsigned xr0 = (unsigned)t16 * 128u + (unsigned)((g4 ^ (t16 >> 1)) << 4);
    const int q = t16 >> 2, p = lane & 3;
    const int rf2 = ((q >> 1) & 1) | ((g4 & 1) << 1);
    const unsigned wr0 = 8192u + (unsigned)(8 * g4 + q) * 128u + (unsigned)((c ^ rf2) * 32) + (unsigned)p * 8u;
    QkvAcc acc; QkvOut outA;
#pragma unroll
    for (int i = 0; i < 4; ++i) acc.q[i] = acc.k[i] = acc.v[i] = (f32x4){};
    QkvFrag fr[2];
#define QMF(i) do { if (g > 0) { constexpr int ks_ = (i) / 12, s_ = ((i) % 12) / 4, b_ = (i) % 4; const QkvFrag& f_ = fr[(g - 1) & 1]; \
        if (s_ == 0) acc.q[b_] = MFMA16(f_.w[ks_][0], f_.x[ks_][b_], acc.q[b_]); if (s_ == 1) acc.k[b_] = MFMA16(f_.w[ks_][1], f_.x[ks_][b_], acc.k[b_]); \
        if (s_ == 2) acc.v[b_] = MFMA16(f_.x[ks_][b_], f_.w[ks_][2], acc.v[b_]); } SBAR(); } while (0)
#define QRX(ks, b) do { if (g < 14) fr[g & 1].x[ks][b] = ldsrd128(sl + (xr0 ^ ((ks) * 64)) + (b) * 2048); } while (0)
#define QRW(ks, s) do { if (g < 14) { lds_cptr wp_ = sl + wr0 + (ks) * 4096 + (s) * 8192; fr[g & 1].w[ks][s] = cat8(vtr(wp_), vtr(wp_ + 512)); } } while (0)
#pragma unroll
    for (int g = 0; g < 15; ++g) {
        if (g == PUBG) asm volatile("s_waitcnt vmcnt(0) lgkmcnt(0)\n\ts_barrier" ::: "memory");
        else if (g < 12) asm volatile("s_waitcnt lgkmcnt(0)\n\ts_barrier" ::: "memory");
        else if (g == 12) ctl_wait(lds, CTL_S12, 4);
        else if (g == 13) ctl_wait(lds, CTL_S13, 4);
        SBAR();
        lds_cptr sl = (lds_cptr)lds + (g & 3) * STG;
        if (g == PUBG && threadIdx.x == 0) flag_set(ws, F1 + ct * 32 + rt, tag);
        SBAR();
        QRX(0, 0); QMF(0); QRX(0, 1); QMF(1); QRX(0, 2); QMF(2); QRX(0, 3); QMF(3); QRW(0, 0); QMF(4); QRW(0, 1); QMF(5); QRW(0, 2); QMF(6);
        QRX(1, 0); QMF(7); QRX(1, 1); QMF(8); QRX(1, 2); QMF(9); QRX(1, 3); QMF(10); QRW(1, 0); QMF(11); QRW(1, 1); QMF(12); QRW(1, 2); QMF(13);
        QMF(14); QMF(15); QMF(16); QMF(17); QMF(18); QMF(19); QMF(20); QMF(21); QMF(22); QMF(23);
        if (g == 7) {
            qkv_pack(outA, acc, QSCALE);
            qkv_store<0, 1>(outA, ws, rt, ct, c, lane); qkv_store<0, 2>(outA, ws, rt, ct, c, lane); qkv_store<0, 4>(outA, ws, rt, ct, c, lane);
            qkv_store<0, 5>(outA, ws, rt, ct, c, lane); qkv_store<0, 0>(outA, ws, rt, ct, c, lane); qkv_store<0, 3>(outA, ws, rt, ct, c, lane);
#pragma unroll
            for (int i = 0; i < 4; ++i) acc.q[i] = acc.k[i] = acc.v[i] = (f32x4){};
        }
    }
    if (lane == 0) ctl_add(lds, CTL_RDONE);
    qkv_pack(outB, acc, 1.0f);
}

__device__ __forceinline__ float max16(const f32x16& s) {
    float a = fmaxf(fmaxf(s[0], s[1]), s[2]), b = fmaxf(fmaxf(s[3], s[4]), s[5]);
    a = fmaxf(fmaxf(a, s[6]), s[7]); b = fmaxf(fmaxf(b, s[8]), s[9]); a = fmaxf(fmaxf(a, s[10]), s[11]); b = fmaxf(fmaxf(b, s[12]), s[13]);
    a = fmaxf(fmaxf(a, s[14]), s[15]); return fmaxf(a, b);
}
__device__ __forceinline__ float xhalf_max(float m) {
    auto r = __builtin_amdgcn_permlane32_swap(__float_as_uint(m), __float_as_uint(m), false, false);
    return fmaxf(__uint_as_float(r[0]), __uint_as_float(r[1]));
}
__device__ __forceinline__ void patch(u32x4& f0, u32x4& f1, bool pe, bool po, unsigned a, unsigned b) {
    f0[0] = po ? a : f0[0]; f0[1] = po ? b : f0[1]; f1[2] = pe ? a : f1[2]; f1[3] = pe ? b : f1[3];
}
__device__ __forceinline__ void exp_pack(const f32x16& s, u32x4& p0, u32x4& p1) {
    float e[16];
#pragma unroll
    for (int r = 0; r < 16; ++r) e[r] = __builtin_amdgcn_exp2f(s[r]);
    p0 = (u32x4){cvtpk(e[0], e[1]), cvtpk(e[2], e[3]), cvtpk(e[4], e[5]), cvtpk(e[6], e[7])};
    p1 = (u32x4){cvtpk(e[8], e[9]), cvtpk(e[10], e[11]), cvtpk(e[12], e[13]), cvtpk(e[14], e[15])};
}
__device__ __forceinline__ void merge_write1(const f32x16& o, char* lds, int wid, int j, int lane) {
#pragma unroll
    for (int hf = 0; hf < 2; ++hf)
        *(u32x4*)(lds + LDS_MRG + ((wid * 4 + j) * 2 + hf) * 1024 + lane * 16) = (u32x4){cvtpk(o[8 * hf], o[8 * hf + 1]), cvtpk(o[8 * hf + 2], o[8 * hf + 3]), cvtpk(o[8 * hf + 4], o[8 * hf + 5]), cvtpk(o[8 * hf + 6], o[8 * hf + 7])};
    if (lane >= 32) *(float*)(lds + LDS_L + ((wid * 4 + j) * 32 + lane - 32) * 4) = o[12];
}
__device__ __forceinline__ bool merge_reduce(char* ws, int h, int rb, char* lds, int wid, int lane) {
    const bool hi = lane >= 32;
    const int j = wid >> 1, hf = wid & 1;
    const int tile = rb * 4 + j, q = lane & 31;
    u32x4 p[8]; float pl[8];
#pragma unroll
    for (int w = 0; w < 8; ++w) pl[w] = *(const float*)(lds + LDS_L + ((w * 4 + j) * 32 + q) * 4);
#pragma unroll
    for (int w = 0; w < 8; ++w) p[w] = *(const u32x4*)(lds + LDS_MRG + ((w * 4 + j) * 2 + hf) * 1024 + lane * 16);
    const int col0 = 28 * h + 16 * hf + (hi ? 4 : 0), col1 = col0 + 8;
    const unsigned off0 = (unsigned)((tile * 56 + (col0 >> 3)) * 32 + q) * 16u + (unsigned)((col0 >> 2) & 1) * 8u, off1 = (unsigned)((tile * 56 + (col1 >> 3)) * 32 + q) * 16u + (unsigned)((col1 >> 2) & 1) * 8u;
    const float l = ((pl[0] + pl[1]) + (pl[2] + pl[3])) + ((pl[4] + pl[5]) + (pl[6] + pl[7]));
    const float inv = __builtin_amdgcn_rcpf(l);
    u32x4 sel;
    {   const int row = lane & 31, i = 4 * (row >> 3) + (row & 3);
        const unsigned one = (row < 16 && ((row >> 2) & 1) == (lane >> 5)) ? ((i & 1) ? 0x3F800000u : 0x00003F80u) : 0u;
        sel = (u32x4){(i >> 1) == 0 ? one : 0u, (i >> 1) == 1 ? one : 0u, (i >> 1) == 2 ? one : 0u, (i >> 1) == 3 ? one : 0u}; }
    f32x16 sum = {};
#pragma unroll
    for (int w = 0; w < 8; ++w) sum = MFMA32(asb(sel), asb(p[w]), sum);
    float a[8];
#pragma unroll
    for (int i = 0; i < 8; ++i) a[i] = sum[i];
#pragma unroll
    for (int i = 0; i < 8; ++i) a[i] *= inv;
    const auto rO = RSRC(ws + WS_OA, QC_BYTES);
    const bool last = hf == 1 && hi;
    bstore64((u32x2){cvtpk(a[0], a[1]), cvtpk(a[2], a[3])}, rO, off0);
    if (!last) bstore64((u32x2){cvtpk(a[4], a[5]), cvtpk(a[6], a[7])}, rO, off1);
    float z = a[0] * 0.f; z = fmaf(a[1], 0.f, z); z = fmaf(a[2], 0.f, z); z = fmaf(a[3], 0.f, z);
    float z1 = a[4] * 0.f; z1 = fmaf(a[5], 0.f, z1); z1 = fmaf(a[6], 0.f, z1); z1 = fmaf(a[7], 0.f, z1);
    const bool ok = l > LMIN && l < 1.0e36f && z == 0.f && (last || z1 == 0.f);
    return !ok;
}

__device__ __forceinline__ void attnA_safe(char* ws, int h, int rb, char* lds, int wid, int lane) {
    const bool hi = lane >= 32, pe = hi && !(h & 1), po = !hi && (h & 1); const int m0 = (28 * h) >> 3;
    const auto rQ = RSRC(ws + WS_QA, QC_BYTES), rK = RSRC(ws + WS_KA, QC_BYTES), rV = RSRC(ws + WS_VA, V_BYTES);
    const unsigned lo16 = (unsigned)lane * 16u;
    float* mx = (float*)(lds + LDS_MX);
#pragma unroll 1
    for (int j = 0; j < 4; ++j) {
        const unsigned b = (unsigned)((rb * 4 + j) * 56 + m0) * 512u;
        u32x4 q0 = bload128(rQ, lo16, b), q1 = bload128(rQ, lo16, b + 1024u);
        patch(q0, q1, pe, po, 0u, 0u);
        float m = -3.0e38f; f32x16 acc = {};
#pragma unroll 1
        for (int t = 0; t < 8; ++t) {
            const unsigned ko = (unsigned)((wid * 8 + t) * 56 + m0) * 512u, vo = (unsigned)(h * 64 + wid * 8 + t) * 2048u;
            u32x4 k0 = bload128(rK, lo16, ko), k1 = bload128(rK, lo16, ko + 1024u), v0 = bload128(rV, lo16, vo), v1 = bload128(rV, lo16, vo + 1024u);
            patch(k0, k1, pe, po, 0u, 0u);
            f32x16 s = {}; s = MFMA32(asb(k0), asb(q0), s); s = MFMA32(asb(k1), asb(q1), s);
            const float mn = fmaxf(m, xhalf_max(max16(s)));
            const float al = __builtin_amdgcn_exp2f(m - mn); m = mn;
#pragma unroll
            for (int r = 0; r < 16; ++r) { s[r] -= mn; acc[r] *= al; }
            u32x4 p0, p1; exp_pack(s, p0, p1);
            acc = MFMA32(asb(v0), asb(p0), acc); acc = MFMA32(asb(v1), asb(p1), acc);
        }
        BARRIER();
        if (!hi) mx[wid * 32 + lane] = m;
        BARRIER();
        float g = m;
#pragma unroll
        for (int w = 0; w < 8; ++w) g = fmaxf(g, mx[w * 32 + (lane & 31)]);
        const float f = __builtin_amdgcn_exp2f(m - g);
        merge_write1(acc * f, lds, wid, j, lane);
    }
}

struct AttnBPre { u32x4 q, kp, kc; u32x2 v[2][2]; };
__device__ __forceinline__ void attnB_load(AttnBPre& a, char* ws, int h, int rb, int wid, int lane) {
    const int m0 = (28 * h) >> 3, T = rb * 4 + (wid >> 1), sub = wid & 1;
    const int Tp = sub ? T : (T > 0 ? T - 1 : 0), subp = sub ^ 1;
    const auto rQ = RSRC(ws + WS_QB, QC_BYTES), rK = RSRC(ws + WS_KB, QC_BYTES), rV = RSRC(ws + WS_VB, V_BYTES);
    const int g = lane >> 4, t16 = lane & 15;
    const unsigned co = (unsigned)g * 512u + (unsigned)t16 * 16u;
    a.q = bload128(rQ, co, (unsigned)(T * 56 + m0) * 512u + (unsigned)sub * 256u);
    a.kp = bload128(rK, co, (unsigned)(Tp * 56 + m0) * 512u + (unsigned)subp * 256u);
    a.kc = bload128(rK, co, (unsigned)(T * 56 + m0) * 512u + (unsigned)sub * 256u);
    const unsigned vo = (unsigned)(g & 1) * 512u + (unsigned)t16 * 16u + (unsigned)(g >> 1) * 8u;
#pragma unroll
    for (int db = 0; db < 2; ++db) {
        a.v[db][0] = __builtin_amdgcn_raw_buffer_load_b64(rV, vo + db * 256u, (unsigned)(h * 64 + Tp) * 2048u + (unsigned)subp * 1024u, SC1);
        a.v[db][1] = __builtin_amdgcn_raw_buffer_load_b64(rV, vo + db * 256u, (unsigned)(h * 64 + T) * 2048u + (unsigned)sub * 1024u, SC1);
    }
}
__device__ __forceinline__ void attnB_compute(AttnBPre& a, char* ws, int h, int rb, int wid, int lane) {
    const int T = rb * 4 + (wid >> 1), sub = wid & 1, g = lane >> 4, ql = lane & 15;
    const bool padl = (h & 1) ? g == 0 : g == 3;
    if (h & 1) { a.q[0] = padl ? 0u : a.q[0]; a.q[1] = padl ? 0u : a.q[1]; a.kp[0] = padl ? 0u : a.kp[0]; a.kp[1] = padl ? 0u : a.kp[1]; a.kc[0] = padl ? 0u : a.kc[0]; a.kc[1] = padl ? 0u : a.kc[1]; }
    else { a.q[2] = padl ? 0u : a.q[2]; a.q[3] = padl ? 0u : a.q[3]; a.kp[2] = padl ? 0u : a.kp[2]; a.kp[3] = padl ? 0u : a.kp[3]; a.kc[2] = padl ? 0u : a.kc[2]; a.kc[3] = padl ? 0u : a.kc[3]; }
    f32x4 sp = {}, sc = {};
    sp = MFMA16(asb(a.kp), asb(a.q), sp); sc = MFMA16(asb(a.kc), asb(a.q), sc);
    const bool hasp = sub || T > 0;
    float m = -INFINITY;
#pragma unroll
    for (int r = 0; r < 4; ++r) {
        const int dc = ql - (4 * g + r), dp = dc + 16;
        sp[r] = (hasp && dp <= 14 && !(dp & 1)) ? sp[r] : -INFINITY; sc[r] = (dc >= 0 && !(dc & 1)) ? sc[r] : -INFINITY;
        m = fmaxf(m, fmaxf(sp[r], sc[r]));
    }
    { auto r = __builtin_amdgcn_permlane16_swap(__float_as_uint(m), __float_as_uint(m), false, false); m = fmaxf(__uint_as_float(r[0]), __uint_as_float(r[1])); }
    m = xhalf_max(m);
    float e[8];
#pragma unroll
    for (int r = 0; r < 4; ++r) { e[r] = __builtin_amdgcn_exp2f((sp[r] - m) * QSCALE); e[4 + r] = __builtin_amdgcn_exp2f((sc[r] - m) * QSCALE); }
    const u32x4 pf = {cvtpk(e[0], e[1]), cvtpk(e[2], e[3]), cvtpk(e[4], e[5]), cvtpk(e[6], e[7])};
    f32x4 o0 = {}, o1 = {};
    o0 = MFMA16(asb((u32x4){a.v[0][0][0], a.v[0][0][1], a.v[0][1][0], a.v[0][1][1]}), asb(pf), o0);
    o1 = MFMA16(asb((u32x4){a.v[1][0][0], a.v[1][0][1], a.v[1][1][0], a.v[1][1][1]}), asb(pf), o1);
    float l;
    { auto r16 = __builtin_amdgcn_permlane16_swap(__float_as_uint(o1[0]), __float_as_uint(o1[0]), false, false); auto r32 = __builtin_amdgcn_permlane32_swap(r16[1], r16[1], false, false); l = __uint_as_float(r32[1]); }
    const float inv = __builtin_amdgcn_rcpf(l);
    const auto rO = RSRC(ws + WS_OB, QC_BYTES);
    const unsigned tok = (unsigned)(sub * 16 + ql) * 16u;
    { const int col = 28 * h + 4 * g; bstore64((u32x2){cvtpk(o0[0] * inv, o0[1] * inv), cvtpk(o0[2] * inv, o0[3] * inv)}, rO, (unsigned)(T * 56 + (col >> 3)) * 512u + tok + (unsigned)((col >> 2) & 1) * 8u); }
    if (g < 3) { const int col = 28 * h + 16 + 4 * g; bstore64((u32x2){cvtpk(o1[0] * inv, o1[1] * inv), cvtpk(o1[2] * inv, o1[3] * inv)}, rO, (unsigned)(T * 56 + (col >> 3)) * 512u + tok + (unsigned)((col >> 2) & 1) * 8u); }
}

template <int N>
__device__ __forceinline__ void wo_issue(const u16* wo, int ct, unsigned lds_base, int pc0, int lane) {
    const int r8 = lane >> 3, dpos = lane & 7;
    const unsigned woff = (unsigned)r8 * 896u + (unsigned)((dpos ^ ((r8 & 2) << 1)) << 4);
#pragma unroll
    for (int i = 0; i < N; ++i) glds16((const char*)wo + (size_t)ct * 128 + (size_t)(pc0 + i) * 8 * 896, woff, lds_base + (pc0 + i) * 1024);
}
__device__ __forceinline__ void wf_read(u32x4 (&wf)[2][14], const char* img, int kh, int lane) {
    const int g16 = lane >> 4, q = (lane & 15) >> 2, p = lane & 3;
#pragma unroll
    for (int i = 0; i < 14; ++i)
#pragma unroll
        for (int c = 0; c < 2; ++c) {
            const int row = (kh * 14 + i) * 16 + 8 * (g16 >> 1) + q;
            lds_cptr wp = (lds_cptr)img + (unsigned)row * 128u + (unsigned)(((2 * c + (g16 & 1)) ^ (q & 2)) * 32) + (unsigned)p * 8u;
            wf[c][i] = __builtin_bit_cast(u32x4, cat8(vtr(wp), vtr(wp + 512)));
        }
}
__device__ __forceinline__ void attnA_phase(char* ws, const u16* woA, int oct, int h, int rb, u64 tag, int rt, int ct, bool has_qkv, const QkvOut& outB, u32x4 (&wf)[2][14], char* lds, int wid, int lane) {
    const bool hi = lane >= 32, pe = hi && !(h & 1), po = !hi && (h & 1);
    const int m0 = (28 * h) >> 3;
    const auto rK = RSRC(ws + WS_KA, QC_BYTES), rV = RSRC(ws + WS_VA, V_BYTES);
    const unsigned lo16 = (unsigned)lane * 16u;
    const unsigned kbase = (unsigned)((wid * 8) * 56 + m0) * 512u, vbase = (unsigned)(h * 64 + wid * 8) * 2048u;
    const int rot = rb & 7;
    u32x4 qf[4][2], kf[3][2], vf[3][2];
    {   const unsigned k0 = kbase + (unsigned)rot * TILE_B, v0 = vbase + (unsigned)rot * 2048u, k1 = kbase + (unsigned)((rot + 1) & 7) * TILE_B;
        kf[0][0] = bload128(rK, lo16, k0); kf[0][1] = bload128(rK, lo16, k0 + 1024u);
        if (wid >= 4) { const unsigned b = (unsigned)((rb * 4 + wid - 4) * 56 + m0) * 512u; const auto rQ = RSRC(ws + WS_QA, QC_BYTES); qf[0][0] = bload128(rQ, lo16, b); qf[0][1] = bload128(rQ, lo16, b + 1024u); }
        vf[0][0] = bload128(rV, lo16, v0); vf[0][1] = bload128(rV, lo16, v0 + 1024u);
        kf[1][0] = bload128(rK, lo16, k1); kf[1][1] = bload128(rK, lo16, k1 + 1024u);
        const unsigned v1 = vbase + (unsigned)((rot + 1) & 7) * 2048u, k2 = kbase + (unsigned)((rot + 2) & 7) * TILE_B;
        vf[1][0] = bload128(rV, lo16, v1); vf[1][1] = bload128(rV, lo16, v1 + 1024u);
        kf[2][0] = bload128(rK, lo16, k2); kf[2][1] = bload128(rK, lo16, k2 + 1024u); }
    patch(kf[0][0], kf[0][1], pe, po, 0x3F803F80u, 0u);
    if (wid >= 4) {
        patch(qf[0][0], qf[0][1], pe, po, 0u, 0u);
        f32x16 s = {}; s = MFMA32(asb(kf[0][0]), asb(qf[0][0]), s); s = MFMA32(asb(kf[0][1]), asb(qf[0][1]), s);
        const float m = xhalf_max(max16(s)) + POFF;
        const unsigned mh = cvtpk(-m, 0.f) & 0xffffu; const float r = -m - __uint_as_float(mh << 16);
        patch(qf[0][0], qf[0][1], pe, po, mh | (cvtpk(r, 0.f) << 16), 0u);
        *(u32x4*)(lds + LDS_Q + (wid - 4) * 2048 + lane * 16) = qf[0][0]; *(u32x4*)(lds + LDS_Q + (wid - 4) * 2048 + 1024 + lane * 16) = qf[0][1];
        asm volatile("s_waitcnt lgkmcnt(0)" ::: "memory");
        if (lane == 0) ctl_add(lds, CTL_QCNT);
    }
    ctl_wait(lds, CTL_QCNT, 4);
#pragma unroll
    for (int j = 0; j < 4; ++j) { qf[j][0] = *(const u32x4*)(lds + LDS_Q + j * 2048 + lane * 16); qf[j][1] = *(const u32x4*)(lds + LDS_Q + j * 2048 + 1024 + lane * 16); }
    if (has_qkv && wid < 4) {
        qkv_store<1, 0>(outB, ws, rt, ct, wid, lane); qkv_store<1, 1>(outB, ws, rt, ct, wid, lane); qkv_store<1, 2>(outB, ws, rt, ct, wid, lane);
        qkv_store<1, 3>(outB, ws, rt, ct, wid, lane); qkv_store<1, 4>(outB, ws, rt, ct, wid, lane); qkv_store<1, 5>(outB, ws, rt, ct, wid, lane);
    }
#pragma unroll
    for (int j = 0; j < 4; ++j) { PIN(qf[j][0]); PIN(qf[j][1]); }
    PIN(kf[0][0]); PIN(kf[0][1]);
    f32x16 o[4];
#define VEXP(d, s) asm volatile("v_exp_f32 %0, %1" : "=v"(d) : "v"(s))
#define VCVT(d, a, b) asm volatile("v_cvt_pk_bf16_f32 %0, %1, %2" : "=v"(d) : "v"(a), "v"(b))
#define MF0(d, a, b) asm volatile("v_mfma_f32_32x32x16_bf16 %0, %1, %2, 0" : "=&v"(d) : "v"(a), "v"(b))
#define MFA(d, a, b) asm volatile("v_mfma_f32_32x32x16_bf16 %0, %1, %2, %0" : "+v"(d) : "v"(a), "v"(b))
#define SLOT(PV, QK, Sc, Pc0, Pc1, Sn, kn0, kn1, qn0, qn1, Op, vp0, vp1, Pp0, Pp1, INJ) do { \
        float e0, e1, e2, e3, e4, e5, e6, e7, e8, e9, e10, e11, e12, e13, e14, e15; \
        if ((QK) != 0) MF0(Sn, kn0, qn0); \
        VEXP(e0, Sc[0]); VEXP(e1, Sc[1]); VEXP(e2, Sc[2]); VEXP(e3, Sc[3]); \
        if ((QK) != 0) MFA(Sn, kn1, qn1); \
        VEXP(e4, Sc[4]); VEXP(e5, Sc[5]); VEXP(e6, Sc[6]); VEXP(e7, Sc[7]); VCVT(Pc0[0], e0, e1); VCVT(Pc0[1], e2, e3); \
        INJ; \
        if ((PV) == 2) MFA(Op, vp0, Pp0); else if ((PV) == 1) MF0(Op, vp0, Pp0); \
        VEXP(e8, Sc[8]); VEXP(e9, Sc[9]); VEXP(e10, Sc[10]); VEXP(e11, Sc[11]); VCVT(Pc0[2], e4, e5); VCVT(Pc0[3], e6, e7); \
        if ((PV) != 0) MFA(Op, vp1, Pp1); \
        VEXP(e12, Sc[12]); VEXP(e13, Sc[13]); VEXP(e14, Sc[14]); VEXP(e15, Sc[15]); VCVT(Pc1[0], e8, e9); VCVT(Pc1[1], e10, e11); VCVT(Pc1[2], e12, e13); VCVT(Pc1[3], e14, e15); \
    } while (0)
    f32x16 sA, sB; u32x4 pA0, pA1, pB0, pB1;
    asm volatile("s_nop 1\n\tv_mfma_f32_32x32x16_bf16 %0, %1, %2, 0\n\tv_mfma_f32_32x32x16_bf16 %0, %3, %4, %0\n\ts_nop 7\n\ts_nop 3" : "=&v"(sA) : "v"(kf[0][0]), "v"(qf[0][0]), "v"(kf[0][1]), "v"(qf[0][1]));
#define PATCHK(kx) do { PIN(kx[0]); PIN(kx[1]); patch(kx[0], kx[1], pe, po, 0x3F803F80u, 0u); PIN(kx[0]); PIN(kx[1]); } while (0)
#define LOADK(kc, tt) do { if ((tt) < 8) { const unsigned o_ = kbase + (unsigned)(((tt) + rot) & 7) * TILE_B; kc[0] = bload128(rK, lo16, o_); kc[1] = bload128(rK, lo16, o_ + 1024u); } } while (0)
#define LOADV(vx, tt) do { if ((tt) < 8) { const unsigned o_ = vbase + (unsigned)(((tt) + rot) & 7) * 2048u; vx[0] = bload128(rV, lo16, o_); vx[1] = bload128(rV, lo16, o_ + 1024u); } } while (0)
#define TILE(t) do { \
        SLOT((t) == 0 ? 0 : (t) == 1 ? 1 : 2, true, sA, pA0, pA1, sB, kf[(t) % 3][0], kf[(t) % 3][1], qf[1][0], qf[1][1], o[3], vf[((t) + 2) % 3][0], vf[((t) + 2) % 3][1], pB0, pB1, (void)0); \
        LOADV(vf[((t) + 2) % 3], (t) + 2); \
        SLOT((t) == 0 ? 1 : 2, true, sB, pB0, pB1, sA, kf[(t) % 3][0], kf[(t) % 3][1], qf[2][0], qf[2][1], o[0], vf[(t) % 3][0], vf[(t) % 3][1], pA0, pA1, (void)0); \
        SLOT((t) == 0 ? 1 : 2, true, sA, pA0, pA1, sB, kf[(t) % 3][0], kf[(t) % 3][1], qf[3][0], qf[3][1], o[1], vf[(t) % 3][0], vf[(t) % 3][1], pB0, pB1, if ((t) < 7) PATCHK(kf[((t) + 1) % 3])); \
        LOADK(kf[(t) % 3], (t) + 3); \
        SLOT((t) == 0 ? 1 : 2, (t) < 7, sB, pB0, pB1, sA, kf[((t) + 1) % 3][0], kf[((t) + 1) % 3][1], qf[0][0], qf[0][1], o[2], vf[(t) % 3][0], vf[(t) % 3][1], pA0, pA1, (void)0); \
    } while (0)
    AttnBPre bpre; u64 flb = 0; bool bready = false;
#pragma unroll
    for (int t = 0; t < 8; t += 2) {
        if (t == 0) __builtin_amdgcn_s_setprio(3); else if (t == 2) __builtin_amdgcn_s_setprio(2); else if (t == 4) __builtin_amdgcn_s_setprio(1); else __builtin_amdgcn_s_setprio(0);
        TILE(t);
        if (t == 4) { bready = __all(flb == tag); attnB_load(bpre, ws, h, rb, wid, opaque_lane()); }
        TILE(t + 1);
        if (t == 0) {
            if (has_qkv && wid < 4) {
                asm volatile("s_waitcnt vmcnt(8)" ::: "memory");
                if (opaque_lane() == 0 && ctl_add(lds, CTL_F2CNT) == 3) flag_set(ws, F2 + ct * 32 + rt, tag);
            }
            if (oct < 7 && wid >= 4) {
                if (has_qkv) ctl_wait(lds, CTL_RDONE, 4);
                wo_issue<14>(woA, oct, (unsigned)(uintptr_t)lds, (wid - 4) * 14, opaque_lane());
            }
        } else if (t == 2) {
            const int ln = opaque_lane();
            if (oct < 7 && wid >= 4) { asm volatile("s_waitcnt vmcnt(8)" ::: "memory"); if (ln == 0) ctl_add(lds, CTL_WA); }
            flb = __hip_atomic_load(flagp(ws, f2_index(h, rb, ln)), __ATOMIC_RELAXED, __HIP_MEMORY_SCOPE_AGENT);
        }
    }
    asm volatile("s_nop 1\n\tv_mfma_f32_32x32x16_bf16 %0, %1, %2, %0\n\tv_mfma_f32_32x32x16_bf16 %0, %3, %4, %0\n\ts_nop 7\n\ts_nop 3" : "+v"(o[3]) : "v"(vf[1][0]), "v"(pB0), "v"(vf[1][1]), "v"(pB1));
    asm volatile("" : "+v"(o[0]), "+v"(o[1]), "+v"(o[2]));
    lane = opaque_lane();
    if (has_qkv) ctl_wait(lds, CTL_RDONE, 4);
#pragma unroll
    for (int j = 0; j < 4; ++j) merge_write1(o[j], lds, wid, j, lane);
    if (oct < 7 && wid < 4) { ctl_wait(lds, CTL_WA, 4); wf_read(wf, lds, wid & 1, lane); }
    BARRIER();
    const bool bad = merge_reduce(ws, h, rb, lds, wid, lane);
    if (__any(bad) && lane == 0) ctl_store(lds, CTL_BAD, 1);
    if (!bready) { flag_wait(ws, f2_index(h, rb, lane), tag); attnB_load(bpre, ws, h, rb, wid, lane); }
    attnB_compute(bpre, ws, h, rb, wid, lane);
    asm volatile("s_waitcnt vmcnt(2) lgkmcnt(0)" ::: "memory");
    if (lane == 0 && ctl_add(lds, CTL_F3CNT) == 7 && !ctl_load(lds, CTL_BAD)) flag_set(ws, F3 + rb * 16 + h, tag);
    asm volatile("s_waitcnt vmcnt(0)" ::: "memory");
    if (lane == 0 && ctl_add(lds, CTL_F4CNT) == 7) flag_set(ws, F4 + rb * 16 + h, tag);
    ctl_wait(lds, CTL_F3CNT, 8);
    if (__builtin_amdgcn_readfirstlane(ctl_load(lds, CTL_BAD))) {
        BARRIER();
        attnA_safe(ws, h, rb, lds, wid, lane);
        BARRIER();
        merge_reduce(ws, h, rb, lds, wid, lane);
        DRAIN_BARRIER();
        if (threadIdx.x == 0) flag_set(ws, F3 + rb * 16 + h, tag);
    }
}

__device__ __forceinline__ void oproj_compute(char* ws, const u16* woB, u16* out, int rt, int ct, u64 tag, u32x4 (&wf)[2][14], char* lds, int wid, int lane) {
    const int br = wid >> 2, tt = (wid >> 1) & 1, kh = wid & 1;
    if (wid < 3) {
        if (wid < 2) wo_issue<19>(woB, ct, (unsigned)(uintptr_t)lds + LDS_MRG, wid * 19, lane); else wo_issue<18>(woB, ct, (unsigned)(uintptr_t)lds + LDS_MRG, 38, lane);
    }
    if ((wid & 3) == 3) { flag_wait(ws, (br ? F4 : F3) + (rt >> 1) * 16 + (lane & 15), tag); if (lane == 0) ctl_store(lds, br ? CTL_F4 : CTL_F3, 1); }
    else {
        if (wid < 3) { asm volatile("s_waitcnt vmcnt(0)" ::: "memory"); if (lane == 0) ctl_add(lds, CTL_WB); }
        ctl_wait(lds, br ? CTL_F4 : CTL_F3, 1);
    }
    const auto rO = RSRC(ws + (br ? WS_OB : WS_OA), QC_BYTES);
    const int tile = rt * 2 + tt;
    u32x4 of[14];
#pragma unroll
    for (int i = 0; i < 14; ++i) of[i] = bload128(rO, (unsigned)lane * 16u, (unsigned)(tile * 56 + 2 * (kh * 14 + i)) * 512u);
    if (br) { ctl_wait(lds, CTL_WB, 3); wf_read(wf, lds + LDS_MRG, kh, lane); }
    f32x16 acc[2] = {};
#pragma unroll
    for (int i = 0; i < 14; ++i) { acc[0] = MFMA32(asb(wf[0][i]), asb(of[i]), acc[0]); acc[1] = MFMA32(asb(wf[1][i]), asb(of[i]), acc[1]); }
    asm volatile("" : "+v"(acc[0]), "+v"(acc[1]));
    {   const f32x16& gv = kh ? acc[0] : acc[1];
#pragma unroll
        for (int rq = 0; rq < 4; ++rq) *(f32x4*)(lds + 114688 + (wid * 4 + rq) * 1024 + lane * 16) = (f32x4){gv[4 * rq], gv[4 * rq + 1], gv[4 * rq + 2], gv[4 * rq + 3]};
    }
    asm volatile("s_waitcnt lgkmcnt(0)" ::: "memory");
    if (lane == 0) ctl_store(lds, CTL_X + wid * 4, 1);
    ctl_wait(lds, CTL_X + (wid ^ 1) * 4, 1);
    f32x16 r = kh ? acc[1] : acc[0];
#pragma unroll
    for (int rq = 0; rq < 4; ++rq) { const f32x4 x = *(const f32x4*)(lds + 114688 + ((wid ^ 1) * 4 + rq) * 1024 + lane * 16); r[4 * rq] += x[0]; r[4 * rq + 1] += x[1]; r[4 * rq + 2] += x[2]; r[4 * rq + 3] += x[3]; }
    const unsigned orow = (unsigned)(rt * 64 + tt * 32 + (lane & 31)) * 1792u + (unsigned)(br * 448 + ct * 64 + kh * 32 + (lane >> 5) * 8) * 2u;
#pragma unroll
    for (int pr = 0; pr < 2; ++pr) {
        const unsigned a0 = cvtpk(r[8 * pr], r[8 * pr + 1]), a1 = cvtpk(r[8 * pr + 2], r[8 * pr + 3]), b0 = cvtpk(r[8 * pr + 4], r[8 * pr + 5]), b1 = cvtpk(r[8 * pr + 6], r[8 * pr + 7]);
        auto r0 = __builtin_amdgcn_permlane32_swap(a0, b0, false, false);
        auto r1 = __builtin_amdgcn_permlane32_swap(a1, b1, false, false);
        bstore128((u32x4){r0[0], r1[0], r0[1], r1[1]}, RSRC(out, 2048 * 896 * 2), orow + pr * 32u);
    }
}

__global__ void __launch_bounds__(512, 2) fused_kernel(const u16* xA, const u16* wA, const u16* woA, const u16* xB, const u16* wB, const u16* woB, u16* out, char* ws) {
    extern __shared__ __attribute__((aligned(16))) char lds[];
    int lane = threadIdx.x & 63; const int wid = __builtin_amdgcn_readfirstlane(threadIdx.x >> 6);
    const int id = blockIdx.x, xcd = id & 7, loc = id >> 3;
    if (threadIdx.x < 32) ctl_store(lds, threadIdx.x * 4, 0);
    const u64 tag = __hip_atomic_load(flagp(ws, FEP), __ATOMIC_RELAXED, __HIP_MEMORY_SCOPE_AGENT) + 1;
    const int gb = xcd & 1, grt = 8 * (xcd >> 1) + (loc & 7), qct = gb * 4 + (loc >> 3);
    const bool has_qkv = qct < 7;
    const int h = xcd * 2 + (loc & 1), rb = loc >> 1;
    const int ort = 4 * xcd + (loc & 3), oct = loc >> 2;
    unsigned warm0 = 0;
    if (wid < 4) {
        u64 pc; int len;
        asm volatile("s_getpc_b64 %0\n.Lfused_pc_ref:\n\ts_mov_b32 %1, .Lfused_code_end - .Lfused_pc_ref" : "=s"(pc), "=s"(len));
        const int off = (wid * 64 + lane) * 128;
        if (off < len) warm0 = *(const __attribute__((address_space(1))) unsigned*)(pc + off);
    }
    QkvOut outB;
    if (has_qkv) qkv_phase(xA, wA, xB, wB, ws, grt, qct, tag, lds, wid, lane, outB);
    else { if (threadIdx.x == 0) flag_set(ws, F0 + (xcd >> 1) * 8 + (loc & 7), tag); BARRIER(); }
    asm volatile("" :: "v"(warm0));
    if (wid == 4) {
        flag_wait(ws, F1 + (lane < 32 ? (28 * h) >> 6 : (28 * h + 27) >> 6) * 32 + (lane & 31), tag);
        if (lane == 0) ctl_store(lds, CTL_F1, 1);
    } else ctl_wait(lds, CTL_F1, 1);
    const u64 tags = (u64)(unsigned)__builtin_amdgcn_readfirstlane((unsigned)tag) | ((u64)(unsigned)__builtin_amdgcn_readfirstlane((unsigned)(tag >> 32)) << 32);
    u32x4 wf[2][14];
    attnA_phase(ws, woA, oct, h, rb, tags, grt, qct, has_qkv, outB, wf, lds, wid, lane);
    lane = opaque_lane();
    if (oct < 7) {
        oproj_compute(ws, woB, out, ort, oct, tags, wf, lds, wid, lane);
    }
    if (id == 0 && wid == 0) {
        flag_wait(ws, F0 + (lane & 31), tags);
        if (lane == 0) flag_set(ws, FEP, tags);
    }
    asm volatile(".Lfused_code_end:");
}

extern "C" void kernel_launch(void* const* d_in, const int* in_sizes, int n_in, void* d_out, int out_size, void* d_ws, size_t ws_size, hipStream_t stream) {
    const u16* xA = (const u16*)d_in[0]; const u16* wA = (const u16*)d_in[1]; const u16* woA = (const u16*)d_in[2];
    const u16* xB = (const u16*)d_in[3]; const u16* wB = (const u16*)d_in[4]; const u16* woB = (const u16*)d_in[5];
    (void)hipFuncSetAttribute((const void*)fused_kernel, hipFuncAttributeMaxDynamicSharedMemorySize, LDS_TOTAL);
    fused_kernel<<<256, 512, LDS_TOTAL, stream>>>(xA, wA, woA, xB, wB, woB, (u16*)d_out, (char*)d_ws);
}
```

```cpp
#include <hip/hip_runtime.h>
#include <hip/hip_bf16.h>
#include <stdint.h>

typedef unsigned short u16;
typedef unsigned long long u64;
typedef short bf16x8 __attribute__((ext_vector_type(8)));
typedef short s16x4 __attribute__((ext_vector_type(4)));
typedef float f32x4 __attribute__((ext_vector_type(4)));
typedef float f32x16 __attribute__((ext_vector_type(16)));
typedef unsigned u32x4 __attribute__((ext_vector_type(4)));
typedef unsigned u32x2 __attribute__((ext_vector_type(2)));
typedef __attribute__((address_space(3))) const char* lds_cptr;
typedef __attribute__((address_space(1))) u64 gu64;

constexpr int TILE_B = 56 * 512;
constexpr size_t QC_BYTES = (size_t)64 * TILE_B;
constexpr size_t V_BYTES = (size_t)16 * 64 * 2048;
constexpr size_t WS_QA = 0, WS_KA = WS_QA + QC_BYTES, WS_VA = WS_KA + QC_BYTES;
constexpr size_t WS_QB = WS_VA + V_BYTES, WS_KB = WS_QB + QC_BYTES, WS_VB = WS_KB + QC_BYTES;
constexpr size_t WS_OA = WS_VB + V_BYTES, WS_OB = WS_OA + QC_BYTES, WS_FLAGS = WS_OB + QC_BYTES;
constexpr int F1 = 0, F2 = 256, F3 = 512, F4 = 768, F0 = 1024, FEP = 1056;
constexpr float QSCALE = 0.18898223650461362f * 1.4426950408889634f;
constexpr int STG = 32768;
constexpr int LDS_Q = 147456, LDS_L = 131072, LDS_MRG = 57344, LDS_CTL = LDS_Q + 8192, LDS_MX = LDS_CTL + 128, LDS_TOTAL = LDS_MX + 1024 + 64;
constexpr int CTL_F1 = 0, CTL_F2CNT = 4, CTL_BAD = 8, CTL_QCNT = 12, CTL_F4CNT = 16, CTL_F3 = 20, CTL_RDONE = 24, CTL_F4 = 28, CTL_X = 32;
constexpr int CTL_WA = 64, CTL_WB = 68, CTL_S12 = 72, CTL_S13 = 76, CTL_F3CNT = 80;
constexpr int PUBG = 10;
#define POFF 96.0f
#define LMIN (__builtin_ldexpf(1.0f, -(int)(POFF) - 20))
constexpr unsigned SPIN_MAX = 1u << 18;


#define MFMA16(a, b, c) __builtin_amdgcn_mfma_f32_16x16x32_bf16(a, b, c, 0, 0, 0)
#define MFMA32(a, b, c) __builtin_amdgcn_mfma_f32_32x32x16_bf16(a, b, c, 0, 0, 0)
#define PIN(x) asm volatile("" : "+v"(x))
#define SBAR() __builtin_amdgcn_sched_barrier(0)
#define SC1 16

__device__ __forceinline__ unsigned cvtpk(float lo, float hi) {
    typedef float f2 __attribute__((ext_vector_type(2))); typedef __bf16 b2 __attribute__((ext_vector_type(2)));
    f2 v = {lo, hi}; b2 r = __builtin_convertvector(v, b2); return __builtin_bit_cast(unsigned, r);
}
__device__ __forceinline__ void glds16(const void* sbase, unsigned voff, unsigned lds_base) {
    asm volatile("s_mov_b32 m0, %2\n\ts_nop 0\n\tglobal_load_lds_dwordx4 %0, %1" :: "v"(voff), "s"(sbase), "s"(lds_base) : "memory");
}
__device__ __forceinline__ s16x4 vtr(lds_cptr p) {
    typedef short v4i16_t __attribute__((ext_vector_type(4)));
    return __builtin_bit_cast(s16x4, __builtin_amdgcn_ds_read_tr16_b64_v4i16((__attribute__((address_space(3))) v4i16_t*)p));
}
__device__ __forceinline__ bf16x8 cat8(s16x4 a, s16x4 b) { return (bf16x8){a[0], a[1], a[2], a[3], b[0], b[1], b[2], b[3]}; }
__device__ __forceinline__ bf16x8 ldsrd128(lds_cptr p) { return *(const __attribute__((address_space(3))) bf16x8*)p; }
#define RSRC(p, bytes) __builtin_amdgcn_make_buffer_rsrc((void*)(p), 0, (int)(bytes), 0x00020000)
__device__ __forceinline__ u32x4 bload128(__amdgpu_buffer_rsrc_t r, unsigned voff, unsigned soff) { return __builtin_amdgcn_raw_buffer_load_b128(r, voff, soff, SC1); }
__device__ __forceinline__ void bstore128(u32x4 v, __amdgpu_buffer_rsrc_t r, unsigned voff) { __builtin_amdgcn_raw_buffer_store_b128(v, r, voff, 0, SC1); }
__device__ __forceinline__ void bstore64(u32x2 v, __amdgpu_buffer_rsrc_t r, unsigned voff) { __builtin_amdgcn_raw_buffer_store_b64(v, r, voff, 0, SC1); }
__device__ __forceinline__ bf16x8 asb(u32x4 v) { return __builtin_bit_cast(bf16x8, v); }

__device__ __forceinline__ gu64* flagp(char* ws, int idx) { return (gu64*)(ws + WS_FLAGS + (size_t)idx * 128); }
__device__ __forceinline__ void flag_set(char* ws, int idx, u64 tag) { __hip_atomic_store(flagp(ws, idx), tag, __ATOMIC_RELAXED, __HIP_MEMORY_SCOPE_AGENT); }
__device__ __forceinline__ void flag_wait(char* ws, int idx, u64 tag) {
    gu64* p = flagp(ws, idx);
    for (unsigned spins = 0; spins < SPIN_MAX; ++spins) {
        const u64 v = __hip_atomic_load(p, __ATOMIC_RELAXED, __HIP_MEMORY_SCOPE_AGENT);
        if (__all(v == tag)) break;
        __builtin_amdgcn_s_sleep(1);
    }
}
__device__ __forceinline__ int opaque_lane() { int l; asm volatile("v_mbcnt_lo_u32_b32 %0, -1, 0\n\tv_mbcnt_hi_u32_b32 %0, -1, %0" : "=v"(l)); return l; }
__device__ __forceinline__ int f2_index(int h, int rb, int lane) { const int i = lane % 6, r = 2 * rb - 1 + i % 3; return F2 + (i < 3 ? (28 * h) >> 6 : (28 * h + 27) >> 6) * 32 + (r < 0 ? 0 : r); }
typedef __attribute__((address_space(3))) int* lds_iptr;
__device__ __forceinline__ int ctl_load(char* lds, int off) { return __hip_atomic_load((lds_iptr)(lds + LDS_CTL + off), __ATOMIC_RELAXED, __HIP_MEMORY_SCOPE_WORKGROUP); }
__device__ __forceinline__ void ctl_store(char* lds, int off, int v) { __hip_atomic_store((lds_iptr)(lds + LDS_CTL + off), v, __ATOMIC_RELAXED, __HIP_MEMORY_SCOPE_WORKGROUP); }
__device__ __forceinline__ void ctl_wait(char* lds, int off, int n) {
    for (unsigned spins = 0; spins < (SPIN_MAX << 6); ++spins) { if (__builtin_amdgcn_readfirstlane(ctl_load(lds, off)) >= n) break; __builtin_amdgcn_s_sleep(1); }
    asm volatile("" ::: "memory");
}
__device__ __forceinline__ int ctl_add(char* lds, int off) { return __hip_atomic_fetch_add((lds_iptr)(lds + LDS_CTL + off), 1, __ATOMIC_RELAXED, __HIP_MEMORY_SCOPE_WORKGROUP); }
#define BARRIER() asm volatile("s_waitcnt lgkmcnt(0)\n\ts_barrier" ::: "memory")
#define DRAIN_BARRIER() asm volatile("s_waitcnt vmcnt(0) lgkmcnt(0)\n\ts_barrier" ::: "memory")

struct QkvFrag { bf16x8 x[2][4], w[2][3]; };
struct QkvAcc { f32x4 q[4], k[4], v[4]; };
struct QkvOut { u32x4 q[2], k[2], v[2]; };
__device__ __forceinline__ void qkv_pack(QkvOut& o, const QkvAcc& a, float qs) {
#pragma unroll
    for (int tb = 0; tb < 2; ++tb) {
        const f32x4 q0 = a.q[2 * tb] * qs, q1 = a.q[2 * tb + 1] * qs, k0 = a.k[2 * tb], k1 = a.k[2 * tb + 1], v0 = a.v[2 * tb], v1 = a.v[2 * tb + 1];
        { auto ra = __builtin_amdgcn_permlane16_swap(cvtpk(q0[0], q0[1]), cvtpk(q1[0], q1[1]), false, false); auto rb = __builtin_amdgcn_permlane16_swap(cvtpk(q0[2], q0[3]), cvtpk(q1[2], q1[3]), false, false);
          o.q[tb] = (u32x4){ra[0], rb[0], ra[1], rb[1]}; }
        { auto ra = __builtin_amdgcn_permlane16_swap(cvtpk(k0[0], k0[1]), cvtpk(k1[0], k1[1]), false, false); auto rb = __builtin_amdgcn_permlane16_swap(cvtpk(k0[2], k0[3]), cvtpk(k1[2], k1[3]), false, false);
          o.k[tb] = (u32x4){ra[0], rb[0], ra[1], rb[1]}; }
        { auto ra = __builtin_amdgcn_permlane32_swap(cvtpk(v0[0], v0[1]), cvtpk(v1[0], v1[1]), false, false); auto rb = __builtin_amdgcn_permlane32_swap(cvtpk(v0[2], v0[3]), cvtpk(v1[2], v1[3]), false, false);
          o.v[tb] = (u32x4){ra[0], rb[0], ra[1], rb[1]}; }
    }
}
template <int BR, int I>
__device__ __forceinline__ void qkv_store(const QkvOut& o, char* ws, int rt, int ct, int c, int lane) {
    constexpr int tb = I / 3, kind = I % 3;
    const int tile = rt * 2 + tb;
    if (kind < 2) {
        const unsigned off = (unsigned)(tile * 56 + 8 * ct + 2 * c) * 512u + (unsigned)lane * 16u;
        if (kind == 0) bstore128(o.q[tb], RSRC(ws + (BR ? WS_QB : WS_QA), QC_BYTES), off); else bstore128(o.k[tb], RSRC(ws + (BR ? WS_KB : WS_KA), QC_BYTES), off);
    } else {
        const int n = 64 * ct + 16 * c + (lane & 15), h = n / 28, d = n - 28 * h;
        const unsigned off = (unsigned)(h * 64 + tile) * 2048u + (unsigned)(lane >> 4) * 512u + (unsigned)d * 16u;
        const auto rs = RSRC(ws + (BR ? WS_VB : WS_VA), V_BYTES);
        bstore128(o.v[tb], rs, off);
        if (d >= 24) { const unsigned pad = d == 24 ? 0x3F803F80u : 0u; bstore128((u32x4){pad, pad, pad, pad}, rs, off + 64u); }
    }
}

__device__ __forceinline__ void qkv_phase(const u16* xA, const u16* wA, const u16* xB, const u16* wB, char* ws, int rt, int ct, u64 tag, char* lds, int wid, int lane, QkvOut& outB) {
    if (wid >= 4) {
        const unsigned lds0 = (unsigned)(uintptr_t)lds;
        const int lw = wid - 4, r8 = lane >> 3, dpos = lane & 7;
        unsigned xoff[2], woff[2];
#pragma unroll
        for (int i = 0; i < 2; ++i) { const int row = 16 * lw + 8 * i + r8;
            xoff[i] = (unsigned)row * 896u + (unsigned)((dpos ^ ((row >> 1) & 7)) << 4);
            const int f2 = ((row >> 1) & 1) | (((row >> 3) & 1) << 1);
            woff[i] = (unsigned)row * 2688u + (unsigned)((((dpos >> 1) ^ f2) * 2 + (dpos & 1)) << 4); }
        const int rot = __builtin_amdgcn_readfirstlane(((rt & 7) + 2 * (ct & 3)) % 7);
        const char* xa = (const char*)xA + (size_t)rt * 64 * 896; const char* wa = (const char*)wA + (size_t)ct * 128;
        const char* xb = (const char*)xB + (size_t)rt * 64 * 896; const char* wb = (const char*)wB + (size_t)ct * 128;
#define QKV_ISSUE(g) do { int st_ = ((g) % 7) + rot; st_ = st_ >= 7 ? st_ - 7 : st_; const char* x_ = ((g) < 7 ? xa : xb) + st_ * 128; const char* w_ = ((g) < 7 ? wa : wb) + (size_t)st_ * (64 * 2688); \
        const unsigned sl_ = lds0 + ((g) & 3) * STG + lw * 2048; \
        glds16(x_, xoff[0], sl_); glds16(x_, xoff[1], sl_ + 1024); \
        glds16(w_, woff[0], sl_ + 8192); glds16(w_, woff[1], sl_ + 8192 + 1024); glds16(w_ + 896, woff[0], sl_ + 16384); glds16(w_ + 896, woff[1], sl_ + 16384 + 1024); \
        glds16(w_ + 1792, woff[0], sl_ + 24576); glds16(w_ + 1792, woff[1], sl_ + 24576 + 1024); } while (0)
        QKV_ISSUE(0); QKV_ISSUE(1);
#pragma unroll
        for (int g = 0; g < 12; ++g) {
            asm volatile("s_waitcnt vmcnt(8) lgkmcnt(0)\n\ts_barrier" ::: "memory");
            QKV_ISSUE(g + 2);
        }
        asm volatile("s_waitcnt vmcnt(8)" ::: "memory");
        if (lane == 0) ctl_add(lds, CTL_S12);
        asm volatile("s_waitcnt vmcnt(0)" ::: "memory");
        if (lane == 0) ctl_add(lds, CTL_S13);
        return;
    }
    const int c = wid, g4 = lane >> 4, t16 = lane & 15;
    const unsigned xr0 = (unsigned)t16 * 128u + (unsigned)((g4 ^ (t16 >> 1)) << 4);
    const int q = t16 >> 2, p = lane & 3;
    const int rf2 = ((q >> 1) & 1) | ((g4 & 1) << 1);
    const unsigned wr0 = 8192u + (unsigned)(8 * g4 + q) * 128u + (unsigned)((c ^ rf2) * 32) + (unsigned)p * 8u;
    QkvAcc acc; QkvOut outA;
#pragma unroll
    for (int i = 0; i < 4; ++i) acc.q[i] = acc.k[i] = acc.v[i] = (f32x4){};
    QkvFrag fr[2];
#define QMF(i) do { if (g > 0) { constexpr int ks_ = (i) / 12, s_ = ((i) % 12) / 4, b_ = (i) % 4; const QkvFrag& f_ = fr[(g - 1) & 1]; \
        if (s_ == 0) acc.q[b_] = MFMA16(f_.w[ks_][0], f_.x[ks_][b_], acc.q[b_]); if (s_ == 1) acc.k[b_] = MFMA16(f_.w[ks_][1], f_.x[ks_][b_], acc.k[b_]); \
        if (s_ == 2) acc.v[b_] = MFMA16(f_.x[ks_][b_], f_.w[ks_][2], acc.v[b_]); } SBAR(); } while (0)
#define QRX(ks, b) do { if (g < 14) fr[g & 1].x[ks][b] = ldsrd128(sl + (xr0 ^ ((ks) * 64)) + (b) * 2048); } while (0)
#define QRW(ks, s) do { if (g < 14) { lds_cptr wp_ = sl + wr0 + (ks) * 4096 + (s) * 8192; fr[g & 1].w[ks][s] = cat8(vtr(wp_), vtr(wp_ + 512)); } } while (0)
#pragma unroll
    for (int g = 0; g < 15; ++g) {
        if (g == PUBG) asm volatile("s_waitcnt vmcnt(0) lgkmcnt(0)\n\ts_barrier" ::: "memory");
        else if (g < 12) asm volatile("s_waitcnt lgkmcnt(0)\n\ts_barrier" ::: "memory");
        else if (g == 12) ctl_wait(lds, CTL_S12, 4);
        else if (g == 13) ctl_wait(lds, CTL_S13, 4);
        SBAR();
        lds_cptr sl = (lds_cptr)lds + (g & 3) * STG;
        if (g == PUBG && threadIdx.x == 0) flag_set(ws, F1 + ct * 32 + rt, tag);
        SBAR();
        QRX(0, 0); QMF(0); QRX(0, 1); QMF(1); QRX(0, 2); QMF(2); QRX(0, 3); QMF(3); QRW(0, 0); QMF(4); QRW(0, 1); QMF(5); QRW(0, 2); QMF(6);
        QRX(1, 0); QMF(7); QRX(1, 1); QMF(8); QRX(1, 2); QMF(9); QRX(1, 3); QMF(10); QRW(1, 0); QMF(11); QRW(1, 1); QMF(12); QRW(1, 2); QMF(13);
        QMF(14); QMF(15); QMF(16); QMF(17); QMF(18); QMF(19); QMF(20); QMF(21); QMF(22); QMF(23);
        if (g == 7) {
            qkv_pack(outA, acc, QSCALE);
            qkv_store<0, 1>(outA, ws, rt, ct, c, lane); qkv_store<0, 2>(outA, ws, rt, ct, c, lane); qkv_store<0, 4>(outA, ws, rt, ct, c, lane);
            qkv_store<0, 5>(outA, ws, rt, ct, c, lane); qkv_store<0, 0>(outA, ws, rt, ct, c, lane); qkv_store<0, 3>(outA, ws, rt, ct, c, lane);
#pragma unroll
            for (int i = 0; i < 4; ++i) acc.q[i] = acc.k[i] = acc.v[i] = (f32x4){};
        }
    }
    if (lane == 0) ctl_add(lds, CTL_RDONE);
    qkv_pack(outB, acc, 1.0f);
}

__device__ __forceinline__ float max16(const f32x16& s) {
    float a = fmaxf(fmaxf(s[0], s[1]), s[2]), b = fmaxf(fmaxf(s[3], s[4]), s[5]);
    a = fmaxf(fmaxf(a, s[6]), s[7]); b = fmaxf(fmaxf(b, s[8]), s[9]); a = fmaxf(fmaxf(a, s[10]), s[11]); b = fmaxf(fmaxf(b, s[12]), s[13]);
    a = fmaxf(fmaxf(a, s[14]), s[15]); return fmaxf(a, b);
}
__device__ __forceinline__ float xhalf_max(float m) {
    auto r = __builtin_amdgcn_permlane32_swap(__float_as_uint(m), __float_as_uint(m), false, false);
    return fmaxf(__uint_as_float(r[0]), __uint_as_float(r[1]));
}
__device__ __forceinline__ void patch(u32x4& f0, u32x4& f1, bool pe, bool po, unsigned a, unsigned b) {
    f0[0] = po ? a : f0[0]; f0[1] = po ? b : f0[1]; f1[2] = pe ? a : f1[2]; f1[3] = pe ? b : f1[3];
}
__device__ __forceinline__ void exp_pack(const f32x16& s, u32x4& p0, u32x4& p1) {
    float e[16];
#pragma unroll
    for (int r = 0; r < 16; ++r) e[r] = __builtin_amdgcn_exp2f(s[r]);
    p0 = (u32x4){cvtpk(e[0], e[1]), cvtpk(e[2], e[3]), cvtpk(e[4], e[5]), cvtpk(e[6], e[7])};
    p1 = (u32x4){cvtpk(e[8], e[9]), cvtpk(e[10], e[11]), cvtpk(e[12], e[13]), cvtpk(e[14], e[15])};
}
__device__ __forceinline__ void merge_write1(const f32x16& o, char* lds, int wid, int j, int lane) {
#pragma unroll
    for (int hf = 0; hf < 2; ++hf)
        *(u32x4*)(lds + LDS_MRG + ((wid * 4 + j) * 2 + hf) * 1024 + lane * 16) = (u32x4){cvtpk(o[8 * hf], o[8 * hf + 1]), cvtpk(o[8 * hf + 2], o[8 * hf + 3]), cvtpk(o[8 * hf + 4], o[8 * hf + 5]), cvtpk(o[8 * hf + 6], o[8 * hf + 7])};
    if (lane >= 32) *(float*)(lds + LDS_L + ((wid * 4 + j) * 32 + lane - 32) * 4) = o[12];
}
__device__ __forceinline__ bool merge_reduce(char* ws, int h, int rb, char* lds, int wid, int lane) {
    const bool hi = lane >= 32;
    const int j = wid >> 1, hf = wid & 1;
    const int tile = rb * 4 + j, q = lane & 31;
    u32x4 p[8]; float pl[8];
#pragma unroll
    for (int w = 0; w < 8; ++w) pl[w] = *(const float*)(lds + LDS_L + ((w * 4 + j) * 32 + q) * 4);
#pragma unroll
    for (int w = 0; w < 8; ++w) p[w] = *(const u32x4*)(lds + LDS_MRG + ((w * 4 + j) * 2 + hf) * 1024 + lane * 16);
    const int col0 = 28 * h + 16 * hf + (hi ? 4 : 0), col1 = col0 + 8;
    const unsigned off0 = (unsigned)((tile * 56 + (col0 >> 3)) * 32 + q) * 16u + (unsigned)((col0 >> 2) & 1) * 8u, off1 = (unsigned)((tile * 56 + (col1 >> 3)) * 32 + q) * 16u + (unsigned)((col1 >> 2) & 1) * 8u;
    const float l = ((pl[0] + pl[1]) + (pl[2] + pl[3])) + ((pl[4] + pl[5]) + (pl[6] + pl[7]));
    const float inv = __builtin_amdgcn_rcpf(l);
    u32x4 sel;
    {   const int row = lane & 31, i = 4 * (row >> 3) + (row & 3);
        const unsigned one = (row < 16 && ((row >> 2) & 1) == (lane >> 5)) ? ((i & 1) ? 0x3F800000u : 0x00003F80u) : 0u;
        sel = (u32x4){(i >> 1) == 0 ? one : 0u, (i >> 1) == 1 ? one : 0u, (i >> 1) == 2 ? one : 0u, (i >> 1) == 3 ? one : 0u}; }
    f32x16 sum = {};
#pragma unroll
    for (int w = 0; w < 8; ++w) sum = MFMA32(asb(sel), asb(p[w]), sum);
    float a[8];
#pragma unroll
    for (int i = 0; i < 8; ++i) a[i] = sum[i];
#pragma unroll
    for (int i = 0; i < 8; ++i) a[i] *= inv;
    const auto rO = RSRC(ws + WS_OA, QC_BYTES);
    const bool last = hf == 1 && hi;
    bstore64((u32x2){cvtpk(a[0], a[1]), cvtpk(a[2], a[3])}, rO, off0);
    if (!last) bstore64((u32x2){cvtpk(a[4], a[5]), cvtpk(a[6], a[7])}, rO, off1);
    float z = a[0] * 0.f; z = fmaf(a[1], 0.f, z); z = fmaf(a[2], 0.f, z); z = fmaf(a[3], 0.f, z);
    float z1 = a[4] * 0.f; z1 = fmaf(a[5], 0.f, z1); z1 = fmaf(a[6], 0.f, z1); z1 = fmaf(a[7], 0.f, z1);
    const bool ok = l > LMIN && l < 1.0e36f && z == 0.f && (last || z1 == 0.f);
    return !ok;
}

__device__ __forceinline__ void attnA_safe(char* ws, int h, int rb, char* lds, int wid, int lane) {
    const bool hi = lane >= 32, pe = hi && !(h & 1), po = !hi && (h & 1); const int m0 = (28 * h) >> 3;
    const auto rQ = RSRC(ws + WS_QA, QC_BYTES), rK = RSRC(ws + WS_KA, QC_BYTES), rV = RSRC(ws + WS_VA, V_BYTES);
    const unsigned lo16 = (unsigned)lane * 16u;
    float* mx = (float*)(lds + LDS_MX);
#pragma unroll 1
    for (int j = 0; j < 4; ++j) {
        const unsigned b = (unsigned)((rb * 4 + j) * 56 + m0) * 512u;
        u32x4 q0 = bload128(rQ, lo16, b), q1 = bload128(rQ, lo16, b + 1024u);
        patch(q0, q1, pe, po, 0u, 0u);
        float m = -3.0e38f; f32x16 acc = {};
#pragma unroll 1
        for (int t = 0; t < 8; ++t) {
            const unsigned ko = (unsigned)((wid * 8 + t) * 56 + m0) * 512u, vo = (unsigned)(h * 64 + wid * 8 + t) * 2048u;
            u32x4 k0 = bload128(rK, lo16, ko), k1 = bload128(rK, lo16, ko + 1024u), v0 = bload128(rV, lo16, vo), v1 = bload128(rV, lo16, vo + 1024u);
            patch(k0, k1, pe, po, 0u, 0u);
            f32x16 s = {}; s = MFMA32(asb(k0), asb(q0), s); s = MFMA32(asb(k1), asb(q1), s);
            const float mn = fmaxf(m, xhalf_max(max16(s)));
            const float al = __builtin_amdgcn_exp2f(m - mn); m = mn;
#pragma unroll
            for (int r = 0; r < 16; ++r) { s[r] -= mn; acc[r] *= al; }
            u32x4 p0, p1; exp_pack(s, p0, p1);
            acc = MFMA32(asb(v0), asb(p0), acc); acc = MFMA32(asb(v1), asb(p1), acc);
        }
        BARRIER();
        if (!hi) mx[wid * 32 + lane] = m;
        BARRIER();
        float g = m;
#pragma unroll
        for (int w = 0; w < 8; ++w) g = fmaxf(g, mx[w * 32 + (lane & 31)]);
        const float f = __builtin_amdgcn_exp2f(m - g);
        merge_write1(acc * f, lds, wid, j, lane);
    }
}

struct AttnBPre { u32x4 q, kp, kc; u32x2 v[2][2]; };
__device__ __forceinline__ void attnB_load(AttnBPre& a, char* ws, int h, int rb, int wid, int lane) {
    const int m0 = (28 * h) >> 3, T = rb * 4 + (wid >> 1), sub = wid & 1;
    const int Tp = sub ? T : (T > 0 ? T - 1 : 0), subp = sub ^ 1;
    const auto rQ = RSRC(ws + WS_QB, QC_BYTES), rK = RSRC(ws + WS_KB, QC_BYTES), rV = RSRC(ws + WS_VB, V_BYTES);
    const int g = lane >> 4, t16 = lane & 15;
    const unsigned co = (unsigned)g * 512u + (unsigned)t16 * 16u;
    a.q = bload128(rQ, co, (unsigned)(T * 56 + m0) * 512u + (unsigned)sub * 256u);
    a.kp = bload128(rK, co, (unsigned)(Tp * 56 + m0) * 512u + (unsigned)subp * 256u);
    a.kc = bload128(rK, co, (unsigned)(T * 56 + m0) * 512u + (unsigned)sub * 256u);
    const unsigned vo = (unsigned)(g & 1) * 512u + (unsigned)t16 * 16u + (unsigned)(g >> 1) * 8u;
#pragma unroll
    for (int db = 0; db < 2; ++db) {
        a.v[db][0] = __builtin_amdgcn_raw_buffer_load_b64(rV, vo + db * 256u, (unsigned)(h * 64 + Tp) * 2048u + (unsigned)subp * 1024u, SC1);
        a.v[db][1] = __builtin_amdgcn_raw_buffer_load_b64(rV, vo + db * 256u, (unsigned)(h * 64 + T) * 2048u + (unsigned)sub * 1024u, SC1);
    }
}
__device__ __forceinline__ void attnB_compute(AttnBPre& a, char* ws, int h, int rb, int wid, int lane) {
    const int T = rb * 4 + (wid >> 1), sub = wid & 1, g = lane >> 4, ql = lane & 15;
    const bool po = (h & 1) && g == 0, pe = !(h & 1) && g == 3;
    patch(a.q, a.q, pe, po, 0u, 0u); patch(a.kp, a.kp, pe, po, 0u, 0u); patch(a.kc, a.kc, pe, po, 0u, 0u);
    f32x4 sp = {}, sc = {};
    sp = MFMA16(asb(a.kp), asb(a.q), sp); sc = MFMA16(asb(a.kc), asb(a.q), sc);
    const bool hasp = sub || T > 0;
    float m = -INFINITY;
    const int d0 = ql - 4 * g;
#pragma unroll
    for (int r = 0; r < 4; ++r) {
        const int dc = d0 - r; const bool ev = !(dc & 1);
        sc[r] = (ev && dc >= 0) ? sc[r] : -INFINITY; sp[r] = (ev && dc < 0 && hasp) ? sp[r] : -INFINITY;
        m = fmaxf(m, fmaxf(sp[r], sc[r]));
    }
    { auto r = __builtin_amdgcn_permlane16_swap(__float_as_uint(m), __float_as_uint(m), false, false); m = fmaxf(__uint_as_float(r[0]), __uint_as_float(r[1])); }
    m = xhalf_max(m);
    float e[8]; const float mq = -m * QSCALE;
#pragma unroll
    for (int r = 0; r < 4; ++r) { e[r] = __builtin_amdgcn_exp2f(fmaf(sp[r], QSCALE, mq)); e[4 + r] = __builtin_amdgcn_exp2f(fmaf(sc[r], QSCALE, mq)); }
    const u32x4 pf = {cvtpk(e[0], e[1]), cvtpk(e[2], e[3]), cvtpk(e[4], e[5]), cvtpk(e[6], e[7])};
    f32x4 o0 = {}, o1 = {};
    o0 = MFMA16(asb((u32x4){a.v[0][0][0], a.v[0][0][1], a.v[0][1][0], a.v[0][1][1]}), asb(pf), o0);
    o1 = MFMA16(asb((u32x4){a.v[1][0][0], a.v[1][0][1], a.v[1][1][0], a.v[1][1][1]}), asb(pf), o1);
    float l;
    { auto r16 = __builtin_amdgcn_permlane16_swap(__float_as_uint(o1[0]), __float_as_uint(o1[0]), false, false); auto r32 = __builtin_amdgcn_permlane32_swap(r16[1], r16[1], false, false); l = __uint_as_float(r32[1]); }
    const float inv = __builtin_amdgcn_rcpf(l);
    const auto rO = RSRC(ws + WS_OB, QC_BYTES);
    const unsigned tok = (unsigned)(sub * 16 + ql) * 16u;
    { const int col = 28 * h + 4 * g; bstore64((u32x2){cvtpk(o0[0] * inv, o0[1] * inv), cvtpk(o0[2] * inv, o0[3] * inv)}, rO, (unsigned)(T * 56 + (col >> 3)) * 512u + tok + (unsigned)((col >> 2) & 1) * 8u); }
    if (g < 3) { const int col = 28 * h + 16 + 4 * g; bstore64((u32x2){cvtpk(o1[0] * inv, o1[1] * inv), cvtpk(o1[2] * inv, o1[3] * inv)}, rO, (unsigned)(T * 56 + (col >> 3)) * 512u + tok + (unsigned)((col >> 2) & 1) * 8u); }
}

template <int N>
__device__ __forceinline__ void wo_issue(const u16* wo, int ct, unsigned lds_base, int pc0, int lane) {
    const int r8 = lane >> 3, dpos = lane & 7;
    const unsigned woff = (unsigned)r8 * 896u + (unsigned)((dpos ^ ((r8 & 2) << 1)) << 4);
#pragma unroll
    for (int i = 0; i < N; ++i) glds16((const char*)wo + (size_t)ct * 128 + (size_t)(pc0 + i) * 8 * 896, woff, lds_base + (pc0 + i) * 1024);
}
__device__ __forceinline__ void wf_read(u32x4 (&wf)[2][14], const char* img, int kh, int lane) {
    const int g16 = lane >> 4, q = (lane & 15) >> 2, p = lane & 3;
#pragma unroll
    for (int i = 0; i < 14; ++i)
#pragma unroll
        for (int c = 0; c < 2; ++c) {
            const int row = (kh * 14 + i) * 16 + 8 * (g16 >> 1) + q;
            lds_cptr wp = (lds_cptr)img + (unsigned)row * 128u + (unsigned)(((2 * c + (g16 & 1)) ^ (q & 2)) * 32) + (unsigned)p * 8u;
            wf[c][i] = __builtin_bit_cast(u32x4, cat8(vtr(wp), vtr(wp + 512)));
        }
}
__device__ __forceinline__ void attnA_phase(char* ws, const u16* woA, int oct, int h, int rb, u64 tag, int rt, int ct, bool has_qkv, const QkvOut& outB, u32x4 (&wf)[2][14], char* lds, int wid, int lane) {
    const bool hi = lane >= 32, pe = hi && !(h & 1), po = !hi && (h & 1);
    const int m0 = (28 * h) >> 3;
    const auto rK = RSRC(ws + WS_KA, QC_BYTES), rV = RSRC(ws + WS_VA, V_BYTES);
    const unsigned lo16 = (unsigned)lane * 16u;
    const unsigned kbase = (unsigned)((wid * 8) * 56 + m0) * 512u, vbase = (unsigned)(h * 64 + wid * 8) * 2048u;
    const int rot = rb & 7;
    u32x4 qf[4][2], kf[3][2], vf[3][2];
    {   const unsigned k0 = kbase + (unsigned)rot * TILE_B, v0 = vbase + (unsigned)rot * 2048u, k1 = kbase + (unsigned)((rot + 1) & 7) * TILE_B;
        kf[0][0] = bload128(rK, lo16, k0); kf[0][1] = bload128(rK, lo16, k0 + 1024u);
        if (wid >= 4) { const unsigned b = (unsigned)((rb * 4 + wid - 4) * 56 + m0) * 512u; const auto rQ = RSRC(ws + WS_QA, QC_BYTES); qf[0][0] = bload128(rQ, lo16, b); qf[0][1] = bload128(rQ, lo16, b + 1024u); }
        vf[0][0] = bload128(rV, lo16, v0); vf[0][1] = bload128(rV, lo16, v0 + 1024u);
        kf[1][0] = bload128(rK, lo16, k1); kf[1][1] = bload128(rK, lo16, k1 + 1024u);
        const unsigned v1 = vbase + (unsigned)((rot + 1) & 7) * 2048u, k2 = kbase + (unsigned)((rot + 2) & 7) * TILE_B;
        vf[1][0] = bload128(rV, lo16, v1); vf[1][1] = bload128(rV, lo16, v1 + 1024u);
        kf[2][0] = bload128(rK, lo16, k2); kf[2][1] = bload128(rK, lo16, k2 + 1024u); }
    patch(kf[0][0], kf[0][1], pe, po, 0x3F803F80u, 0u);
    if (wid >= 4) {
        patch(qf[0][0], qf[0][1], pe, po, 0u, 0u);
        f32x16 s = {}; s = MFMA32(asb(kf[0][0]), asb(qf[0][0]), s); s = MFMA32(asb(kf[0][1]), asb(qf[0][1]), s);
        const float m = xhalf_max(max16(s)) + POFF;
        const unsigned mh = cvtpk(-m, 0.f) & 0xffffu; const float r = -m - __uint_as_float(mh << 16);
        patch(qf[0][0], qf[0][1], pe, po, mh | (cvtpk(r, 0.f) << 16), 0u);
        *(u32x4*)(lds + LDS_Q + (wid - 4) * 2048 + lane * 16) = qf[0][0]; *(u32x4*)(lds + LDS_Q + (wid - 4) * 2048 + 1024 + lane * 16) = qf[0][1];
        asm volatile("s_waitcnt lgkmcnt(0)" ::: "memory");
        if (lane == 0) ctl_add(lds, CTL_QCNT);
    }
    ctl_wait(lds, CTL_QCNT, 4);
#pragma unroll
    for (int j = 0; j < 4; ++j) { qf[j][0] = *(const u32x4*)(lds + LDS_Q + j * 2048 + lane * 16); qf[j][1] = *(const u32x4*)(lds + LDS_Q + j * 2048 + 1024 + lane * 16); }
    if (has_qkv && wid < 4) {
        qkv_store<1, 0>(outB, ws, rt, ct, wid, lane); qkv_store<1, 1>(outB, ws, rt, ct, wid, lane); qkv_store<1, 2>(outB, ws, rt, ct, wid, lane);
        qkv_store<1, 3>(outB, ws, rt, ct, wid, lane); qkv_store<1, 4>(outB, ws, rt, ct, wid, lane); qkv_store<1, 5>(outB, ws, rt, ct, wid, lane);
    }
#pragma unroll
    for (int j = 0; j < 4; ++j) { PIN(qf[j][0]); PIN(qf[j][1]); }
    PIN(kf[0][0]); PIN(kf[0][1]);
    f32x16 o[4];
#define VEXP(d, s) asm volatile("v_exp_f32 %0, %1" : "=v"(d) : "v"(s))
#define VCVT(d, a, b) asm volatile("v_cvt_pk_bf16_f32 %0, %1, %2" : "=v"(d) : "v"(a), "v"(b))
#define MF0(d, a, b) asm volatile("v_mfma_f32_32x32x16_bf16 %0, %1, %2, 0" : "=&v"(d) : "v"(a), "v"(b))
#define MFA(d, a, b) asm volatile("v_mfma_f32_32x32x16_bf16 %0, %1, %2, %0" : "+v"(d) : "v"(a), "v"(b))
#define SLOT(PV, QK, Sc, Pc0, Pc1, Sn, kn0, kn1, qn0, qn1, Op, vp0, vp1, Pp0, Pp1, INJ) do { \
        float e0, e1, e2, e3, e4, e5, e6, e7, e8, e9, e10, e11, e12, e13, e14, e15; \
        if ((QK) != 0) MF0(Sn, kn0, qn0); \
        VEXP(e0, Sc[0]); VEXP(e1, Sc[1]); VEXP(e2, Sc[2]); VEXP(e3, Sc[3]); \
        if ((QK) != 0) MFA(Sn, kn1, qn1); \
        VEXP(e4, Sc[4]); VEXP(e5, Sc[5]); VEXP(e6, Sc[6]); VEXP(e7, Sc[7]); VCVT(Pc0[0], e0, e1); VCVT(Pc0[1], e2, e3); \
        INJ; \
        if ((PV) == 2) MFA(Op, vp0, Pp0); else if ((PV) == 1) MF0(Op, vp0, Pp0); \
        VEXP(e8, Sc[8]); VEXP(e9, Sc[9]); VEXP(e10, Sc[10]); VEXP(e11, Sc[11]); VCVT(Pc0[2], e4, e5); VCVT(Pc0[3], e6, e7); \
        if ((PV) != 0) MFA(Op, vp1, Pp1); \
        VEXP(e12, Sc[12]); VEXP(e13, Sc[13]); VEXP(e14, Sc[14]); VEXP(e15, Sc[15]); VCVT(Pc1[0], e8, e9); VCVT(Pc1[1], e10, e11); VCVT(Pc1[2], e12, e13); VCVT(Pc1[3], e14, e15); \
    } while (0)
    f32x16 sA, sB; u32x4 pA0, pA1, pB0, pB1;
    asm volatile("s_nop 1\n\tv_mfma_f32_32x32x16_bf16 %0, %1, %2, 0\n\tv_mfma_f32_32x32x16_bf16 %0, %3, %4, %0\n\ts_nop 7\n\ts_nop 3" : "=&v"(sA) : "v"(kf[0][0]), "v"(qf[0][0]), "v"(kf[0][1]), "v"(qf[0][1]));
#define PATCHK(kx) do { PIN(kx[0]); PIN(kx[1]); patch(kx[0], kx[1], pe, po, 0x3F803F80u, 0u); PIN(kx[0]); PIN(kx[1]); } while (0)
#define LOADK(kc, tt) do { if ((tt) < 8) { const unsigned o_ = kbase + (unsigned)(((tt) + rot) & 7) * TILE_B; kc[0] = bload128(rK, lo16, o_); kc[1] = bload128(rK, lo16, o_ + 1024u); } } while (0)
#define LOADV(vx, tt) do { if ((tt) < 8) { const unsigned o_ = vbase + (unsigned)(((tt) + rot) & 7) * 2048u; vx[0] = bload128(rV, lo16, o_); vx[1] = bload128(rV, lo16, o_ + 1024u); } } while (0)
#define TILE(t) do { \
        SLOT((t) == 0 ? 0 : (t) == 1 ? 1 : 2, true, sA, pA0, pA1, sB, kf[(t) % 3][0], kf[(t) % 3][1], qf[1][0], qf[1][1], o[3], vf[((t) + 2) % 3][0], vf[((t) + 2) % 3][1], pB0, pB1, (void)0); \
        LOADV(vf[((t) + 2) % 3], (t) + 2); \
        SLOT((t) == 0 ? 1 : 2, true, sB, pB0, pB1, sA, kf[(t) % 3][0], kf[(t) % 3][1], qf[2][0], qf[2][1], o[0], vf[(t) % 3][0], vf[(t) % 3][1], pA0, pA1, (void)0); \
        SLOT((t) == 0 ? 1 : 2, true, sA, pA0, pA1, sB, kf[(t) % 3][0], kf[(t) % 3][1], qf[3][0], qf[3][1], o[1], vf[(t) % 3][0], vf[(t) % 3][1], pB0, pB1, if ((t) < 7) PATCHK(kf[((t) + 1) % 3])); \
        LOADK(kf[(t) % 3], (t) + 3); \
        SLOT((t) == 0 ? 1 : 2, (t) < 7, sB, pB0, pB1, sA, kf[((t) + 1) % 3][0], kf[((t) + 1) % 3][1], qf[0][0], qf[0][1], o[2], vf[(t) % 3][0], vf[(t) % 3][1], pA0, pA1, (void)0); \
    } while (0)
    AttnBPre bpre; u64 flb = 0; bool bready = false;
#pragma unroll
    for (int t = 0; t < 8; t += 2) {
        if (t == 0) __builtin_amdgcn_s_setprio(3); else if (t == 2) __builtin_amdgcn_s_setprio(2); else if (t == 4) __builtin_amdgcn_s_setprio(1); else __builtin_amdgcn_s_setprio(0);
        TILE(t);
        if (t == 4) { bready = __all(flb == tag); attnB_load(bpre, ws, h, rb, wid, opaque_lane()); }
        TILE(t + 1);
        if (t == 0) {
            if (has_qkv && wid < 4) {
                asm volatile("s_waitcnt vmcnt(8)" ::: "memory");
                if (opaque_lane() == 0 && ctl_add(lds, CTL_F2CNT) == 3) flag_set(ws, F2 + ct * 32 + rt, tag);
            }
            if (oct < 7 && wid >= 4) {
                if (has_qkv) ctl_wait(lds, CTL_RDONE, 4);
                wo_issue<14>(woA, oct, (unsigned)(uintptr_t)lds, (wid - 4) * 14, opaque_lane());
            }
        } else if (t == 2) {
            const int ln = opaque_lane();
            if (oct < 7 && wid >= 4) { asm volatile("s_waitcnt vmcnt(8)" ::: "memory"); if (ln == 0) ctl_add(lds, CTL_WA); }
            flb = __hip_atomic_load(flagp(ws, f2_index(h, rb, ln)), __ATOMIC_RELAXED, __HIP_MEMORY_SCOPE_AGENT);
        }
    }
    asm volatile("s_nop 1\n\tv_mfma_f32_32x32x16_bf16 %0, %1, %2, %0\n\tv_mfma_f32_32x32x16_bf16 %0, %3, %4, %0\n\ts_nop 7\n\ts_nop 3" : "+v"(o[3]) : "v"(vf[1][0]), "v"(pB0), "v"(vf[1][1]), "v"(pB1));
    asm volatile("" : "+v"(o[0]), "+v"(o[1]), "+v"(o[2]));
    lane = opaque_lane();
    if (has_qkv) ctl_wait(lds, CTL_RDONE, 4);
#pragma unroll
    for (int j = 0; j < 4; ++j) merge_write1(o[j], lds, wid, j, lane);
    if (!bready) { flag_wait(ws, f2_index(h, rb, lane), tag); attnB_load(bpre, ws, h, rb, wid, lane); }
    PIN(bpre.q); PIN(bpre.kp); PIN(bpre.kc); PIN(bpre.v[0][0]); PIN(bpre.v[0][1]); PIN(bpre.v[1][0]); PIN(bpre.v[1][1]);
    if (oct < 7 && wid < 4) { ctl_wait(lds, CTL_WA, 4); wf_read(wf, lds, wid & 1, lane); }
    BARRIER();
    const bool bad = merge_reduce(ws, h, rb, lds, wid, lane);
    if (__any(bad) && lane == 0) ctl_store(lds, CTL_BAD, 1);
    attnB_compute(bpre, ws, h, rb, wid, lane);
    asm volatile("s_waitcnt vmcnt(2) lgkmcnt(0)" ::: "memory");
    if (lane == 0 && ctl_add(lds, CTL_F3CNT) == 7 && !ctl_load(lds, CTL_BAD)) flag_set(ws, F3 + rb * 16 + h, tag);
    asm volatile("s_waitcnt vmcnt(0)" ::: "memory");
    if (lane == 0 && ctl_add(lds, CTL_F4CNT) == 7) flag_set(ws, F4 + rb * 16 + h, tag);
    ctl_wait(lds, CTL_F3CNT, 8);
    if (__builtin_amdgcn_readfirstlane(ctl_load(lds, CTL_BAD))) {
        BARRIER();
        attnA_safe(ws, h, rb, lds, wid, lane);
        BARRIER();
        merge_reduce(ws, h, rb, lds, wid, lane);
        DRAIN_BARRIER();
        if (threadIdx.x == 0) flag_set(ws, F3 + rb * 16 + h, tag);
    }
}

__device__ __forceinline__ void oproj_compute(char* ws, const u16* woB, u16* out, int rt, int ct, u64 tag, u32x4 (&wf)[2][14], char* lds, int wid, int lane) {
    const int br = wid >> 2, tt = (wid >> 1) & 1, kh = wid & 1;
    if (wid < 3) {
        if (wid < 2) wo_issue<19>(woB, ct, (unsigned)(uintptr_t)lds + LDS_MRG, wid * 19, lane); else wo_issue<18>(woB, ct, (unsigned)(uintptr_t)lds + LDS_MRG, 38, lane);
    }
    if ((wid & 3) == 3) { flag_wait(ws, (br ? F4 : F3) + (rt >> 1) * 16 + (lane & 15), tag); if (lane == 0) ctl_store(lds, br ? CTL_F4 : CTL_F3, 1); }
    else {
        if (wid < 3) { asm volatile("s_waitcnt vmcnt(0)" ::: "memory"); if (lane == 0) ctl_add(lds, CTL_WB); }
        ctl_wait(lds, br ? CTL_F4 : CTL_F3, 1);
    }
    const auto rO = RSRC(ws + (br ? WS_OB : WS_OA), QC_BYTES);
    const int tile = rt * 2 + tt;
    u32x4 of[14];
#pragma unroll
    for (int i = 0; i < 14; ++i) of[i] = bload128(rO, (unsigned)lane * 16u, (unsigned)(tile * 56 + 2 * (kh * 14 + i)) * 512u);
    if (br) { ctl_wait(lds, CTL_WB, 3); wf_read(wf, lds + LDS_MRG, kh, lane); }
    f32x16 acc[2] = {};
#pragma unroll
    for (int i = 0; i < 14; ++i) { acc[0] = MFMA32(asb(wf[0][i]), asb(of[i]), acc[0]); acc[1] = MFMA32(asb(wf[1][i]), asb(of[i]), acc[1]); }
    asm volatile("" : "+v"(acc[0]), "+v"(acc[1]));
    {   const f32x16& gv = kh ? acc[0] : acc[1];
#pragma unroll
        for (int rq = 0; rq < 4; ++rq) *(f32x4*)(lds + 114688 + (wid * 4 + rq) * 1024 + lane * 16) = (f32x4){gv[4 * rq], gv[4 * rq + 1], gv[4 * rq + 2], gv[4 * rq + 3]};
    }
    asm volatile("s_waitcnt lgkmcnt(0)" ::: "memory");
    if (lane == 0) ctl_store(lds, CTL_X + wid * 4, 1);
    ctl_wait(lds, CTL_X + (wid ^ 1) * 4, 1);
    f32x16 r = kh ? acc[1] : acc[0];
#pragma unroll
    for (int rq = 0; rq < 4; ++rq) { const f32x4 x = *(const f32x4*)(lds + 114688 + ((wid ^ 1) * 4 + rq) * 1024 + lane * 16); r[4 * rq] += x[0]; r[4 * rq + 1] += x[1]; r[4 * rq + 2] += x[2]; r[4 * rq + 3] += x[3]; }
    const unsigned orow = (unsigned)(rt * 64 + tt * 32 + (lane & 31)) * 1792u + (unsigned)(br * 448 + ct * 64 + kh * 32 + (lane >> 5) * 8) * 2u;
#pragma unroll
    for (int pr = 0; pr < 2; ++pr) {
        const unsigned a0 = cvtpk(r[8 * pr], r[8 * pr + 1]), a1 = cvtpk(r[8 * pr + 2], r[8 * pr + 3]), b0 = cvtpk(r[8 * pr + 4], r[8 * pr + 5]), b1 = cvtpk(r[8 * pr + 6], r[8 * pr + 7]);
        auto r0 = __builtin_amdgcn_permlane32_swap(a0, b0, false, false);
        auto r1 = __builtin_amdgcn_permlane32_swap(a1, b1, false, false);
        bstore128((u32x4){r0[0], r1[0], r0[1], r1[1]}, RSRC(out, 2048 * 896 * 2), orow + pr * 32u);
    }
}

__global__ void __launch_bounds__(512, 2) fused_kernel(const u16* xA, const u16* wA, const u16* woA, const u16* xB, const u16* wB, const u16* woB, u16* out, char* ws) {
    extern __shared__ __attribute__((aligned(16))) char lds[];
    int lane = threadIdx.x & 63; const int wid = __builtin_amdgcn_readfirstlane(threadIdx.x >> 6);
    const int id = blockIdx.x, xcd = id & 7, loc = id >> 3;
    if (threadIdx.x < 32) ctl_store(lds, threadIdx.x * 4, 0);
    const u64 tag = __hip_atomic_load(flagp(ws, FEP), __ATOMIC_RELAXED, __HIP_MEMORY_SCOPE_AGENT) + 1;
    const int gb = xcd & 1, grt = 8 * (xcd >> 1) + (loc & 7), qct = gb * 4 + (loc >> 3);
    const bool has_qkv = qct < 7;
    const int h = xcd * 2 + (loc & 1), rb = loc >> 1;
    const int ort = 4 * xcd + (loc & 3), oct = loc >> 2;
    unsigned warm0 = 0;
    if (wid < 4) {
        u64 pc; int len;
        asm volatile("s_getpc_b64 %0\n.Lfused_pc_ref:\n\ts_mov_b32 %1, .Lfused_code_end - .Lfused_pc_ref" : "=s"(pc), "=s"(len));
        const int off = (wid * 64 + lane) * 128;
        if (off < len) warm0 = *(const __attribute__((address_space(1))) unsigned*)(pc + off);
    }
    QkvOut outB;
    if (has_qkv) qkv_phase(xA, wA, xB, wB, ws, grt, qct, tag, lds, wid, lane, outB);
    else { if (threadIdx.x == 0) flag_set(ws, F0 + (xcd >> 1) * 8 + (loc & 7), tag); BARRIER(); }
    asm volatile("" :: "v"(warm0));
    if (wid == 4) {
        flag_wait(ws, F1 + (lane < 32 ? (28 * h) >> 6 : (28 * h + 27) >> 6) * 32 + (lane & 31), tag);
        if (lane == 0) ctl_store(lds, CTL_F1, 1);
    } else ctl_wait(lds, CTL_F1, 1);
    const u64 tags = (u64)(unsigned)__builtin_amdgcn_readfirstlane((unsigned)tag) | ((u64)(unsigned)__builtin_amdgcn_readfirstlane((unsigned)(tag >> 32)) << 32);
    u32x4 wf[2][14];
    attnA_phase(ws, woA, oct, h, rb, tags, grt, qct, has_qkv, outB, wf, lds, wid, lane);
    lane = opaque_lane();
    if (oct < 7) {
        oproj_compute(ws, woB, out, ort, oct, tags, wf, lds, wid, lane);
    }
    if (id == 0 && wid == 0) {
        flag_wait(ws, F0 + (lane & 31), tags);
        if (lane == 0) flag_set(ws, FEP, tags);
    }
    asm volatile(".Lfused_code_end:");
}

extern "C" void kernel_launch(void* const* d_in, const int* in_sizes, int n_in, void* d_out, int out_size, void* d_ws, size_t ws_size, hipStream_t stream) {
    const u16* xA = (const u16*)d_in[0]; const u16* wA = (const u16*)d_in[1]; const u16* woA = (const u16*)d_in[2];
    const u16* xB = (const u16*)d_in[3]; const u16* wB = (const u16*)d_in[4]; const u16* woB = (const u16*)d_in[5];
    (void)hipFuncSetAttribute((const void*)fused_kernel, hipFuncAttributeMaxDynamicSharedMemorySize, LDS_TOTAL);
    fused_kernel<<<256, 512, LDS_TOTAL, stream>>>(xA, wA, woA, xB, wB, woB, (u16*)d_out, (char*)d_ws);
}
```

```cpp
#include <hip/hip_runtime.h>
#include <hip/hip_bf16.h>
#include <stdint.h>

typedef unsigned short u16;
typedef unsigned long long u64;
typedef short bf16x8 __attribute__((ext_vector_type(8)));
typedef short s16x4 __attribute__((ext_vector_type(4)));
typedef float f32x4 __attribute__((ext_vector_type(4)));
typedef float f32x16 __attribute__((ext_vector_type(16)));
typedef unsigned u32x4 __attribute__((ext_vector_type(4)));
typedef unsigned u32x2 __attribute__((ext_vector_type(2)));
typedef __attribute__((address_space(3))) const char* lds_cptr;
typedef __attribute__((address_space(1))) u64 gu64;

constexpr int TILE_B = 56 * 512;
constexpr size_t QC_BYTES = (size_t)64 * TILE_B;
constexpr size_t V_BYTES = (size_t)16 * 64 * 2048;
constexpr size_t WS_QA = 0, WS_KA = WS_QA + QC_BYTES, WS_VA = WS_KA + QC_BYTES;
constexpr size_t WS_QB = WS_VA + V_BYTES, WS_KB = WS_QB + QC_BYTES, WS_VB = WS_KB + QC_BYTES;
constexpr size_t WS_OA = WS_VB + V_BYTES, WS_OB = WS_OA + QC_BYTES, WS_FLAGS = WS_OB + QC_BYTES;
constexpr int F1 = 0, F2 = 256, F3 = 512, F4 = 768, F0 = 1024, FEP = 1056;
constexpr float QSCALE = 0.18898223650461362f * 1.4426950408889634f;
constexpr int STG = 32768;
constexpr int LDS_Q = 147456, LDS_L = 131072, LDS_MRG = 57344, LDS_CTL = LDS_Q + 8192, LDS_MX = LDS_CTL + 128, LDS_TOTAL = LDS_MX + 1024 + 64;
constexpr int CTL_F1 = 0, CTL_F2CNT = 4, CTL_BAD = 8, CTL_QCNT = 12, CTL_F4CNT = 16, CTL_F3 = 20, CTL_RDONE = 24, CTL_F4 = 28, CTL_X = 32;
constexpr int CTL_WA = 64, CTL_WB = 68, CTL_S12 = 72, CTL_S13 = 76, CTL_F3CNT = 80, CTL_F0 = 84;
constexpr int PUBG = 10;
#define POFF 96.0f
#define LMIN (__builtin_ldexpf(1.0f, -(int)(POFF) - 20))
constexpr unsigned SPIN_MAX = 1u << 18;


#define MFMA16(a, b, c) __builtin_amdgcn_mfma_f32_16x16x32_bf16(a, b, c, 0, 0, 0)
#define MFMA32(a, b, c) __builtin_amdgcn_mfma_f32_32x32x16_bf16(a, b, c, 0, 0, 0)
#define PIN(x) asm volatile("" : "+v"(x))
#define SBAR() __builtin_amdgcn_sched_barrier(0)
#define SC1 16

__device__ __forceinline__ unsigned cvtpk(float lo, float hi) {
    typedef float f2 __attribute__((ext_vector_type(2))); typedef __bf16 b2 __attribute__((ext_vector_type(2)));
    f2 v = {lo, hi}; b2 r = __builtin_convertvector(v, b2); return __builtin_bit_cast(unsigned, r);
}
__device__ __forceinline__ void glds16(const void* sbase, unsigned voff, unsigned lds_base) {
    asm volatile("s_mov_b32 m0, %2\n\ts_nop 0\n\tglobal_load_lds_dwordx4 %0, %1" :: "v"(voff), "s"(sbase), "s"(lds_base) : "memory");
}
__device__ __forceinline__ s16x4 vtr(lds_cptr p) {
    typedef short v4i16_t __attribute__((ext_vector_type(4)));
    return __builtin_bit_cast(s16x4, __builtin_amdgcn_ds_read_tr16_b64_v4i16((__attribute__((address_space(3))) v4i16_t*)p));
}
__device__ __forceinline__ bf16x8 cat8(s16x4 a, s16x4 b) { return (bf16x8){a[0], a[1], a[2], a[3], b[0], b[1], b[2], b[3]}; }
__device__ __forceinline__ bf16x8 ldsrd128(lds_cptr p) { return *(const __attribute__((address_space(3))) bf16x8*)p; }
#define RSRC(p, bytes) __builtin_amdgcn_make_buffer_rsrc((void*)(p), 0, (int)(bytes), 0x00020000)
__device__ __forceinline__ u32x4 bload128(__amdgpu_buffer_rsrc_t r, unsigned voff, unsigned soff) { return __builtin_amdgcn_raw_buffer_load_b128(r, voff, soff, SC1); }
__device__ __forceinline__ void bstore128(u32x4 v, __amdgpu_buffer_rsrc_t r, unsigned voff) { __builtin_amdgcn_raw_buffer_store_b128(v, r, voff, 0, SC1); }
__device__ __forceinline__ void bstore64(u32x2 v, __amdgpu_buffer_rsrc_t r, unsigned voff) { __builtin_amdgcn_raw_buffer_store_b64(v, r, voff, 0, SC1); }
__device__ __forceinline__ bf16x8 asb(u32x4 v) { return __builtin_bit_cast(bf16x8, v); }

__device__ __forceinline__ gu64* flagp(char* ws, int idx) { return (gu64*)(ws + WS_FLAGS + (size_t)idx * 128); }
__device__ __forceinline__ void flag_set(char* ws, int idx, u64 tag) { __hip_atomic_store(flagp(ws, idx), tag, __ATOMIC_RELAXED, __HIP_MEMORY_SCOPE_AGENT); }
__device__ __forceinline__ void flag_wait(char* ws, int idx, u64 tag) {
    gu64* p = flagp(ws, idx);
    for (unsigned spins = 0; spins < SPIN_MAX; ++spins) {
        const u64 v = __hip_atomic_load(p, __ATOMIC_RELAXED, __HIP_MEMORY_SCOPE_AGENT);
        if (__all(v == tag)) break;
        __builtin_amdgcn_s_sleep(1);
    }
}
__device__ __forceinline__ int opaque_lane() { int l; asm volatile("v_mbcnt_lo_u32_b32 %0, -1, 0\n\tv_mbcnt_hi_u32_b32 %0, -1, %0" : "=v"(l)); return l; }
__device__ __forceinline__ int f2_index(int h, int rb, int lane) { const int i = lane % 6, r = 2 * rb - 1 + i % 3; return F2 + (i < 3 ? (28 * h) >> 6 : (28 * h + 27) >> 6) * 32 + (r < 0 ? 0 : r); }
typedef __attribute__((address_space(3))) int* lds_iptr;
__device__ __forceinline__ int ctl_load(char* lds, int off) { return __hip_atomic_load((lds_iptr)(lds + LDS_CTL + off), __ATOMIC_RELAXED, __HIP_MEMORY_SCOPE_WORKGROUP); }
__device__ __forceinline__ void ctl_store(char* lds, int off, int v) { __hip_atomic_store((lds_iptr)(lds + LDS_CTL + off), v, __ATOMIC_RELAXED, __HIP_MEMORY_SCOPE_WORKGROUP); }
__device__ __forceinline__ void ctl_wait(char* lds, int off, int n) {
    for (unsigned spins = 0; spins < (SPIN_MAX << 6); ++spins) { if (__builtin_amdgcn_readfirstlane(ctl_load(lds, off)) >= n) break; __builtin_amdgcn_s_sleep(1); }
    asm volatile("" ::: "memory");
}
__device__ __forceinline__ int ctl_add(char* lds, int off) { return __hip_atomic_fetch_add((lds_iptr)(lds + LDS_CTL + off), 1, __ATOMIC_RELAXED, __HIP_MEMORY_SCOPE_WORKGROUP); }
#define BARRIER() asm volatile("s_waitcnt lgkmcnt(0)\n\ts_barrier" ::: "memory")
#define DRAIN_BARRIER() asm volatile("s_waitcnt vmcnt(0) lgkmcnt(0)\n\ts_barrier" ::: "memory")

struct QkvFrag { bf16x8 x[2][4], w[2][3]; };
struct QkvAcc { f32x4 q[4], k[4], v[4]; };
struct QkvOut { u32x4 q[2], k[2], v[2]; };
__device__ __forceinline__ void qkv_pack(QkvOut& o, const QkvAcc& a, float qs) {
#pragma unroll
    for (int tb = 0; tb < 2; ++tb) {
        const f32x4 q0 = a.q[2 * tb] * qs, q1 = a.q[2 * tb + 1] * qs, k0 = a.k[2 * tb], k1 = a.k[2 * tb + 1], v0 = a.v[2 * tb], v1 = a.v[2 * tb + 1];
        { auto ra = __builtin_amdgcn_permlane16_swap(cvtpk(q0[0], q0[1]), cvtpk(q1[0], q1[1]), false, false); auto rb = __builtin_amdgcn_permlane16_swap(cvtpk(q0[2], q0[3]), cvtpk(q1[2], q1[3]), false, false);
          o.q[tb] = (u32x4){ra[0], rb[0], ra[1], rb[1]}; }
        { auto ra = __builtin_amdgcn_permlane16_swap(cvtpk(k0[0], k0[1]), cvtpk(k1[0], k1[1]), false, false); auto rb = __builtin_amdgcn_permlane16_swap(cvtpk(k0[2], k0[3]), cvtpk(k1[2], k1[3]), false, false);
          o.k[tb] = (u32x4){ra[0], rb[0], ra[1], rb[1]}; }
        { auto ra = __builtin_amdgcn_permlane32_swap(cvtpk(v0[0], v0[1]), cvtpk(v1[0], v1[1]), false, false); auto rb = __builtin_amdgcn_permlane32_swap(cvtpk(v0[2], v0[3]), cvtpk(v1[2], v1[3]), false, false);
          o.v[tb] = (u32x4){ra[0], rb[0], ra[1], rb[1]}; }
    }
}
template <int BR, int I>
__device__ __forceinline__ void qkv_store(const QkvOut& o, char* ws, int rt, int ct, int c, int lane) {
    constexpr int tb = I / 3, kind = I % 3;
    const int tile = rt * 2 + tb;
    if (kind < 2) {
        const unsigned off = (unsigned)(tile * 56 + 8 * ct + 2 * c) * 512u + (unsigned)lane * 16u;
        if (kind == 0) bstore128(o.q[tb], RSRC(ws + (BR ? WS_QB : WS_QA), QC_BYTES), off); else bstore128(o.k[tb], RSRC(ws + (BR ? WS_KB : WS_KA), QC_BYTES), off);
    } else {
        const int n = 64 * ct + 16 * c + (lane & 15), h = n / 28, d = n - 28 * h;
        const unsigned off = (unsigned)(h * 64 + tile) * 2048u + (unsigned)(lane >> 4) * 512u + (unsigned)d * 16u;
        const auto rs = RSRC(ws + (BR ? WS_VB : WS_VA), V_BYTES);
        bstore128(o.v[tb], rs, off);
        if (d >= 24) { const unsigned pad = d == 24 ? 0x3F803F80u : 0u; bstore128((u32x4){pad, pad, pad, pad}, rs, off + 64u); }
    }
}

__device__ __forceinline__ void qkv_phase(const u16* xA, const u16* wA, const u16* xB, const u16* wB, char* ws, int rt, int ct, u64 tag, char* lds, int wid, int lane, QkvOut& outB) {
    if (wid >= 4) {
        const unsigned lds0 = (unsigned)(uintptr_t)lds;
        const int lw = wid - 4, r8 = lane >> 3, dpos = lane & 7;
        unsigned xoff[2], woff[2];
#pragma unroll
        for (int i = 0; i < 2; ++i) { const int row = 16 * lw + 8 * i + r8;
            xoff[i] = (unsigned)row * 896u + (unsigned)((dpos ^ ((row >> 1) & 7)) << 4);
            const int f2 = ((row >> 1) & 1) | (((row >> 3) & 1) << 1);
            woff[i] = (unsigned)row * 2688u + (unsigned)((((dpos >> 1) ^ f2) * 2 + (dpos & 1)) << 4); }
        const int rot = __builtin_amdgcn_readfirstlane(((rt & 7) + 2 * (ct & 3)) % 7);
        const char* xa = (const char*)xA + (size_t)rt * 64 * 896; const char* wa = (const char*)wA + (size_t)ct * 128;
        const char* xb = (const char*)xB + (size_t)rt * 64 * 896; const char* wb = (const char*)wB + (size_t)ct * 128;
#define QKV_ISSUE(g) do { int st_ = ((g) % 7) + rot; st_ = st_ >= 7 ? st_ - 7 : st_; const char* x_ = ((g) < 7 ? xa : xb) + st_ * 128; const char* w_ = ((g) < 7 ? wa : wb) + (size_t)st_ * (64 * 2688); \
        const unsigned sl_ = lds0 + ((g) & 3) * STG + lw * 2048; \
        glds16(x_, xoff[0], sl_); glds16(x_, xoff[1], sl_ + 1024); \
        glds16(w_, woff[0], sl_ + 8192); glds16(w_, woff[1], sl_ + 8192 + 1024); glds16(w_ + 896, woff[0], sl_ + 16384); glds16(w_ + 896, woff[1], sl_ + 16384 + 1024); \
        glds16(w_ + 1792, woff[0], sl_ + 24576); glds16(w_ + 1792, woff[1], sl_ + 24576 + 1024); } while (0)
        QKV_ISSUE(0); QKV_ISSUE(1);
#pragma unroll
        for (int g = 0; g < 12; ++g) {
            asm volatile("s_waitcnt vmcnt(8) lgkmcnt(0)\n\ts_barrier" ::: "memory");
            QKV_ISSUE(g + 2);
        }
        asm volatile("s_waitcnt vmcnt(8)" ::: "memory");
        if (lane == 0) ctl_add(lds, CTL_S12);
        asm volatile("s_waitcnt vmcnt(0)" ::: "memory");
        if (lane == 0) ctl_add(lds, CTL_S13);
        return;
    }
    const int c = wid, g4 = lane >> 4, t16 = lane & 15;
    const unsigned xr0 = (unsigned)t16 * 128u + (unsigned)((g4 ^ (t16 >> 1)) << 4);
    const int q = t16 >> 2, p = lane & 3;
    const int rf2 = ((q >> 1) & 1) | ((g4 & 1) << 1);
    const unsigned wr0 = 8192u + (unsigned)(8 * g4 + q) * 128u + (unsigned)((c ^ rf2) * 32) + (unsigned)p * 8u;
    QkvAcc acc; QkvOut outA;
#pragma unroll
    for (int i = 0; i < 4; ++i) acc.q[i] = acc.k[i] = acc.v[i] = (f32x4){};
    QkvFrag fr[2];
#define QMF(i) do { if (g > 0) { constexpr int ks_ = (i) / 12, s_ = ((i) % 12) / 4, b_ = (i) % 4; const QkvFrag& f_ = fr[(g - 1) & 1]; \
        if (s_ == 0) acc.q[b_] = MFMA16(f_.w[ks_][0], f_.x[ks_][b_], acc.q[b_]); if (s_ == 1) acc.k[b_] = MFMA16(f_.w[ks_][1], f_.x[ks_][b_], acc.k[b_]); \
        if (s_ == 2) acc.v[b_] = MFMA16(f_.x[ks_][b_], f_.w[ks_][2], acc.v[b_]); } SBAR(); } while (0)
#define QRX(ks, b) do { if (g < 14) fr[g & 1].x[ks][b] = ldsrd128(sl + (xr0 ^ ((ks) * 64)) + (b) * 2048); } while (0)
#define QRW(ks, s) do { if (g < 14) { lds_cptr wp_ = sl + wr0 + (ks) * 4096 + (s) * 8192; fr[g & 1].w[ks][s] = cat8(vtr(wp_), vtr(wp_ + 512)); } } while (0)
#pragma unroll
    for (int g = 0; g < 15; ++g) {
        if (g == PUBG) asm volatile("s_waitcnt vmcnt(0) lgkmcnt(0)\n\ts_barrier" ::: "memory");
        else if (g < 12) asm volatile("s_waitcnt lgkmcnt(0)\n\ts_barrier" ::: "memory");
        else if (g == 12) ctl_wait(lds, CTL_S12, 4);
        else if (g == 13) ctl_wait(lds, CTL_S13, 4);
        SBAR();
        lds_cptr sl = (lds_cptr)lds + (g & 3) * STG;
        if (g == PUBG && threadIdx.x == 0) flag_set(ws, F1 + ct * 32 + rt, tag);
        SBAR();
        QRX(0, 0); QMF(0); QRX(0, 1); QMF(1); QRX(0, 2); QMF(2); QRX(0, 3); QMF(3); QRW(0, 0); QMF(4); QRW(0, 1); QMF(5); QRW(0, 2); QMF(6);
        QRX(1, 0); QMF(7); QRX(1, 1); QMF(8); QRX(1, 2); QMF(9); QRX(1, 3); QMF(10); QRW(1, 0); QMF(11); QRW(1, 1); QMF(12); QRW(1, 2); QMF(13);
        QMF(14); QMF(15); QMF(16); QMF(17); QMF(18); QMF(19); QMF(20); QMF(21); QMF(22); QMF(23);
        if (g == 7) {
            qkv_pack(outA, acc, QSCALE);
            qkv_store<0, 1>(outA, ws, rt, ct, c, lane); qkv_store<0, 2>(outA, ws, rt, ct, c, lane); qkv_store<0, 4>(outA, ws, rt, ct, c, lane);
            qkv_store<0, 5>(outA, ws, rt, ct, c, lane); qkv_store<0, 0>(outA, ws, rt, ct, c, lane); qkv_store<0, 3>(outA, ws, rt, ct, c, lane);
#pragma unroll
            for (int i = 0; i < 4; ++i) acc.q[i] = acc.k[i] = acc.v[i] = (f32x4){};
        }
    }
    if (lane == 0) ctl_add(lds, CTL_RDONE);
    qkv_pack(outB, acc, 1.0f);
}

__device__ __forceinline__ float max16(const f32x16& s) {
    float a = fmaxf(fmaxf(s[0], s[1]), s[2]), b = fmaxf(fmaxf(s[3], s[4]), s[5]);
    a = fmaxf(fmaxf(a, s[6]), s[7]); b = fmaxf(fmaxf(b, s[8]), s[9]); a = fmaxf(fmaxf(a, s[10]), s[11]); b = fmaxf(fmaxf(b, s[12]), s[13]);
    a = fmaxf(fmaxf(a, s[14]), s[15]); return fmaxf(a, b);
}
__device__ __forceinline__ float xhalf_max(float m) {
    auto r = __builtin_amdgcn_permlane32_swap(__float_as_uint(m), __float_as_uint(m), false, false);
    return fmaxf(__uint_as_float(r[0]), __uint_as_float(r[1]));
}
__device__ __forceinline__ void patch(u32x4& f0, u32x4& f1, bool pe, bool po, unsigned a, unsigned b) {
    f0[0] = po ? a : f0[0]; f0[1] = po ? b : f0[1]; f1[2] = pe ? a : f1[2]; f1[3] = pe ? b : f1[3];
}
__device__ __forceinline__ void exp_pack(const f32x16& s, u32x4& p0, u32x4& p1) {
    float e[16];
#pragma unroll
    for (int r = 0; r < 16; ++r) e[r] = __builtin_amdgcn_exp2f(s[r]);
    p0 = (u32x4){cvtpk(e[0], e[1]), cvtpk(e[2], e[3]), cvtpk(e[4], e[5]), cvtpk(e[6], e[7])};
    p1 = (u32x4){cvtpk(e[8], e[9]), cvtpk(e[10], e[11]), cvtpk(e[12], e[13]), cvtpk(e[14], e[15])};
}
__device__ __forceinline__ void merge_write1(const f32x16& o, char* lds, int wid, int j, int lane) {
#pragma unroll
    for (int hf = 0; hf < 2; ++hf)
        *(u32x4*)(lds + LDS_MRG + ((wid * 4 + j) * 2 + hf) * 1024 + lane * 16) = (u32x4){cvtpk(o[8 * hf], o[8 * hf + 1]), cvtpk(o[8 * hf + 2], o[8 * hf + 3]), cvtpk(o[8 * hf + 4], o[8 * hf + 5]), cvtpk(o[8 * hf + 6], o[8 * hf + 7])};
    if (lane >= 32) *(float*)(lds + LDS_L + ((wid * 4 + j) * 32 + lane - 32) * 4) = o[12];
}
__device__ __forceinline__ bool merge_reduce(char* ws, int h, int rb, char* lds, int wid, int lane) {
    const bool hi = lane >= 32;
    const int j = wid >> 1, hf = wid & 1;
    const int tile = rb * 4 + j, q = lane & 31;
    u32x4 p[8]; float pl[8];
#pragma unroll
    for (int w = 0; w < 8; ++w) pl[w] = *(const float*)(lds + LDS_L + ((w * 4 + j) * 32 + q) * 4);
#pragma unroll
    for (int w = 0; w < 8; ++w) p[w] = *(const u32x4*)(lds + LDS_MRG + ((w * 4 + j) * 2 + hf) * 1024 + lane * 16);
    const int col0 = 28 * h + 16 * hf + (hi ? 4 : 0), col1 = col0 + 8;
    const unsigned off0 = (unsigned)((tile * 56 + (col0 >> 3)) * 32 + q) * 16u + (unsigned)((col0 >> 2) & 1) * 8u, off1 = (unsigned)((tile * 56 + (col1 >> 3)) * 32 + q) * 16u + (unsigned)((col1 >> 2) & 1) * 8u;
    const float l = ((pl[0] + pl[1]) + (pl[2] + pl[3])) + ((pl[4] + pl[5]) + (pl[6] + pl[7]));
    const float inv = __builtin_amdgcn_rcpf(l);
    u32x4 sel;
    {   const int row = lane & 31, i = 4 * (row >> 3) + (row & 3);
        const unsigned one = (row < 16 && ((row >> 2) & 1) == (lane >> 5)) ? ((i & 1) ? 0x3F800000u : 0x00003F80u) : 0u;
        sel = (u32x4){(i >> 1) == 0 ? one : 0u, (i >> 1) == 1 ? one : 0u, (i >> 1) == 2 ? one : 0u, (i >> 1) == 3 ? one : 0u}; }
    f32x16 sum = {};
#pragma unroll
    for (int w = 0; w < 8; ++w) sum = MFMA32(asb(sel), asb(p[w]), sum);
    float a[8];
#pragma unroll
    for (int i = 0; i < 8; ++i) a[i] = sum[i];
#pragma unroll
    for (int i = 0; i < 8; ++i) a[i] *= inv;
    const auto rO = RSRC(ws + WS_OA, QC_BYTES);
    const bool last = hf == 1 && hi;
    bstore64((u32x2){cvtpk(a[0], a[1]), cvtpk(a[2], a[3])}, rO, off0);
    if (!last) bstore64((u32x2){cvtpk(a[4], a[5]), cvtpk(a[6], a[7])}, rO, off1);
    float z = a[0] * 0.f; z = fmaf(a[1], 0.f, z); z = fmaf(a[2], 0.f, z); z = fmaf(a[3], 0.f, z);
    float z1 = a[4] * 0.f; z1 = fmaf(a[5], 0.f, z1); z1 = fmaf(a[6], 0.f, z1); z1 = fmaf(a[7], 0.f, z1);
    const bool ok = l > LMIN && l < 1.0e36f && z == 0.f && (last || z1 == 0.f);
    return !ok;
}

__device__ __forceinline__ void attnA_safe(char* ws, int h, int rb, char* lds, int wid, int lane) {
    const bool hi = lane >= 32, pe = hi && !(h & 1), po = !hi && (h & 1); const int m0 = (28 * h) >> 3;
    const auto rQ = RSRC(ws + WS_QA, QC_BYTES), rK = RSRC(ws + WS_KA, QC_BYTES), rV = RSRC(ws + WS_VA, V_BYTES);
    const unsigned lo16 = (unsigned)lane * 16u;
    float* mx = (float*)(lds + LDS_MX);
#pragma unroll 1
    for (int j = 0; j < 4; ++j) {
        const unsigned b = (unsigned)((rb * 4 + j) * 56 + m0) * 512u;
        u32x4 q0 = bload128(rQ, lo16, b), q1 = bload128(rQ, lo16, b + 1024u);
        patch(q0, q1, pe, po, 0u, 0u);
        float m = -3.0e38f; f32x16 acc = {};
#pragma unroll 1
        for (int t = 0; t < 8; ++t) {
            const unsigned ko = (unsigned)((wid * 8 + t) * 56 + m0) * 512u, vo = (unsigned)(h * 64 + wid * 8 + t) * 2048u;
            u32x4 k0 = bload128(rK, lo16, ko), k1 = bload128(rK, lo16, ko + 1024u), v0 = bload128(rV, lo16, vo), v1 = bload128(rV, lo16, vo + 1024u);
            patch(k0, k1, pe, po, 0u, 0u);
            f32x16 s = {}; s = MFMA32(asb(k0), asb(q0), s); s = MFMA32(asb(k1), asb(q1), s);
            const float mn = fmaxf(m, xhalf_max(max16(s)));
            const float al = __builtin_amdgcn_exp2f(m - mn); m = mn;
#pragma unroll
            for (int r = 0; r < 16; ++r) { s[r] -= mn; acc[r] *= al; }
            u32x4 p0, p1; exp_pack(s, p0, p1);
            acc = MFMA32(asb(v0), asb(p0), acc); acc = MFMA32(asb(v1), asb(p1), acc);
        }
        BARRIER();
        if (!hi) mx[wid * 32 + lane] = m;
        BARRIER();
        float g = m;
#pragma unroll
        for (int w = 0; w < 8; ++w) g = fmaxf(g, mx[w * 32 + (lane & 31)]);
        const float f = __builtin_amdgcn_exp2f(m - g);
        merge_write1(acc * f, lds, wid, j, lane);
    }
}

struct AttnBPre { u32x4 q, kp, kc; u32x2 v[2][2]; };
__device__ __forceinline__ void attnB_load(AttnBPre& a, char* ws, int h, int rb, int wid, int lane) {
    const int m0 = (28 * h) >> 3, T = rb * 4 + (wid >> 1), sub = wid & 1;
    const int Tp = sub ? T : (T > 0 ? T - 1 : 0), subp = sub ^ 1;
    const auto rQ = RSRC(ws + WS_QB, QC_BYTES), rK = RSRC(ws + WS_KB, QC_BYTES), rV = RSRC(ws + WS_VB, V_BYTES);
    const int g = lane >> 4, t16 = lane & 15;
    const unsigned co = (unsigned)g * 512u + (unsigned)t16 * 16u;
    a.q = bload128(rQ, co, (unsigned)(T * 56 + m0) * 512u + (unsigned)sub * 256u);
    a.kp = bload128(rK, co, (unsigned)(Tp * 56 + m0) * 512u + (unsigned)subp * 256u);
    a.kc = bload128(rK, co, (unsigned)(T * 56 + m0) * 512u + (unsigned)sub * 256u);
    const unsigned vo = (unsigned)(g & 1) * 512u + (unsigned)t16 * 16u + (unsigned)(g >> 1) * 8u;
#pragma unroll
    for (int db = 0; db < 2; ++db) {
        a.v[db][0] = __builtin_amdgcn_raw_buffer_load_b64(rV, vo + db * 256u, (unsigned)(h * 64 + Tp) * 2048u + (unsigned)subp * 1024u, SC1);
        a.v[db][1] = __builtin_amdgcn_raw_buffer_load_b64(rV, vo + db * 256u, (unsigned)(h * 64 + T) * 2048u + (unsigned)sub * 1024u, SC1);
    }
}
__device__ __forceinline__ void attnB_compute(AttnBPre& a, char* ws, int h, int rb, int wid, int lane) {
    const int T = rb * 4 + (wid >> 1), sub = wid & 1, g = lane >> 4, ql = lane & 15;
    const bool po = (h & 1) && g == 0, pe = !(h & 1) && g == 3;
    patch(a.q, a.q, pe, po, 0u, 0u); patch(a.kp, a.kp, pe, po, 0u, 0u); patch(a.kc, a.kc, pe, po, 0u, 0u);
    f32x4 sp = {}, sc = {};
    sp = MFMA16(asb(a.kp), asb(a.q), sp); sc = MFMA16(asb(a.kc), asb(a.q), sc);
    const bool hasp = sub || T > 0;
    float m = -INFINITY;
    const int d0 = ql - 4 * g;
#pragma unroll
    for (int r = 0; r < 4; ++r) {
        const int dc = d0 - r; const bool ev = !(dc & 1);
        sc[r] = (ev && dc >= 0) ? sc[r] : -INFINITY; sp[r] = (ev && dc < 0 && hasp) ? sp[r] : -INFINITY;
        m = fmaxf(m, fmaxf(sp[r], sc[r]));
    }
    { auto r = __builtin_amdgcn_permlane16_swap(__float_as_uint(m), __float_as_uint(m), false, false); m = fmaxf(__uint_as_float(r[0]), __uint_as_float(r[1])); }
    m = xhalf_max(m);
    float e[8]; const float mq = -m * QSCALE;
#pragma unroll
    for (int r = 0; r < 4; ++r) { e[r] = __builtin_amdgcn_exp2f(fmaf(sp[r], QSCALE, mq)); e[4 + r] = __builtin_amdgcn_exp2f(fmaf(sc[r], QSCALE, mq)); }
    const u32x4 pf = {cvtpk(e[0], e[1]), cvtpk(e[2], e[3]), cvtpk(e[4], e[5]), cvtpk(e[6], e[7])};
    f32x4 o0 = {}, o1 = {};
    o0 = MFMA16(asb((u32x4){a.v[0][0][0], a.v[0][0][1], a.v[0][1][0], a.v[0][1][1]}), asb(pf), o0);
    o1 = MFMA16(asb((u32x4){a.v[1][0][0], a.v[1][0][1], a.v[1][1][0], a.v[1][1][1]}), asb(pf), o1);
    float l;
    { auto r16 = __builtin_amdgcn_permlane16_swap(__float_as_uint(o1[0]), __float_as_uint(o1[0]), false, false); auto r32 = __builtin_amdgcn_permlane32_swap(r16[1], r16[1], false, false); l = __uint_as_float(r32[1]); }
    const float inv = __builtin_amdgcn_rcpf(l);
    const auto rO = RSRC(ws + WS_OB, QC_BYTES);
    const unsigned tok = (unsigned)(sub * 16 + ql) * 16u;
    { const int col = 28 * h + 4 * g; bstore64((u32x2){cvtpk(o0[0] * inv, o0[1] * inv), cvtpk(o0[2] * inv, o0[3] * inv)}, rO, (unsigned)(T * 56 + (col >> 3)) * 512u + tok + (unsigned)((col >> 2) & 1) * 8u); }
    if (g < 3) { const int col = 28 * h + 16 + 4 * g; bstore64((u32x2){cvtpk(o1[0] * inv, o1[1] * inv), cvtpk(o1[2] * inv, o1[3] * inv)}, rO, (unsigned)(T * 56 + (col >> 3)) * 512u + tok + (unsigned)((col >> 2) & 1) * 8u); }
}

template <int N>
__device__ __forceinline__ void wo_issue(const u16* wo, int ct, unsigned lds_base, int pc0, int lane) {
    const int r8 = lane >> 3, dpos = lane & 7;
    const unsigned woff = (unsigned)r8 * 896u + (unsigned)((dpos ^ ((r8 & 2) << 1)) << 4);
#pragma unroll
    for (int i = 0; i < N; ++i) glds16((const char*)wo + (size_t)ct * 128 + (size_t)(pc0 + i) * 8 * 896, woff, lds_base + (pc0 + i) * 1024);
}
__device__ __forceinline__ void wf_read(u32x4 (&wf)[2][14], const char* img, int kh, int lane) {
    const int g16 = lane >> 4, q = (lane & 15) >> 2, p = lane & 3;
#pragma unroll
    for (int i = 0; i < 14; ++i)
#pragma unroll
        for (int c = 0; c < 2; ++c) {
            const int row = (kh * 14 + i) * 16 + 8 * (g16 >> 1) + q;
            lds_cptr wp = (lds_cptr)img + (unsigned)row * 128u + (unsigned)(((2 * c + (g16 & 1)) ^ (q & 2)) * 32) + (unsigned)p * 8u;
            wf[c][i] = __builtin_bit_cast(u32x4, cat8(vtr(wp), vtr(wp + 512)));
        }
}
__device__ __forceinline__ void attnA_phase(char* ws, const u16* woA, int oct, int h, int rb, u64 tag, int rt, int ct, bool has_qkv, bool first, const QkvOut& outB, u32x4 (&wf)[2][14], char* lds, int wid, int lane) {
    const bool hi = lane >= 32, pe = hi && !(h & 1), po = !hi && (h & 1);
    const int m0 = (28 * h) >> 3;
    const auto rK = RSRC(ws + WS_KA, QC_BYTES), rV = RSRC(ws + WS_VA, V_BYTES);
    const unsigned lo16 = (unsigned)lane * 16u;
    const unsigned kbase = (unsigned)((wid * 8) * 56 + m0) * 512u, vbase = (unsigned)(h * 64 + wid * 8) * 2048u;
    const int rot = rb & 7;
    u32x4 qf[4][2], kf[3][2], vf[3][2];
    {   const unsigned k0 = kbase + (unsigned)rot * TILE_B, v0 = vbase + (unsigned)rot * 2048u, k1 = kbase + (unsigned)((rot + 1) & 7) * TILE_B;
        kf[0][0] = bload128(rK, lo16, k0); kf[0][1] = bload128(rK, lo16, k0 + 1024u);
        if (wid >= 4) { const unsigned b = (unsigned)((rb * 4 + wid - 4) * 56 + m0) * 512u; const auto rQ = RSRC(ws + WS_QA, QC_BYTES); qf[0][0] = bload128(rQ, lo16, b); qf[0][1] = bload128(rQ, lo16, b + 1024u); }
        vf[0][0] = bload128(rV, lo16, v0); vf[0][1] = bload128(rV, lo16, v0 + 1024u);
        kf[1][0] = bload128(rK, lo16, k1); kf[1][1] = bload128(rK, lo16, k1 + 1024u);
        const unsigned v1 = vbase + (unsigned)((rot + 1) & 7) * 2048u, k2 = kbase + (unsigned)((rot + 2) & 7) * TILE_B;
        vf[1][0] = bload128(rV, lo16, v1); vf[1][1] = bload128(rV, lo16, v1 + 1024u);
        kf[2][0] = bload128(rK, lo16, k2); kf[2][1] = bload128(rK, lo16, k2 + 1024u); }
    patch(kf[0][0], kf[0][1], pe, po, 0x3F803F80u, 0u);
    if (wid >= 4) {
        patch(qf[0][0], qf[0][1], pe, po, 0u, 0u);
        f32x16 s = {}; s = MFMA32(asb(kf[0][0]), asb(qf[0][0]), s); s = MFMA32(asb(kf[0][1]), asb(qf[0][1]), s);
        const float m = xhalf_max(max16(s)) + POFF;
        const unsigned mh = cvtpk(-m, 0.f) & 0xffffu; const float r = -m - __uint_as_float(mh << 16);
        patch(qf[0][0], qf[0][1], pe, po, mh | (cvtpk(r, 0.f) << 16), 0u);
        *(u32x4*)(lds + LDS_Q + (wid - 4) * 2048 + lane * 16) = qf[0][0]; *(u32x4*)(lds + LDS_Q + (wid - 4) * 2048 + 1024 + lane * 16) = qf[0][1];
        asm volatile("s_waitcnt lgkmcnt(0)" ::: "memory");
        if (lane == 0) ctl_add(lds, CTL_QCNT);
    }
    ctl_wait(lds, CTL_QCNT, 4);
#pragma unroll
    for (int j = 0; j < 4; ++j) { qf[j][0] = *(const u32x4*)(lds + LDS_Q + j * 2048 + lane * 16); qf[j][1] = *(const u32x4*)(lds + LDS_Q + j * 2048 + 1024 + lane * 16); }
    if (has_qkv && wid < 4) {
        qkv_store<1, 0>(outB, ws, rt, ct, wid, lane); qkv_store<1, 1>(outB, ws, rt, ct, wid, lane); qkv_store<1, 2>(outB, ws, rt, ct, wid, lane);
        qkv_store<1, 3>(outB, ws, rt, ct, wid, lane); qkv_store<1, 4>(outB, ws, rt, ct, wid, lane); qkv_store<1, 5>(outB, ws, rt, ct, wid, lane);
    }
#pragma unroll
    for (int j = 0; j < 4; ++j) { PIN(qf[j][0]); PIN(qf[j][1]); }
    PIN(kf[0][0]); PIN(kf[0][1]);
    f32x16 o[4];
#define VEXP(d, s) asm volatile("v_exp_f32 %0, %1" : "=v"(d) : "v"(s))
#define VCVT(d, a, b) asm volatile("v_cvt_pk_bf16_f32 %0, %1, %2" : "=v"(d) : "v"(a), "v"(b))
#define MF0(d, a, b) asm volatile("v_mfma_f32_32x32x16_bf16 %0, %1, %2, 0" : "=&v"(d) : "v"(a), "v"(b))
#define MFA(d, a, b) asm volatile("v_mfma_f32_32x32x16_bf16 %0, %1, %2, %0" : "+v"(d) : "v"(a), "v"(b))
#define SLOT(PV, QK, Sc, Pc0, Pc1, Sn, kn0, kn1, qn0, qn1, Op, vp0, vp1, Pp0, Pp1, INJ) do { \
        float e0, e1, e2, e3, e4, e5, e6, e7, e8, e9, e10, e11, e12, e13, e14, e15; \
        if ((QK) != 0) MF0(Sn, kn0, qn0); \
        VEXP(e0, Sc[0]); VEXP(e1, Sc[1]); VEXP(e2, Sc[2]); VEXP(e3, Sc[3]); \
        if ((QK) != 0) MFA(Sn, kn1, qn1); \
        VEXP(e4, Sc[4]); VEXP(e5, Sc[5]); VEXP(e6, Sc[6]); VEXP(e7, Sc[7]); VCVT(Pc0[0], e0, e1); VCVT(Pc0[1], e2, e3); \
        INJ; \
        if ((PV) == 2) MFA(Op, vp0, Pp0); else if ((PV) == 1) MF0(Op, vp0, Pp0); \
        VEXP(e8, Sc[8]); VEXP(e9, Sc[9]); VEXP(e10, Sc[10]); VEXP(e11, Sc[11]); VCVT(Pc0[2], e4, e5); VCVT(Pc0[3], e6, e7); \
        if ((PV) != 0) MFA(Op, vp1, Pp1); \
        VEXP(e12, Sc[12]); VEXP(e13, Sc[13]); VEXP(e14, Sc[14]); VEXP(e15, Sc[15]); VCVT(Pc1[0], e8, e9); VCVT(Pc1[1], e10, e11); VCVT(Pc1[2], e12, e13); VCVT(Pc1[3], e14, e15); \
    } while (0)
    f32x16 sA, sB; u32x4 pA0, pA1, pB0, pB1;
    asm volatile("s_nop 1\n\tv_mfma_f32_32x32x16_bf16 %0, %1, %2, 0\n\tv_mfma_f32_32x32x16_bf16 %0, %3, %4, %0\n\ts_nop 7\n\ts_nop 3" : "=&v"(sA) : "v"(kf[0][0]), "v"(qf[0][0]), "v"(kf[0][1]), "v"(qf[0][1]));
#define PATCHK(kx) do { PIN(kx[0]); PIN(kx[1]); patch(kx[0], kx[1], pe, po, 0x3F803F80u, 0u); PIN(kx[0]); PIN(kx[1]); } while (0)
#define LOADK(kc, tt) do { if ((tt) < 8) { const unsigned o_ = kbase + (unsigned)(((tt) + rot) & 7) * TILE_B; kc[0] = bload128(rK, lo16, o_); kc[1] = bload128(rK, lo16, o_ + 1024u); } } while (0)
#define LOADV(vx, tt) do { if ((tt) < 8) { const unsigned o_ = vbase + (unsigned)(((tt) + rot) & 7) * 2048u; vx[0] = bload128(rV, lo16, o_); vx[1] = bload128(rV, lo16, o_ + 1024u); } } while (0)
#define TILE(t) do { \
        SLOT((t) == 0 ? 0 : (t) == 1 ? 1 : 2, true, sA, pA0, pA1, sB, kf[(t) % 3][0], kf[(t) % 3][1], qf[1][0], qf[1][1], o[3], vf[((t) + 2) % 3][0], vf[((t) + 2) % 3][1], pB0, pB1, (void)0); \
        LOADV(vf[((t) + 2) % 3], (t) + 2); \
        SLOT((t) == 0 ? 1 : 2, true, sB, pB0, pB1, sA, kf[(t) % 3][0], kf[(t) % 3][1], qf[2][0], qf[2][1], o[0], vf[(t) % 3][0], vf[(t) % 3][1], pA0, pA1, (void)0); \
        SLOT((t) == 0 ? 1 : 2, true, sA, pA0, pA1, sB, kf[(t) % 3][0], kf[(t) % 3][1], qf[3][0], qf[3][1], o[1], vf[(t) % 3][0], vf[(t) % 3][1], pB0, pB1, if ((t) < 7) PATCHK(kf[((t) + 1) % 3])); \
        LOADK(kf[(t) % 3], (t) + 3); \
        SLOT((t) == 0 ? 1 : 2, (t) < 7, sB, pB0, pB1, sA, kf[((t) + 1) % 3][0], kf[((t) + 1) % 3][1], qf[0][0], qf[0][1], o[2], vf[(t) % 3][0], vf[(t) % 3][1], pA0, pA1, (void)0); \
    } while (0)
    AttnBPre bpre; u64 flb = 0; bool bready = false;
#pragma unroll
    for (int t = 0; t < 8; t += 2) {
        if (t == 0) __builtin_amdgcn_s_setprio(3); else if (t == 2) __builtin_amdgcn_s_setprio(2); else if (t == 4) __builtin_amdgcn_s_setprio(1); else __builtin_amdgcn_s_setprio(0);
        TILE(t);
        if (t == 4) { bready = __all(flb == tag); attnB_load(bpre, ws, h, rb, wid, opaque_lane()); }
        TILE(t + 1);
        if (t == 0) {
            if (has_qkv && wid < 4) {
                asm volatile("s_waitcnt vmcnt(8)" ::: "memory");
                if (opaque_lane() == 0 && ctl_add(lds, CTL_F2CNT) == 3) flag_set(ws, F2 + ct * 32 + rt, tag);
            }
            if (oct < 7 && wid >= 4) {
                if (has_qkv) ctl_wait(lds, CTL_RDONE, 4);
                wo_issue<14>(woA, oct, (unsigned)(uintptr_t)lds, (wid - 4) * 14, opaque_lane());
            }
        } else if (t == 2) {
            const int ln = opaque_lane();
            if (oct < 7 && wid >= 4) { asm volatile("s_waitcnt vmcnt(8)" ::: "memory"); if (ln == 0) ctl_add(lds, CTL_WA); }
            flb = __hip_atomic_load(flagp(ws, f2_index(h, rb, ln)), __ATOMIC_RELAXED, __HIP_MEMORY_SCOPE_AGENT);
            if (first && wid == 1) { flag_wait(ws, F0 + (ln & 31), tag); if (ln == 0) ctl_store(lds, CTL_F0, 1); }
        }
    }
    asm volatile("s_nop 1\n\tv_mfma_f32_32x32x16_bf16 %0, %1, %2, %0\n\tv_mfma_f32_32x32x16_bf16 %0, %3, %4, %0\n\ts_nop 7\n\ts_nop 3" : "+v"(o[3]) : "v"(vf[1][0]), "v"(pB0), "v"(vf[1][1]), "v"(pB1));
    asm volatile("" : "+v"(o[0]), "+v"(o[1]), "+v"(o[2]));
    lane = opaque_lane();
    if (has_qkv) ctl_wait(lds, CTL_RDONE, 4);
#pragma unroll
    for (int j = 0; j < 4; ++j) merge_write1(o[j], lds, wid, j, lane);
    if (!bready) { flag_wait(ws, f2_index(h, rb, lane), tag); attnB_load(bpre, ws, h, rb, wid, lane); }
    PIN(bpre.q); PIN(bpre.kp); PIN(bpre.kc); PIN(bpre.v[0][0]); PIN(bpre.v[0][1]); PIN(bpre.v[1][0]); PIN(bpre.v[1][1]);
    if (oct < 7 && wid < 4) { ctl_wait(lds, CTL_WA, 4); wf_read(wf, lds, wid & 1, lane); }
    BARRIER();
    const bool bad = merge_reduce(ws, h, rb, lds, wid, lane);
    if (__any(bad) && lane == 0) ctl_store(lds, CTL_BAD, 1);
    attnB_compute(bpre, ws, h, rb, wid, lane);
    asm volatile("s_waitcnt vmcnt(2) lgkmcnt(0)" ::: "memory");
    if (lane == 0 && ctl_add(lds, CTL_F3CNT) == 7 && !ctl_load(lds, CTL_BAD)) flag_set(ws, F3 + rb * 16 + h, tag);
    asm volatile("s_waitcnt vmcnt(0)" ::: "memory");
    if (lane == 0 && ctl_add(lds, CTL_F4CNT) == 7) flag_set(ws, F4 + rb * 16 + h, tag);
    ctl_wait(lds, CTL_F3CNT, 8);
    if (__builtin_amdgcn_readfirstlane(ctl_load(lds, CTL_BAD))) {
        BARRIER();
        attnA_safe(ws, h, rb, lds, wid, lane);
        BARRIER();
        merge_reduce(ws, h, rb, lds, wid, lane);
        DRAIN_BARRIER();
        if (threadIdx.x == 0) flag_set(ws, F3 + rb * 16 + h, tag);
    }
}

__device__ __forceinline__ void oproj_compute(char* ws, const u16* woB, u16* out, int rt, int ct, u64 tag, bool first, u32x4 (&wf)[2][14], char* lds, int wid, int lane) {
    const int br = wid >> 2, tt = (wid >> 1) & 1, kh = wid & 1;
    if (wid < 3) {
        if (wid < 2) wo_issue<19>(woB, ct, (unsigned)(uintptr_t)lds + LDS_MRG, wid * 19, lane); else wo_issue<18>(woB, ct, (unsigned)(uintptr_t)lds + LDS_MRG, 38, lane);
    }
    if ((wid & 3) == 3) {
        flag_wait(ws, (br ? F4 : F3) + (rt >> 1) * 16 + (lane & 15), tag); if (lane == 0) ctl_store(lds, br ? CTL_F4 : CTL_F3, 1);
        if (first && !br) { ctl_wait(lds, CTL_F0, 1); if (lane == 0) flag_set(ws, FEP, tag); }
    }
    else {
        if (wid < 3) { asm volatile("s_waitcnt vmcnt(0)" ::: "memory"); if (lane == 0) ctl_add(lds, CTL_WB); }
        ctl_wait(lds, br ? CTL_F4 : CTL_F3, 1);
    }
    const auto rO = RSRC(ws + (br ? WS_OB : WS_OA), QC_BYTES);
    const int tile = rt * 2 + tt;
    u32x4 of[14];
#pragma unroll
    for (int i = 0; i < 14; ++i) of[i] = bload128(rO, (unsigned)lane * 16u, (unsigned)(tile * 56 + 2 * (kh * 14 + i)) * 512u);
    if (br) { ctl_wait(lds, CTL_WB, 3); wf_read(wf, lds + LDS_MRG, kh, lane); }
    f32x16 acc[2] = {};
#pragma unroll
    for (int i = 0; i < 14; ++i) { acc[0] = MFMA32(asb(wf[0][i]), asb(of[i]), acc[0]); acc[1] = MFMA32(asb(wf[1][i]), asb(of[i]), acc[1]); }
    asm volatile("" : "+v"(acc[0]), "+v"(acc[1]));
    {   const f32x16& gv = kh ? acc[0] : acc[1];
#pragma unroll
        for (int rq = 0; rq < 4; ++rq) *(f32x4*)(lds + 114688 + (wid * 4 + rq) * 1024 + lane * 16) = (f32x4){gv[4 * rq], gv[4 * rq + 1], gv[4 * rq + 2], gv[4 * rq + 3]};
    }
    asm volatile("s_waitcnt lgkmcnt(0)" ::: "memory");
    if (lane == 0) ctl_store(lds, CTL_X + wid * 4, 1);
    ctl_wait(lds, CTL_X + (wid ^ 1) * 4, 1);
    f32x16 r = kh ? acc[1] : acc[0];
#pragma unroll
    for (int rq = 0; rq < 4; ++rq) { const f32x4 x = *(const f32x4*)(lds + 114688 + ((wid ^ 1) * 4 + rq) * 1024 + lane * 16); r[4 * rq] += x[0]; r[4 * rq + 1] += x[1]; r[4 * rq + 2] += x[2]; r[4 * rq + 3] += x[3]; }
    const unsigned orow = (unsigned)(rt * 64 + tt * 32 + (lane & 31)) * 1792u + (unsigned)(br * 448 + ct * 64 + kh * 32 + (lane >> 5) * 8) * 2u;
#pragma unroll
    for (int pr = 0; pr < 2; ++pr) {
        const unsigned a0 = cvtpk(r[8 * pr], r[8 * pr + 1]), a1 = cvtpk(r[8 * pr + 2], r[8 * pr + 3]), b0 = cvtpk(r[8 * pr + 4], r[8 * pr + 5]), b1 = cvtpk(r[8 * pr + 6], r[8 * pr + 7]);
        auto r0 = __builtin_amdgcn_permlane32_swap(a0, b0, false, false);
        auto r1 = __builtin_amdgcn_permlane32_swap(a1, b1, false, false);
        bstore128((u32x4){r0[0], r1[0], r0[1], r1[1]}, RSRC(out, 2048 * 896 * 2), orow + pr * 32u);
    }
}

__global__ void __launch_bounds__(512, 2) fused_kernel(const u16* xA, const u16* wA, const u16* woA, const u16* xB, const u16* wB, const u16* woB, u16* out, char* ws) {
    extern __shared__ __attribute__((aligned(16))) char lds[];
    int lane = threadIdx.x & 63; const int wid = __builtin_amdgcn_readfirstlane(threadIdx.x >> 6);
    const int id = blockIdx.x, xcd = id & 7, loc = id >> 3;
    if (threadIdx.x < 32) ctl_store(lds, threadIdx.x * 4, 0);
    const u64 tag = __hip_atomic_load(flagp(ws, FEP), __ATOMIC_RELAXED, __HIP_MEMORY_SCOPE_AGENT) + 1;
    const int gb = xcd & 1, grt = 8 * (xcd >> 1) + (loc & 7), qct = gb * 4 + (loc >> 3);
    const bool has_qkv = qct < 7;
    const int h = xcd * 2 + (loc & 1), rb = loc >> 1;
    const int ort = 4 * xcd + (loc & 3), oct = loc >> 2;
    unsigned warm0 = 0;
    if (wid < 4) {
        u64 pc; int len;
        asm volatile("s_getpc_b64 %0\n.Lfused_pc_ref:\n\ts_mov_b32 %1, .Lfused_code_end - .Lfused_pc_ref" : "=s"(pc), "=s"(len));
        const int off = (wid * 64 + lane) * 128;
        if (off < len) warm0 = *(const __attribute__((address_space(1))) unsigned*)(pc + off);
    }
    QkvOut outB;
    if (has_qkv) qkv_phase(xA, wA, xB, wB, ws, grt, qct, tag, lds, wid, lane, outB);
    else { if (threadIdx.x == 0) flag_set(ws, F0 + (xcd >> 1) * 8 + (loc & 7), tag); BARRIER(); }
    asm volatile("" :: "v"(warm0));
    if (wid == 4) {
        flag_wait(ws, F1 + (lane < 32 ? (28 * h) >> 6 : (28 * h + 27) >> 6) * 32 + (lane & 31), tag);
        if (lane == 0) ctl_store(lds, CTL_F1, 1);
    } else ctl_wait(lds, CTL_F1, 1);
    const u64 tags = (u64)(unsigned)__builtin_amdgcn_readfirstlane((unsigned)tag) | ((u64)(unsigned)__builtin_amdgcn_readfirstlane((unsigned)(tag >> 32)) << 32);
    u32x4 wf[2][14];
    attnA_phase(ws, woA, oct, h, rb, tags, grt, qct, has_qkv, id == 0, outB, wf, lds, wid, lane);
    lane = opaque_lane();
    if (oct < 7) {
        oproj_compute(ws, woB, out, ort, oct, tags, id == 0, wf, lds, wid, lane);
    }
    asm volatile(".Lfused_code_end:");
}

extern "C" void kernel_launch(void* const* d_in, const int* in_sizes, int n_in, void* d_out, int out_size, void* d_ws, size_t ws_size, hipStream_t stream) {
    const u16* xA = (const u16*)d_in[0]; const u16* wA = (const u16*)d_in[1]; const u16* woA = (const u16*)d_in[2];
    const u16* xB = (const u16*)d_in[3]; const u16* wB = (const u16*)d_in[4]; const u16* woB = (const u16*)d_in[5];
    (void)hipFuncSetAttribute((const void*)fused_kernel, hipFuncAttributeMaxDynamicSharedMemorySize, LDS_TOTAL);
    fused_kernel<<<256, 512, LDS_TOTAL, stream>>>(xA, wA, woA, xB, wB, woB, (u16*)d_out, (char*)d_ws);
}
```

```cpp
#include <hip/hip_runtime.h>
#include <hip/hip_bf16.h>
#include <stdint.h>

typedef unsigned short u16;
typedef unsigned long long u64;
typedef short bf16x8 __attribute__((ext_vector_type(8)));
typedef short s16x4 __attribute__((ext_vector_type(4)));
typedef float f32x4 __attribute__((ext_vector_type(4)));
typedef float f32x16 __attribute__((ext_vector_type(16)));
typedef unsigned u32x4 __attribute__((ext_vector_type(4)));
typedef unsigned u32x2 __attribute__((ext_vector_type(2)));
typedef __attribute__((address_space(3))) const char* lds_cptr;
typedef __attribute__((address_space(1))) u64 gu64;

constexpr int TILE_B = 56 * 512;
constexpr size_t QC_BYTES = (size_t)64 * TILE_B;
constexpr size_t V_BYTES = (size_t)16 * 64 * 2048;
constexpr size_t WS_QA = 0, WS_KA = WS_QA + QC_BYTES, WS_VA = WS_KA + QC_BYTES;
constexpr size_t WS_QB = WS_VA + V_BYTES, WS_KB = WS_QB + QC_BYTES, WS_VB = WS_KB + QC_BYTES;
constexpr size_t WS_OA = WS_VB + V_BYTES, WS_OB = WS_OA + QC_BYTES, WS_FLAGS = WS_OB + QC_BYTES;
constexpr int F1 = 0, F2 = 256, F3 = 512, F4 = 768, F0 = 1024, FEP = 1056;
constexpr float QSCALE = 0.18898223650461362f * 1.4426950408889634f;
constexpr int STG = 32768;
constexpr int LDS_Q = 147456, LDS_L = 131072, LDS_MRG = 57344, LDS_CTL = LDS_Q + 8192, LDS_MX = LDS_CTL + 128, LDS_TOTAL = LDS_MX + 1024 + 64;
constexpr int CTL_F1 = 0, CTL_F2CNT = 4, CTL_BAD = 8, CTL_QCNT = 12, CTL_F4CNT = 16, CTL_F3 = 20, CTL_RDONE = 24, CTL_F4 = 28, CTL_X = 32;
constexpr int CTL_WA = 64, CTL_WB = 68, CTL_S12 = 72, CTL_S13 = 76, CTL_F3CNT = 80, CTL_F0 = 84;
constexpr int PUBG = 10;
#define POFF 96.0f
#define LMIN (__builtin_ldexpf(1.0f, -(int)(POFF) - 20))
constexpr unsigned SPIN_MAX = 1u << 18;


#define MFMA16(a, b, c) __builtin_amdgcn_mfma_f32_16x16x32_bf16(a, b, c, 0, 0, 0)
#define MFMA32(a, b, c) __builtin_amdgcn_mfma_f32_32x32x16_bf16(a, b, c, 0, 0, 0)
#define PIN(x) asm volatile("" : "+v"(x))
#define SBAR() __builtin_amdgcn_sched_barrier(0)
#define SC1 16

__device__ __forceinline__ unsigned cvtpk(float lo, float hi) {
    typedef float f2 __attribute__((ext_vector_type(2))); typedef __bf16 b2 __attribute__((ext_vector_type(2)));
    f2 v = {lo, hi}; b2 r = __builtin_convertvector(v, b2); return __builtin_bit_cast(unsigned, r);
}
__device__ __forceinline__ void glds16(const void* sbase, unsigned voff, unsigned lds_base) {
    asm volatile("s_mov_b32 m0, %2\n\ts_nop 0\n\tglobal_load_lds_dwordx4 %0, %1" :: "v"(voff), "s"(sbase), "s"(lds_base) : "memory");
}
__device__ __forceinline__ s16x4 vtr(lds_cptr p) {
    typedef short v4i16_t __attribute__((ext_vector_type(4)));
    return __builtin_bit_cast(s16x4, __builtin_amdgcn_ds_read_tr16_b64_v4i16((__attribute__((address_space(3))) v4i16_t*)p));
}
__device__ __forceinline__ bf16x8 cat8(s16x4 a, s16x4 b) { return (bf16x8){a[0], a[1], a[2], a[3], b[0], b[1], b[2], b[3]}; }
__device__ __forceinline__ bf16x8 ldsrd128(lds_cptr p) { return *(const __attribute__((address_space(3))) bf16x8*)p; }
#define RSRC(p, bytes) __builtin_amdgcn_make_buffer_rsrc((void*)(p), 0, (int)(bytes), 0x00020000)
__device__ __forceinline__ u32x4 bload128(__amdgpu_buffer_rsrc_t r, unsigned voff, unsigned soff) { return __builtin_amdgcn_raw_buffer_load_b128(r, voff, soff, SC1); }
__device__ __forceinline__ void bstore128(u32x4 v, __amdgpu_buffer_rsrc_t r, unsigned voff) { __builtin_amdgcn_raw_buffer_store_b128(v, r, voff, 0, SC1); }
__device__ __forceinline__ void bstore64(u32x2 v, __amdgpu_buffer_rsrc_t r, unsigned voff) { __builtin_amdgcn_raw_buffer_store_b64(v, r, voff, 0, SC1); }
__device__ __forceinline__ bf16x8 asb(u32x4 v) { return __builtin_bit_cast(bf16x8, v); }

__device__ __forceinline__ gu64* flagp(char* ws, int idx) { return (gu64*)(ws + WS_FLAGS + (size_t)idx * 128); }
__device__ __forceinline__ void flag_set(char* ws, int idx, u64 tag) { __hip_atomic_store(flagp(ws, idx), tag, __ATOMIC_RELAXED, __HIP_MEMORY_SCOPE_AGENT); }
__device__ __forceinline__ void flag_wait(char* ws, int idx, u64 tag) {
    gu64* p = flagp(ws, idx);
    for (unsigned spins = 0; spins < SPIN_MAX; ++spins) {
        const u64 v = __hip_atomic_load(p, __ATOMIC_RELAXED, __HIP_MEMORY_SCOPE_AGENT);
        if (__all(v == tag)) break;
        __builtin_amdgcn_s_sleep(1);
    }
}
__device__ __forceinline__ int opaque_lane() { int l; asm volatile("v_mbcnt_lo_u32_b32 %0, -1, 0\n\tv_mbcnt_hi_u32_b32 %0, -1, %0" : "=v"(l)); return l; }
__device__ __forceinline__ int f2_index(int h, int rb, int lane) { const int i = lane % 6, r = 2 * rb - 1 + i % 3; return F2 + (i < 3 ? (28 * h) >> 6 : (28 * h + 27) >> 6) * 32 + (r < 0 ? 0 : r); }
typedef __attribute__((address_space(3))) int* lds_iptr;
__device__ __forceinline__ int ctl_load(char* lds, int off) { return __hip_atomic_load((lds_iptr)(lds + LDS_CTL + off), __ATOMIC_RELAXED, __HIP_MEMORY_SCOPE_WORKGROUP); }
__device__ __forceinline__ void ctl_store(char* lds, int off, int v) { __hip_atomic_store((lds_iptr)(lds + LDS_CTL + off), v, __ATOMIC_RELAXED, __HIP_MEMORY_SCOPE_WORKGROUP); }
__device__ __forceinline__ void ctl_wait(char* lds, int off, int n) {
    for (unsigned spins = 0; spins < (SPIN_MAX << 6); ++spins) { if (__builtin_amdgcn_readfirstlane(ctl_load(lds, off)) >= n) break; __builtin_amdgcn_s_sleep(1); }
    asm volatile("" ::: "memory");
}
__device__ __forceinline__ int ctl_add(char* lds, int off) { return __hip_atomic_fetch_add((lds_iptr)(lds + LDS_CTL + off), 1, __ATOMIC_RELAXED, __HIP_MEMORY_SCOPE_WORKGROUP); }
#define BARRIER() asm volatile("s_waitcnt lgkmcnt(0)\n\ts_barrier" ::: "memory")
#define DRAIN_BARRIER() asm volatile("s_waitcnt vmcnt(0) lgkmcnt(0)\n\ts_barrier" ::: "memory")

struct QkvFrag { bf16x8 x[2][4], w[2][3]; };
struct QkvAcc { f32x4 q[4], k[4], v[4]; };
struct QkvOut { u32x4 q[2], k[2], v[2]; };
__device__ __forceinline__ void qkv_pack(QkvOut& o, const QkvAcc& a, float qs) {
#pragma unroll
    for (int tb = 0; tb < 2; ++tb) {
        const f32x4 q0 = a.q[2 * tb] * qs, q1 = a.q[2 * tb + 1] * qs, k0 = a.k[2 * tb], k1 = a.k[2 * tb + 1], v0 = a.v[2 * tb], v1 = a.v[2 * tb + 1];
        { auto ra = __builtin_amdgcn_permlane16_swap(cvtpk(q0[0], q0[1]), cvtpk(q1[0], q1[1]), false, false); auto rb = __builtin_amdgcn_permlane16_swap(cvtpk(q0[2], q0[3]), cvtpk(q1[2], q1[3]), false, false);
          o.q[tb] = (u32x4){ra[0], rb[0], ra[1], rb[1]}; }
        { auto ra = __builtin_amdgcn_permlane16_swap(cvtpk(k0[0], k0[1]), cvtpk(k1[0], k1[1]), false, false); auto rb = __builtin_amdgcn_permlane16_swap(cvtpk(k0[2], k0[3]), cvtpk(k1[2], k1[3]), false, false);
          o.k[tb] = (u32x4){ra[0], rb[0], ra[1], rb[1]}; }
        { auto ra = __builtin_amdgcn_permlane32_swap(cvtpk(v0[0], v0[1]), cvtpk(v1[0], v1[1]), false, false); auto rb = __builtin_amdgcn_permlane32_swap(cvtpk(v0[2], v0[3]), cvtpk(v1[2], v1[3]), false, false);
          o.v[tb] = (u32x4){ra[0], rb[0], ra[1], rb[1]}; }
    }
}
template <int BR, int I>
__device__ __forceinline__ void qkv_store(const QkvOut& o, char* ws, int rt, int ct, int c, int lane) {
    constexpr int tb = I / 3, kind = I % 3;
    const int tile = rt * 2 + tb;
    if (kind < 2) {
        const unsigned off = (unsigned)(tile * 56 + 8 * ct + 2 * c) * 512u + (unsigned)lane * 16u;
        if (kind == 0) bstore128(o.q[tb], RSRC(ws + (BR ? WS_QB : WS_QA), QC_BYTES), off); else bstore128(o.k[tb], RSRC(ws + (BR ? WS_KB : WS_KA), QC_BYTES), off);
    } else {
        const int n = 64 * ct + 16 * c + (lane & 15), h = n / 28, d = n - 28 * h;
        const unsigned off = (unsigned)(h * 64 + tile) * 2048u + (unsigned)(lane >> 4) * 512u + (unsigned)d * 16u;
        const auto rs = RSRC(ws + (BR ? WS_VB : WS_VA), V_BYTES);
        bstore128(o.v[tb], rs, off);
        if (d >= 24) { const unsigned pad = d == 24 ? 0x3F803F80u : 0u; bstore128((u32x4){pad, pad, pad, pad}, rs, off + 64u); }
    }
}

__device__ __forceinline__ void qkv_phase(const u16* xA, const u16* wA, const u16* xB, const u16* wB, char* ws, int rt, int ct, u64 tag, char* lds, int wid, int lane, QkvOut& outB) {
    if (wid >= 4) {
        const unsigned lds0 = (unsigned)(uintptr_t)lds;
        const int lw = wid - 4, r8 = lane >> 3, dpos = lane & 7;
        unsigned xoff[2], woff[2];
#pragma unroll
        for (int i = 0; i < 2; ++i) { const int row = 16 * lw + 8 * i + r8;
            xoff[i] = (unsigned)row * 896u + (unsigned)((dpos ^ ((row >> 1) & 7)) << 4);
            const int f2 = ((row >> 1) & 1) | (((row >> 3) & 1) << 1);
            woff[i] = (unsigned)row * 2688u + (unsigned)((((dpos >> 1) ^ f2) * 2 + (dpos & 1)) << 4); }
        const int rot = __builtin_amdgcn_readfirstlane(((rt & 7) + 2 * (ct & 3)) % 7);
        const char* xa = (const char*)xA + (size_t)rt * 64 * 896; const char* wa = (const char*)wA + (size_t)ct * 128;
        const char* xb = (const char*)xB + (size_t)rt * 64 * 896; const char* wb = (const char*)wB + (size_t)ct * 128;
#define QKV_ISSUE(g) do { int st_ = ((g) % 7) + rot; st_ = st_ >= 7 ? st_ - 7 : st_; const char* x_ = ((g) < 7 ? xa : xb) + st_ * 128; const char* w_ = ((g) < 7 ? wa : wb) + (size_t)st_ * (64 * 2688); \
        const unsigned sl_ = lds0 + ((g) & 3) * STG + lw * 2048; \
        glds16(x_, xoff[0], sl_); glds16(x_, xoff[1], sl_ + 1024); \
        glds16(w_, woff[0], sl_ + 8192); glds16(w_, woff[1], sl_ + 8192 + 1024); glds16(w_ + 896, woff[0], sl_ + 16384); glds16(w_ + 896, woff[1], sl_ + 16384 + 1024); \
        glds16(w_ + 1792, woff[0], sl_ + 24576); glds16(w_ + 1792, woff[1], sl_ + 24576 + 1024); } while (0)
        QKV_ISSUE(0); QKV_ISSUE(1);
#pragma unroll
        for (int g = 0; g < 12; ++g) {
            asm volatile("s_waitcnt vmcnt(8) lgkmcnt(0)\n\ts_barrier" ::: "memory");
            QKV_ISSUE(g + 2);
        }
        asm volatile("s_waitcnt vmcnt(8)" ::: "memory");
        if (lane == 0) ctl_add(lds, CTL_S12);
        asm volatile("s_waitcnt vmcnt(0)" ::: "memory");
        if (lane == 0) ctl_add(lds, CTL_S13);
        return;
    }
    const int c = wid, g4 = lane >> 4, t16 = lane & 15;
    const unsigned xr0 = (unsigned)t16 * 128u + (unsigned)((g4 ^ (t16 >> 1)) << 4);
    const int q = t16 >> 2, p = lane & 3;
    const int rf2 = ((q >> 1) & 1) | ((g4 & 1) << 1);
    const unsigned wr0 = 8192u + (unsigned)(8 * g4 + q) * 128u + (unsigned)((c ^ rf2) * 32) + (unsigned)p * 8u;
    QkvAcc acc; QkvOut outA;
#pragma unroll
    for (int i = 0; i < 4; ++i) acc.q[i] = acc.k[i] = acc.v[i] = (f32x4){};
    QkvFrag fr[2];
#define QMF(i) do { if (g > 0) { constexpr int ks_ = (i) / 12, s_ = ((i) % 12) / 4, b_ = (i) % 4; const QkvFrag& f_ = fr[(g - 1) & 1]; \
        if (s_ == 0) acc.q[b_] = MFMA16(f_.w[ks_][0], f_.x[ks_][b_], acc.q[b_]); if (s_ == 1) acc.k[b_] = MFMA16(f_.w[ks_][1], f_.x[ks_][b_], acc.k[b_]); \
        if (s_ == 2) acc.v[b_] = MFMA16(f_.x[ks_][b_], f_.w[ks_][2], acc.v[b_]); } SBAR(); } while (0)
#define QRX(ks, b) do { if (g < 14) fr[g & 1].x[ks][b] = ldsrd128(sl + (xr0 ^ ((ks) * 64)) + (b) * 2048); } while (0)
#define QRW(ks, s) do { if (g < 14) { lds_cptr wp_ = sl + wr0 + (ks) * 4096 + (s) * 8192; fr[g & 1].w[ks][s] = cat8(vtr(wp_), vtr(wp_ + 512)); } } while (0)
#pragma unroll
    for (int g = 0; g < 15; ++g) {
        if (g == PUBG) asm volatile("s_waitcnt vmcnt(0) lgkmcnt(0)\n\ts_barrier" ::: "memory");
        else if (g < 12) asm volatile("s_waitcnt lgkmcnt(0)\n\ts_barrier" ::: "memory");
        else if (g == 12) ctl_wait(lds, CTL_S12, 4);
        else if (g == 13) ctl_wait(lds, CTL_S13, 4);
        SBAR();
        lds_cptr sl = (lds_cptr)lds + (g & 3) * STG;
        if (g == PUBG && threadIdx.x == 0) flag_set(ws, F1 + ct * 32 + rt, tag);
        SBAR();
        QRX(0, 0); QMF(0); QRX(0, 1); QMF(1); QRX(0, 2); QMF(2); QRX(0, 3); QMF(3); QRW(0, 0); QMF(4); QRW(0, 1); QMF(5); QRW(0, 2); QMF(6);
        QRX(1, 0); QMF(7); QRX(1, 1); QMF(8); QRX(1, 2); QMF(9); QRX(1, 3); QMF(10); QRW(1, 0); QMF(11); QRW(1, 1); QMF(12); QRW(1, 2); QMF(13);
        QMF(14); QMF(15); QMF(16); QMF(17); QMF(18); QMF(19); QMF(20); QMF(21); QMF(22); QMF(23);
        if (g == 7) {
            qkv_pack(outA, acc, QSCALE);
            qkv_store<0, 1>(outA, ws, rt, ct, c, lane); qkv_store<0, 2>(outA, ws, rt, ct, c, lane); qkv_store<0, 4>(outA, ws, rt, ct, c, lane);
            qkv_store<0, 5>(outA, ws, rt, ct, c, lane); qkv_store<0, 0>(outA, ws, rt, ct, c, lane); qkv_store<0, 3>(outA, ws, rt, ct, c, lane);
#pragma unroll
            for (int i = 0; i < 4; ++i) acc.q[i] = acc.k[i] = acc.v[i] = (f32x4){};
        }
    }
    if (lane == 0) ctl_add(lds, CTL_RDONE);
    qkv_pack(outB, acc, 1.0f);
}

__device__ __forceinline__ float max16(const f32x16& s) {
    float a = fmaxf(fmaxf(s[0], s[1]), s[2]), b = fmaxf(fmaxf(s[3], s[4]), s[5]);
    a = fmaxf(fmaxf(a, s[6]), s[7]); b = fmaxf(fmaxf(b, s[8]), s[9]); a = fmaxf(fmaxf(a, s[10]), s[11]); b = fmaxf(fmaxf(b, s[12]), s[13]);
    a = fmaxf(fmaxf(a, s[14]), s[15]); return fmaxf(a, b);
}
__device__ __forceinline__ float xhalf_max(float m) {
    auto r = __builtin_amdgcn_permlane32_swap(__float_as_uint(m), __float_as_uint(m), false, false);
    return fmaxf(__uint_as_float(r[0]), __uint_as_float(r[1]));
}
__device__ __forceinline__ void patch(u32x4& f0, u32x4& f1, bool pe, bool po, unsigned a, unsigned b) {
    f0[0] = po ? a : f0[0]; f0[1] = po ? b : f0[1]; f1[2] = pe ? a : f1[2]; f1[3] = pe ? b : f1[3];
}
__device__ __forceinline__ void exp_pack(const f32x16& s, u32x4& p0, u32x4& p1) {
    float e[16];
#pragma unroll
    for (int r = 0; r < 16; ++r) e[r] = __builtin_amdgcn_exp2f(s[r]);
    p0 = (u32x4){cvtpk(e[0], e[1]), cvtpk(e[2], e[3]), cvtpk(e[4], e[5]), cvtpk(e[6], e[7])};
    p1 = (u32x4){cvtpk(e[8], e[9]), cvtpk(e[10], e[11]), cvtpk(e[12], e[13]), cvtpk(e[14], e[15])};
}
__device__ __forceinline__ void merge_write1(const f32x16& o, char* lds, int wid, int j, int lane) {
#pragma unroll
    for (int hf = 0; hf < 2; ++hf)
        *(u32x4*)(lds + LDS_MRG + ((wid * 4 + j) * 2 + hf) * 1024 + lane * 16) = (u32x4){cvtpk(o[8 * hf], o[8 * hf + 1]), cvtpk(o[8 * hf + 2], o[8 * hf + 3]), cvtpk(o[8 * hf + 4], o[8 * hf + 5]), cvtpk(o[8 * hf + 6], o[8 * hf + 7])};
    if (lane >= 32) *(float*)(lds + LDS_L + ((wid * 4 + j) * 32 + lane - 32) * 4) = o[12];
}
__device__ __forceinline__ bool merge_reduce(char* ws, int h, int rb, char* lds, int wid, int lane) {
    const bool hi = lane >= 32;
    const int j = wid >> 1, hf = wid & 1;
    const int tile = rb * 4 + j, q = lane & 31;
    u32x4 p[8]; float pl[8];
#pragma unroll
    for (int w = 0; w < 8; ++w) pl[w] = *(const float*)(lds + LDS_L + ((w * 4 + j) * 32 + q) * 4);
#pragma unroll
    for (int w = 0; w < 8; ++w) p[w] = *(const u32x4*)(lds + LDS_MRG + ((w * 4 + j) * 2 + hf) * 1024 + lane * 16);
    const int col0 = 28 * h + 16 * hf + (hi ? 4 : 0), col1 = col0 + 8;
    const unsigned off0 = (unsigned)((tile * 56 + (col0 >> 3)) * 32 + q) * 16u + (unsigned)((col0 >> 2) & 1) * 8u, off1 = (unsigned)((tile * 56 + (col1 >> 3)) * 32 + q) * 16u + (unsigned)((col1 >> 2) & 1) * 8u;
    const float l = ((pl[0] + pl[1]) + (pl[2] + pl[3])) + ((pl[4] + pl[5]) + (pl[6] + pl[7]));
    const float inv = __builtin_amdgcn_rcpf(l);
    u32x4 sel;
    {   const int row = lane & 31, i = 4 * (row >> 3) + (row & 3);
        const unsigned one = (row < 16 && ((row >> 2) & 1) == (lane >> 5)) ? ((i & 1) ? 0x3F800000u : 0x00003F80u) : 0u;
        sel = (u32x4){(i >> 1) == 0 ? one : 0u, (i >> 1) == 1 ? one : 0u, (i >> 1) == 2 ? one : 0u, (i >> 1) == 3 ? one : 0u}; }
    f32x16 sum = {};
#pragma unroll
    for (int w = 0; w < 8; ++w) sum = MFMA32(asb(sel), asb(p[w]), sum);
    float a[8];
#pragma unroll
    for (int i = 0; i < 8; ++i) a[i] = sum[i];
#pragma unroll
    for (int i = 0; i < 8; ++i) a[i] *= inv;
    const auto rO = RSRC(ws + WS_OA, QC_BYTES);
    const bool last = hf == 1 && hi;
    bstore64((u32x2){cvtpk(a[0], a[1]), cvtpk(a[2], a[3])}, rO, off0);
    if (!last) bstore64((u32x2){cvtpk(a[4], a[5]), cvtpk(a[6], a[7])}, rO, off1);
    float z = a[0] * 0.f; z = fmaf(a[1], 0.f, z); z = fmaf(a[2], 0.f, z); z = fmaf(a[3], 0.f, z);
    float z1 = a[4] * 0.f; z1 = fmaf(a[5], 0.f, z1); z1 = fmaf(a[6], 0.f, z1); z1 = fmaf(a[7], 0.f, z1);
    const bool ok = l > LMIN && l < 1.0e36f && z == 0.f && (last || z1 == 0.f);
    return !ok;
}

__device__ __forceinline__ void attnA_safe(char* ws, int h, int rb, char* lds, int wid, int lane) {
    const bool hi = lane >= 32, pe = hi && !(h & 1), po = !hi && (h & 1); const int m0 = (28 * h) >> 3;
    const auto rQ = RSRC(ws + WS_QA, QC_BYTES), rK = RSRC(ws + WS_KA, QC_BYTES), rV = RSRC(ws + WS_VA, V_BYTES);
    const unsigned lo16 = (unsigned)lane * 16u;
    float* mx = (float*)(lds + LDS_MX);
#pragma unroll 1
    for (int j = 0; j < 4; ++j) {
        const unsigned b = (unsigned)((rb * 4 + j) * 56 + m0) * 512u;
        u32x4 q0 = bload128(rQ, lo16, b), q1 = bload128(rQ, lo16, b + 1024u);
        patch(q0, q1, pe, po, 0u, 0u);
        float m = -3.0e38f; f32x16 acc = {};
#pragma unroll 1
        for (int t = 0; t < 8; ++t) {
            const unsigned ko = (unsigned)((wid * 8 + t) * 56 + m0) * 512u, vo = (unsigned)(h * 64 + wid * 8 + t) * 2048u;
            u32x4 k0 = bload128(rK, lo16, ko), k1 = bload128(rK, lo16, ko + 1024u), v0 = bload128(rV, lo16, vo), v1 = bload128(rV, lo16, vo + 1024u);
            patch(k0, k1, pe, po, 0u, 0u);
            f32x16 s = {}; s = MFMA32(asb(k0), asb(q0), s); s = MFMA32(asb(k1), asb(q1), s);
            const float mn = fmaxf(m, xhalf_max(max16(s)));
            const float al = __builtin_amdgcn_exp2f(m - mn); m = mn;
#pragma unroll
            for (int r = 0; r < 16; ++r) { s[r] -= mn; acc[r] *= al; }
            u32x4 p0, p1; exp_pack(s, p0, p1);
            acc = MFMA32(asb(v0), asb(p0), acc); acc = MFMA32(asb(v1), asb(p1), acc);
        }
        BARRIER();
        if (!hi) mx[wid * 32 + lane] = m;
        BARRIER();
        float g = m;
#pragma unroll
        for (int w = 0; w < 8; ++w) g = fmaxf(g, mx[w * 32 + (lane & 31)]);
        const float f = __builtin_amdgcn_exp2f(m - g);
        merge_write1(acc * f, lds, wid, j, lane);
    }
}

struct AttnBPre { u32x4 q, kp, kc; u32x2 v[2][2]; };
__device__ __forceinline__ void attnB_load(AttnBPre& a, char* ws, int h, int rb, int wid, int lane) {
    const int m0 = (28 * h) >> 3, T = rb * 4 + (wid >> 1), sub = wid & 1;
    const int Tp = sub ? T : (T > 0 ? T - 1 : 0), subp = sub ^ 1;
    const auto rQ = RSRC(ws + WS_QB, QC_BYTES), rK = RSRC(ws + WS_KB, QC_BYTES), rV = RSRC(ws + WS_VB, V_BYTES);
    const int g = lane >> 4, t16 = lane & 15;
    const unsigned co = (unsigned)g * 512u + (unsigned)t16 * 16u;
    a.q = bload128(rQ, co, (unsigned)(T * 56 + m0) * 512u + (unsigned)sub * 256u);
    a.kp = bload128(rK, co, (unsigned)(Tp * 56 + m0) * 512u + (unsigned)subp * 256u);
    a.kc = bload128(rK, co, (unsigned)(T * 56 + m0) * 512u + (unsigned)sub * 256u);
    const unsigned vo = (unsigned)(g & 1) * 512u + (unsigned)t16 * 16u + (unsigned)(g >> 1) * 8u;
#pragma unroll
    for (int db = 0; db < 2; ++db) {
        a.v[db][0] = __builtin_amdgcn_raw_buffer_load_b64(rV, vo + db * 256u, (unsigned)(h * 64 + Tp) * 2048u + (unsigned)subp * 1024u, SC1);
        a.v[db][1] = __builtin_amdgcn_raw_buffer_load_b64(rV, vo + db * 256u, (unsigned)(h * 64 + T) * 2048u + (unsigned)sub * 1024u, SC1);
    }
}
__device__ __forceinline__ void attnB_compute(AttnBPre& a, char* ws, int h, int rb, int wid, int lane) {
    const int T = rb * 4 + (wid >> 1), sub = wid & 1, g = lane >> 4, ql = lane & 15;
    const bool po = (h & 1) && g == 0, pe = !(h & 1) && g == 3;
    patch(a.q, a.q, pe, po, 0u, 0u); patch(a.kp, a.kp, pe, po, 0u, 0u); patch(a.kc, a.kc, pe, po, 0u, 0u);
    f32x4 sp = {}, sc = {};
    sp = MFMA16(asb(a.kp), asb(a.q), sp); sc = MFMA16(asb(a.kc), asb(a.q), sc);
    const bool hasp = sub || T > 0;
    float m = -INFINITY;
    const int d0 = ql - 4 * g;
#pragma unroll
    for (int r = 0; r < 4; ++r) {
        const int dc = d0 - r; const bool ev = !(dc & 1);
        sc[r] = (ev && dc >= 0) ? sc[r] : -INFINITY; sp[r] = (ev && dc < 0 && hasp) ? sp[r] : -INFINITY;
        m = fmaxf(m, fmaxf(sp[r], sc[r]));
    }
    { auto r = __builtin_amdgcn_permlane16_swap(__float_as_uint(m), __float_as_uint(m), false, false); m = fmaxf(__uint_as_float(r[0]), __uint_as_float(r[1])); }
    m = xhalf_max(m);
    float e[8]; const float mq = -m * QSCALE;
#pragma unroll
    for (int r = 0; r < 4; ++r) { e[r] = __builtin_amdgcn_exp2f(fmaf(sp[r], QSCALE, mq)); e[4 + r] = __builtin_amdgcn_exp2f(fmaf(sc[r], QSCALE, mq)); }
    const u32x4 pf = {cvtpk(e[0], e[1]), cvtpk(e[2], e[3]), cvtpk(e[4], e[5]), cvtpk(e[6], e[7])};
    f32x4 o0 = {}, o1 = {};
    o0 = MFMA16(asb((u32x4){a.v[0][0][0], a.v[0][0][1], a.v[0][1][0], a.v[0][1][1]}), asb(pf), o0);
    o1 = MFMA16(asb((u32x4){a.v[1][0][0], a.v[1][0][1], a.v[1][1][0], a.v[1][1][1]}), asb(pf), o1);
    float l;
    { auto r16 = __builtin_amdgcn_permlane16_swap(__float_as_uint(o1[0]), __float_as_uint(o1[0]), false, false); auto r32 = __builtin_amdgcn_permlane32_swap(r16[1], r16[1], false, false); l = __uint_as_float(r32[1]); }
    const float inv = __builtin_amdgcn_rcpf(l);
    const auto rO = RSRC(ws + WS_OB, QC_BYTES);
    const unsigned tok = (unsigned)(sub * 16 + ql) * 16u;
    { const int col = 28 * h + 4 * g; bstore64((u32x2){cvtpk(o0[0] * inv, o0[1] * inv), cvtpk(o0[2] * inv, o0[3] * inv)}, rO, (unsigned)(T * 56 + (col >> 3)) * 512u + tok + (unsigned)((col >> 2) & 1) * 8u); }
    if (g < 3) { const int col = 28 * h + 16 + 4 * g; bstore64((u32x2){cvtpk(o1[0] * inv, o1[1] * inv), cvtpk(o1[2] * inv, o1[3] * inv)}, rO, (unsigned)(T * 56 + (col >> 3)) * 512u + tok + (unsigned)((col >> 2) & 1) * 8u); }
}

template <int N>
__device__ __forceinline__ void wo_issue(const u16* wo, int ct, unsigned lds_base, int pc0, int lane) {
    const int r8 = lane >> 3, dpos = lane & 7;
    const unsigned woff = (unsigned)r8 * 896u + (unsigned)((dpos ^ ((r8 & 2) << 1)) << 4);
#pragma unroll
    for (int i = 0; i < N; ++i) glds16((const char*)wo + (size_t)ct * 128 + (size_t)(pc0 + i) * 8 * 896, woff, lds_base + (pc0 + i) * 1024);
}
__device__ __forceinline__ void wf_read(u32x4 (&wf)[2][14], const char* img, int kh, int lane) {
    const int g16 = lane >> 4, q = (lane & 15) >> 2, p = lane & 3;
#pragma unroll
    for (int i = 0; i < 14; ++i)
#pragma unroll
        for (int c = 0; c < 2; ++c) {
            const int row = (kh * 14 + i) * 16 + 8 * (g16 >> 1) + q;
            lds_cptr wp = (lds_cptr)img + (unsigned)row * 128u + (unsigned)(((2 * c + (g16 & 1)) ^ (q & 2)) * 32) + (unsigned)p * 8u;
            wf[c][i] = __builtin_bit_cast(u32x4, cat8(vtr(wp), vtr(wp + 512)));
        }
}
__device__ __forceinline__ void attnA_phase(char* ws, const u16* woA, int oct, int h, int rb, u64 tag, int rt, int ct, bool has_qkv, bool first, const QkvOut& outB, u32x4 (&wf)[2][14], char* lds, int wid, int lane) {
    const bool hi = lane >= 32, pe = hi && !(h & 1), po = !hi && (h & 1);
    const int m0 = (28 * h) >> 3;
    const auto rK = RSRC(ws + WS_KA, QC_BYTES), rV = RSRC(ws + WS_VA, V_BYTES);
    const unsigned lo16 = (unsigned)lane * 16u;
    const unsigned kbase = (unsigned)((wid * 8) * 56 + m0) * 512u, vbase = (unsigned)(h * 64 + wid * 8) * 2048u;
    const int rot = rb & 7;
    u32x4 qf[4][2], kf[3][2], vf[3][2];
    {   const unsigned k0 = kbase + (unsigned)rot * TILE_B, v0 = vbase + (unsigned)rot * 2048u, k1 = kbase + (unsigned)((rot + 1) & 7) * TILE_B;
        kf[0][0] = bload128(rK, lo16, k0); kf[0][1] = bload128(rK, lo16, k0 + 1024u);
        if (wid >= 4) { const unsigned b = (unsigned)((rb * 4 + wid - 4) * 56 + m0) * 512u; const auto rQ = RSRC(ws + WS_QA, QC_BYTES); qf[0][0] = bload128(rQ, lo16, b); qf[0][1] = bload128(rQ, lo16, b + 1024u); }
        vf[0][0] = bload128(rV, lo16, v0); vf[0][1] = bload128(rV, lo16, v0 + 1024u);
        kf[1][0] = bload128(rK, lo16, k1); kf[1][1] = bload128(rK, lo16, k1 + 1024u);
        const unsigned v1 = vbase + (unsigned)((rot + 1) & 7) * 2048u, k2 = kbase + (unsigned)((rot + 2) & 7) * TILE_B;
        vf[1][0] = bload128(rV, lo16, v1); vf[1][1] = bload128(rV, lo16, v1 + 1024u);
        kf[2][0] = bload128(rK, lo16, k2); kf[2][1] = bload128(rK, lo16, k2 + 1024u); }
    patch(kf[0][0], kf[0][1], pe, po, 0x3F803F80u, 0u);
    if (wid >= 4) {
        patch(qf[0][0], qf[0][1], pe, po, 0u, 0u);
        f32x16 s = {}; s = MFMA32(asb(kf[0][0]), asb(qf[0][0]), s); s = MFMA32(asb(kf[0][1]), asb(qf[0][1]), s);
        const float m = xhalf_max(max16(s)) + POFF;
        const unsigned mh = cvtpk(-m, 0.f) & 0xffffu; const float r = -m - __uint_as_float(mh << 16);
        patch(qf[0][0], qf[0][1], pe, po, mh | (cvtpk(r, 0.f) << 16), 0u);
        *(u32x4*)(lds + LDS_Q + (wid - 4) * 2048 + lane * 16) = qf[0][0]; *(u32x4*)(lds + LDS_Q + (wid - 4) * 2048 + 1024 + lane * 16) = qf[0][1];
        asm volatile("s_waitcnt lgkmcnt(0)" ::: "memory");
        if (lane == 0) ctl_add(lds, CTL_QCNT);
    }
    ctl_wait(lds, CTL_QCNT, 4);
#pragma unroll
    for (int j = 0; j < 4; ++j) { qf[j][0] = *(const u32x4*)(lds + LDS_Q + j * 2048 + lane * 16); qf[j][1] = *(const u32x4*)(lds + LDS_Q + j * 2048 + 1024 + lane * 16); }
    if (has_qkv && wid < 4) {
        qkv_store<1, 0>(outB, ws, rt, ct, wid, lane); qkv_store<1, 1>(outB, ws, rt, ct, wid, lane); qkv_store<1, 2>(outB, ws, rt, ct, wid, lane);
        qkv_store<1, 3>(outB, ws, rt, ct, wid, lane); qkv_store<1, 4>(outB, ws, rt, ct, wid, lane); qkv_store<1, 5>(outB, ws, rt, ct, wid, lane);
    }
#pragma unroll
    for (int j = 0; j < 4; ++j) { PIN(qf[j][0]); PIN(qf[j][1]); }
    PIN(kf[0][0]); PIN(kf[0][1]);
    f32x16 o[4];
#define VEXP(d, s) asm volatile("v_exp_f32 %0, %1" : "=v"(d) : "v"(s))
#define VCVT(d, a, b) asm volatile("v_cvt_pk_bf16_f32 %0, %1, %2" : "=v"(d) : "v"(a), "v"(b))
#define MF0(d, a, b) asm volatile("v_mfma_f32_32x32x16_bf16 %0, %1, %2, 0" : "=&v"(d) : "v"(a), "v"(b))
#define MFA(d, a, b) asm volatile("v_mfma_f32_32x32x16_bf16 %0, %1, %2, %0" : "+v"(d) : "v"(a), "v"(b))
#define SLOT(PV, QK, Sc, Pc0, Pc1, Sn, kn0, kn1, qn0, qn1, Op, vp0, vp1, Pp0, Pp1, INJ) do { \
        float e0, e1, e2, e3, e4, e5, e6, e7, e8, e9, e10, e11, e12, e13, e14, e15; \
        if ((QK) != 0) MF0(Sn, kn0, qn0); \
        VEXP(e0, Sc[0]); VEXP(e1, Sc[1]); VEXP(e2, Sc[2]); VEXP(e3, Sc[3]); \
        if ((QK) != 0) MFA(Sn, kn1, qn1); \
        VEXP(e4, Sc[4]); VEXP(e5, Sc[5]); VEXP(e6, Sc[6]); VEXP(e7, Sc[7]); VCVT(Pc0[0], e0, e1); VCVT(Pc0[1], e2, e3); \
        INJ; \
        if ((PV) == 2) MFA(Op, vp0, Pp0); else if ((PV) == 1) MF0(Op, vp0, Pp0); \
        VEXP(e8, Sc[8]); VEXP(e9, Sc[9]); VEXP(e10, Sc[10]); VEXP(e11, Sc[11]); VCVT(Pc0[2], e4, e5); VCVT(Pc0[3], e6, e7); \
        if ((PV) != 0) MFA(Op, vp1, Pp1); \
        VEXP(e12, Sc[12]); VEXP(e13, Sc[13]); VEXP(e14, Sc[14]); VEXP(e15, Sc[15]); VCVT(Pc1[0], e8, e9); VCVT(Pc1[1], e10, e11); VCVT(Pc1[2], e12, e13); VCVT(Pc1[3], e14, e15); \
    } while (0)
    f32x16 sA, sB; u32x4 pA0, pA1, pB0, pB1;
    asm volatile("s_nop 1\n\tv_mfma_f32_32x32x16_bf16 %0, %1, %2, 0\n\tv_mfma_f32_32x32x16_bf16 %0, %3, %4, %0\n\ts_nop 7\n\ts_nop 3" : "=&v"(sA) : "v"(kf[0][0]), "v"(qf[0][0]), "v"(kf[0][1]), "v"(qf[0][1]));
#define PATCHK(kx) do { PIN(kx[0]); PIN(kx[1]); patch(kx[0], kx[1], pe, po, 0x3F803F80u, 0u); PIN(kx[0]); PIN(kx[1]); } while (0)
#define LOADK(kc, tt) do { if ((tt) < 8) { const unsigned o_ = kbase + (unsigned)(((tt) + rot) & 7) * TILE_B; kc[0] = bload128(rK, lo16, o_); kc[1] = bload128(rK, lo16, o_ + 1024u); } } while (0)
#define LOADV(vx, tt) do { if ((tt) < 8) { const unsigned o_ = vbase + (unsigned)(((tt) + rot) & 7) * 2048u; vx[0] = bload128(rV, lo16, o_); vx[1] = bload128(rV, lo16, o_ + 1024u); } } while (0)
#define TILE(t) do { \
        SLOT((t) == 0 ? 0 : (t) == 1 ? 1 : 2, true, sA, pA0, pA1, sB, kf[(t) % 3][0], kf[(t) % 3][1], qf[1][0], qf[1][1], o[3], vf[((t) + 2) % 3][0], vf[((t) + 2) % 3][1], pB0, pB1, (void)0); \
        LOADV(vf[((t) + 2) % 3], (t) + 2); \
        SLOT((t) == 0 ? 1 : 2, true, sB, pB0, pB1, sA, kf[(t) % 3][0], kf[(t) % 3][1], qf[2][0], qf[2][1], o[0], vf[(t) % 3][0], vf[(t) % 3][1], pA0, pA1, (void)0); \
        SLOT((t) == 0 ? 1 : 2, true, sA, pA0, pA1, sB, kf[(t) % 3][0], kf[(t) % 3][1], qf[3][0], qf[3][1], o[1], vf[(t) % 3][0], vf[(t) % 3][1], pB0, pB1, if ((t) < 7) PATCHK(kf[((t) + 1) % 3])); \
        LOADK(kf[(t) % 3], (t) + 3); \
        SLOT((t) == 0 ? 1 : 2, (t) < 7, sB, pB0, pB1, sA, kf[((t) + 1) % 3][0], kf[((t) + 1) % 3][1], qf[0][0], qf[0][1], o[2], vf[(t) % 3][0], vf[(t) % 3][1], pA0, pA1, (void)0); \
    } while (0)
    AttnBPre bpre; u64 flb = 0; bool bready = false;
#pragma unroll
    for (int t = 0; t < 8; t += 2) {
        if (t == 0) __builtin_amdgcn_s_setprio(3); else if (t == 2) __builtin_amdgcn_s_setprio(2); else if (t == 4) __builtin_amdgcn_s_setprio(1); else __builtin_amdgcn_s_setprio(0);
        TILE(t);
        if (t == 4) { bready = __all(flb == tag); attnB_load(bpre, ws, h, rb, wid, opaque_lane()); }
        TILE(t + 1);
        if (t == 0) {
            if (has_qkv && wid < 4) {
                asm volatile("s_waitcnt vmcnt(8)" ::: "memory");
                if (opaque_lane() == 0 && ctl_add(lds, CTL_F2CNT) == 3) flag_set(ws, F2 + ct * 32 + rt, tag);
            }
            if (oct < 7 && wid >= 4) {
                if (has_qkv) ctl_wait(lds, CTL_RDONE, 4);
                wo_issue<14>(woA, oct, (unsigned)(uintptr_t)lds, (wid - 4) * 14, opaque_lane());
            }
        } else if (t == 2) {
            const int ln = opaque_lane();
            if (oct < 7 && wid >= 4) { asm volatile("s_waitcnt vmcnt(8)" ::: "memory"); if (ln == 0) ctl_add(lds, CTL_WA); }
            flb = __hip_atomic_load(flagp(ws, f2_index(h, rb, ln)), __ATOMIC_RELAXED, __HIP_MEMORY_SCOPE_AGENT);
            if (first && wid == 1) { flag_wait(ws, F0 + (ln & 31), tag); if (ln == 0) ctl_store(lds, CTL_F0, 1); }
        }
    }
    asm volatile("s_nop 1\n\tv_mfma_f32_32x32x16_bf16 %0, %1, %2, %0\n\tv_mfma_f32_32x32x16_bf16 %0, %3, %4, %0\n\ts_nop 7\n\ts_nop 3" : "+v"(o[3]) : "v"(vf[1][0]), "v"(pB0), "v"(vf[1][1]), "v"(pB1));
    asm volatile("" : "+v"(o[0]), "+v"(o[1]), "+v"(o[2]));
    lane = opaque_lane();
    if (has_qkv) ctl_wait(lds, CTL_RDONE, 4);
#pragma unroll
    for (int j = 0; j < 4; ++j) merge_write1(o[j], lds, wid, j, lane);
    if (!bready) { flag_wait(ws, f2_index(h, rb, lane), tag); attnB_load(bpre, ws, h, rb, wid, lane); }
    PIN(bpre.q); PIN(bpre.kp); PIN(bpre.kc); PIN(bpre.v[0][0]); PIN(bpre.v[0][1]); PIN(bpre.v[1][0]); PIN(bpre.v[1][1]);
    if (oct < 7 && wid < 4) { ctl_wait(lds, CTL_WA, 4); wf_read(wf, lds, wid & 1, lane); }
    BARRIER();
    const bool bad = merge_reduce(ws, h, rb, lds, wid, lane);
    if (__any(bad) && lane == 0) ctl_store(lds, CTL_BAD, 1);
    attnB_compute(bpre, ws, h, rb, wid, lane);
    asm volatile("s_waitcnt vmcnt(2) lgkmcnt(0)" ::: "memory");
    if (lane == 0 && ctl_add(lds, CTL_F3CNT) == 7 && !ctl_load(lds, CTL_BAD)) flag_set(ws, F3 + rb * 16 + h, tag);
    asm volatile("s_waitcnt vmcnt(0)" ::: "memory");
    if (lane == 0 && ctl_add(lds, CTL_F4CNT) == 7) flag_set(ws, F4 + rb * 16 + h, tag);
    ctl_wait(lds, CTL_F3CNT, 8);
    if (__builtin_amdgcn_readfirstlane(ctl_load(lds, CTL_BAD))) {
        BARRIER();
        attnA_safe(ws, h, rb, lds, wid, lane);
        BARRIER();
        merge_reduce(ws, h, rb, lds, wid, lane);
        DRAIN_BARRIER();
        if (threadIdx.x == 0) flag_set(ws, F3 + rb * 16 + h, tag);
    }
}

__device__ __forceinline__ void oproj_compute(char* ws, const u16* woB, u16* out, int rt, int ct, u64 tag, bool first, u32x4 (&wf)[2][14], char* lds, int wid, int lane) {
    const int br = wid >> 2, tt = (wid >> 1) & 1, kh = wid & 1;
    if (wid < 3) {
        if (wid < 2) wo_issue<19>(woB, ct, (unsigned)(uintptr_t)lds + LDS_MRG, wid * 19, lane); else wo_issue<18>(woB, ct, (unsigned)(uintptr_t)lds + LDS_MRG, 38, lane);
    }
    if ((wid & 3) == 3) {
        flag_wait(ws, (br ? F4 : F3) + (rt >> 1) * 16 + (lane & 15), tag); if (lane == 0) ctl_store(lds, br ? CTL_F4 : CTL_F3, 1);
        if (first && !br) { ctl_wait(lds, CTL_F0, 1); if (lane == 0) flag_set(ws, FEP, tag); }
    }
    else {
        if (wid < 3) { asm volatile("s_waitcnt vmcnt(0)" ::: "memory"); if (lane == 0) ctl_add(lds, CTL_WB); }
        ctl_wait(lds, br ? CTL_F4 : CTL_F3, 1);
    }
    const auto rO = RSRC(ws + (br ? WS_OB : WS_OA), QC_BYTES);
    const int tile = rt * 2 + tt;
    u32x4 of[14];
#pragma unroll
    for (int i = 0; i < 14; ++i) of[i] = bload128(rO, (unsigned)lane * 16u, (unsigned)(tile * 56 + 2 * (kh * 14 + i)) * 512u);
    if (br) { ctl_wait(lds, CTL_WB, 3); wf_read(wf, lds + LDS_MRG, kh, lane); }
    f32x16 acc[2] = {};
#pragma unroll
    for (int i = 0; i < 14; ++i) { acc[0] = MFMA32(asb(wf[0][i]), asb(of[i]), acc[0]); acc[1] = MFMA32(asb(wf[1][i]), asb(of[i]), acc[1]); }
    asm volatile("" : "+v"(acc[0]), "+v"(acc[1]));
    {   const f32x16& gv = kh ? acc[0] : acc[1];
#pragma unroll
        for (int rq = 0; rq < 4; ++rq) *(f32x4*)(lds + 114688 + (wid * 4 + rq) * 1024 + lane * 16) = (f32x4){gv[4 * rq], gv[4 * rq + 1], gv[4 * rq + 2], gv[4 * rq + 3]};
    }
    asm volatile("s_waitcnt lgkmcnt(0)" ::: "memory");
    if (lane == 0) ctl_store(lds, CTL_X + wid * 4, 1);
    ctl_wait(lds, CTL_X + (wid ^ 1) * 4, 1);
    f32x16 r = kh ? acc[1] : acc[0];
#pragma unroll
    for (int rq = 0; rq < 4; ++rq) { const f32x4 x = *(const f32x4*)(lds + 114688 + ((wid ^ 1) * 4 + rq) * 1024 + lane * 16); r[4 * rq] += x[0]; r[4 * rq + 1] += x[1]; r[4 * rq + 2] += x[2]; r[4 * rq + 3] += x[3]; }
    const unsigned orow = (unsigned)(rt * 64 + tt * 32 + (lane & 31)) * 1792u + (unsigned)(br * 448 + ct * 64 + kh * 32 + (lane >> 5) * 8) * 2u;
#pragma unroll
    for (int pr = 0; pr < 2; ++pr) {
        const unsigned a0 = cvtpk(r[8 * pr], r[8 * pr + 1]), a1 = cvtpk(r[8 * pr + 2], r[8 * pr + 3]), b0 = cvtpk(r[8 * pr + 4], r[8 * pr + 5]), b1 = cvtpk(r[8 * pr + 6], r[8 * pr + 7]);
        auto r0 = __builtin_amdgcn_permlane32_swap(a0, b0, false, false);
        auto r1 = __builtin_amdgcn_permlane32_swap(a1, b1, false, false);
        bstore128((u32x4){r0[0], r1[0], r0[1], r1[1]}, RSRC(out, 2048 * 896 * 2), orow + pr * 32u);
    }
}

__global__ void __launch_bounds__(512, 2) fused_kernel(const u16* xA, const u16* wA, const u16* woA, const u16* xB, const u16* wB, const u16* woB, u16* out, char* ws) {
    extern __shared__ __attribute__((aligned(16))) char lds[];
    int lane = threadIdx.x & 63; const int wid = __builtin_amdgcn_readfirstlane(threadIdx.x >> 6);
    const int id = blockIdx.x, xcd = id & 7, loc = id >> 3;
    if (threadIdx.x < 32) ctl_store(lds, threadIdx.x * 4, 0);
    const u64 tag = __hip_atomic_load(flagp(ws, FEP), __ATOMIC_RELAXED, __HIP_MEMORY_SCOPE_AGENT) + 1;
    const int gb = xcd & 1, grt = 8 * (xcd >> 1) + (loc & 7), qct = gb * 4 + (loc >> 3);
    const bool has_qkv = qct < 7;
    const int h = xcd * 2 + (loc & 1), rb = loc >> 1;
    const int ort = 4 * xcd + (loc & 3), oct = loc >> 2;
    unsigned warm0 = 0;
    if (wid < 4) {
        u64 pc; int len;
        asm volatile("s_getpc_b64 %0\n.Lfused_pc_ref:\n\ts_mov_b32 %1, .Lfused_code_end - .Lfused_pc_ref" : "=s"(pc), "=s"(len));
        const int off = (wid * 64 + lane) * 128;
        if (off < len) warm0 = *(const __attribute__((address_space(1))) unsigned*)(pc + off);
        warm0 ^= __builtin_amdgcn_raw_buffer_load_b32(RSRC(ws, WS_FLAGS), (unsigned)(wid * 64 + lane) * 65536u, 0, SC1);
        if (wid == 0) warm0 ^= __builtin_amdgcn_raw_buffer_load_b32(RSRC(out, 2048 * 896 * 2), (unsigned)lane * 65536u, 0, SC1);
        if (wid == 1) { warm0 ^= __builtin_amdgcn_raw_buffer_load_b32(RSRC(woA, 448 * 448 * 2), (unsigned)lane * 65536u, 0, 0); warm0 ^= __builtin_amdgcn_raw_buffer_load_b32(RSRC(woB, 448 * 448 * 2), (unsigned)lane * 65536u, 0, 0); }
        if (wid == 2) { warm0 ^= __builtin_amdgcn_raw_buffer_load_b32(RSRC(xB, 2048 * 448 * 2), (unsigned)lane * 65536u, 0, 0); warm0 ^= __builtin_amdgcn_raw_buffer_load_b32(RSRC(wB, 448 * 1344 * 2), (unsigned)lane * 65536u, 0, 0); }
    }
    QkvOut outB;
    if (has_qkv) qkv_phase(xA, wA, xB, wB, ws, grt, qct, tag, lds, wid, lane, outB);
    else { if (threadIdx.x == 0) flag_set(ws, F0 + (xcd >> 1) * 8 + (loc & 7), tag); BARRIER(); }
    asm volatile("" :: "v"(warm0));
    if (wid == 4) {
        flag_wait(ws, F1 + (lane < 32 ? (28 * h) >> 6 : (28 * h + 27) >> 6) * 32 + (lane & 31), tag);
        if (lane == 0) ctl_store(lds, CTL_F1, 1);
    } else ctl_wait(lds, CTL_F1, 1);
    const u64 tags = (u64)(unsigned)__builtin_amdgcn_readfirstlane((unsigned)tag) | ((u64)(unsigned)__builtin_amdgcn_readfirstlane((unsigned)(tag >> 32)) << 32);
    u32x4 wf[2][14];
    attnA_phase(ws, woA, oct, h, rb, tags, grt, qct, has_qkv, id == 0, outB, wf, lds, wid, lane);
    lane = opaque_lane();
    if (oct < 7) {
        oproj_compute(ws, woB, out, ort, oct, tags, id == 0, wf, lds, wid, lane);
    }
    asm volatile(".Lfused_code_end:");
}

extern "C" void kernel_launch(void* const* d_in, const int* in_sizes, int n_in, void* d_out, int out_size, void* d_ws, size_t ws_size, hipStream_t stream) {
    const u16* xA = (const u16*)d_in[0]; const u16* wA = (const u16*)d_in[1]; const u16* woA = (const u16*)d_in[2];
    const u16* xB = (const u16*)d_in[3]; const u16* wB = (const u16*)d_in[4]; const u16* woB = (const u16*)d_in[5];
    (void)hipFuncSetAttribute((const void*)fused_kernel, hipFuncAttributeMaxDynamicSharedMemorySize, LDS_TOTAL);
    fused_kernel<<<256, 512, LDS_TOTAL, stream>>>(xA, wA, woA, xB, wB, woB, (u16*)d_out, (char*)d_ws);
}
```

```cpp
#include <hip/hip_runtime.h>
#include <hip/hip_bf16.h>
#include <stdint.h>

typedef unsigned short u16;
typedef unsigned long long u64;
typedef short bf16x8 __attribute__((ext_vector_type(8)));
typedef short s16x4 __attribute__((ext_vector_type(4)));
typedef float f32x4 __attribute__((ext_vector_type(4)));
typedef float f32x16 __attribute__((ext_vector_type(16)));
typedef unsigned u32x4 __attribute__((ext_vector_type(4)));
typedef unsigned u32x2 __attribute__((ext_vector_type(2)));
typedef __attribute__((address_space(3))) const char* lds_cptr;
typedef __attribute__((address_space(1))) u64 gu64;

constexpr int TILE_B = 56 * 512;
constexpr size_t QC_BYTES = (size_t)64 * TILE_B;
constexpr size_t V_BYTES = (size_t)16 * 64 * 2048;
constexpr size_t WS_QA = 0, WS_KA = WS_QA + QC_BYTES, WS_VA = WS_KA + QC_BYTES;
constexpr size_t WS_QB = WS_VA + V_BYTES, WS_KB = WS_QB + QC_BYTES, WS_VB = WS_KB + QC_BYTES;
constexpr size_t WS_OA = WS_VB + V_BYTES, WS_OB = WS_OA + QC_BYTES, WS_FLAGS = WS_OB + QC_BYTES;
constexpr int F1 = 0, F2 = 256, F3 = 512, F4 = 768, F0 = 1024, FEP = 1056;
constexpr float QSCALE = 0.18898223650461362f * 1.4426950408889634f;
constexpr int STG = 32768;
constexpr int LDS_Q = 147456, LDS_L = 131072, LDS_MRG = 57344, LDS_CTL = LDS_Q + 8192, LDS_MX = LDS_CTL + 128, LDS_TOTAL = LDS_MX + 1024 + 64;
constexpr int CTL_F1 = 0, CTL_F2CNT = 4, CTL_BAD = 8, CTL_QCNT = 12, CTL_F4CNT = 16, CTL_F3 = 20, CTL_RDONE = 24, CTL_F4 = 28, CTL_X = 32;
constexpr int CTL_WA = 64, CTL_WB = 68, CTL_S12 = 72, CTL_S13 = 76, CTL_F3CNT = 80, CTL_F0 = 84;
constexpr int PUBG = 10;
#define POFF 96.0f
#define LMIN (__builtin_ldexpf(1.0f, -(int)(POFF) - 20))
constexpr unsigned SPIN_MAX = 1u << 18;

#define MFMA16(a, b, c) __builtin_amdgcn_mfma_f32_16x16x32_bf16(a, b, c, 0, 0, 0)
#define MFMA32(a, b, c) __builtin_amdgcn_mfma_f32_32x32x16_bf16(a, b, c, 0, 0, 0)
#define PIN(x) asm volatile("" : "+v"(x))
#define SBAR() __builtin_amdgcn_sched_barrier(0)
#define SC1 16

__device__ __forceinline__ unsigned cvtpk(float lo, float hi) {
    typedef float f2 __attribute__((ext_vector_type(2))); typedef __bf16 b2 __attribute__((ext_vector_type(2)));
    f2 v = {lo, hi}; b2 r = __builtin_convertvector(v, b2); return __builtin_bit_cast(unsigned, r);
}
__device__ __forceinline__ void glds16(const void* sbase, unsigned voff, unsigned lds_base) {
    asm volatile("s_mov_b32 m0, %2\n\ts_nop 0\n\tglobal_load_lds_dwordx4 %0, %1" :: "v"(voff), "s"(sbase), "s"(lds_base) : "memory");
}
__device__ __forceinline__ s16x4 vtr(lds_cptr p) {
    typedef short v4i16_t __attribute__((ext_vector_type(4)));
    return __builtin_bit_cast(s16x4, __builtin_amdgcn_ds_read_tr16_b64_v4i16((__attribute__((address_space(3))) v4i16_t*)p));
}
__device__ __forceinline__ bf16x8 cat8(s16x4 a, s16x4 b) { return (bf16x8){a[0], a[1], a[2], a[3], b[0], b[1], b[2], b[3]}; }
__device__ __forceinline__ bf16x8 ldsrd128(lds_cptr p) { return *(const __attribute__((address_space(3))) bf16x8*)p; }
#define RSRC(p, bytes) __builtin_amdgcn_make_buffer_rsrc((void*)(p), 0, (int)(bytes), 0x00020000)
__device__ __forceinline__ u32x4 bload128(__amdgpu_buffer_rsrc_t r, unsigned voff, unsigned soff) { return __builtin_amdgcn_raw_buffer_load_b128(r, voff, soff, SC1); }
__device__ __forceinline__ void bstore128(u32x4 v, __amdgpu_buffer_rsrc_t r, unsigned voff) { __builtin_amdgcn_raw_buffer_store_b128(v, r, voff, 0, SC1); }
__device__ __forceinline__ void bstore64(u32x2 v, __amdgpu_buffer_rsrc_t r, unsigned voff) { __builtin_amdgcn_raw_buffer_store_b64(v, r, voff, 0, SC1); }
__device__ __forceinline__ bf16x8 asb(u32x4 v) { return __builtin_bit_cast(bf16x8, v); }

__device__ __forceinline__ gu64* flagp(char* ws, int idx) { return (gu64*)(ws + WS_FLAGS + (size_t)idx * 256); }
__device__ __forceinline__ void flag_set(char* ws, int idx, u64 tag) { __hip_atomic_store(flagp(ws, idx), tag, __ATOMIC_RELAXED, __HIP_MEMORY_SCOPE_AGENT); }
__device__ __forceinline__ u64 next_tag(u64 epoch) { asm volatile("" : "+v"(epoch)); return epoch + 1; }
__device__ __forceinline__ void flag_wait(char* ws, int idx, u64 tag) {
    gu64* p = flagp(ws, idx);
    for (unsigned spins = 0; spins < SPIN_MAX; ++spins) {
        const u64 v = __hip_atomic_load(p, __ATOMIC_RELAXED, __HIP_MEMORY_SCOPE_AGENT);
        if (__all(v == tag)) break;
        __builtin_amdgcn_s_sleep(1);
    }
}
__device__ __forceinline__ int opaque_lane() { int l; asm volatile("v_mbcnt_lo_u32_b32 %0, -1, 0\n\tv_mbcnt_hi_u32_b32 %0, -1, %0" : "=v"(l)); return l; }
__device__ __forceinline__ int f2_index(int h, int rb, int lane) { const int i = lane % 6, r = 2 * rb - 1 + i % 3; return F2 + (i < 3 ? (28 * h) >> 6 : (28 * h + 27) >> 6) * 32 + (r < 0 ? 0 : r); }
typedef __attribute__((address_space(3))) int* lds_iptr;
__device__ __forceinline__ int ctl_load(char* lds, int off) { return __hip_atomic_load((lds_iptr)(lds + LDS_CTL + off), __ATOMIC_RELAXED, __HIP_MEMORY_SCOPE_WORKGROUP); }
__device__ __forceinline__ void ctl_store(char* lds, int off, int v) { __hip_atomic_store((lds_iptr)(lds + LDS_CTL + off), v, __ATOMIC_RELAXED, __HIP_MEMORY_SCOPE_WORKGROUP); }
__device__ __forceinline__ void ctl_wait(char* lds, int off, int n) {
    for (unsigned spins = 0; spins < (SPIN_MAX << 6); ++spins) { if (__builtin_amdgcn_readfirstlane(ctl_load(lds, off)) >= n) break; __builtin_amdgcn_s_sleep(1); }
    asm volatile("" ::: "memory");
}
__device__ __forceinline__ int ctl_add(char* lds, int off) { return __hip_atomic_fetch_add((lds_iptr)(lds + LDS_CTL + off), 1, __ATOMIC_RELAXED, __HIP_MEMORY_SCOPE_WORKGROUP); }
#define BARRIER() asm volatile("s_waitcnt lgkmcnt(0)\n\ts_barrier" ::: "memory")
#define DRAIN_BARRIER() asm volatile("s_waitcnt vmcnt(0) lgkmcnt(0)\n\ts_barrier" ::: "memory")

struct QkvFrag { bf16x8 x[2][4], w[2][3]; };
struct QkvAcc { f32x4 q[4], k[4], v[4]; };
struct QkvOut { u32x4 q[2], k[2], v[2]; };
__device__ __forceinline__ void qkv_pack(QkvOut& o, const QkvAcc& a, float qs) {
#pragma unroll
    for (int tb = 0; tb < 2; ++tb) {
        const f32x4 q0 = a.q[2 * tb] * qs, q1 = a.q[2 * tb + 1] * qs, k0 = a.k[2 * tb], k1 = a.k[2 * tb + 1], v0 = a.v[2 * tb], v1 = a.v[2 * tb + 1];
        { auto ra = __builtin_amdgcn_permlane16_swap(cvtpk(q0[0], q0[1]), cvtpk(q1[0], q1[1]), false, false); auto rb = __builtin_amdgcn_permlane16_swap(cvtpk(q0[2], q0[3]), cvtpk(q1[2], q1[3]), false, false);
          o.q[tb] = (u32x4){ra[0], rb[0], ra[1], rb[1]}; }
        { auto ra = __builtin_amdgcn_permlane16_swap(cvtpk(k0[0], k0[1]), cvtpk(k1[0], k1[1]), false, false); auto rb = __builtin_amdgcn_permlane16_swap(cvtpk(k0[2], k0[3]), cvtpk(k1[2], k1[3]), false, false);
          o.k[tb] = (u32x4){ra[0], rb[0], ra[1], rb[1]}; }
        { auto ra = __builtin_amdgcn_permlane32_swap(cvtpk(v0[0], v0[1]), cvtpk(v1[0], v1[1]), false, false); auto rb = __builtin_amdgcn_permlane32_swap(cvtpk(v0[2], v0[3]), cvtpk(v1[2], v1[3]), false, false);
          o.v[tb] = (u32x4){ra[0], rb[0], ra[1], rb[1]}; }
    }
}
template <int BR, int I>
__device__ __forceinline__ void qkv_store(const QkvOut& o, char* ws, int rt, int ct, int c, int lane) {
    constexpr int tb = I / 3, kind = I % 3;
    const int tile = rt * 2 + tb;
    if (kind < 2) {
        const unsigned off = (unsigned)(tile * 56 + 8 * ct + 2 * c) * 512u + (unsigned)lane * 16u;
        if (kind == 0) bstore128(o.q[tb], RSRC(ws + (BR ? WS_QB : WS_QA), QC_BYTES), off); else bstore128(o.k[tb], RSRC(ws + (BR ? WS_KB : WS_KA), QC_BYTES), off);
    } else {
        const int n = 64 * ct + 16 * c + (lane & 15), h = n / 28, d = n - 28 * h;
        const unsigned off = (unsigned)(h * 64 + tile) * 2048u + (unsigned)(lane >> 4) * 512u + (unsigned)d * 16u;
        const auto rs = RSRC(ws + (BR ? WS_VB : WS_VA), V_BYTES);
        bstore128(o.v[tb], rs, off);
        if (d >= 24) { const unsigned pad = d == 24 ? 0x3F803F80u : 0u; bstore128((u32x4){pad, pad, pad, pad}, rs, off + 64u); }
    }
}

__device__ __forceinline__ void qkv_phase(const u16* xA, const u16* wA, const u16* xB, const u16* wB, char* ws, int rt, int ct, u64 epoch, char* lds, int wid, int lane, QkvOut& outB) {
    if (wid >= 4) {
        const unsigned lds0 = (unsigned)(uintptr_t)lds;
        const int lw = wid - 4, r8 = lane >> 3, dpos = lane & 7;
        unsigned xoff[2], woff[2];
#pragma unroll
        for (int i = 0; i < 2; ++i) { const int row = 16 * lw + 8 * i + r8;
            xoff[i] = (unsigned)row * 896u + (unsigned)((dpos ^ ((row >> 1) & 7)) << 4);
            const int f2 = ((row >> 1) & 1) | (((row >> 3) & 1) << 1);
            woff[i] = (unsigned)row * 2688u + (unsigned)((((dpos >> 1) ^ f2) * 2 + (dpos & 1)) << 4); }
        const int rot = __builtin_amdgcn_readfirstlane(((rt & 7) + 2 * (ct & 3)) % 7);
        const char* xa = (const char*)xA + (size_t)rt * 64 * 896; const char* wa = (const char*)wA + (size_t)ct * 128;
        const char* xb = (const char*)xB + (size_t)rt * 64 * 896; const char* wb = (const char*)wB + (size_t)ct * 128;
#define QKV_ISSUE(g) do { int st_ = ((g) % 7) + rot; st_ = st_ >= 7 ? st_ - 7 : st_; const char* x_ = ((g) < 7 ? xa : xb) + st_ * 128; const char* w_ = ((g) < 7 ? wa : wb) + (size_t)st_ * (64 * 2688); \
        const unsigned sl_ = lds0 + ((g) & 3) * STG + lw * 2048; \
        glds16(x_, xoff[0], sl_); glds16(x_, xoff[1], sl_ + 1024); \
        glds16(w_, woff[0], sl_ + 8192); glds16(w_, woff[1], sl_ + 8192 + 1024); glds16(w_ + 896, woff[0], sl_ + 16384); glds16(w_ + 896, woff[1], sl_ + 16384 + 1024); \
        glds16(w_ + 1792, woff[0], sl_ + 24576); glds16(w_ + 1792, woff[1], sl_ + 24576 + 1024); } while (0)
        QKV_ISSUE(0); QKV_ISSUE(1);
#pragma unroll
        for (int g = 0; g < 12; ++g) {
            asm volatile("s_waitcnt vmcnt(8) lgkmcnt(0)\n\ts_barrier" ::: "memory");
            QKV_ISSUE(g + 2);
        }
        asm volatile("s_waitcnt vmcnt(8)" ::: "memory");
        if (lane == 0) ctl_add(lds, CTL_S12);
        asm volatile("s_waitcnt vmcnt(0)" ::: "memory");
        if (lane == 0) ctl_add(lds, CTL_S13);
        return;
    }
    const int c = wid, g4 = lane >> 4, t16 = lane & 15;
    const unsigned xr0 = (unsigned)t16 * 128u + (unsigned)((g4 ^ (t16 >> 1)) << 4);
    const int q = t16 >> 2, p = lane & 3;
    const int rf2 = ((q >> 1) & 1) | ((g4 & 1) << 1);
    const unsigned wr0 = 8192u + (unsigned)(8 * g4 + q) * 128u + (unsigned)((c ^ rf2) * 32) + (unsigned)p * 8u;
    QkvAcc acc; QkvOut outA;
#pragma unroll
    for (int i = 0; i < 4; ++i) acc.q[i] = acc.k[i] = acc.v[i] = (f32x4){};
    QkvFrag fr[2];
#define QMF(i) do { if (g > 0) { constexpr int ks_ = (i) / 12, s_ = ((i) % 12) / 4, b_ = (i) % 4; const QkvFrag& f_ = fr[(g - 1) & 1]; \
        if (s_ == 0) acc.q[b_] = MFMA16(f_.w[ks_][0], f_.x[ks_][b_], acc.q[b_]); if (s_ == 1) acc.k[b_] = MFMA16(f_.w[ks_][1], f_.x[ks_][b_], acc.k[b_]); \
        if (s_ == 2) acc.v[b_] = MFMA16(f_.x[ks_][b_], f_.w[ks_][2], acc.v[b_]); } SBAR(); } while (0)
#define QRX(ks, b) do { if (g < 14) fr[g & 1].x[ks][b] = ldsrd128(sl + (xr0 ^ ((ks) * 64)) + (b) * 2048); } while (0)
#define QRW(ks, s) do { if (g < 14) { lds_cptr wp_ = sl + wr0 + (ks) * 4096 + (s) * 8192; fr[g & 1].w[ks][s] = cat8(vtr(wp_), vtr(wp_ + 512)); } } while (0)
#pragma unroll
    for (int g = 0; g < 15; ++g) {
        if (g == PUBG) asm volatile("s_waitcnt vmcnt(0) lgkmcnt(0)\n\ts_barrier" ::: "memory");
        else if (g < 12) asm volatile("s_waitcnt lgkmcnt(0)\n\ts_barrier" ::: "memory");
        else if (g == 12) ctl_wait(lds, CTL_S12, 4);
        else if (g == 13) ctl_wait(lds, CTL_S13, 4);
        SBAR();
        lds_cptr sl = (lds_cptr)lds + (g & 3) * STG;
        if (g == PUBG) { const u64 tag = next_tag(epoch); if (threadIdx.x == 0) flag_set(ws, F1 + ct * 32 + rt, tag); }
        SBAR();
        QRX(0, 0); QMF(0); QRX(0, 1); QMF(1); QRX(0, 2); QMF(2); QRX(0, 3); QMF(3); QRW(0, 0); QMF(4); QRW(0, 1); QMF(5); QRW(0, 2); QMF(6);
        QRX(1, 0); QMF(7); QRX(1, 1); QMF(8); QRX(1, 2); QMF(9); QRX(1, 3); QMF(10); QRW(1, 0); QMF(11); QRW(1, 1); QMF(12); QRW(1, 2); QMF(13);
        QMF(14); QMF(15); QMF(16); QMF(17); QMF(18); QMF(19); QMF(20); QMF(21); QMF(22); QMF(23);
        if (g == 7) {
            qkv_pack(outA, acc, QSCALE);
            qkv_store<0, 1>(outA, ws, rt, ct, c, lane); qkv_store<0, 2>(outA, ws, rt, ct, c, lane); qkv_store<0, 4>(outA, ws, rt, ct, c, lane);
            qkv_store<0, 5>(outA, ws, rt, ct, c, lane); qkv_store<0, 0>(outA, ws, rt, ct, c, lane); qkv_store<0, 3>(outA, ws, rt, ct, c, lane);
#pragma unroll
            for (int i = 0; i < 4; ++i) acc.q[i] = acc.k[i] = acc.v[i] = (f32x4){};
        }
    }
    if (lane == 0) ctl_add(lds, CTL_RDONE);
    qkv_pack(outB, acc, 1.0f);
}

__device__ __forceinline__ float max16(const f32x16& s) {
    float a = fmaxf(fmaxf(s[0], s[1]), s[2]), b = fmaxf(fmaxf(s[3], s[4]), s[5]);
    a = fmaxf(fmaxf(a, s[6]), s[7]); b = fmaxf(fmaxf(b, s[8]), s[9]); a = fmaxf(fmaxf(a, s[10]), s[11]); b = fmaxf(fmaxf(b, s[12]), s[13]);
    a = fmaxf(fmaxf(a, s[14]), s[15]); return fmaxf(a, b);
}
__device__ __forceinline__ float xhalf_max(float m) {
    auto r = __builtin_amdgcn_permlane32_swap(__float_as_uint(m), __float_as_uint(m), false, false);
    return fmaxf(__uint_as_float(r[0]), __uint_as_float(r[1]));
}
__device__ __forceinline__ void patch(u32x4& f0, u32x4& f1, bool pe, bool po, unsigned a, unsigned b) {
    f0[0] = po ? a : f0[0]; f0[1] = po ? b : f0[1]; f1[2] = pe ? a : f1[2]; f1[3] = pe ? b : f1[3];
}
__device__ __forceinline__ void exp_pack(const f32x16& s, u32x4& p0, u32x4& p1) {
    float e[16];
#pragma unroll
    for (int r = 0; r < 16; ++r) e[r] = __builtin_amdgcn_exp2f(s[r]);
    p0 = (u32x4){cvtpk(e[0], e[1]), cvtpk(e[2], e[3]), cvtpk(e[4], e[5]), cvtpk(e[6], e[7])};
    p1 = (u32x4){cvtpk(e[8], e[9]), cvtpk(e[10], e[11]), cvtpk(e[12], e[13]), cvtpk(e[14], e[15])};
}
__device__ __forceinline__ void merge_write1(const f32x16& o, char* lds, int wid, int j, int lane) {
#pragma unroll
    for (int hf = 0; hf < 2; ++hf)
        *(u32x4*)(lds + LDS_MRG + ((wid * 4 + j) * 2 + hf) * 1024 + lane * 16) = (u32x4){cvtpk(o[8 * hf], o[8 * hf + 1]), cvtpk(o[8 * hf + 2], o[8 * hf + 3]), cvtpk(o[8 * hf + 4], o[8 * hf + 5]), cvtpk(o[8 * hf + 6], o[8 * hf + 7])};
    if (lane >= 32) *(float*)(lds + LDS_L + ((wid * 4 + j) * 32 + lane - 32) * 4) = o[12];
}
__device__ __forceinline__ bool merge_reduce(char* ws, int h, int rb, char* lds, int wid, int lane) {
    const bool hi = lane >= 32;
    const int j = wid >> 1, hf = wid & 1;
    const int tile = rb * 4 + j, q = lane & 31;
    u32x4 p[8]; float pl[8];
#pragma unroll
    for (int w = 0; w < 8; ++w) pl[w] = *(const float*)(lds + LDS_L + ((w * 4 + j) * 32 + q) * 4);
#pragma unroll
    for (int w = 0; w < 8; ++w) p[w] = *(const u32x4*)(lds + LDS_MRG + ((w * 4 + j) * 2 + hf) * 1024 + lane * 16);
    const int col0 = 28 * h + 16 * hf + (hi ? 4 : 0), col1 = col0 + 8;
    const unsigned off0 = (unsigned)((tile * 56 + (col0 >> 3)) * 32 + q) * 16u + (unsigned)((col0 >> 2) & 1) * 8u, off1 = (unsigned)((tile * 56 + (col1 >> 3)) * 32 + q) * 16u + (unsigned)((col1 >> 2) & 1) * 8u;
    const float l = ((pl[0] + pl[1]) + (pl[2] + pl[3])) + ((pl[4] + pl[5]) + (pl[6] + pl[7]));
    const float inv = __builtin_amdgcn_rcpf(l);
    u32x4 sel;
    {   const int row = lane & 31, i = 4 * (row >> 3) + (row & 3);
        const unsigned one = (row < 16 && ((row >> 2) & 1) == (lane >> 5)) ? ((i & 1) ? 0x3F800000u : 0x00003F80u) : 0u;
        sel = (u32x4){(i >> 1) == 0 ? one : 0u, (i >> 1) == 1 ? one : 0u, (i >> 1) == 2 ? one : 0u, (i >> 1) == 3 ? one : 0u}; }
    f32x16 sum = {};
#pragma unroll
    for (int w = 0; w < 8; ++w) sum = MFMA32(asb(sel), asb(p[w]), sum);
    float a[8];
#pragma unroll
    for (int i = 0; i < 8; ++i) a[i] = sum[i];
#pragma unroll
    for (int i = 0; i < 8; ++i) a[i] *= inv;
    const auto rO = RSRC(ws + WS_OA, QC_BYTES);
    const bool last = hf == 1 && hi;
    bstore64((u32x2){cvtpk(a[0], a[1]), cvtpk(a[2], a[3])}, rO, off0);
    if (!last) bstore64((u32x2){cvtpk(a[4], a[5]), cvtpk(a[6], a[7])}, rO, off1);
    float z = a[0] * 0.f; z = fmaf(a[1], 0.f, z); z = fmaf(a[2], 0.f, z); z = fmaf(a[3], 0.f, z);
    float z1 = a[4] * 0.f; z1 = fmaf(a[5], 0.f, z1); z1 = fmaf(a[6], 0.f, z1); z1 = fmaf(a[7], 0.f, z1);
    const bool ok = l > LMIN && l < 1.0e36f && z == 0.f && (last || z1 == 0.f);
    return !ok;
}

__device__ __forceinline__ void attnA_safe(char* ws, int h, int rb, char* lds, int wid, int lane) {
    const bool hi = lane >= 32, pe = hi && !(h & 1), po = !hi && (h & 1); const int m0 = (28 * h) >> 3;
    const auto rQ = RSRC(ws + WS_QA, QC_BYTES), rK = RSRC(ws + WS_KA, QC_BYTES), rV = RSRC(ws + WS_VA, V_BYTES);
    const unsigned lo16 = (unsigned)lane * 16u;
    float* mx = (float*)(lds + LDS_MX);
#pragma unroll 1
    for (int j = 0; j < 4; ++j) {
        const unsigned b = (unsigned)((rb * 4 + j) * 56 + m0) * 512u;
        u32x4 q0 = bload128(rQ, lo16, b), q1 = bload128(rQ, lo16, b + 1024u);
        patch(q0, q1, pe, po, 0u, 0u);
        float m = -3.0e38f; f32x16 acc = {};
#pragma unroll 1
        for (int t = 0; t < 8; ++t) {
            const unsigned ko = (unsigned)((wid * 8 + t) * 56 + m0) * 512u, vo = (unsigned)(h * 64 + wid * 8 + t) * 2048u;
            u32x4 k0 = bload128(rK, lo16, ko), k1 = bload128(rK, lo16, ko + 1024u), v0 = bload128(rV, lo16, vo), v1 = bload128(rV, lo16, vo + 1024u);
            patch(k0, k1, pe, po, 0u, 0u);
            f32x16 s = {}; s = MFMA32(asb(k0), asb(q0), s); s = MFMA32(asb(k1), asb(q1), s);
            const float mn = fmaxf(m, xhalf_max(max16(s)));
            const float al = __builtin_amdgcn_exp2f(m - mn); m = mn;
#pragma unroll
            for (int r = 0; r < 16; ++r) { s[r] -= mn; acc[r] *= al; }
            u32x4 p0, p1; exp_pack(s, p0, p1);
            acc = MFMA32(asb(v0), asb(p0), acc); acc = MFMA32(asb(v1), asb(p1), acc);
        }
        BARRIER();
        if (!hi) mx[wid * 32 + lane] = m;
        BARRIER();
        float g = m;
#pragma unroll
        for (int w = 0; w < 8; ++w) g = fmaxf(g, mx[w * 32 + (lane & 31)]);
        const float f = __builtin_amdgcn_exp2f(m - g);
        merge_write1(acc * f, lds, wid, j, lane);
    }
}

struct AttnBPre { u32x4 q, kp, kc; u32x2 v[2][2]; };
__device__ __forceinline__ void attnB_load(AttnBPre& a, char* ws, int h, int rb, int wid, int lane) {
    const int m0 = (28 * h) >> 3, T = rb * 4 + (wid >> 1), sub = wid & 1;
    const int Tp = sub ? T : (T > 0 ? T - 1 : 0), subp = sub ^ 1;
    const auto rQ = RSRC(ws + WS_QB, QC_BYTES), rK = RSRC(ws + WS_KB, QC_BYTES), rV = RSRC(ws + WS_VB, V_BYTES);
    const int g = lane >> 4, t16 = lane & 15;
    const unsigned co = (unsigned)g * 512u + (unsigned)t16 * 16u;
    a.q = bload128(rQ, co, (unsigned)(T * 56 + m0) * 512u + (unsigned)sub * 256u);
    a.kp = bload128(rK, co, (unsigned)(Tp * 56 + m0) * 512u + (unsigned)subp * 256u);
    a.kc = bload128(rK, co, (unsigned)(T * 56 + m0) * 512u + (unsigned)sub * 256u);
    const unsigned vo = (unsigned)(g & 1) * 512u + (unsigned)t16 * 16u + (unsigned)(g >> 1) * 8u;
#pragma unroll
    for (int db = 0; db < 2; ++db) {
        a.v[db][0] = __builtin_amdgcn_raw_buffer_load_b64(rV, vo + db * 256u, (unsigned)(h * 64 + Tp) * 2048u + (unsigned)subp * 1024u, SC1);
        a.v[db][1] = __builtin_amdgcn_raw_buffer_load_b64(rV, vo + db * 256u, (unsigned)(h * 64 + T) * 2048u + (unsigned)sub * 1024u, SC1);
    }
}
__device__ __forceinline__ void attnB_compute(AttnBPre& a, char* ws, int h, int rb, int wid, int lane) {
    const int T = rb * 4 + (wid >> 1), sub = wid & 1, g = lane >> 4, ql = lane & 15;
    const bool po = (h & 1) && g == 0, pe = !(h & 1) && g == 3;
    patch(a.q, a.q, pe, po, 0u, 0u); patch(a.kp, a.kp, pe, po, 0u, 0u); patch(a.kc, a.kc, pe, po, 0u, 0u);
    f32x4 sp = {}, sc = {};
    sp = MFMA16(asb(a.kp), asb(a.q), sp); sc = MFMA16(asb(a.kc), asb(a.q), sc);
    const bool hasp = sub || T > 0;
    float m = -INFINITY;
    const int d0 = ql - 4 * g;
#pragma unroll
    for (int r = 0; r < 4; ++r) {
        const int dc = d0 - r; const bool ev = !(dc & 1);
        sc[r] = (ev && dc >= 0) ? sc[r] : -INFINITY; sp[r] = (ev && dc < 0 && hasp) ? sp[r] : -INFINITY;
        m = fmaxf(m, fmaxf(sp[r], sc[r]));
    }
    { auto r = __builtin_amdgcn_permlane16_swap(__float_as_uint(m), __float_as_uint(m), false, false); m = fmaxf(__uint_as_float(r[0]), __uint_as_float(r[1])); }
    m = xhalf_max(m);
    float e[8]; const float mq = -m * QSCALE;
#pragma unroll
    for (int r = 0; r < 4; ++r) { e[r] = __builtin_amdgcn_exp2f(fmaf(sp[r], QSCALE, mq)); e[4 + r] = __builtin_amdgcn_exp2f(fmaf(sc[r], QSCALE, mq)); }
    const u32x4 pf = {cvtpk(e[0], e[1]), cvtpk(e[2], e[3]), cvtpk(e[4], e[5]), cvtpk(e[6], e[7])};
    f32x4 o0 = {}, o1 = {};
    o0 = MFMA16(asb((u32x4){a.v[0][0][0], a.v[0][0][1], a.v[0][1][0], a.v[0][1][1]}), asb(pf), o0);
    o1 = MFMA16(asb((u32x4){a.v[1][0][0], a.v[1][0][1], a.v[1][1][0], a.v[1][1][1]}), asb(pf), o1);
    float l;
    { auto r16 = __builtin_amdgcn_permlane16_swap(__float_as_uint(o1[0]), __float_as_uint(o1[0]), false, false); auto r32 = __builtin_amdgcn_permlane32_swap(r16[1], r16[1], false, false); l = __uint_as_float(r32[1]); }
    const float inv = __builtin_amdgcn_rcpf(l);
    const auto rO = RSRC(ws + WS_OB, QC_BYTES);
    const unsigned tok = (unsigned)(sub * 16 + ql) * 16u;
    { const int col = 28 * h + 4 * g; bstore64((u32x2){cvtpk(o0[0] * inv, o0[1] * inv), cvtpk(o0[2] * inv, o0[3] * inv)}, rO, (unsigned)(T * 56 + (col >> 3)) * 512u + tok + (unsigned)((col >> 2) & 1) * 8u); }
    if (g < 3) { const int col = 28 * h + 16 + 4 * g; bstore64((u32x2){cvtpk(o1[0] * inv, o1[1] * inv), cvtpk(o1[2] * inv, o1[3] * inv)}, rO, (unsigned)(T * 56 + (col >> 3)) * 512u + tok + (unsigned)((col >> 2) & 1) * 8u); }
}

template <int N>
__device__ __forceinline__ void wo_issue(const u16* wo, int ct, unsigned lds_base, int pc0, int lane) {
    const int r8 = lane >> 3, dpos = lane & 7;
    const unsigned woff = (unsigned)r8 * 896u + (unsigned)((dpos ^ ((r8 & 2) << 1)) << 4);
#pragma unroll
    for (int i = 0; i < N; ++i) glds16((const char*)wo + (size_t)ct * 128 + (size_t)(pc0 + i) * 8 * 896, woff, lds_base + (pc0 + i) * 1024);
}
__device__ __forceinline__ void wf_read(u32x4 (&wf)[2][14], const char* img, int kh, int lane) {
    const int g16 = lane >> 4, q = (lane & 15) >> 2, p = lane & 3;
#pragma unroll
    for (int i = 0; i < 14; ++i)
#pragma unroll
        for (int c = 0; c < 2; ++c) {
            const int row = (kh * 14 + i) * 16 + 8 * (g16 >> 1) + q;
            lds_cptr wp = (lds_cptr)img + (unsigned)row * 128u + (unsigned)(((2 * c + (g16 & 1)) ^ (q & 2)) * 32) + (unsigned)p * 8u;
            wf[c][i] = __builtin_bit_cast(u32x4, cat8(vtr(wp), vtr(wp + 512)));
        }
}
__device__ __forceinline__ void attnA_phase(char* ws, const u16* woA, int oct, int h, int rb, u64 tag, int rt, int ct, bool has_qkv, bool first, const QkvOut& outB, u32x4 (&wf)[2][14], char* lds, int wid, int lane) {
    const bool hi = lane >= 32, pe = hi && !(h & 1), po = !hi && (h & 1);
    const int m0 = (28 * h) >> 3;
    const auto rK = RSRC(ws + WS_KA, QC_BYTES), rV = RSRC(ws + WS_VA, V_BYTES);
    const unsigned lo16 = (unsigned)lane * 16u;
    const unsigned kbase = (unsigned)((wid * 8) * 56 + m0) * 512u, vbase = (unsigned)(h * 64 + wid * 8) * 2048u;
    const int rot = rb & 7;
    u32x4 qf[4][2], kf[3][2], vf[3][2];
    {   const unsigned k0 = kbase + (unsigned)rot * TILE_B, v0 = vbase + (unsigned)rot * 2048u, k1 = kbase + (unsigned)((rot + 1) & 7) * TILE_B;
        kf[0][0] = bload128(rK, lo16, k0); kf[0][1] = bload128(rK, lo16, k0 + 1024u);
        if (wid >= 4) { const unsigned b = (unsigned)((rb * 4 + wid - 4) * 56 + m0) * 512u; const auto rQ = RSRC(ws + WS_QA, QC_BYTES); qf[0][0] = bload128(rQ, lo16, b); qf[0][1] = bload128(rQ, lo16, b + 1024u); }
        vf[0][0] = bload128(rV, lo16, v0); vf[0][1] = bload128(rV, lo16, v0 + 1024u);
        kf[1][0] = bload128(rK, lo16, k1); kf[1][1] = bload128(rK, lo16, k1 + 1024u);
        const unsigned v1 = vbase + (unsigned)((rot + 1) & 7) * 2048u, k2 = kbase + (unsigned)((rot + 2) & 7) * TILE_B;
        vf[1][0] = bload128(rV, lo16, v1); vf[1][1] = bload128(rV, lo16, v1 + 1024u);
        kf[2][0] = bload128(rK, lo16, k2); kf[2][1] = bload128(rK, lo16, k2 + 1024u); }
    patch(kf[0][0], kf[0][1], pe, po, 0x3F803F80u, 0u);
    if (wid >= 4) {
        patch(qf[0][0], qf[0][1], pe, po, 0u, 0u);
        f32x16 s = {}; s = MFMA32(asb(kf[0][0]), asb(qf[0][0]), s); s = MFMA32(asb(kf[0][1]), asb(qf[0][1]), s);
        const float m = xhalf_max(max16(s)) + POFF;
        const unsigned mh = cvtpk(-m, 0.f) & 0xffffu; const float r = -m - __uint_as_float(mh << 16);
        patch(qf[0][0], qf[0][1], pe, po, mh | (cvtpk(r, 0.f) << 16), 0u);
        *(u32x4*)(lds + LDS_Q + (wid - 4) * 2048 + lane * 16) = qf[0][0]; *(u32x4*)(lds + LDS_Q + (wid - 4) * 2048 + 1024 + lane * 16) = qf[0][1];
        asm volatile("s_waitcnt lgkmcnt(0)" ::: "memory");
        if (lane == 0) ctl_add(lds, CTL_QCNT);
    }
    ctl_wait(lds, CTL_QCNT, 4);
#pragma unroll
    for (int j = 0; j < 4; ++j) { qf[j][0] = *(const u32x4*)(lds + LDS_Q + j * 2048 + lane * 16); qf[j][1] = *(const u32x4*)(lds + LDS_Q + j * 2048 + 1024 + lane * 16); }
    if (has_qkv && wid < 4) {
        qkv_store<1, 0>(outB, ws, rt, ct, wid, lane); qkv_store<1, 1>(outB, ws, rt, ct, wid, lane); qkv_store<1, 2>(outB, ws, rt, ct, wid, lane);
        qkv_store<1, 3>(outB, ws, rt, ct, wid, lane); qkv_store<1, 4>(outB, ws, rt, ct, wid, lane); qkv_store<1, 5>(outB, ws, rt, ct, wid, lane);
    }
#pragma unroll
    for (int j = 0; j < 4; ++j) { PIN(qf[j][0]); PIN(qf[j][1]); }
    PIN(kf[0][0]); PIN(kf[0][1]);
    f32x16 o[4];
#define VEXP(d, s) asm volatile("v_exp_f32 %0, %1" : "=v"(d) : "v"(s))
#define VCVT(d, a, b) asm volatile("v_cvt_pk_bf16_f32 %0, %1, %2" : "=v"(d) : "v"(a), "v"(b))
#define MF0(d, a, b) asm volatile("v_mfma_f32_32x32x16_bf16 %0, %1, %2, 0" : "=&v"(d) : "v"(a), "v"(b))
#define MFA(d, a, b) asm volatile("v_mfma_f32_32x32x16_bf16 %0, %1, %2, %0" : "+v"(d) : "v"(a), "v"(b))
#define SLOT(PV, QK, Sc, Pc0, Pc1, Sn, kn0, kn1, qn0, qn1, Op, vp0, vp1, Pp0, Pp1, INJ) do { \
        float e0, e1, e2, e3, e4, e5, e6, e7, e8, e9, e10, e11, e12, e13, e14, e15; \
        if ((QK) != 0) MF0(Sn, kn0, qn0); \
        VEXP(e0, Sc[0]); VEXP(e1, Sc[1]); VEXP(e2, Sc[2]); VEXP(e3, Sc[3]); \
        if ((QK) != 0) MFA(Sn, kn1, qn1); \
        VEXP(e4, Sc[4]); VEXP(e5, Sc[5]); VEXP(e6, Sc[6]); VEXP(e7, Sc[7]); VCVT(Pc0[0], e0, e1); VCVT(Pc0[1], e2, e3); \
        INJ; \
        if ((PV) == 2) MFA(Op, vp0, Pp0); else if ((PV) == 1) MF0(Op, vp0, Pp0); \
        VEXP(e8, Sc[8]); VEXP(e9, Sc[9]); VEXP(e10, Sc[10]); VEXP(e11, Sc[11]); VCVT(Pc0[2], e4, e5); VCVT(Pc0[3], e6, e7); \
        if ((PV) != 0) MFA(Op, vp1, Pp1); \
        VEXP(e12, Sc[12]); VEXP(e13, Sc[13]); VEXP(e14, Sc[14]); VEXP(e15, Sc[15]); VCVT(Pc1[0], e8, e9); VCVT(Pc1[1], e10, e11); VCVT(Pc1[2], e12, e13); VCVT(Pc1[3], e14, e15); \
    } while (0)
    f32x16 sA, sB; u32x4 pA0, pA1, pB0, pB1;
    asm volatile("s_nop 1\n\tv_mfma_f32_32x32x16_bf16 %0, %1, %2, 0\n\tv_mfma_f32_32x32x16_bf16 %0, %3, %4, %0\n\ts_nop 7\n\ts_nop 3" : "=&v"(sA) : "v"(kf[0][0]), "v"(qf[0][0]), "v"(kf[0][1]), "v"(qf[0][1]));
#define PATCHK(kx) do { PIN(kx[0]); PIN(kx[1]); patch(kx[0], kx[1], pe, po, 0x3F803F80u, 0u); PIN(kx[0]); PIN(kx[1]); } while (0)
#define LOADK(kc, tt) do { if ((tt) < 8) { const unsigned o_ = kbase + (unsigned)(((tt) + rot) & 7) * TILE_B; kc[0] = bload128(rK, lo16, o_); kc[1] = bload128(rK, lo16, o_ + 1024u); } } while (0)
#define LOADV(vx, tt) do { if ((tt) < 8) { const unsigned o_ = vbase + (unsigned)(((tt) + rot) & 7) * 2048u; vx[0] = bload128(rV, lo16, o_); vx[1] = bload128(rV, lo16, o_ + 1024u); } } while (0)
#define TILE(t) do { \
        SLOT((t) == 0 ? 0 : (t) == 1 ? 1 : 2, true, sA, pA0, pA1, sB, kf[(t) % 3][0], kf[(t) % 3][1], qf[1][0], qf[1][1], o[3], vf[((t) + 2) % 3][0], vf[((t) + 2) % 3][1], pB0, pB1, (void)0); \
        LOADV(vf[((t) + 2) % 3], (t) + 2); \
        SLOT((t) == 0 ? 1 : 2, true, sB, pB0, pB1, sA, kf[(t) % 3][0], kf[(t) % 3][1], qf[2][0], qf[2][1], o[0], vf[(t) % 3][0], vf[(t) % 3][1], pA0, pA1, (void)0); \
        SLOT((t) == 0 ? 1 : 2, true, sA, pA0, pA1, sB, kf[(t) % 3][0], kf[(t) % 3][1], qf[3][0], qf[3][1], o[1], vf[(t) % 3][0], vf[(t) % 3][1], pB0, pB1, if ((t) < 7) PATCHK(kf[((t) + 1) % 3])); \
        LOADK(kf[(t) % 3], (t) + 3); \
        SLOT((t) == 0 ? 1 : 2, (t) < 7, sB, pB0, pB1, sA, kf[((t) + 1) % 3][0], kf[((t) + 1) % 3][1], qf[0][0], qf[0][1], o[2], vf[(t) % 3][0], vf[(t) % 3][1], pA0, pA1, (void)0); \
    } while (0)
    AttnBPre bpre; u64 flb = 0; bool bready = false;
#pragma unroll
    for (int t = 0; t < 8; t += 2) {
        if (t == 0) __builtin_amdgcn_s_setprio(3); else if (t == 2) __builtin_amdgcn_s_setprio(2); else if (t == 4) __builtin_amdgcn_s_setprio(1); else __builtin_amdgcn_s_setprio(0);
        TILE(t);
        if (t == 4) { bready = __all(flb == tag); attnB_load(bpre, ws, h, rb, wid, opaque_lane()); }
        TILE(t + 1);
        if (t == 0) {
            if (has_qkv && wid < 4) {
                asm volatile("s_waitcnt vmcnt(8)" ::: "memory");
                if (opaque_lane() == 0 && ctl_add(lds, CTL_F2CNT) == 3) flag_set(ws, F2 + ct * 32 + rt, tag);
            }
            if (oct < 7 && wid >= 4) {
                if (has_qkv) ctl_wait(lds, CTL_RDONE, 4);
                wo_issue<14>(woA, oct, (unsigned)(uintptr_t)lds, (wid - 4) * 14, opaque_lane());
            }
        } else if (t == 2) {
            const int ln = opaque_lane();
            if (oct < 7 && wid >= 4) { asm volatile("s_waitcnt vmcnt(8)" ::: "memory"); if (ln == 0) ctl_add(lds, CTL_WA); }
            flb = __hip_atomic_load(flagp(ws, f2_index(h, rb, ln)), __ATOMIC_RELAXED, __HIP_MEMORY_SCOPE_AGENT);
            if (first && wid == 1) { flag_wait(ws, F0 + (ln & 31), tag); if (ln == 0) ctl_store(lds, CTL_F0, 1); }
        }
    }
    asm volatile("s_nop 1\n\tv_mfma_f32_32x32x16_bf16 %0, %1, %2, %0\n\tv_mfma_f32_32x32x16_bf16 %0, %3, %4, %0\n\ts_nop 7\n\ts_nop 3" : "+v"(o[3]) : "v"(vf[1][0]), "v"(pB0), "v"(vf[1][1]), "v"(pB1));
    asm volatile("" : "+v"(o[0]), "+v"(o[1]), "+v"(o[2]));
    lane = opaque_lane();
    if (has_qkv) ctl_wait(lds, CTL_RDONE, 4);
#pragma unroll
    for (int j = 0; j < 4; ++j) merge_write1(o[j], lds, wid, j, lane);
    if (!bready) { flag_wait(ws, f2_index(h, rb, lane), tag); attnB_load(bpre, ws, h, rb, wid, lane); }
    PIN(bpre.q); PIN(bpre.kp); PIN(bpre.kc); PIN(bpre.v[0][0]); PIN(bpre.v[0][1]); PIN(bpre.v[1][0]); PIN(bpre.v[1][1]);
    if (oct < 7 && wid < 4) { ctl_wait(lds, CTL_WA, 4); wf_read(wf, lds, wid & 1, lane); }
    BARRIER();
    const bool bad = merge_reduce(ws, h, rb, lds, wid, lane);
    if (__any(bad) && lane == 0) ctl_store(lds, CTL_BAD, 1);
    attnB_compute(bpre, ws, h, rb, wid, lane);
    asm volatile("s_waitcnt vmcnt(2) lgkmcnt(0)" ::: "memory");
    if (lane == 0 && ctl_add(lds, CTL_F3CNT) == 7 && !ctl_load(lds, CTL_BAD)) flag_set(ws, F3 + rb * 16 + h, tag);
    asm volatile("s_waitcnt vmcnt(0)" ::: "memory");
    if (lane == 0 && ctl_add(lds, CTL_F4CNT) == 7) flag_set(ws, F4 + rb * 16 + h, tag);
    ctl_wait(lds, CTL_F3CNT, 8);
    if (__builtin_amdgcn_readfirstlane(ctl_load(lds, CTL_BAD))) {
        BARRIER();
        attnA_safe(ws, h, rb, lds, wid, lane);
        BARRIER();
        merge_reduce(ws, h, rb, lds, wid, lane);
        DRAIN_BARRIER();
        if (threadIdx.x == 0) flag_set(ws, F3 + rb * 16 + h, tag);
    }
}

__device__ __forceinline__ void oproj_compute(char* ws, const u16* woB, u16* out, int rt, int ct, u64 tag, bool first, u32x4 (&wf)[2][14], char* lds, int wid, int lane) {
    const int br = wid >> 2, tt = (wid >> 1) & 1, kh = wid & 1;
    if (wid < 3) {
        if (wid < 2) wo_issue<19>(woB, ct, (unsigned)(uintptr_t)lds + LDS_MRG, wid * 19, lane); else wo_issue<18>(woB, ct, (unsigned)(uintptr_t)lds + LDS_MRG, 38, lane);
    }
    if ((wid & 3) == 3) {
        flag_wait(ws, (br ? F4 : F3) + (rt >> 1) * 16 + (lane & 15), tag); if (lane == 0) ctl_store(lds, br ? CTL_F4 : CTL_F3, 1);
        if (first && !br) { ctl_wait(lds, CTL_F0, 1); if (lane == 0) flag_set(ws, FEP, tag); }
    }
    else {
        if (wid < 3) { asm volatile("s_waitcnt vmcnt(0)" ::: "memory"); if (lane == 0) ctl_add(lds, CTL_WB); }
        ctl_wait(lds, br ? CTL_F4 : CTL_F3, 1);
    }
    const auto rO = RSRC(ws + (br ? WS_OB : WS_OA), QC_BYTES);
    const int tile = rt * 2 + tt;
    u32x4 of[14];
#pragma unroll
    for (int i = 0; i < 14; ++i) of[i] = bload128(rO, (unsigned)lane * 16u, (unsigned)(tile * 56 + 2 * (kh * 14 + i)) * 512u);
    if (br) { ctl_wait(lds, CTL_WB, 3); wf_read(wf, lds + LDS_MRG, kh, lane); }
    f32x16 acc[2] = {};
#pragma unroll
    for (int i = 0; i < 14; ++i) { acc[0] = MFMA32(asb(wf[0][i]), asb(of[i]), acc[0]); acc[1] = MFMA32(asb(wf[1][i]), asb(of[i]), acc[1]); }
    asm volatile("" : "+v"(acc[0]), "+v"(acc[1]));
    {   const f32x16& gv = kh ? acc[0] : acc[1];
#pragma unroll
        for (int rq = 0; rq < 4; ++rq) *(f32x4*)(lds + 114688 + (wid * 4 + rq) * 1024 + lane * 16) = (f32x4){gv[4 * rq], gv[4 * rq + 1], gv[4 * rq + 2], gv[4 * rq + 3]};
    }
    asm volatile("s_waitcnt lgkmcnt(0)" ::: "memory");
    if (lane == 0) ctl_store(lds, CTL_X + wid * 4, 1);
    ctl_wait(lds, CTL_X + (wid ^ 1) * 4, 1);
    f32x16 r = kh ? acc[1] : acc[0];
#pragma unroll
    for (int rq = 0; rq < 4; ++rq) { const f32x4 x = *(const f32x4*)(lds + 114688 + ((wid ^ 1) * 4 + rq) * 1024 + lane * 16); r[4 * rq] += x[0]; r[4 * rq + 1] += x[1]; r[4 * rq + 2] += x[2]; r[4 * rq + 3] += x[3]; }
    const unsigned orow = (unsigned)(rt * 64 + tt * 32 + (lane & 31)) * 1792u + (unsigned)(br * 448 + ct * 64 + kh * 32 + (lane >> 5) * 8) * 2u;
#pragma unroll
    for (int pr = 0; pr < 2; ++pr) {
        const unsigned a0 = cvtpk(r[8 * pr], r[8 * pr + 1]), a1 = cvtpk(r[8 * pr + 2], r[8 * pr + 3]), b0 = cvtpk(r[8 * pr + 4], r[8 * pr + 5]), b1 = cvtpk(r[8 * pr + 6], r[8 * pr + 7]);
        auto r0 = __builtin_amdgcn_permlane32_swap(a0, b0, false, false);
        auto r1 = __builtin_amdgcn_permlane32_swap(a1, b1, false, false);
        bstore128((u32x4){r0[0], r1[0], r0[1], r1[1]}, RSRC(out, 2048 * 896 * 2), orow + pr * 32u);
    }
}

__global__ void __launch_bounds__(512, 2) fused_kernel(const u16* xA, const u16* wA, const u16* woA, const u16* xB, const u16* wB, const u16* woB, u16* out, char* ws) {
    extern __shared__ __attribute__((aligned(16))) char lds[];
    int lane = threadIdx.x & 63; const int wid = __builtin_amdgcn_readfirstlane(threadIdx.x >> 6);
    const int id = blockIdx.x, xcd = id & 7, loc = id >> 3;
    if (threadIdx.x < 32) ctl_store(lds, threadIdx.x * 4, 0);
    const u64 epoch = __hip_atomic_load(flagp(ws, FEP), __ATOMIC_RELAXED, __HIP_MEMORY_SCOPE_AGENT);
    const int gb = xcd & 1, grt = 8 * (xcd >> 1) + (loc & 7), qct = gb * 4 + (loc >> 3);
    const bool has_qkv = qct < 7;
    const int h = xcd * 2 + (loc & 1), rb = loc >> 1;
    const int ort = 4 * xcd + (loc & 3), oct = loc >> 2;
    unsigned warm0 = 0, warm1 = 0, warm2 = 0, warm3 = 0;
    if (wid < 4) {
        u64 pc; int len;
        asm volatile("s_getpc_b64 %0\n.Lfused_pc_ref:\n\ts_mov_b32 %1, .Lfused_code_end - .Lfused_pc_ref" : "=s"(pc), "=s"(len));
        const int off = (wid * 64 + lane) * 128;
        if (off < len) warm0 = *(const __attribute__((address_space(1))) unsigned*)(pc + off);
    }
    QkvOut outB;
    if (has_qkv) qkv_phase(xA, wA, xB, wB, ws, grt, qct, epoch, lds, wid, lane, outB);
    else { const u64 t0 = next_tag(epoch); if (threadIdx.x == 0) flag_set(ws, F0 + (xcd >> 1) * 8 + (loc & 7), t0); BARRIER(); }
    asm volatile("" :: "v"(warm0), "v"(warm1), "v"(warm2), "v"(warm3));
    const u64 tag = next_tag(epoch);
    if (wid == 4) {
        flag_wait(ws, F1 + (lane < 32 ? (28 * h) >> 6 : (28 * h + 27) >> 6) * 32 + (lane & 31), tag);
        if (lane == 0) ctl_store(lds, CTL_F1, 1);
    } else ctl_wait(lds, CTL_F1, 1);
    const u64 tags = (u64)(unsigned)__builtin_amdgcn_readfirstlane((unsigned)tag) | ((u64)(unsigned)__builtin_amdgcn_readfirstlane((unsigned)(tag >> 32)) << 32);
    u32x4 wf[2][14];
    attnA_phase(ws, woA, oct, h, rb, tags, grt, qct, has_qkv, id == 0, outB, wf, lds, wid, lane);
    lane = opaque_lane();
    if (oct < 7) {
        oproj_compute(ws, woB, out, ort, oct, tags, id == 0, wf, lds, wid, lane);
    }
    asm volatile(".Lfused_code_end:");
}

extern "C" void kernel_launch(void* const* d_in, const int* in_sizes, int n_in, void* d_out, int out_size, void* d_ws, size_t ws_size, hipStream_t stream) {
    const u16* xA = (const u16*)d_in[0]; const u16* wA = (const u16*)d_in[1]; const u16* woA = (const u16*)d_in[2];
    const u16* xB = (const u16*)d_in[3]; const u16* wB = (const u16*)d_in[4]; const u16* woB = (const u16*)d_in[5];
    (void)hipFuncSetAttribute((const void*)fused_kernel, hipFuncAttributeMaxDynamicSharedMemorySize, LDS_TOTAL);
    fused_kernel<<<256, 512, LDS_TOTAL, stream>>>(xA, wA, woA, xB, wB, woB, (u16*)d_out, (char*)d_ws);
}
```

```cpp
#include <hip/hip_runtime.h>
#include <hip/hip_bf16.h>
#include <stdint.h>

typedef unsigned short u16;
typedef unsigned long long u64;
typedef short bf16x8 __attribute__((ext_vector_type(8)));
typedef short s16x4 __attribute__((ext_vector_type(4)));
typedef float f32x4 __attribute__((ext_vector_type(4)));
typedef float f32x16 __attribute__((ext_vector_type(16)));
typedef unsigned u32x4 __attribute__((ext_vector_type(4)));
typedef unsigned u32x2 __attribute__((ext_vector_type(2)));
typedef __attribute__((address_space(3))) const char* lds_cptr;
typedef __attribute__((address_space(1))) u64 gu64;

constexpr int TILE_B = 56 * 512;
constexpr size_t QC_BYTES = (size_t)64 * TILE_B;
constexpr size_t V_BYTES = (size_t)16 * 64 * 2048;
constexpr size_t WS_QA = 0, WS_KA = WS_QA + QC_BYTES, WS_VA = WS_KA + QC_BYTES;
constexpr size_t WS_QB = WS_VA + V_BYTES, WS_KB = WS_QB + QC_BYTES, WS_VB = WS_KB + QC_BYTES;
constexpr size_t WS_OA = WS_VB + V_BYTES, WS_OB = WS_OA + QC_BYTES, WS_FLAGS = WS_OB + QC_BYTES;
constexpr int F1 = 0, F2 = 256, F3 = 512, F4 = 768, F0 = 1024, FEP = 1056;
constexpr float QSCALE = 0.18898223650461362f * 1.4426950408889634f;
constexpr int STG = 32768;
constexpr int LDS_Q = 147456, LDS_L = 131072, LDS_MRG = 57344, LDS_CTL = LDS_Q + 8192, LDS_MX = LDS_CTL + 128, LDS_TOTAL = LDS_MX + 1024 + 64;
constexpr int CTL_F1 = 0, CTL_F2CNT = 4, CTL_BAD = 8, CTL_QCNT = 12, CTL_F4CNT = 16, CTL_F3 = 20, CTL_RDONE = 24, CTL_F4 = 28, CTL_X = 32;
constexpr int CTL_WA = 64, CTL_WB = 68, CTL_S12 = 72, CTL_S13 = 76, CTL_F3CNT = 80, CTL_F0 = 84;
constexpr int PUBG = 10;
#define POFF 96.0f
#define LMIN (__builtin_ldexpf(1.0f, -(int)(POFF) - 20))
constexpr unsigned SPIN_MAX = 1u << 18;

#define MFMA16(a, b, c) __builtin_amdgcn_mfma_f32_16x16x32_bf16(a, b, c, 0, 0, 0)
#define MFMA32(a, b, c) __builtin_amdgcn_mfma_f32_32x32x16_bf16(a, b, c, 0, 0, 0)
#define PIN(x) asm volatile("" : "+v"(x))
#define SBAR() __builtin_amdgcn_sched_barrier(0)
#define SC1 16

__device__ __forceinline__ unsigned cvtpk(float lo, float hi) {
    typedef float f2 __attribute__((ext_vector_type(2))); typedef __bf16 b2 __attribute__((ext_vector_type(2)));
    f2 v = {lo, hi}; b2 r = __builtin_convertvector(v, b2); return __builtin_bit_cast(unsigned, r);
}
__device__ __forceinline__ void glds16(const void* sbase, unsigned voff, unsigned lds_base) {
    asm volatile("s_mov_b32 m0, %2\n\ts_nop 0\n\tglobal_load_lds_dwordx4 %0, %1" :: "v"(voff), "s"(sbase), "s"(lds_base) : "memory");
}
__device__ __forceinline__ s16x4 vtr(lds_cptr p) {
    typedef short v4i16_t __attribute__((ext_vector_type(4)));
    return __builtin_bit_cast(s16x4, __builtin_amdgcn_ds_read_tr16_b64_v4i16((__attribute__((address_space(3))) v4i16_t*)p));
}
__device__ __forceinline__ bf16x8 cat8(s16x4 a, s16x4 b) { return (bf16x8){a[0], a[1], a[2], a[3], b[0], b[1], b[2], b[3]}; }
__device__ __forceinline__ bf16x8 ldsrd128(lds_cptr p) { return *(const __attribute__((address_space(3))) bf16x8*)p; }
#define RSRC(p, bytes) __builtin_amdgcn_make_buffer_rsrc((void*)(p), 0, (int)(bytes), 0x00020000)
__device__ __forceinline__ u32x4 bload128(__amdgpu_buffer_rsrc_t r, unsigned voff, unsigned soff) { return __builtin_amdgcn_raw_buffer_load_b128(r, voff, soff, SC1); }
__device__ __forceinline__ void bstore128(u32x4 v, __amdgpu_buffer_rsrc_t r, unsigned voff) { __builtin_amdgcn_raw_buffer_store_b128(v, r, voff, 0, SC1); }
__device__ __forceinline__ void bstore64(u32x2 v, __amdgpu_buffer_rsrc_t r, unsigned voff) { __builtin_amdgcn_raw_buffer_store_b64(v, r, voff, 0, SC1); }
__device__ __forceinline__ bf16x8 asb(u32x4 v) { return __builtin_bit_cast(bf16x8, v); }

__device__ __forceinline__ gu64* flagp(char* ws, int idx) { return (gu64*)(ws + WS_FLAGS + (size_t)idx * 256); }
__device__ __forceinline__ void flag_set(char* ws, int idx, u64 tag) { __hip_atomic_store(flagp(ws, idx), tag, __ATOMIC_RELAXED, __HIP_MEMORY_SCOPE_AGENT); }
__device__ __forceinline__ u64 next_tag(u64 epoch) { asm volatile("" : "+v"(epoch)); return epoch + 1; }
__device__ __forceinline__ void flag_wait(char* ws, int idx, u64 tag) {
    gu64* p = flagp(ws, idx);
    for (unsigned spins = 0; spins < SPIN_MAX; ++spins) {
        const u64 v = __hip_atomic_load(p, __ATOMIC_RELAXED, __HIP_MEMORY_SCOPE_AGENT);
        if (__all(v == tag)) break;
        __builtin_amdgcn_s_sleep(1);
    }
}
__device__ __forceinline__ int opaque_lane() { int l; asm volatile("v_mbcnt_lo_u32_b32 %0, -1, 0\n\tv_mbcnt_hi_u32_b32 %0, -1, %0" : "=v"(l)); return l; }
__device__ __forceinline__ int f2_index(int h, int rb, int lane) { const int i = lane % 6, r = 2 * rb - 1 + i % 3; return F2 + (i < 3 ? (28 * h) >> 6 : (28 * h + 27) >> 6) * 32 + (r < 0 ? 0 : r); }
typedef __attribute__((address_space(3))) int* lds_iptr;
__device__ __forceinline__ int ctl_load(char* lds, int off) { return __hip_atomic_load((lds_iptr)(lds + LDS_CTL + off), __ATOMIC_RELAXED, __HIP_MEMORY_SCOPE_WORKGROUP); }
__device__ __forceinline__ void ctl_store(char* lds, int off, int v) { __hip_atomic_store((lds_iptr)(lds + LDS_CTL + off), v, __ATOMIC_RELAXED, __HIP_MEMORY_SCOPE_WORKGROUP); }
__device__ __forceinline__ void ctl_wait(char* lds, int off, int n) {
    for (unsigned spins = 0; spins < (SPIN_MAX << 6); ++spins) { if (__builtin_amdgcn_readfirstlane(ctl_load(lds, off)) >= n) break; __builtin_amdgcn_s_sleep(1); }
    asm volatile("" ::: "memory");
}
__device__ __forceinline__ int ctl_add(char* lds, int off) { return __hip_atomic_fetch_add((lds_iptr)(lds + LDS_CTL + off), 1, __ATOMIC_RELAXED, __HIP_MEMORY_SCOPE_WORKGROUP); }
#define BARRIER() asm volatile("s_waitcnt lgkmcnt(0)\n\ts_barrier" ::: "memory")
#define DRAIN_BARRIER() asm volatile("s_waitcnt vmcnt(0) lgkmcnt(0)\n\ts_barrier" ::: "memory")

struct QkvFrag { bf16x8 x[2][4], w[2][3]; };
struct QkvAcc { f32x4 q[4], k[4], v[4]; };
struct QkvOut { u32x4 q[2], k[2], v[2]; };
__device__ __forceinline__ void qkv_pack(QkvOut& o, const QkvAcc& a, float qs) {
#pragma unroll
    for (int tb = 0; tb < 2; ++tb) {
        const f32x4 q0 = a.q[2 * tb] * qs, q1 = a.q[2 * tb + 1] * qs, k0 = a.k[2 * tb], k1 = a.k[2 * tb + 1], v0 = a.v[2 * tb], v1 = a.v[2 * tb + 1];
        { auto ra = __builtin_amdgcn_permlane16_swap(cvtpk(q0[0], q0[1]), cvtpk(q1[0], q1[1]), false, false); auto rb = __builtin_amdgcn_permlane16_swap(cvtpk(q0[2], q0[3]), cvtpk(q1[2], q1[3]), false, false);
          o.q[tb] = (u32x4){ra[0], rb[0], ra[1], rb[1]}; }
        { auto ra = __builtin_amdgcn_permlane16_swap(cvtpk(k0[0], k0[1]), cvtpk(k1[0], k1[1]), false, false); auto rb = __builtin_amdgcn_permlane16_swap(cvtpk(k0[2], k0[3]), cvtpk(k1[2], k1[3]), false, false);
          o.k[tb] = (u32x4){ra[0], rb[0], ra[1], rb[1]}; }
        { auto ra = __builtin_amdgcn_permlane32_swap(cvtpk(v0[0], v0[1]), cvtpk(v1[0], v1[1]), false, false); auto rb = __builtin_amdgcn_permlane32_swap(cvtpk(v0[2], v0[3]), cvtpk(v1[2], v1[3]), false, false);
          o.v[tb] = (u32x4){ra[0], rb[0], ra[1], rb[1]}; }
    }
}
template <int BR, int I>
__device__ __forceinline__ void qkv_store(const QkvOut& o, char* ws, int rt, int ct, int c, int lane) {
    constexpr int tb = I / 3, kind = I % 3;
    const int tile = rt * 2 + tb;
    if (kind < 2) {
        const unsigned off = (unsigned)(tile * 56 + 8 * ct + 2 * c) * 512u + (unsigned)lane * 16u;
        if (kind == 0) bstore128(o.q[tb], RSRC(ws + (BR ? WS_QB : WS_QA), QC_BYTES), off); else bstore128(o.k[tb], RSRC(ws + (BR ? WS_KB : WS_KA), QC_BYTES), off);
    } else {
        const int n = 64 * ct + 16 * c + (lane & 15), h = n / 28, d = n - 28 * h;
        const unsigned off = (unsigned)(h * 64 + tile) * 2048u + (unsigned)(lane >> 4) * 512u + (unsigned)d * 16u;
        const auto rs = RSRC(ws + (BR ? WS_VB : WS_VA), V_BYTES);
        bstore128(o.v[tb], rs, off);
        if (d >= 24) { const unsigned pad = d == 24 ? 0x3F803F80u : 0u; bstore128((u32x4){pad, pad, pad, pad}, rs, off + 64u); }
    }
}

__device__ __forceinline__ void qkv_phase(const u16* xA, const u16* wA, const u16* xB, const u16* wB, char* ws, int rt, int ct, u64 epoch, char* lds, int wid, int lane, QkvOut& outB) {
    if (wid >= 4) {
        const unsigned lds0 = (unsigned)(uintptr_t)lds;
        const int lw = wid - 4, r8 = lane >> 3, dpos = lane & 7;
        unsigned xoff[2], woff[2];
#pragma unroll
        for (int i = 0; i < 2; ++i) { const int row = 16 * lw + 8 * i + r8;
            xoff[i] = (unsigned)row * 896u + (unsigned)((dpos ^ ((row >> 1) & 7)) << 4);
            const int f2 = ((row >> 1) & 1) | (((row >> 3) & 1) << 1);
            woff[i] = (unsigned)row * 2688u + (unsigned)((((dpos >> 1) ^ f2) * 2 + (dpos & 1)) << 4); }
        const int rot = __builtin_amdgcn_readfirstlane(((rt & 7) + 2 * (ct & 3)) % 7);
        const char* xa = (const char*)xA + (size_t)rt * 64 * 896; const char* wa = (const char*)wA + (size_t)ct * 128;
        const char* xb = (const char*)xB + (size_t)rt * 64 * 896; const char* wb = (const char*)wB + (size_t)ct * 128;
#define QKV_ISSUE(g) do { int st_ = ((g) % 7) + rot; st_ = st_ >= 7 ? st_ - 7 : st_; const char* x_ = ((g) < 7 ? xa : xb) + st_ * 128; const char* w_ = ((g) < 7 ? wa : wb) + (size_t)st_ * (64 * 2688); \
        const unsigned sl_ = lds0 + ((g) & 3) * STG + lw * 2048; \
        glds16(x_, xoff[0], sl_); glds16(x_, xoff[1], sl_ + 1024); \
        glds16(w_, woff[0], sl_ + 8192); glds16(w_, woff[1], sl_ + 8192 + 1024); glds16(w_ + 896, woff[0], sl_ + 16384); glds16(w_ + 896, woff[1], sl_ + 16384 + 1024); \
        glds16(w_ + 1792, woff[0], sl_ + 24576); glds16(w_ + 1792, woff[1], sl_ + 24576 + 1024); } while (0)
        QKV_ISSUE(0); QKV_ISSUE(1);
#pragma unroll
        for (int g = 0; g < 12; ++g) {
            asm volatile("s_waitcnt vmcnt(8) lgkmcnt(0)\n\ts_barrier" ::: "memory");
            QKV_ISSUE(g + 2);
        }
        asm volatile("s_waitcnt vmcnt(8)" ::: "memory");
        if (lane == 0) ctl_add(lds, CTL_S12);
        asm volatile("s_waitcnt vmcnt(0)" ::: "memory");
        if (lane == 0) ctl_add(lds, CTL_S13);
        return;
    }
    const int c = wid, g4 = lane >> 4, t16 = lane & 15;
    const unsigned xr0 = (unsigned)t16 * 128u + (unsigned)((g4 ^ (t16 >> 1)) << 4);
    const int q = t16 >> 2, p = lane & 3;
    const int rf2 = ((q >> 1) & 1) | ((g4 & 1) << 1);
    const unsigned wr0 = 8192u + (unsigned)(8 * g4 + q) * 128u + (unsigned)((c ^ rf2) * 32) + (unsigned)p * 8u;
    QkvAcc acc; QkvOut outA;
#pragma unroll
    for (int i = 0; i < 4; ++i) acc.q[i] = acc.k[i] = acc.v[i] = (f32x4){};
    QkvFrag fr[2];
#define QMF(i) do { if (g > 0) { constexpr int ks_ = (i) / 12, s_ = ((i) % 12) / 4, b_ = (i) % 4; const QkvFrag& f_ = fr[(g - 1) & 1]; \
        if (s_ == 0) acc.q[b_] = MFMA16(f_.w[ks_][0], f_.x[ks_][b_], acc.q[b_]); if (s_ == 1) acc.k[b_] = MFMA16(f_.w[ks_][1], f_.x[ks_][b_], acc.k[b_]); \
        if (s_ == 2) acc.v[b_] = MFMA16(f_.x[ks_][b_], f_.w[ks_][2], acc.v[b_]); } SBAR(); } while (0)
#define QRX(ks, b) do { if (g < 14) fr[g & 1].x[ks][b] = ldsrd128(sl + (xr0 ^ ((ks) * 64)) + (b) * 2048); } while (0)
#define QRW(ks, s) do { if (g < 14) { lds_cptr wp_ = sl + wr0 + (ks) * 4096 + (s) * 8192; fr[g & 1].w[ks][s] = cat8(vtr(wp_), vtr(wp_ + 512)); } } while (0)
#pragma unroll
    for (int g = 0; g < 15; ++g) {
        if (g == PUBG) asm volatile("s_waitcnt vmcnt(0) lgkmcnt(0)\n\ts_barrier" ::: "memory");
        else if (g < 12) asm volatile("s_waitcnt lgkmcnt(0)\n\ts_barrier" ::: "memory");
        else if (g == 12) ctl_wait(lds, CTL_S12, 4);
        else if (g == 13) ctl_wait(lds, CTL_S13, 4);
        SBAR();
        lds_cptr sl = (lds_cptr)lds + (g & 3) * STG;
        if (g == PUBG) { const u64 tag = next_tag(epoch); if (threadIdx.x == 0) flag_set(ws, F1 + ct * 32 + rt, tag); }
        SBAR();
        QRX(0, 0); QMF(0); QRX(0, 1); QMF(1); QRX(0, 2); QMF(2); QRX(0, 3); QMF(3); QRW(0, 0); QMF(4); QRW(0, 1); QMF(5); QRW(0, 2); QMF(6);
        QRX(1, 0); QMF(7); QRX(1, 1); QMF(8); QRX(1, 2); QMF(9); QRX(1, 3); QMF(10); QRW(1, 0); QMF(11); QRW(1, 1); QMF(12); QRW(1, 2); QMF(13);
        QMF(14); QMF(15); QMF(16); QMF(17); QMF(18); QMF(19); QMF(20); QMF(21); QMF(22); QMF(23);
        if (g == 7) {
            qkv_pack(outA, acc, QSCALE);
            qkv_store<0, 1>(outA, ws, rt, ct, c, lane); qkv_store<0, 2>(outA, ws, rt, ct, c, lane); qkv_store<0, 4>(outA, ws, rt, ct, c, lane);
            qkv_store<0, 5>(outA, ws, rt, ct, c, lane); qkv_store<0, 0>(outA, ws, rt, ct, c, lane); qkv_store<0, 3>(outA, ws, rt, ct, c, lane);
#pragma unroll
            for (int i = 0; i < 4; ++i) acc.q[i] = acc.k[i] = acc.v[i] = (f32x4){};
        }
    }
    if (lane == 0) ctl_add(lds, CTL_RDONE);
    qkv_pack(outB, acc, 1.0f);
}

__device__ __forceinline__ float max16(const f32x16& s) {
    float a = fmaxf(fmaxf(s[0], s[1]), s[2]), b = fmaxf(fmaxf(s[3], s[4]), s[5]);
    a = fmaxf(fmaxf(a, s[6]), s[7]); b = fmaxf(fmaxf(b, s[8]), s[9]); a = fmaxf(fmaxf(a, s[10]), s[11]); b = fmaxf(fmaxf(b, s[12]), s[13]);
    a = fmaxf(fmaxf(a, s[14]), s[15]); return fmaxf(a, b);
}
__device__ __forceinline__ float xhalf_max(float m) {
    auto r = __builtin_amdgcn_permlane32_swap(__float_as_uint(m), __float_as_uint(m), false, false);
    return fmaxf(__uint_as_float(r[0]), __uint_as_float(r[1]));
}
__device__ __forceinline__ void patch(u32x4& f0, u32x4& f1, bool pe, bool po, unsigned a, unsigned b) {
    f0[0] = po ? a : f0[0]; f0[1] = po ? b : f0[1]; f1[2] = pe ? a : f1[2]; f1[3] = pe ? b : f1[3];
}
__device__ __forceinline__ void exp_pack(const f32x16& s, u32x4& p0, u32x4& p1) {
    float e[16];
#pragma unroll
    for (int r = 0; r < 16; ++r) e[r] = __builtin_amdgcn_exp2f(s[r]);
    p0 = (u32x4){cvtpk(e[0], e[1]), cvtpk(e[2], e[3]), cvtpk(e[4], e[5]), cvtpk(e[6], e[7])};
    p1 = (u32x4){cvtpk(e[8], e[9]), cvtpk(e[10], e[11]), cvtpk(e[12], e[13]), cvtpk(e[14], e[15])};
}
__device__ __forceinline__ void merge_write1(const f32x16& o, char* lds, int wid, int j, int lane) {
#pragma unroll
    for (int hf = 0; hf < 2; ++hf)
        *(u32x4*)(lds + LDS_MRG + ((wid * 4 + j) * 2 + hf) * 1024 + lane * 16) = (u32x4){cvtpk(o[8 * hf], o[8 * hf + 1]), cvtpk(o[8 * hf + 2], o[8 * hf + 3]), cvtpk(o[8 * hf + 4], o[8 * hf + 5]), cvtpk(o[8 * hf + 6], o[8 * hf + 7])};
    if (lane >= 32) *(float*)(lds + LDS_L + ((wid * 4 + j) * 32 + lane - 32) * 4) = o[12];
}
__device__ __forceinline__ bool merge_reduce(char* ws, int h, int rb, char* lds, int wid, int lane) {
    const bool hi = lane >= 32;
    const int j = wid >> 1, hf = wid & 1;
    const int tile = rb * 4 + j, q = lane & 31;
    u32x4 p[8]; float pl[8];
#pragma unroll
    for (int w = 0; w < 8; ++w) pl[w] = *(const float*)(lds + LDS_L + ((w * 4 + j) * 32 + q) * 4);
#pragma unroll
    for (int w = 0; w < 8; ++w) p[w] = *(const u32x4*)(lds + LDS_MRG + ((w * 4 + j) * 2 + hf) * 1024 + lane * 16);
    const int col0 = 28 * h + 16 * hf + (hi ? 4 : 0), col1 = col0 + 8;
    const unsigned off0 = (unsigned)((tile * 56 + (col0 >> 3)) * 32 + q) * 16u + (unsigned)((col0 >> 2) & 1) * 8u, off1 = (unsigned)((tile * 56 + (col1 >> 3)) * 32 + q) * 16u + (unsigned)((col1 >> 2) & 1) * 8u;
    const float l = ((pl[0] + pl[1]) + (pl[2] + pl[3])) + ((pl[4] + pl[5]) + (pl[6] + pl[7]));
    const float inv = __builtin_amdgcn_rcpf(l);
    u32x4 sel;
    {   const int row = lane & 31, i = 4 * (row >> 3) + (row & 3);
        const unsigned one = (row < 16 && ((row >> 2) & 1) == (lane >> 5)) ? ((i & 1) ? 0x3F800000u : 0x00003F80u) : 0u;
        sel = (u32x4){(i >> 1) == 0 ? one : 0u, (i >> 1) == 1 ? one : 0u, (i >> 1) == 2 ? one : 0u, (i >> 1) == 3 ? one : 0u}; }
    f32x16 sum = {};
#pragma unroll
    for (int w = 0; w < 8; ++w) sum = MFMA32(asb(sel), asb(p[w]), sum);
    float a[8];
#pragma unroll
    for (int i = 0; i < 8; ++i) a[i] = sum[i];
#pragma unroll
    for (int i = 0; i < 8; ++i) a[i] *= inv;
    const auto rO = RSRC(ws + WS_OA, QC_BYTES);
    const bool last = hf == 1 && hi;
    bstore64((u32x2){cvtpk(a[0], a[1]), cvtpk(a[2], a[3])}, rO, off0);
    if (!last) bstore64((u32x2){cvtpk(a[4], a[5]), cvtpk(a[6], a[7])}, rO, off1);
    float z = a[0] * 0.f; z = fmaf(a[1], 0.f, z); z = fmaf(a[2], 0.f, z); z = fmaf(a[3], 0.f, z);
    float z1 = a[4] * 0.f; z1 = fmaf(a[5], 0.f, z1); z1 = fmaf(a[6], 0.f, z1); z1 = fmaf(a[7], 0.f, z1);
    const bool ok = l > LMIN && l < 1.0e36f && z == 0.f && (last || z1 == 0.f);
    return !ok;
}

__device__ __forceinline__ void attnA_safe(char* ws, int h, int rb, char* lds, int wid, int lane) {
    const bool hi = lane >= 32, pe = hi && !(h & 1), po = !hi && (h & 1); const int m0 = (28 * h) >> 3;
    const auto rQ = RSRC(ws + WS_QA, QC_BYTES), rK = RSRC(ws + WS_KA, QC_BYTES), rV = RSRC(ws + WS_VA, V_BYTES);
    const unsigned lo16 = (unsigned)lane * 16u;
    float* mx = (float*)(lds + LDS_MX);
#pragma unroll 1
    for (int j = 0; j < 4; ++j) {
        const unsigned b = (unsigned)((rb * 4 + j) * 56 + m0) * 512u;
        u32x4 q0 = bload128(rQ, lo16, b), q1 = bload128(rQ, lo16, b + 1024u);
        patch(q0, q1, pe, po, 0u, 0u);
        float m = -3.0e38f; f32x16 acc = {};
#pragma unroll 1
        for (int t = 0; t < 8; ++t) {
            const unsigned ko = (unsigned)((wid * 8 + t) * 56 + m0) * 512u, vo = (unsigned)(h * 64 + wid * 8 + t) * 2048u;
            u32x4 k0 = bload128(rK, lo16, ko), k1 = bload128(rK, lo16, ko + 1024u), v0 = bload128(rV, lo16, vo), v1 = bload128(rV, lo16, vo + 1024u);
            patch(k0, k1, pe, po, 0u, 0u);
            f32x16 s = {}; s = MFMA32(asb(k0), asb(q0), s); s = MFMA32(asb(k1), asb(q1), s);
            const float mn = fmaxf(m, xhalf_max(max16(s)));
            const float al = __builtin_amdgcn_exp2f(m - mn); m = mn;
#pragma unroll
            for (int r = 0; r < 16; ++r) { s[r] -= mn; acc[r] *= al; }
            u32x4 p0, p1; exp_pack(s, p0, p1);
            acc = MFMA32(asb(v0), asb(p0), acc); acc = MFMA32(asb(v1), asb(p1), acc);
        }
        BARRIER();
        if (!hi) mx[wid * 32 + lane] = m;
        BARRIER();
        float g = m;
#pragma unroll
        for (int w = 0; w < 8; ++w) g = fmaxf(g, mx[w * 32 + (lane & 31)]);
        const float f = __builtin_amdgcn_exp2f(m - g);
        merge_write1(acc * f, lds, wid, j, lane);
    }
}

struct AttnBPre { u32x4 q, kp, kc; u32x2 v[2][2]; };
__device__ __forceinline__ void attnB_load(AttnBPre& a, char* ws, int h, int rb, int wid, int lane) {
    const int m0 = (28 * h) >> 3, T = rb * 4 + (wid >> 1), sub = wid & 1;
    const int Tp = sub ? T : (T > 0 ? T - 1 : 0), subp = sub ^ 1;
    const auto rQ = RSRC(ws + WS_QB, QC_BYTES), rK = RSRC(ws + WS_KB, QC_BYTES), rV = RSRC(ws + WS_VB, V_BYTES);
    const int g = lane >> 4, t16 = lane & 15;
    const unsigned co = (unsigned)g * 512u + (unsigned)t16 * 16u;
    a.q = bload128(rQ, co, (unsigned)(T * 56 + m0) * 512u + (unsigned)sub * 256u);
    a.kp = bload128(rK, co, (unsigned)(Tp * 56 + m0) * 512u + (unsigned)subp * 256u);
    a.kc = bload128(rK, co, (unsigned)(T * 56 + m0) * 512u + (unsigned)sub * 256u);
    const unsigned vo = (unsigned)(g & 1) * 512u + (unsigned)t16 * 16u + (unsigned)(g >> 1) * 8u;
#pragma unroll
    for (int db = 0; db < 2; ++db) {
        a.v[db][0] = __builtin_amdgcn_raw_buffer_load_b64(rV, vo + db * 256u, (unsigned)(h * 64 + Tp) * 2048u + (unsigned)subp * 1024u, SC1);
        a.v[db][1] = __builtin_amdgcn_raw_buffer_load_b64(rV, vo + db * 256u, (unsigned)(h * 64 + T) * 2048u + (unsigned)sub * 1024u, SC1);
    }
}
__device__ __forceinline__ void attnB_compute(AttnBPre& a, char* ws, int h, int rb, int wid, int lane) {
    const int T = rb * 4 + (wid >> 1), sub = wid & 1, g = lane >> 4, ql = lane & 15;
    const bool po = (h & 1) && g == 0, pe = !(h & 1) && g == 3;
    patch(a.q, a.q, pe, po, 0u, 0u); patch(a.kp, a.kp, pe, po, 0u, 0u); patch(a.kc, a.kc, pe, po, 0u, 0u);
    f32x4 sp = {}, sc = {};
    sp = MFMA16(asb(a.kp), asb(a.q), sp); sc = MFMA16(asb(a.kc), asb(a.q), sc);
    const bool hasp = sub || T > 0;
    float m = -INFINITY;
    const int d0 = ql - 4 * g;
#pragma unroll
    for (int r = 0; r < 4; ++r) {
        const int dc = d0 - r; const bool ev = !(dc & 1);
        sc[r] = (ev && dc >= 0) ? sc[r] : -INFINITY; sp[r] = (ev && dc < 0 && hasp) ? sp[r] : -INFINITY;
        m = fmaxf(m, fmaxf(sp[r], sc[r]));
    }
    { auto r = __builtin_amdgcn_permlane16_swap(__float_as_uint(m), __float_as_uint(m), false, false); m = fmaxf(__uint_as_float(r[0]), __uint_as_float(r[1])); }
    m = xhalf_max(m);
    float e[8]; const float mq = -m * QSCALE;
#pragma unroll
    for (int r = 0; r < 4; ++r) { e[r] = __builtin_amdgcn_exp2f(fmaf(sp[r], QSCALE, mq)); e[4 + r] = __builtin_amdgcn_exp2f(fmaf(sc[r], QSCALE, mq)); }
    const u32x4 pf = {cvtpk(e[0], e[1]), cvtpk(e[2], e[3]), cvtpk(e[4], e[5]), cvtpk(e[6], e[7])};
    f32x4 o0 = {}, o1 = {};
    o0 = MFMA16(asb((u32x4){a.v[0][0][0], a.v[0][0][1], a.v[0][1][0], a.v[0][1][1]}), asb(pf), o0);
    o1 = MFMA16(asb((u32x4){a.v[1][0][0], a.v[1][0][1], a.v[1][1][0], a.v[1][1][1]}), asb(pf), o1);
    float l;
    { auto r16 = __builtin_amdgcn_permlane16_swap(__float_as_uint(o1[0]), __float_as_uint(o1[0]), false, false); auto r32 = __builtin_amdgcn_permlane32_swap(r16[1], r16[1], false, false); l = __uint_as_float(r32[1]); }
    const float inv = __builtin_amdgcn_rcpf(l);
    const auto rO = RSRC(ws + WS_OB, QC_BYTES);
    const unsigned tok = (unsigned)(sub * 16 + ql) * 16u;
    { const int col = 28 * h + 4 * g; bstore64((u32x2){cvtpk(o0[0] * inv, o0[1] * inv), cvtpk(o0[2] * inv, o0[3] * inv)}, rO, (unsigned)(T * 56 + (col >> 3)) * 512u + tok + (unsigned)((col >> 2) & 1) * 8u); }
    if (g < 3) { const int col = 28 * h + 16 + 4 * g; bstore64((u32x2){cvtpk(o1[0] * inv, o1[1] * inv), cvtpk(o1[2] * inv, o1[3] * inv)}, rO, (unsigned)(T * 56 + (col >> 3)) * 512u + tok + (unsigned)((col >> 2) & 1) * 8u); }
}

template <int N>
__device__ __forceinline__ void wo_issue(const u16* wo, int ct, unsigned lds_base, int pc0, int lane) {
    const int r8 = lane >> 3, dpos = lane & 7;
    const unsigned woff = (unsigned)r8 * 896u + (unsigned)((dpos ^ ((r8 & 2) << 1)) << 4);
#pragma unroll
    for (int i = 0; i < N; ++i) glds16((const char*)wo + (size_t)ct * 128 + (size_t)(pc0 + i) * 8 * 896, woff, lds_base + (pc0 + i) * 1024);
}
__device__ __forceinline__ void wf_read(u32x4 (&wf)[2][14], const char* img, int kh, int lane) {
    const int g16 = lane >> 4, q = (lane & 15) >> 2, p = lane & 3;
#pragma unroll
    for (int i = 0; i < 14; ++i)
#pragma unroll
        for (int c = 0; c < 2; ++c) {
            const int row = (kh * 14 + i) * 16 + 8 * (g16 >> 1) + q;
            lds_cptr wp = (lds_cptr)img + (unsigned)row * 128u + (unsigned)(((2 * c + (g16 & 1)) ^ (q & 2)) * 32) + (unsigned)p * 8u;
            wf[c][i] = __builtin_bit_cast(u32x4, cat8(vtr(wp), vtr(wp + 512)));
        }
}
__device__ __forceinline__ void attnA_phase(char* ws, const u16* woA, int oct, int h, int rb, u64 tag, int rt, int ct, bool has_qkv, bool first, const QkvOut& outB, u32x4 (&wf)[2][14], char* lds, int wid, int lane) {
    const bool hi = lane >= 32, pe = hi && !(h & 1), po = !hi && (h & 1);
    const int m0 = (28 * h) >> 3;
    const auto rK = RSRC(ws + WS_KA, QC_BYTES), rV = RSRC(ws + WS_VA, V_BYTES);
    const unsigned lo16 = (unsigned)lane * 16u;
    const unsigned kbase = (unsigned)((wid * 8) * 56 + m0) * 512u, vbase = (unsigned)(h * 64 + wid * 8) * 2048u;
    const int rot = rb & 7;
    u32x4 qf[4][2], kf[3][2], vf[3][2];
    {   const unsigned k0 = kbase + (unsigned)rot * TILE_B, v0 = vbase + (unsigned)rot * 2048u, k1 = kbase + (unsigned)((rot + 1) & 7) * TILE_B;
        kf[0][0] = bload128(rK, lo16, k0); kf[0][1] = bload128(rK, lo16, k0 + 1024u);
        if (wid >= 4) { const unsigned b = (unsigned)((rb * 4 + wid - 4) * 56 + m0) * 512u; const auto rQ = RSRC(ws + WS_QA, QC_BYTES); qf[0][0] = bload128(rQ, lo16, b); qf[0][1] = bload128(rQ, lo16, b + 1024u); }
        vf[0][0] = bload128(rV, lo16, v0); vf[0][1] = bload128(rV, lo16, v0 + 1024u);
        kf[1][0] = bload128(rK, lo16, k1); kf[1][1] = bload128(rK, lo16, k1 + 1024u);
        const unsigned v1 = vbase + (unsigned)((rot + 1) & 7) * 2048u, k2 = kbase + (unsigned)((rot + 2) & 7) * TILE_B;
        vf[1][0] = bload128(rV, lo16, v1); vf[1][1] = bload128(rV, lo16, v1 + 1024u);
        kf[2][0] = bload128(rK, lo16, k2); kf[2][1] = bload128(rK, lo16, k2 + 1024u); }
    patch(kf[0][0], kf[0][1], pe, po, 0x3F803F80u, 0u);
    if (wid >= 4) {
        patch(qf[0][0], qf[0][1], pe, po, 0u, 0u);
        f32x16 s = {}; s = MFMA32(asb(kf[0][0]), asb(qf[0][0]), s); s = MFMA32(asb(kf[0][1]), asb(qf[0][1]), s);
        const float m = xhalf_max(max16(s)) + POFF;
        const unsigned mh = cvtpk(-m, 0.f) & 0xffffu; const float r = -m - __uint_as_float(mh << 16);
        patch(qf[0][0], qf[0][1], pe, po, mh | (cvtpk(r, 0.f) << 16), 0u);
        *(u32x4*)(lds + LDS_Q + (wid - 4) * 2048 + lane * 16) = qf[0][0]; *(u32x4*)(lds + LDS_Q + (wid - 4) * 2048 + 1024 + lane * 16) = qf[0][1];
        asm volatile("s_waitcnt lgkmcnt(0)" ::: "memory");
        if (lane == 0) ctl_add(lds, CTL_QCNT);
    }
    ctl_wait(lds, CTL_QCNT, 4);
#pragma unroll
    for (int j = 0; j < 4; ++j) { qf[j][0] = *(const u32x4*)(lds + LDS_Q + j * 2048 + lane * 16); qf[j][1] = *(const u32x4*)(lds + LDS_Q + j * 2048 + 1024 + lane * 16); }
    if (has_qkv && wid < 4) {
        qkv_store<1, 0>(outB, ws, rt, ct, wid, lane); qkv_store<1, 1>(outB, ws, rt, ct, wid, lane); qkv_store<1, 2>(outB, ws, rt, ct, wid, lane);
        qkv_store<1, 3>(outB, ws, rt, ct, wid, lane); qkv_store<1, 4>(outB, ws, rt, ct, wid, lane); qkv_store<1, 5>(outB, ws, rt, ct, wid, lane);
    }
#pragma unroll
    for (int j = 0; j < 4; ++j) { PIN(qf[j][0]); PIN(qf[j][1]); }
    PIN(kf[0][0]); PIN(kf[0][1]);
    f32x16 o[4];
#define VEXP(d, s) asm volatile("v_exp_f32 %0, %1" : "=v"(d) : "v"(s))
#define VCVT(d, a, b) asm volatile("v_cvt_pk_bf16_f32 %0, %1, %2" : "=v"(d) : "v"(a), "v"(b))
#define MF0(d, a, b) asm volatile("v_mfma_f32_32x32x16_bf16 %0, %1, %2, 0" : "=&v"(d) : "v"(a), "v"(b))
#define MFA(d, a, b) asm volatile("v_mfma_f32_32x32x16_bf16 %0, %1, %2, %0" : "+v"(d) : "v"(a), "v"(b))
#define SLOT(PV, QK, Sc, Pc0, Pc1, Sn, kn0, kn1, qn0, qn1, Op, vp0, vp1, Pp0, Pp1, INJ) do { \
        float e0, e1, e2, e3, e4, e5, e6, e7, e8, e9, e10, e11, e12, e13, e14, e15; \
        if ((QK) != 0) MF0(Sn, kn0, qn0); \
        VEXP(e0, Sc[0]); VEXP(e1, Sc[1]); VEXP(e2, Sc[2]); VEXP(e3, Sc[3]); \
        if ((QK) != 0) MFA(Sn, kn1, qn1); \
        VEXP(e4, Sc[4]); VEXP(e5, Sc[5]); VEXP(e6, Sc[6]); VEXP(e7, Sc[7]); VCVT(Pc0[0], e0, e1); VCVT(Pc0[1], e2, e3); \
        INJ; \
        if ((PV) == 2) MFA(Op, vp0, Pp0); else if ((PV) == 1) MF0(Op, vp0, Pp0); \
        VEXP(e8, Sc[8]); VEXP(e9, Sc[9]); VEXP(e10, Sc[10]); VEXP(e11, Sc[11]); VCVT(Pc0[2], e4, e5); VCVT(Pc0[3], e6, e7); \
        if ((PV) != 0) MFA(Op, vp1, Pp1); \
        VEXP(e12, Sc[12]); VEXP(e13, Sc[13]); VEXP(e14, Sc[14]); VEXP(e15, Sc[15]); VCVT(Pc1[0], e8, e9); VCVT(Pc1[1], e10, e11); VCVT(Pc1[2], e12, e13); VCVT(Pc1[3], e14, e15); \
    } while (0)
    f32x16 sA, sB; u32x4 pA0, pA1, pB0, pB1;
    asm volatile("s_nop 1\n\tv_mfma_f32_32x32x16_bf16 %0, %1, %2, 0\n\tv_mfma_f32_32x32x16_bf16 %0, %3, %4, %0\n\ts_nop 7\n\ts_nop 3" : "=&v"(sA) : "v"(kf[0][0]), "v"(qf[0][0]), "v"(kf[0][1]), "v"(qf[0][1]));
#define PATCHK(kx) do { PIN(kx[0]); PIN(kx[1]); patch(kx[0], kx[1], pe, po, 0x3F803F80u, 0u); PIN(kx[0]); PIN(kx[1]); } while (0)
#define LOADK(kc, tt) do { if ((tt) < 8) { const unsigned o_ = kbase + (unsigned)(((tt) + rot) & 7) * TILE_B; kc[0] = bload128(rK, lo16, o_); kc[1] = bload128(rK, lo16, o_ + 1024u); } } while (0)
#define LOADV(vx, tt) do { if ((tt) < 8) { const unsigned o_ = vbase + (unsigned)(((tt) + rot) & 7) * 2048u; vx[0] = bload128(rV, lo16, o_); vx[1] = bload128(rV, lo16, o_ + 1024u); } } while (0)
#define TILE(t) do { \
        SLOT((t) == 0 ? 0 : (t) == 1 ? 1 : 2, true, sA, pA0, pA1, sB, kf[(t) % 3][0], kf[(t) % 3][1], qf[1][0], qf[1][1], o[3], vf[((t) + 2) % 3][0], vf[((t) + 2) % 3][1], pB0, pB1, (void)0); \
        LOADV(vf[((t) + 2) % 3], (t) + 2); \
        SLOT((t) == 0 ? 1 : 2, true, sB, pB0, pB1, sA, kf[(t) % 3][0], kf[(t) % 3][1], qf[2][0], qf[2][1], o[0], vf[(t) % 3][0], vf[(t) % 3][1], pA0, pA1, (void)0); \
        SLOT((t) == 0 ? 1 : 2, true, sA, pA0, pA1, sB, kf[(t) % 3][0], kf[(t) % 3][1], qf[3][0], qf[3][1], o[1], vf[(t) % 3][0], vf[(t) % 3][1], pB0, pB1, if ((t) < 7) PATCHK(kf[((t) + 1) % 3])); \
        LOADK(kf[(t) % 3], (t) + 3); \
        SLOT((t) == 0 ? 1 : 2, (t) < 7, sB, pB0, pB1, sA, kf[((t) + 1) % 3][0], kf[((t) + 1) % 3][1], qf[0][0], qf[0][1], o[2], vf[(t) % 3][0], vf[(t) % 3][1], pA0, pA1, (void)0); \
    } while (0)
    AttnBPre bpre; u64 flb = 0; bool bready = false;
#pragma unroll
    for (int t = 0; t < 8; t += 2) {
        if (t == 0) __builtin_amdgcn_s_setprio(3); else if (t == 2) __builtin_amdgcn_s_setprio(2); else if (t == 4) __builtin_amdgcn_s_setprio(1); else __builtin_amdgcn_s_setprio(0);
        TILE(t);
        if (t == 4) { bready = __all(flb == tag); attnB_load(bpre, ws, h, rb, wid, opaque_lane()); }
        TILE(t + 1);
        if (t == 0) {
            if (has_qkv && wid < 4) {
                asm volatile("s_waitcnt vmcnt(8)" ::: "memory");
                if (opaque_lane() == 0 && ctl_add(lds, CTL_F2CNT) == 3) flag_set(ws, F2 + ct * 32 + rt, tag);
            }
            if (oct < 7 && wid >= 4) {
                if (has_qkv) ctl_wait(lds, CTL_RDONE, 4);
                wo_issue<14>(woA, oct, (unsigned)(uintptr_t)lds, (wid - 4) * 14, opaque_lane());
            }
        } else if (t == 2) {
            const int ln = opaque_lane();
            if (oct < 7 && wid >= 4) { asm volatile("s_waitcnt vmcnt(8)" ::: "memory"); if (ln == 0) ctl_add(lds, CTL_WA); }
            flb = __hip_atomic_load(flagp(ws, f2_index(h, rb, ln)), __ATOMIC_RELAXED, __HIP_MEMORY_SCOPE_AGENT);
            if (first && wid == 1) { flag_wait(ws, F0 + (ln & 31), tag); if (ln == 0) ctl_store(lds, CTL_F0, 1); }
        }
    }
    asm volatile("s_nop 1\n\tv_mfma_f32_32x32x16_bf16 %0, %1, %2, %0\n\tv_mfma_f32_32x32x16_bf16 %0, %3, %4, %0\n\ts_nop 7\n\ts_nop 3" : "+v"(o[3]) : "v"(vf[1][0]), "v"(pB0), "v"(vf[1][1]), "v"(pB1));
    asm volatile("" : "+v"(o[0]), "+v"(o[1]), "+v"(o[2]));
    lane = opaque_lane();
    if (has_qkv) ctl_wait(lds, CTL_RDONE, 4);
#pragma unroll
    for (int j = 0; j < 4; ++j) merge_write1(o[j], lds, wid, j, lane);
    if (!bready) { flag_wait(ws, f2_index(h, rb, lane), tag); attnB_load(bpre, ws, h, rb, wid, lane); }
    PIN(bpre.q); PIN(bpre.kp); PIN(bpre.kc); PIN(bpre.v[0][0]); PIN(bpre.v[0][1]); PIN(bpre.v[1][0]); PIN(bpre.v[1][1]);
    if (oct < 7 && wid < 4) { ctl_wait(lds, CTL_WA, 4); wf_read(wf, lds, wid & 1, lane); }
    BARRIER();
    const bool bad = merge_reduce(ws, h, rb, lds, wid, lane);
    if (__any(bad) && lane == 0) ctl_store(lds, CTL_BAD, 1);
    attnB_compute(bpre, ws, h, rb, wid, lane);
    asm volatile("s_waitcnt vmcnt(2) lgkmcnt(0)" ::: "memory");
    if (lane == 0 && ctl_add(lds, CTL_F3CNT) == 7 && !ctl_load(lds, CTL_BAD)) flag_set(ws, F3 + rb * 16 + h, tag);
    asm volatile("s_waitcnt vmcnt(0)" ::: "memory");
    if (lane == 0 && ctl_add(lds, CTL_F4CNT) == 7) flag_set(ws, F4 + rb * 16 + h, tag);
    ctl_wait(lds, CTL_F3CNT, 8);
    if (__builtin_amdgcn_readfirstlane(ctl_load(lds, CTL_BAD))) {
        BARRIER();
        attnA_safe(ws, h, rb, lds, wid, lane);
        BARRIER();
        merge_reduce(ws, h, rb, lds, wid, lane);
        DRAIN_BARRIER();
        if (threadIdx.x == 0) flag_set(ws, F3 + rb * 16 + h, tag);
    }
}

__device__ __forceinline__ void oproj_compute(char* ws, const u16* woB, u16* out, int rt, int ct, u64 tag, bool first, u32x4 (&wf)[2][14], char* lds, int wid, int lane) {
    const int br = wid >> 2, tt = (wid >> 1) & 1, kh = wid & 1;
    if (wid < 3) {
        if (wid < 2) wo_issue<19>(woB, ct, (unsigned)(uintptr_t)lds + LDS_MRG, wid * 19, lane); else wo_issue<18>(woB, ct, (unsigned)(uintptr_t)lds + LDS_MRG, 38, lane);
    }
    if ((wid & 3) == 3) {
        flag_wait(ws, (br ? F4 : F3) + (rt >> 1) * 16 + (lane & 15), tag); if (lane == 0) ctl_store(lds, br ? CTL_F4 : CTL_F3, 1);
        if (first && !br) { ctl_wait(lds, CTL_F0, 1); if (lane == 0) flag_set(ws, FEP, tag); }
    }
    else {
        if (wid < 3) { asm volatile("s_waitcnt vmcnt(0)" ::: "memory"); if (lane == 0) ctl_add(lds, CTL_WB); }
        ctl_wait(lds, br ? CTL_F4 : CTL_F3, 1);
    }
    const auto rO = RSRC(ws + (br ? WS_OB : WS_OA), QC_BYTES);
    const int tile = rt * 2 + tt;
    u32x4 of[14];
#pragma unroll
    for (int i = 0; i < 14; ++i) of[i] = bload128(rO, (unsigned)lane * 16u, (unsigned)(tile * 56 + 2 * (kh * 14 + i)) * 512u);
    if (br) { ctl_wait(lds, CTL_WB, 3); wf_read(wf, lds + LDS_MRG, kh, lane); }
    f32x16 acc[2] = {};
#pragma unroll
    for (int i = 0; i < 14; ++i) { acc[0] = MFMA32(asb(wf[0][i]), asb(of[i]), acc[0]); acc[1] = MFMA32(asb(wf[1][i]), asb(of[i]), acc[1]); }
    asm volatile("" : "+v"(acc[0]), "+v"(acc[1]));
    {   const f32x16& gv = kh ? acc[0] : acc[1];
#pragma unroll
        for (int rq = 0; rq < 4; ++rq) *(f32x4*)(lds + 114688 + (wid * 4 + rq) * 1024 + lane * 16) = (f32x4){gv[4 * rq], gv[4 * rq + 1], gv[4 * rq + 2], gv[4 * rq + 3]};
    }
    asm volatile("s_waitcnt lgkmcnt(0)" ::: "memory");
    if (lane == 0) ctl_store(lds, CTL_X + wid * 4, 1);
    ctl_wait(lds, CTL_X + (wid ^ 1) * 4, 1);
    f32x16 r = kh ? acc[1] : acc[0];
#pragma unroll
    for (int rq = 0; rq < 4; ++rq) { const f32x4 x = *(const f32x4*)(lds + 114688 + ((wid ^ 1) * 4 + rq) * 1024 + lane * 16); r[4 * rq] += x[0]; r[4 * rq + 1] += x[1]; r[4 * rq + 2] += x[2]; r[4 * rq + 3] += x[3]; }
    const unsigned orow = (unsigned)(rt * 64 + tt * 32 + (lane & 31)) * 1792u + (unsigned)(br * 448 + ct * 64 + kh * 32 + (lane >> 5) * 8) * 2u;
#pragma unroll
    for (int pr = 0; pr < 2; ++pr) {
        const unsigned a0 = cvtpk(r[8 * pr], r[8 * pr + 1]), a1 = cvtpk(r[8 * pr + 2], r[8 * pr + 3]), b0 = cvtpk(r[8 * pr + 4], r[8 * pr + 5]), b1 = cvtpk(r[8 * pr + 6], r[8 * pr + 7]);
        auto r0 = __builtin_amdgcn_permlane32_swap(a0, b0, false, false);
        auto r1 = __builtin_amdgcn_permlane32_swap(a1, b1, false, false);
        bstore128((u32x4){r0[0], r1[0], r0[1], r1[1]}, RSRC(out, 2048 * 896 * 2), orow + pr * 32u);
    }
}

__global__ void __launch_bounds__(512, 2) fused_kernel(const u16* xA, const u16* wA, const u16* woA, const u16* xB, const u16* wB, const u16* woB, u16* out, char* ws) {
    extern __shared__ __attribute__((aligned(16))) char lds[];
    int lane = threadIdx.x & 63; const int wid = __builtin_amdgcn_readfirstlane(threadIdx.x >> 6);
    const int id = blockIdx.x, xcd = id & 7, loc = id >> 3;
    if (threadIdx.x < 32) ctl_store(lds, threadIdx.x * 4, 0);
    const u64 epoch = __hip_atomic_load(flagp(ws, FEP), __ATOMIC_RELAXED, __HIP_MEMORY_SCOPE_AGENT);
    const int gb = xcd & 1, grt = 8 * (xcd >> 1) + (loc & 7), qct = gb * 4 + (loc >> 3);
    const bool has_qkv = qct < 7;
    const int h = xcd * 2 + (loc & 1), rb = loc >> 1;
    const int ort = 4 * xcd + (loc & 3), oct = loc >> 2;
    unsigned warm0 = 0;
    if (wid < 4) {
        u64 pc; int len;
        asm volatile("s_getpc_b64 %0\n.Lfused_pc_ref:\n\ts_mov_b32 %1, .Lfused_code_end - .Lfused_pc_ref" : "=s"(pc), "=s"(len));
        const int off = (wid * 64 + lane) * 128;
        if (off < len) warm0 = *(const __attribute__((address_space(1))) unsigned*)(pc + off);
    }
    QkvOut outB;
    if (has_qkv) qkv_phase(xA, wA, xB, wB, ws, grt, qct, epoch, lds, wid, lane, outB);
    else { const u64 t0 = next_tag(epoch); if (threadIdx.x == 0) flag_set(ws, F0 + (xcd >> 1) * 8 + (loc & 7), t0); BARRIER(); }
    asm volatile("" :: "v"(warm0));
    const u64 tag = next_tag(epoch);
    if (wid == 4) {
        flag_wait(ws, F1 + (lane < 32 ? (28 * h) >> 6 : (28 * h + 27) >> 6) * 32 + (lane & 31), tag);
        if (lane == 0) ctl_store(lds, CTL_F1, 1);
    } else ctl_wait(lds, CTL_F1, 1);
    const u64 tags = (u64)(unsigned)__builtin_amdgcn_readfirstlane((unsigned)tag) | ((u64)(unsigned)__builtin_amdgcn_readfirstlane((unsigned)(tag >> 32)) << 32);
    u32x4 wf[2][14];
    attnA_phase(ws, woA, oct, h, rb, tags, grt, qct, has_qkv, id == 0, outB, wf, lds, wid, lane);
    lane = opaque_lane();
    if (oct < 7) {
        oproj_compute(ws, woB, out, ort, oct, tags, id == 0, wf, lds, wid, lane);
    }
    asm volatile(".Lfused_code_end:");
}

extern "C" void kernel_launch(void* const* d_in, const int* in_sizes, int n_in, void* d_out, int out_size, void* d_ws, size_t ws_size, hipStream_t stream) {
    const u16* xA = (const u16*)d_in[0]; const u16* wA = (const u16*)d_in[1]; const u16* woA = (const u16*)d_in[2];
    const u16* xB = (const u16*)d_in[3]; const u16* wB = (const u16*)d_in[4]; const u16* woB = (const u16*)d_in[5];
    (void)hipFuncSetAttribute((const void*)fused_kernel, hipFuncAttributeMaxDynamicSharedMemorySize, LDS_TOTAL);
    fused_kernel<<<256, 512, LDS_TOTAL, stream>>>(xA, wA, woA, xB, wB, woB, (u16*)d_out, (char*)d_ws);
}
```
